```python
import torch

def _softmax(x, axis=-1):
    return torch.softmax(x, dim=axis)
def _gelu(x):
    return torch.nn.functional.gelu(x, approximate="tanh")
def _silu(x):
    return torch.nn.functional.silu(x)
def _sigmoid(x):
    return torch.sigmoid(x)
def _layernorm(x, axis=-1, eps=1e-5):
    m = x.mean(dim=axis, keepdim=True)
    v = x.var(dim=axis, keepdim=True, unbiased=False)
    return (x - m) / torch.sqrt(v + eps)
def _scatter_add(tgt, idx, src, axis=0):
    return tgt.clone().index_add_(axis, idx, src)
def _segment_sum(src, seg, n):
    out = torch.zeros((n,) + src.shape[1:], dtype=src.dtype)
    return out.index_add_(0, seg, src)
def _pad(x, pw):
    flat = tuple(v for pair in reversed(pw) for v in pair)
    return torch.nn.functional.pad(x, flat)

kernel_name = 'interleave_layers__mha_block__glu_gated_reglu__prompt'

def setup_inputs():

    _w = (lambda a: (lambda t: t.to(torch.bfloat16) if t.is_floating_point() else t)(torch.as_tensor(a)))
    def _A_setup():
        B, S, D, H = 1, 2048, 640, 32
        return {
            "x": _w((lambda *s: torch.randn(*s, dtype=torch.bfloat16) * 0.05)(B, S, D)),
            "wqkv": _w((lambda *s: torch.randn(*s, dtype=torch.bfloat16) * 0.05)(D, 3 * D)),
            "wo": _w((lambda *s: torch.randn(*s, dtype=torch.bfloat16) * 0.05)(D, D)),
        }
    _A_in = _A_setup()

    def _B_setup():
        B, S, D, F = 1, 2048, 640, 1728
        return {
            "x": _w((lambda *s: torch.randn(*s, dtype=torch.bfloat16) * 0.05)(B, S, D)),
            "W_gate": _w((lambda *s: torch.randn(*s, dtype=torch.bfloat16) * 0.8)(D, F) * (2.0 / D) ** 0.5),
            "W_up": _w((lambda *s: torch.randn(*s, dtype=torch.bfloat16) * 0.8)(D, F) * (2.0 / D) ** 0.5),
            "W_down": _w((lambda *s: torch.randn(*s, dtype=torch.bfloat16) * 0.8)(F, D) * (1.0 / F) ** 0.5),
        }
    _B_in = _B_setup()

    return {
        "A_x": _A_in["x"],
        "A_wqkv": _A_in["wqkv"],
        "A_wo": _A_in["wo"],
        "B_x": _B_in["x"],
        "B_W_gate": _B_in["W_gate"],
        "B_W_up": _B_in["W_up"],
        "B_W_down": _B_in["W_down"],
    }

def reference(A_x, A_wqkv, A_wo, B_x, B_W_gate, B_W_up, B_W_down):
    def _A_ref(x, wqkv, wo):
        B, S, D = x.shape
        H = 32
        Dh = D // H
        qkv = (lambda x, *d: x.permute(*d))((x @ wqkv).reshape(B, S, 3, H, Dh), 2, 0, 3, 1, 4)
        q, k, v = qkv[0], qkv[1], qkv[2]
        scores = torch.einsum("bhqd,bhkd->bhqk", q, k) * (Dh ** -0.5)
        p = _softmax(scores, axis=-1)
        o = torch.einsum("bhqk,bhkd->bhqd", p, v)
        return (lambda x, *d: x.permute(*d))(o, 0, 2, 1, 3).reshape(B, S, D) @ wo

    def _B_ref(x, W_gate, W_up, W_down):
        gate = torch.einsum("bsd,df->bsf", x, W_gate)
        up = torch.einsum("bsd,df->bsf", x, W_up)
        gate = gate * (lambda x, dt: x.to(dt))(gate > 0, gate.dtype)
        h = gate * up
        return torch.einsum("bsf,fd->bsd", h, W_down)

    x = A_x
    x = x + _A_ref(_layernorm(x, axis=-1), A_wqkv, A_wo)
    x = x + _B_ref(_layernorm(x, axis=-1), B_W_gate, B_W_up, B_W_down)
    x = x + _A_ref(_layernorm(x, axis=-1), A_wqkv, A_wo)
    x = x + _B_ref(_layernorm(x, axis=-1), B_W_gate, B_W_up, B_W_down)
    return x
```

```cpp
#include <hip/hip_runtime.h>
#include <hip/hip_bf16.h>
#include <stdint.h>

typedef __attribute__((ext_vector_type(8))) __bf16 bf16x8;
typedef __attribute__((ext_vector_type(16))) __bf16 bf16x16;
typedef __attribute__((ext_vector_type(4))) short s16x4;
typedef __attribute__((ext_vector_type(2))) __bf16 bf16x2;
typedef __attribute__((ext_vector_type(2))) float f32x2;
typedef __attribute__((ext_vector_type(4))) float f32x4;
typedef __attribute__((ext_vector_type(8))) float f32x8;
typedef __attribute__((ext_vector_type(16))) float f32x16;
typedef __attribute__((ext_vector_type(2))) unsigned u32x2;
typedef __attribute__((ext_vector_type(4))) unsigned u32x4;
typedef __attribute__((address_space(3))) char* lds_ptr;
#define DI __device__ __forceinline__

constexpr int S_ = 2048, D_ = 640, F_ = 1728;
constexpr unsigned WS_WQKV = 0;
constexpr unsigned WS_WO   = WS_WQKV + 2048u * 640 * 2;
constexpr unsigned WS_WGU  = WS_WO + 640u * 640 * 2;
constexpr unsigned WS_WD   = WS_WGU + 3456u * 640 * 2;
constexpr unsigned WS_X0   = WS_WD + 640u * 1728 * 2;
constexpr unsigned XB      = 2048u * 640 * 2;
constexpr unsigned WS_X1   = WS_X0 + XB;
constexpr unsigned WS_X2   = WS_X1 + XB;
constexpr unsigned WS_X3   = WS_X2 + XB;
constexpr unsigned WS_Q    = WS_X3 + XB;
constexpr unsigned WS_K    = WS_Q + XB;
constexpr unsigned WS_V    = WS_K + XB;
constexpr unsigned WS_O    = WS_V + XB;
constexpr unsigned WS_H    = WS_O + XB;
constexpr unsigned WS_CSQ  = WS_H + 2048u * 1728 * 2;
constexpr unsigned WS_CSG  = WS_CSQ + 2048u * 4;
constexpr unsigned WS_ST0  = WS_CSG + 4096u * 4;
constexpr unsigned STB     = 2048u * 8 * 8;
constexpr unsigned WS_ST1  = WS_ST0 + STB;
constexpr unsigned WS_ST2  = WS_ST1 + STB;
constexpr unsigned WS_ST3  = WS_ST2 + STB;

DI unsigned pk2(float a, float b) { f32x2 v = {a, b}; bf16x2 r = __builtin_convertvector(v, bf16x2); return __builtin_bit_cast(unsigned, r); }
DI float bflo(unsigned u) { return __uint_as_float(u << 16); }
DI float bfhi(unsigned u) { return __uint_as_float(u & 0xffff0000u); }
DI f32x4 mfma16(bf16x8 a, bf16x8 b, f32x4 c) { return __builtin_amdgcn_mfma_f32_16x16x32_bf16(a, b, c, 0, 0, 0); }
DI bf16x8 ldsr16(lds_ptr p) { return *(const __attribute__((address_space(3))) bf16x8*)p; }
#define BARRIER() asm volatile("s_waitcnt lgkmcnt(0)\n\ts_barrier" ::: "memory")
template <int N> DI void wait_vm() { asm volatile("s_waitcnt vmcnt(%0)" ::"n"(N) : "memory"); }
template <int OFF> DI void glds(unsigned voff, const char* sbase) {
  asm volatile("global_load_lds_dwordx4 %0, %1 offset:%2" ::"v"(voff), "s"(sbase), "n"(OFF) : "memory");
}
DI void set_m0(unsigned v) { asm volatile("s_mov_b32 m0, %0\n\ts_nop 0" ::"s"(v) : "memory"); }

__global__ __launch_bounds__(256) void prep_kernel(const uint16_t* __restrict__ x, const uint16_t* __restrict__ wqkv,
                                                   const uint16_t* __restrict__ wo, const uint16_t* __restrict__ wg,
                                                   const uint16_t* __restrict__ wu, const uint16_t* __restrict__ wd, char* __restrict__ ws) {
  const int lane = threadIdx.x & 63, i = lane & 15, g = lane >> 4;
  const int u = blockIdx.x * 4 + (threadIdx.x >> 6);
  if (u < 504) {
    const uint16_t* src; int ld, col, ks0 = 0, nks = 20, kd8 = 80, nt; unsigned dst; float* cs = nullptr; bool perm = false;
    if (u < 128) {
      nt = u; const int hp = u >> 3, c = (u & 7) * 16 + i;
      col = c < 120 ? (c / 40) * 640 + hp * 40 + (c % 40) : -1;
      src = wqkv; ld = 1920; dst = WS_WQKV; cs = (float*)(ws + WS_CSQ);
    } else if (u < 168) {
      nt = u - 128; col = nt * 16 + i; src = wo; ld = 640; dst = WS_WO; perm = true;
    } else if (u < 384) {
      nt = u - 168; col = (nt >> 1) * 16 + i; src = (nt & 1) ? wu : wg; ld = 1728; dst = WS_WGU; cs = (float*)(ws + WS_CSG);
    } else {
      const int v = u - 384; nt = (v / 12) * 4 + (v & 3); col = nt * 16 + i; src = wd; ld = 640; dst = WS_WD;
      ks0 = ((v >> 2) % 3) * 18; nks = 18; kd8 = 216;
    }
    float sum = 0.f;
#pragma unroll 2
    for (int ks = ks0; ks < ks0 + nks; ++ks) {
      unsigned e[8];
#pragma unroll
      for (int j = 0; j < 8; ++j) {
        int k = ks * 32 + g * 8 + j;
        if (perm) k = k < 512 ? (k >> 4) * 20 + (k & 15) : (((k - 512) >> 3) * 2 + (((k - 512) >> 2) & 1)) * 20 + 16 + (k & 3);
        e[j] = col >= 0 ? src[(size_t)k * ld + col] : 0;
      }
      u32x4 o;
#pragma unroll
      for (int j = 0; j < 4; ++j) { o[j] = e[2 * j] | (e[2 * j + 1] << 16); sum += bflo(e[2 * j]) + bflo(e[2 * j + 1]); }
      *(u32x4*)(ws + dst + ((size_t)nt * kd8 + ks * 4) * 256 + lane * 16) = o;
    }
    if (cs) { sum += __shfl_xor(sum, 16); sum += __shfl_xor(sum, 32); if (g == 0) cs[nt * 16 + i] = sum; }
  } else if (u < 632) {
    const int mt = u - 504;
    u32x4 v[20]; float sum = 0.f;
#pragma unroll
    for (int s = 0; s < 20; ++s) v[s] = *(const u32x4*)(x + (size_t)(mt * 16 + i) * 640 + s * 32 + g * 8);
#pragma unroll
    for (int s = 0; s < 20; ++s) {
      *(u32x4*)(ws + WS_X0 + ((size_t)mt * 80 + s * 4) * 256 + lane * 16) = v[s];
#pragma unroll
      for (int j = 0; j < 4; ++j) sum += bflo(v[s][j]) + bfhi(v[s][j]);
    }
    sum += __shfl_xor(sum, 16); sum += __shfl_xor(sum, 32);
    const float mean = sum * (1.f / 640.f); float m2 = 0.f;
#pragma unroll
    for (int s = 0; s < 20; ++s)
#pragma unroll
      for (int j = 0; j < 4; ++j) { const float a = bflo(v[s][j]) - mean, b = bfhi(v[s][j]) - mean; m2 += a * a + b * b; }
    m2 += __shfl_xor(m2, 16); m2 += __shfl_xor(m2, 32);
    f32x4 o = {sum * 0.125f, m2 * 0.125f, sum * 0.125f, m2 * 0.125f};
    *(f32x4*)(ws + WS_ST0 + (size_t)(mt * 16 + i) * 64 + g * 16) = o;
  }
}

template <int MT, int NT, int KS, int NST, int NLW, int NS>
DI void gemm_loader(const char* ws, unsigned a_off, unsigned a_stride, unsigned b_off, unsigned b_stride, int lw, int lane) {
  constexpr int P = (MT + NT) * KS, PP = P / NLW, STAGE = P * 1024;
  static_assert(P % NLW == 0 && (NST - 1) * PP <= 63, "loader split");
  unsigned voff[PP];
#pragma unroll
  for (int p = 0; p < PP; ++p) {
    const int q = lw * PP + p, t = q / KS, ks = q % KS;
    voff[p] = (t < MT ? a_off + t * a_stride : b_off + (t - MT) * b_stride) + ks * 1024 + lane * 16 - (p & 3) * 1024 + 4096;
  }
  const char* base = ws - 4096;
  const unsigned l0 = lw * PP * 1024;
  auto issue = [&](int slot) {
#pragma unroll
    for (int p = 0; p < PP; ++p) {
      if ((p & 3) == 0) set_m0(l0 + slot * STAGE + p * 1024);
      if ((p & 3) == 0) glds<0>(voff[p], base);
      if ((p & 3) == 1) glds<1024>(voff[p], base);
      if ((p & 3) == 2) glds<2048>(voff[p], base);
      if ((p & 3) == 3) glds<3072>(voff[p], base);
    }
    base += KS * 1024;
  };
#pragma unroll
  for (int s = 0; s < NST; ++s) issue(s);
  wait_vm<(NST - 1) * PP>();
  BARRIER();
  int slot = 0;
  for (int k = 1; k <= NS - NST; ++k) {
    wait_vm<(NST - 2) * PP>();
    BARRIER();
    issue(slot);
    slot = slot == NST - 1 ? 0 : slot + 1;
  }
#pragma unroll
  for (int r = NST - 2; r >= 0; --r) {
    if (r == 7) wait_vm<(7 * PP > 63 ? 63 : 7 * PP)>();
    if (r == 6) wait_vm<(6 * PP > 63 ? 63 : 6 * PP)>();
    if (r == 5) wait_vm<(5 * PP > 63 ? 63 : 5 * PP)>();
    if (r == 4) wait_vm<(4 * PP > 63 ? 63 : 4 * PP)>();
    if (r == 3) wait_vm<(3 * PP > 63 ? 63 : 3 * PP)>();
    if (r == 2) wait_vm<2 * PP>();
    if (r == 1) wait_vm<PP>();
    if (r == 0) wait_vm<0>();
    BARRIER();
  }
}

template <int MT, int NT, int NST, int NS, int WA, int WB, class MM>
DI void gemm_consume(lds_ptr lds, int a0, int b0, int lane, MM mm) {
  constexpr int KS = 2, STAGE = (MT + NT) * KS * 1024;
  bf16x8 xa0[WA], wb0[WB], xa1[WA], wb1[WB];
  lds_ptr pa = lds + a0 * KS * 1024 + lane * 16, pb = lds + (MT + b0) * KS * 1024 + lane * 16;
  BARRIER();
#pragma unroll
  for (int t = 0; t < WA; ++t) xa0[t] = ldsr16(pa + t * 2048);
#pragma unroll
  for (int t = 0; t < WB; ++t) wb0[t] = ldsr16(pb + t * 2048);
  int slot = 0;
  for (int st = 0; st < NS; ++st) {
#pragma unroll
    for (int t = 0; t < WA; ++t) xa1[t] = ldsr16(pa + t * 2048 + 1024);
#pragma unroll
    for (int t = 0; t < WB; ++t) wb1[t] = ldsr16(pb + t * 2048 + 1024);
    mm(xa0, wb0);
    if (st + 1 < NS) {
      const int d = slot == NST - 1 ? -(NST - 1) * STAGE : STAGE;
      slot = slot == NST - 1 ? 0 : slot + 1;
      pa += d; pb += d;
      BARRIER();
#pragma unroll
      for (int t = 0; t < WA; ++t) xa0[t] = ldsr16(pa + t * 2048);
#pragma unroll
      for (int t = 0; t < WB; ++t) wb0[t] = ldsr16(pb + t * 2048);
    }
    mm(xa1, wb1);
  }
}

DI f32x2 row_stats(const char* st, int row) {
  f32x4 p[4];
#pragma unroll
  for (int j = 0; j < 4; ++j) p[j] = *(const f32x4*)(st + (size_t)row * 64 + j * 16);
  float sum = 0.f;
#pragma unroll
  for (int j = 0; j < 4; ++j) sum += p[j][0] + p[j][2];
  const float mean = sum * (1.f / 640.f); float m2 = 0.f;
#pragma unroll
  for (int j = 0; j < 4; ++j) {
    const float a = p[j][0] * (1.f / 80.f) - mean, b = p[j][2] * (1.f / 80.f) - mean;
    m2 += p[j][1] + p[j][3] + 80.f * (a * a + b * b);
  }
  return f32x2{mean, rsqrtf(m2 * (1.f / 640.f) + 1e-5f)};
}

constexpr int QKV_NST = 4, QKV_LDS = QKV_NST * 32768 + 1024;
__global__ __launch_bounds__(512) void qkv_kernel(char* __restrict__ ws, unsigned x_off, unsigned st_off) {
  extern __shared__ __attribute__((aligned(16))) char smem[];
  lds_ptr lds = (lds_ptr)smem;
  const int lane = threadIdx.x & 63, w = __builtin_amdgcn_readfirstlane(threadIdx.x >> 6);
  const int xcd = blockIdx.x & 7, j = blockIdx.x >> 3;
  const int panel = (xcd & 3) * 4 + (j & 3), hp = (xcd >> 2) * 8 + (j >> 2);
  if (w >= 4) {
    gemm_loader<8, 8, 2, QKV_NST, 4, 10>(ws, x_off + panel * 8 * 20480, 20480, WS_WQKV + hp * 8 * 20480, 20480, w - 4, lane);
    return;
  }
  __attribute__((address_space(3))) f32x2* sst = (__attribute__((address_space(3))) f32x2*)(lds + QKV_NST * 32768);
  if (lane < 32) sst[w * 32 + lane] = row_stats(ws + st_off, panel * 128 + w * 32 + lane);
  const int i = lane & 15, g = lane >> 4;
  const float* cs = (const float*)(ws + WS_CSQ) + hp * 128;
  f32x4 cs4[5]; float cs1[3];
#pragma unroll
  for (int t = 0; t < 5; ++t) cs4[t] = *(const f32x4*)(cs + t * 16 + g * 4);
#pragma unroll
  for (int t = 0; t < 3; ++t) cs1[t] = cs[80 + t * 16 + i];
  f32x4 acc[2][8];
#pragma unroll
  for (int m = 0; m < 2; ++m)
#pragma unroll
    for (int t = 0; t < 8; ++t) acc[m][t] = f32x4{0.f, 0.f, 0.f, 0.f};
  gemm_consume<8, 8, QKV_NST, 10, 2, 8>(lds, w * 2, 0, lane, [&](const bf16x8* xa, const bf16x8* wb) {
#pragma unroll
    for (int t = 0; t < 8; ++t)
#pragma unroll
      for (int m = 0; m < 2; ++m) acc[m][t] = t < 5 ? mfma16(wb[t], xa[m], acc[m][t]) : mfma16(xa[m], wb[t], acc[m][t]);
  });
  const float qs = 0.22360679774997896f * 1.4426950408889634f;
#pragma unroll
  for (int m = 0; m < 2; ++m) {
    const int tok0 = panel * 128 + w * 32 + m * 16;
    {
      const f32x2 ms = sst[w * 32 + m * 16 + i];
      const int tok = tok0 + i;
#pragma unroll
      for (int t = 0; t < 5; ++t) {
        const int c = t * 16 + g * 4, isk = c >= 40, cc = c - isk * 40, hh = cc >= 20, d0 = cc - hh * 20;
        const float sc = isk ? ms[1] : ms[1] * qs;
        u32x2 o;
        o[0] = pk2((acc[m][t][0] - ms[0] * cs4[t][0]) * sc, (acc[m][t][1] - ms[0] * cs4[t][1]) * sc);
        o[1] = pk2((acc[m][t][2] - ms[0] * cs4[t][2]) * sc, (acc[m][t][3] - ms[0] * cs4[t][3]) * sc);
        const unsigned off = (isk ? WS_K : WS_Q) + (hp * 2 + hh) * 81920 + (tok >> 5) * 1280 +
                             (d0 < 16 ? (d0 >> 3) * 512 + (tok & 31) * 16 + (d0 & 4) * 2 : 1024 + (tok & 31) * 8);
        *(u32x2*)(ws + off) = o;
      }
    }
    {
      f32x2 ms[4];
#pragma unroll
      for (int r = 0; r < 4; ++r) ms[r] = sst[w * 32 + m * 16 + g * 4 + r];
#pragma unroll
      for (int t = 0; t < 3; ++t) {
        const int cv = t * 16 + i, hh = cv >= 20, d = cv - hh * 20;
        u32x2 o;
        o[0] = pk2((acc[m][5 + t][0] - ms[0][0] * cs1[t]) * ms[0][1], (acc[m][5 + t][1] - ms[1][0] * cs1[t]) * ms[1][1]);
        o[1] = pk2((acc[m][5 + t][2] - ms[2][0] * cs1[t]) * ms[2][1], (acc[m][5 + t][3] - ms[3][0] * cs1[t]) * ms[3][1]);
        if (cv < 40) *(u32x2*)(ws + WS_V + (hp * 2 + hh) * 81920 + (tok0 >> 4) * 640 + (g >> 1) * 320 + d * 16 + (g & 1) * 8) = o;
      }
    }
  }
}

constexpr int AT_NKP = 2, AT_NW = 4 * AT_NKP, AT_SLOT = 40960, AT_ONES = 3 * AT_SLOT, AT_LDS = AT_ONES + 20480 + 1024;
DI f32x16 qk_tile(bf16x8 k1, s16x4 k2, bf16x8 q1, s16x4 q2) {
  f32x16 s = __builtin_amdgcn_mfma_f32_32x32x8bf16_1k(k2, q2, f32x16{}, 0, 0, 0);
  return __builtin_amdgcn_mfma_f32_32x32x16_bf16(k1, q1, s, 0, 0, 0);
}
DI f32x16 pv_tile(bf16x8 v0, bf16x8 v1, f32x16 p, f32x16 o) {
  const f32x8 lo = __builtin_shufflevector(p, p, 0, 1, 2, 3, 4, 5, 6, 7), hi = __builtin_shufflevector(p, p, 8, 9, 10, 11, 12, 13, 14, 15);
  o = __builtin_amdgcn_smfmac_f32_32x32x32_bf16(v0, __builtin_bit_cast(bf16x16, lo), o, 0xDDDD, 0, 0);
  return __builtin_amdgcn_smfmac_f32_32x32x32_bf16(v1, __builtin_bit_cast(bf16x16, hi), o, 0xDDDD, 0, 0);
}
DI float swap32(float v) {
  auto r = __builtin_amdgcn_permlane32_swap(__float_as_uint(v), __float_as_uint(v), false, false);
  return __uint_as_float((threadIdx.x & 32) ? r[0] : r[1]);
}
__global__ __launch_bounds__(64 * AT_NW) void attn_kernel(char* __restrict__ ws) {
  extern __shared__ __attribute__((aligned(16))) char smem[];
  lds_ptr lds = (lds_ptr)smem;
  const int lane = threadIdx.x & 63, w = __builtin_amdgcn_readfirstlane(threadIdx.x >> 6);
  const int xcd = blockIdx.x & 7, j = blockIdx.x >> 3;
  const int head = xcd * 4 + (j & 3), qb = j >> 2;
  const int qp = w & 3, kp = w >> 2, r32 = lane & 31, h = lane >> 5;
  const char* kv = ws + ((w & 4) ? WS_V : WS_K) + head * 81920 + (w & 3) * 5120;
  auto issue = [&](int grp, int slot) {
    if (w < 8) {
      const char* b = kv + grp * 20480;
      const unsigned l = slot * AT_SLOT + w * 5120;
      set_m0(l); glds<0>(lane * 16, b); glds<1024>(lane * 16, b); glds<2048>(lane * 16, b); glds<3072>(lane * 16, b);
      set_m0(l + 4096); glds<0>(lane * 16, b + 4096);
    }
  };
  bf16x8 q1[2]; s16x4 q2[2];
  const char* qimg = ws + WS_Q + head * 81920 + (qb * 8 + qp * 2) * 1280;
#pragma unroll
  for (int t = 0; t < 2; ++t) {
    q1[t] = *(const bf16x8*)(qimg + t * 1280 + lane * 16);
    q2[t] = *(const s16x4*)(qimg + t * 1280 + 1024 + r32 * 8);
    if (h) q2[t] = s16x4{0, 0, 0, 0};
  }
  asm volatile("" : "+v"(q1[0]), "+v"(q1[1]), "+v"(q2[0]), "+v"(q2[1]));
  issue(0, 0); issue(1, 1); issue(2, 2);
  for (int o = threadIdx.x * 16; o < 20480 + 1024; o += AT_NW * 64 * 16)
    *(__attribute__((address_space(3))) u32x4*)(lds + AT_ONES + o) = u32x4{0x3f803f80u, 0x3f803f80u, 0x3f803f80u, 0x3f803f80u};
  const bool ones = r32 >= 20 && r32 < 28;
  const int voff = h * 320 + (r32 < 20 ? r32 : ones ? 20 : r32 - 12) * 16 + kp * 1280;
  f32x16 o[2] = {f32x16{}, f32x16{}};
  auto group = [&](int slot) {
    lds_ptr pk1 = lds + slot * AT_SLOT + kp * 1280 + lane * 16, pk2_ = lds + slot * AT_SLOT + kp * 1280 + 1024 + r32 * 8;
    lds_ptr pv = lds + (ones ? AT_ONES : slot * AT_SLOT + 20480) + voff;
#pragma unroll
    for (int t = 0; t < 16 / AT_NKP; ++t) {
      const int to = t * AT_NKP * 1280;
      const bf16x8 k1 = ldsr16(pk1 + to);
      const s16x4 k2 = *(const __attribute__((address_space(3))) s16x4*)(pk2_ + to);
      const bf16x8 v0 = ldsr16(pv + to), v1 = ldsr16(pv + to + 640);
#pragma unroll
      for (int q = 0; q < 2; ++q) {
        f32x16 s = qk_tile(k1, k2, q1[q], q2[q]);
#pragma unroll
        for (int r = 0; r < 16; ++r) s[r] = __builtin_amdgcn_exp2f(s[r]);
        o[q] = pv_tile(v0, v1, s, o[q]);
      }
    }
  };
  wait_vm<10>(); BARRIER();
  group(0);
  wait_vm<0>(); BARRIER();
  issue(3, 0);
  group(1);
  group(2);
  wait_vm<0>(); BARRIER();
  group(0);
  BARRIER();
#pragma unroll
  for (int q = 0; q < 2; ++q)
#pragma unroll
    for (int c = 0; c < 3; ++c)
      *(__attribute__((address_space(3))) f32x4*)(lds + ((w * 2 + q) * 3 + c) * 1024 + lane * 16) =
          f32x4{o[q][4 * c], o[q][4 * c + 1], o[q][4 * c + 2], o[q][4 * c + 3]};
  BARRIER();
  if (w >= 8) return;
  const int qt = (w & 3) * 2 + (w >> 2);
  f32x4 a[3];
#pragma unroll
  for (int c = 0; c < 3; ++c) {
    a[c] = f32x4{0.f, 0.f, 0.f, 0.f};
#pragma unroll
    for (int p = 0; p < AT_NKP; ++p)
      a[c] += *(const __attribute__((address_space(3))) f32x4*)(lds + (((p * 4 + (qt >> 1)) * 2 + (qt & 1)) * 3 + c) * 1024 + lane * 16);
  }
  float l = h ? a[2][0] : 0.f;
  l += swap32(l);
  if (__builtin_expect(__any(!(l >= 5.421e-20f && l <= 1.8447e19f)), 0)) {
    const char* qi = ws + WS_Q + head * 81920 + (qb * 8 + qt) * 1280;
    const bf16x8 fq1 = *(const bf16x8*)(qi + lane * 16);
    s16x4 fq2 = *(const s16x4*)(qi + 1024 + r32 * 8);
    if (h) fq2 = s16x4{0, 0, 0, 0};
    const char* ki = ws + WS_K + head * 81920;
    const char* vi = ws + WS_V + head * 81920 + h * 320 + (r32 < 20 ? r32 : 0) * 16;
    f32x16 oo = f32x16{};
    float m = -INFINITY;
    const bf16x8 one8 = __builtin_bit_cast(bf16x8, u32x4{0x3f803f80u, 0x3f803f80u, 0x3f803f80u, 0x3f803f80u});
    for (int t = 0; t < 64; ++t) {
      const bf16x8 k1 = *(const bf16x8*)(ki + t * 1280 + lane * 16);
      const s16x4 k2 = *(const s16x4*)(ki + t * 1280 + 1024 + r32 * 8);
      bf16x8 v0 = *(const bf16x8*)(vi + t * 1280), v1 = *(const bf16x8*)(vi + t * 1280 + 640);
      if (r32 >= 20) { v0 = one8; v1 = one8; }
      f32x16 s = qk_tile(k1, k2, fq1, fq2);
      float tm = s[0];
#pragma unroll
      for (int r = 1; r < 16; ++r) tm = fmaxf(tm, s[r]);
      tm = fmaxf(tm, swap32(tm));
      const float mn = fmaxf(m, tm), al = __builtin_amdgcn_exp2f(m - mn);
      m = mn;
#pragma unroll
      for (int r = 0; r < 16; ++r) { s[r] = __builtin_amdgcn_exp2f(s[r] - mn); oo[r] *= al; }
      oo = pv_tile(v0, v1, s, oo);
    }
#pragma unroll
    for (int c = 0; c < 3; ++c) a[c] = f32x4{oo[4 * c], oo[4 * c + 1], oo[4 * c + 2], oo[4 * c + 3]};
    l = h ? a[2][0] : 0.f;
    l += swap32(l);
  }
  const float inv = 1.f / l;
  unsigned A0 = pk2(a[0][0] * inv, a[0][1] * inv), A1 = pk2(a[0][2] * inv, a[0][3] * inv);
  unsigned B0 = pk2(a[1][0] * inv, a[1][1] * inv), B1 = pk2(a[1][2] * inv, a[1][3] * inv);
  const u32x2 C = {pk2(a[2][0] * inv, a[2][1] * inv), pk2(a[2][2] * inv, a[2][3] * inv)};
  auto s0 = __builtin_amdgcn_permlane32_swap(A0, B0, false, false);
  auto s1 = __builtin_amdgcn_permlane32_swap(A1, B1, false, false);
  const int tok = qb * 256 + qt * 32 + r32;
  char* ob = ws + WS_O + (tok >> 4) * 20480 + (tok & 15) * 16;
  *(u32x4*)(ob + (head * 2 + h) * 256) = u32x4{s0[0], s1[0], s0[1], s1[1]};
  if (!h) *(u32x2*)(ob + (64 + (head >> 1)) * 256 + (head & 1) * 8) = C;
}

constexpr int PR_NST = 8, PR_NLW = 3, PR_LDS = PR_NST * 18432;
template <int NS, bool FINAL>
__global__ __launch_bounds__(64 * (4 + PR_NLW)) void proj_kernel(char* __restrict__ ws, unsigned a_off, unsigned w_off, unsigned res_off,
                                                                 unsigned out_off, unsigned st_off, uint16_t* __restrict__ out) {
  extern __shared__ __attribute__((aligned(16))) char smem[];
  lds_ptr lds = (lds_ptr)smem;
  const int lane = threadIdx.x & 63, w = __builtin_amdgcn_readfirstlane(threadIdx.x >> 6);
  const int xcd = blockIdx.x & 7, j = blockIdx.x >> 3;
  const int panel = (xcd & 3) * 8 + (j & 7), cb = (xcd >> 2) * 4 + (j >> 3);
  constexpr unsigned TS = NS * 2048;
  if (w >= 4) {
    gemm_loader<4, 5, 2, PR_NST, PR_NLW, NS>(ws, a_off + panel * 4 * TS, TS, w_off + cb * 5 * TS, TS, w - 4, lane);
    return;
  }
  const int i = lane & 15, g = lane >> 4, mt = panel * 4 + w;
  const unsigned xo = (mt * 80 + cb * 10 + (g >> 1)) * 256 + i * 16 + (g & 1) * 8;
  u32x2 res[5];
#pragma unroll
  for (int t = 0; t < 5; ++t) res[t] = *(const u32x2*)(ws + res_off + xo + t * 512);
  f32x4 acc[5];
#pragma unroll
  for (int t = 0; t < 5; ++t) acc[t] = f32x4{0.f, 0.f, 0.f, 0.f};
  gemm_consume<4, 5, PR_NST, NS, 1, 5>(lds, w, 0, lane, [&](const bf16x8* xa, const bf16x8* wb) {
#pragma unroll
    for (int t = 0; t < 5; ++t) acc[t] = mfma16(wb[t], xa[0], acc[t]);
  });
  u32x2 o[5];
#pragma unroll
  for (int t = 0; t < 5; ++t) {
    o[t][0] = pk2(acc[t][0] + bflo(res[t][0]), acc[t][1] + bfhi(res[t][0]));
    o[t][1] = pk2(acc[t][2] + bflo(res[t][1]), acc[t][3] + bfhi(res[t][1]));
    if (FINAL) *(u32x2*)(out + (size_t)(mt * 16 + i) * 640 + cb * 80 + t * 16 + g * 4) = o[t];
    else *(u32x2*)(ws + out_off + xo + t * 512) = o[t];
  }
  if (!FINAL) {
    float sum = 0.f;
#pragma unroll
    for (int t = 0; t < 5; ++t) sum += bflo(o[t][0]) + bfhi(o[t][0]) + bflo(o[t][1]) + bfhi(o[t][1]);
    sum += __shfl_xor(sum, 16); sum += __shfl_xor(sum, 32);
    const float mean = sum * (1.f / 80.f); float m2 = 0.f;
#pragma unroll
    for (int t = 0; t < 5; ++t) {
      const float a = bflo(o[t][0]) - mean, b = bfhi(o[t][0]) - mean, c = bflo(o[t][1]) - mean, d = bfhi(o[t][1]) - mean;
      m2 += a * a + b * b + c * c + d * d;
    }
    m2 += __shfl_xor(m2, 16); m2 += __shfl_xor(m2, 32);
    if (g == 0) *(f32x2*)(ws + st_off + (size_t)(mt * 16 + i) * 64 + cb * 8) = f32x2{sum, m2};
  }
}

constexpr int GU_NST = 3, GU_LDS = GU_NST * 45056 + 1024;
template <int NF>
DI void gu_consumer(char* ws, lds_ptr lds, int panel, int ft0, int f0, int mq, int lane) {
  const int i = lane & 15, g = lane >> 4;
  const float* cs = (const float*)(ws + WS_CSG) + (ft0 + f0) * 32 + g * 4;
  f32x4 cs4[2 * NF];
#pragma unroll
  for (int t = 0; t < 2 * NF; ++t) cs4[t] = *(const f32x4*)(cs + t * 16);
  f32x4 acc[2][2 * NF];
#pragma unroll
  for (int m = 0; m < 2; ++m)
#pragma unroll
    for (int t = 0; t < 2 * NF; ++t) acc[m][t] = f32x4{0.f, 0.f, 0.f, 0.f};
  gemm_consume<8, 14, GU_NST, 10, 2, 2 * NF>(lds, mq * 2, f0 * 2, lane, [&](const bf16x8* xa, const bf16x8* wb) {
#pragma unroll
    for (int t = 0; t < 2 * NF; ++t)
#pragma unroll
      for (int m = 0; m < 2; ++m) acc[m][t] = mfma16(wb[t], xa[m], acc[m][t]);
  });
  const __attribute__((address_space(3))) f32x2* sst = (const __attribute__((address_space(3))) f32x2*)(lds + GU_NST * 45056);
#pragma unroll
  for (int m = 0; m < 2; ++m) {
    const f32x2 ms = sst[mq * 32 + m * 16 + i];
    const int mt = panel * 8 + mq * 2 + m;
#pragma unroll
    for (int f = 0; f < NF; ++f) {
      float hv[4];
#pragma unroll
      for (int r = 0; r < 4; ++r) {
        const float gt = (acc[m][2 * f][r] - ms[0] * cs4[2 * f][r]) * ms[1];
        const float up = (acc[m][2 * f + 1][r] - ms[0] * cs4[2 * f + 1][r]) * ms[1];
        hv[r] = fmaxf(gt, 0.f) * up;
      }
      *(u32x2*)(ws + WS_H + ((size_t)mt * 216 + (ft0 + f0 + f) * 2 + (g >> 1)) * 256 + i * 16 + (g & 1) * 8) =
          u32x2{pk2(hv[0], hv[1]), pk2(hv[2], hv[3])};
    }
  }
}
__global__ __launch_bounds__(768) void gu_kernel(char* __restrict__ ws, unsigned x_off, unsigned st_off) {
  extern __shared__ __attribute__((aligned(16))) char smem[];
  lds_ptr lds = (lds_ptr)smem;
  const int lane = threadIdx.x & 63, w = __builtin_amdgcn_readfirstlane(threadIdx.x >> 6);
  const int xcd = blockIdx.x & 7, j = blockIdx.x >> 3;
  const int panel = (xcd & 1) * 8 + (j & 7), cb = (xcd >> 1) * 4 + (j >> 3), ft0 = (cb * 27) >> 2;
  if (w >= 8) {
    gemm_loader<8, 14, 2, GU_NST, 4, 10>(ws, x_off + panel * 8 * 20480, 20480, WS_WGU + ft0 * 2 * 20480, 20480, w - 8, lane);
    return;
  }
  if (lane < 16) ((__attribute__((address_space(3))) f32x2*)(lds + GU_NST * 45056))[w * 16 + lane] = row_stats(ws + st_off, panel * 128 + w * 16 + lane);
  if (w < 4) gu_consumer<4>(ws, lds, panel, ft0, 0, w, lane);
  else gu_consumer<3>(ws, lds, panel, ft0, 4, w - 4, lane);
}

extern "C" void kernel_launch(void* const* d_in, const int* in_sizes, int n_in, void* d_out, int out_size, void* d_ws, size_t ws_size,
                              hipStream_t stream) {
  char* ws = (char*)d_ws;
  uint16_t* out = (uint16_t*)d_out;
  (void)hipFuncSetAttribute((const void*)qkv_kernel, hipFuncAttributeMaxDynamicSharedMemorySize, QKV_LDS);
  (void)hipFuncSetAttribute((const void*)attn_kernel, hipFuncAttributeMaxDynamicSharedMemorySize, AT_LDS);
  (void)hipFuncSetAttribute((const void*)proj_kernel<10, false>, hipFuncAttributeMaxDynamicSharedMemorySize, PR_LDS);
  (void)hipFuncSetAttribute((const void*)proj_kernel<27, false>, hipFuncAttributeMaxDynamicSharedMemorySize, PR_LDS);
  (void)hipFuncSetAttribute((const void*)proj_kernel<27, true>, hipFuncAttributeMaxDynamicSharedMemorySize, PR_LDS);
  (void)hipFuncSetAttribute((const void*)gu_kernel, hipFuncAttributeMaxDynamicSharedMemorySize, GU_LDS);
  prep_kernel<<<158, 256, 0, stream>>>((const uint16_t*)d_in[0], (const uint16_t*)d_in[1], (const uint16_t*)d_in[2],
                                       (const uint16_t*)d_in[4], (const uint16_t*)d_in[5], (const uint16_t*)d_in[6], ws);
  constexpr int PT = 64 * (4 + PR_NLW);
  qkv_kernel<<<256, 512, QKV_LDS, stream>>>(ws, WS_X0, WS_ST0);
  attn_kernel<<<256, 64 * AT_NW, AT_LDS, stream>>>(ws);
  proj_kernel<10, false><<<256, PT, PR_LDS, stream>>>(ws, WS_O, WS_WO, WS_X0, WS_X1, WS_ST1, out);
  gu_kernel<<<256, 768, GU_LDS, stream>>>(ws, WS_X1, WS_ST1);
  proj_kernel<27, false><<<256, PT, PR_LDS, stream>>>(ws, WS_H, WS_WD, WS_X1, WS_X2, WS_ST2, out);
  qkv_kernel<<<256, 512, QKV_LDS, stream>>>(ws, WS_X2, WS_ST2);
  attn_kernel<<<256, 64 * AT_NW, AT_LDS, stream>>>(ws);
  proj_kernel<10, false><<<256, PT, PR_LDS, stream>>>(ws, WS_O, WS_WO, WS_X2, WS_X3, WS_ST3, out);
  gu_kernel<<<256, 768, GU_LDS, stream>>>(ws, WS_X3, WS_ST3);
  proj_kernel<27, true><<<256, PT, PR_LDS, stream>>>(ws, WS_H, WS_WD, WS_X3, WS_X3, WS_ST3, out);
}
```

```cpp
#include <hip/hip_runtime.h>
#include <hip/hip_bf16.h>
#include <stdint.h>

typedef __attribute__((ext_vector_type(8))) __bf16 bf16x8;
typedef __attribute__((ext_vector_type(16))) __bf16 bf16x16;
typedef __attribute__((ext_vector_type(4))) short s16x4;
typedef __attribute__((ext_vector_type(8))) short s16x8;
typedef __attribute__((ext_vector_type(2))) __bf16 bf16x2;
typedef __attribute__((ext_vector_type(2))) float f32x2;
typedef __attribute__((ext_vector_type(4))) float f32x4;
typedef __attribute__((ext_vector_type(8))) float f32x8;
typedef __attribute__((ext_vector_type(16))) float f32x16;
typedef __attribute__((ext_vector_type(2))) unsigned u32x2;
typedef __attribute__((ext_vector_type(4))) unsigned u32x4;
typedef unsigned long long u64;
#define LAS __attribute__((address_space(3)))
#define GAS __attribute__((address_space(1)))
typedef LAS char* lds_ptr;
typedef __amdgpu_buffer_rsrc_t rsrc_t;
#define DI __device__ __forceinline__

constexpr unsigned XB = 2048u * 640 * 2;
constexpr unsigned WS_EPOCH = 0;
constexpr unsigned WS_FLAG = 4096;
constexpr int FL_P0 = 0, FL_RT = 1, FL_QKV = 2, FL_ATT = 3, FL_WO = 4, FL_GU = 5, FL_DN = 6;
constexpr unsigned WS_WO  = WS_FLAG + 12u * 32768;
constexpr unsigned WS_WGU = WS_WO + 640u * 640 * 2;
constexpr unsigned WS_WD  = WS_WGU + 3456u * 640 * 2;
constexpr unsigned WS_CSQ = WS_WD + 640u * 1728 * 2;
constexpr unsigned WS_CSG = WS_CSQ + 8u * 1920 * 4;
constexpr unsigned WS_ST  = WS_CSG + 4u * 3456 * 4;
constexpr unsigned STB = 2048u * 64;
constexpr unsigned WS_X2  = WS_ST + 4 * STB;
constexpr unsigned WS_L   = WS_X2 + XB;
constexpr unsigned L_Q1 = 0, L_Q2 = L_Q1 + 32u * 65536, L_K1 = L_Q2 + 16u * 32768, L_K2 = L_K1 + 32u * 65536, L_V = L_K2 + 16u * 32768,
                   L_O = L_V + XB, L_X1 = L_O + XB, L_H = L_X1 + XB, L_SZ = L_H + 2048u * 1728 * 2;
constexpr unsigned WS_END = WS_L + 2 * L_SZ;

constexpr int LDS_BYTES = 163840, MISC = 159744, M_SST = MISC, M_SCS = MISC + 1024, M_CNT = MISC + 2048, M_CNT2 = M_CNT + 8, M_CNT3 = M_CNT + 16, M_CNT4 = M_CNT + 24, M_SIG = M_CNT + 32, M_ISS = M_CNT + 40;
constexpr int QKV_NB = 5, QKV_NA = 4, QKV_A = QKV_NB * 16384;
constexpr int WO_NB = 10, DN_NB = 15;
constexpr int GU_NB = 7, GU_NA = 7, GU_A = GU_NB * 14336;
constexpr int AT_SLOT = 45056, AT_CONST = 3 * AT_SLOT, AT_SCR = AT_CONST + 10752, AT_MERGE = 3 * AT_SLOT;
static_assert(QKV_A + QKV_NA * 16384 <= MISC && DN_NB * 10240 <= MISC && GU_A + GU_NA * 8192 <= MISC, "lds");
static_assert(WO_NB * 10240 <= AT_MERGE && AT_MERGE + 24576 <= MISC && AT_SCR + 3 * 4096 <= MISC, "lds");

DI unsigned pk2(float a, float b) { f32x2 v = {a, b}; bf16x2 r = __builtin_convertvector(v, bf16x2); return __builtin_bit_cast(unsigned, r); }
DI float bflo(unsigned u) { return __uint_as_float(u << 16); }
DI float bfhi(unsigned u) { return __uint_as_float(u & 0xffff0000u); }
DI f32x4 mfma16(bf16x8 a, bf16x8 b, f32x4 c) { return __builtin_amdgcn_mfma_f32_16x16x32_bf16(a, b, c, 0, 0, 0); }
DI f32x16 mfma32(bf16x8 a, bf16x8 b, f32x16 c) { return __builtin_amdgcn_mfma_f32_32x32x16_bf16(a, b, c, 0, 0, 0); }
DI bf16x8 ldsr16(lds_ptr p) { return *(const LAS bf16x8*)p; }
DI bf16x8 ldstr(lds_ptr p, int second) {
  const s16x4 a = __builtin_amdgcn_ds_read_tr16_b64_v4i16((LAS s16x4*)p), b = __builtin_amdgcn_ds_read_tr16_b64_v4i16((LAS s16x4*)(p + second));
  return __builtin_bit_cast(bf16x8, (s16x8)__builtin_shufflevector(a, b, 0, 1, 2, 3, 4, 5, 6, 7));
}
#define BARRIER() asm volatile("s_waitcnt lgkmcnt(0)\n\ts_barrier" ::: "memory")
template <int N> DI void wait_vm() { asm volatile("s_waitcnt vmcnt(%0)" ::"n"(N > 63 ? 63 : N) : "memory"); }
#define WDUMP(id)
#define WRESET()
#define BARRIER_L() BARRIER()
#define BARRIER_NW() asm volatile("s_barrier" ::: "memory")
#define SBAR() __builtin_amdgcn_sched_barrier(0)
#define PIN(x) asm volatile("" : "+v"(x))
#define GL_HEAD "s_mov_b32 m0, %0\n\ts_nop 3\n\t"
#define GL(v, off) "global_load_lds_dwordx4 %" #v ", %1 offset:" #off " sc1\n\t"
DI void glds1(unsigned m, const char* sb, unsigned v0) { asm volatile(GL_HEAD GL(2, 0)::"s"(m), "s"(sb), "v"(v0) : "memory"); }
DI void glds2(unsigned m, const char* sb, unsigned v0, unsigned v1) {
  asm volatile(GL_HEAD GL(2, 0) GL(3, 1024)::"s"(m), "s"(sb), "v"(v0), "v"(v1) : "memory");
}
DI void glds3(unsigned m, const char* sb, unsigned v0, unsigned v1, unsigned v2) {
  asm volatile(GL_HEAD GL(2, 0) GL(3, 1024) GL(4, 2048)::"s"(m), "s"(sb), "v"(v0), "v"(v1), "v"(v2) : "memory");
}
DI void glds4(unsigned m, const char* sb, unsigned v0, unsigned v1, unsigned v2, unsigned v3) {
  asm volatile(GL_HEAD GL(2, 0) GL(3, 1024) GL(4, 2048) GL(5, 3072)::"s"(m), "s"(sb), "v"(v0), "v"(v1), "v"(v2), "v"(v3) : "memory");
}
DI void st16(rsrc_t r, unsigned off, u32x4 v) { __builtin_amdgcn_raw_buffer_store_b128(v, r, off, 0, 16); }
DI void st8(rsrc_t r, unsigned off, u32x2 v) { __builtin_amdgcn_raw_buffer_store_b64(v, r, off, 0, 16); }
DI void st4(rsrc_t r, unsigned off, unsigned v) { __builtin_amdgcn_raw_buffer_store_b32(v, r, off, 0, 16); }
DI u32x4 ld16(rsrc_t r, unsigned off) { return __builtin_amdgcn_raw_buffer_load_b128(r, off, 0, 16); }
DI u32x2 ld8(rsrc_t r, unsigned off) { return __builtin_amdgcn_raw_buffer_load_b64(r, off, 0, 16); }
DI f32x2 ld8_async(unsigned voff, const char* sbase) {
  f32x2 v; asm volatile("s_nop 4\n\tglobal_load_dwordx2 %0, %1, %2 sc1" : "=v"(v) : "v"(voff), "s"(sbase) : "memory"); return v;
}
DI u32x4 pair32(unsigned a0, unsigned a1, unsigned b0, unsigned b1) {
  auto x = __builtin_amdgcn_permlane32_swap(a0, b0, false, false);
  auto y = __builtin_amdgcn_permlane32_swap(a1, b1, false, false);
  return u32x4{x[0], y[0], x[1], y[1]};
}
template <int N> DI float ror16(float v) { return __int_as_float(__builtin_amdgcn_update_dpp(0, __float_as_int(v), 0x120 + N, 0xf, 0xf, false)); }
DI float sum32(float v) {
  v += ror16<8>(v); v += ror16<4>(v); v += ror16<2>(v); v += ror16<1>(v);
  auto a = __builtin_amdgcn_permlane16_swap(__float_as_uint(v), __float_as_uint(v), false, false);
  return __uint_as_float(a[0]) + __uint_as_float(a[1]);
}
DI u32x4 pair16(unsigned a0, unsigned a1, unsigned b0, unsigned b1) {
  auto x = __builtin_amdgcn_permlane16_swap(a0, b0, false, false);
  auto y = __builtin_amdgcn_permlane16_swap(a1, b1, false, false);
  return u32x4{x[0], y[0], x[1], y[1]};
}

DI float rows4(float v) {
  auto a = __builtin_amdgcn_permlane16_swap(__float_as_uint(v), __float_as_uint(v), false, false);
  v = __uint_as_float(a[0]) + __uint_as_float(a[1]);
  auto b = __builtin_amdgcn_permlane32_swap(__float_as_uint(v), __float_as_uint(v), false, false);
  return __uint_as_float(b[0]) + __uint_as_float(b[1]);
}

#define STAMP(id)
#define STAMPC(id)
#define SLOT(n)

#define PACE 9
#define GATE_DRAIN 0
#define SPANST(id)
struct Ctx {
  char* ws; rsrc_t r; lds_ptr lds; u64 tgt; int lane, xcd, j;
};
DI Ctx fresh(const Ctx& c0) { Ctx c = c0; asm volatile("" : "+v"(c.lane), "+s"(c.xcd), "+s"(c.j), "+s"(c.ws)); return c; }
DI int opq(int v) { asm volatile("" : "+s"(v)); return v; }
constexpr int SPIN_CAP = 1 << 18;

DI void publish(const Ctx& c, int cnt_off, unsigned n, int arr, int idx, bool mark = false) {
  if (!GATE_DRAIN && mark && c.lane == 0) __hip_atomic_fetch_add((LAS unsigned*)(c.lds + M_ISS), 1u, __ATOMIC_RELAXED, __HIP_MEMORY_SCOPE_WORKGROUP);
  wait_vm<0>();
  if (GATE_DRAIN && mark && c.lane == 0) __hip_atomic_fetch_add((LAS unsigned*)(c.lds + M_ISS), 1u, __ATOMIC_RELAXED, __HIP_MEMORY_SCOPE_WORKGROUP);
  if (c.lane == 0) {
    const unsigned old = __hip_atomic_fetch_add((LAS unsigned*)(c.lds + cnt_off), 1u, __ATOMIC_RELAXED, __HIP_MEMORY_SCOPE_WORKGROUP);
    if (old % n == n - 1)
      __hip_atomic_store((GAS u64*)(c.ws + WS_FLAG + (arr * 256 + idx) * 128), c.tgt, __ATOMIC_RELAXED, __HIP_MEMORY_SCOPE_AGENT);
  }
}
DI void poll(const Ctx& c, int arr, int idx) {
  const GAS u64* p = (const GAS u64*)(c.ws + WS_FLAG + (arr * 256 + (idx < 0 ? 0 : idx)) * 128);
#pragma unroll 1
  for (int spin = 0; spin < SPIN_CAP; ++spin) {
    const u64 v = __hip_atomic_load(p, __ATOMIC_RELAXED, __HIP_MEMORY_SCOPE_AGENT);
    if (__all(idx < 0 || v == c.tgt)) break;
    __builtin_amdgcn_s_sleep(1);
  }
  asm volatile("" ::: "memory");
}
DI void sig_set(const Ctx& c, unsigned v) { if (c.lane == 0) __hip_atomic_store((LAS unsigned*)(c.lds + M_SIG), v, __ATOMIC_RELAXED, __HIP_MEMORY_SCOPE_WORKGROUP); }
DI void sig_wait(const Ctx& c, unsigned v, int off = M_SIG) {
#pragma unroll 1
  for (int spin = 0; spin < SPIN_CAP * 8; ++spin) {
    if (__builtin_amdgcn_readfirstlane(__hip_atomic_load((LAS unsigned*)(c.lds + off), __ATOMIC_RELAXED, __HIP_MEMORY_SCOPE_WORKGROUP)) >= v) break;
    __builtin_amdgcn_s_sleep(1);
  }
  asm volatile("" ::: "memory");
}

template <int PP, int NSLOT, int NS, int LAG = 1>
struct Ring {
  unsigned voff[PP];
  const char* base;
  unsigned adv, l0;
  int slot;
  DI void init(const char* b, unsigned a, unsigned lds0, int half) {
    base = b - 4096; adv = a; l0 = lds0 + half * PP * 1024; slot = 0;
#pragma unroll
    for (int p = 0; p < PP; ++p) voff[p] += 4096 - (p & 3) * 1024;
  }
  DI void issue() {
    const unsigned m = l0 + slot * (2 * PP * 1024);
    glds4(m, base, voff[0], voff[1], voff[2], voff[3]);
    if constexpr (PP == 5) glds1(m + 4096, base, voff[4]);
    if constexpr (PP == 7) glds3(m + 4096, base, voff[4], voff[5], voff[6]);
    if constexpr (PP == 8) glds4(m + 4096, base, voff[4], voff[5], voff[6], voff[7]);
    base += adv; slot = slot == NSLOT - 1 ? 0 : slot + 1;
  }
  template <int N = NSLOT> DI void fill() {
#pragma unroll 1
    for (int s = 0; s < N; ++s) issue();
  }
  DI void fill_paced() {
#pragma unroll 1
    for (int s = 0; s < NSLOT; ++s) { issue(); wait_vm<PACE>(); }
  }
  template <int R, int E> DI void range() { if constexpr (R >= E) { wait_vm<R * PP>(); BARRIER_L(); range<R - 1, E>(); } }
  template <int D0 = NSLOT> DI void head() {
    wait_vm<(D0 - 1) * PP>(); BARRIER_L(); WRESET();
    if constexpr (D0 < NSLOT) fill<NSLOT - D0>();
    range<NSLOT - 2, NSLOT - LAG>();
    steady();
  }
  DI void steady() {
#pragma unroll 1
    for (int k = LAG; k < NS - NSLOT + LAG; ++k) { wait_vm<(NSLOT - 1 - LAG) * PP>(); BARRIER_L(); issue(); }
  }
  template <int D0 = NSLOT> DI void run() { head<D0>(); range<NSLOT - 1 - LAG, 0>(); BARRIER_L(); }
};

DI void ln_rows(const Ctx& c, int row, const f32x2 (&p)[8]) {
  f32x2 t = p[0];
#pragma unroll
  for (int i = 1; i < 8; ++i) t += p[i];
  const float m8 = t[0] * 0.125f; float q = 0.f;
#pragma unroll
  for (int i = 0; i < 8; ++i) { const float a = p[i][0] - m8; q += a * a; }
  const float rstd = __builtin_amdgcn_rsqf((t[1] + q * (1.f / 80.f)) * (1.f / 640.f) + 1e-5f);
  *(LAS float*)(c.lds + M_SST + row * 4) = rstd;
  *(LAS float*)(c.lds + M_SST + 512 + row * 4) = -t[0] * (1.f / 640.f) * rstd;
}
template <int NP> DI void ln_cols(const Ctx& c, int n4, unsigned off, unsigned pitch) {
  u32x4 q[NP];
#pragma unroll
  for (int i = 0; i < NP; ++i) q[i] = ld16(c.r, off + i * pitch + (c.lane < n4 ? c.lane : 0) * 16);
  f32x4 cs = __builtin_bit_cast(f32x4, q[0]);
#pragma unroll
  for (int i = 1; i < NP; ++i) cs += __builtin_bit_cast(f32x4, q[i]);
  if (c.lane < n4) *(LAS f32x4*)(c.lds + M_SCS + c.lane * 16) = cs;
}

DI int qkv_panel(const Ctx& c) { return (c.xcd & 3) * 4 + (c.j & 3); }
DI int qkv_cb(const Ctx& c) { return (c.xcd >> 2) * 8 + (c.j >> 2); }
DI int att_head(const Ctx& c) { return c.xcd * 4 + (c.j & 3); }
DI int att_qb(const Ctx& c) { return c.j >> 2; }
DI int pr_panel(const Ctx& c) { return (c.xcd & 3) * 8 + (c.j & 7); }
DI int pr_cb(const Ctx& c) { return (c.xcd >> 2) * 4 + (c.j >> 3); }
DI int gu_panel(const Ctx& c) { return (c.xcd & 1) * 8 + (c.j & 7); }
DI int gu_cb(const Ctx& c) { return (c.xcd >> 1) * 4 + (c.j >> 3); }

DI void phase0(const Ctx& c, int hw, const uint16_t* x, const uint16_t* wqkv) {
  const int lane = c.lane, r32 = lane & 31, b = blockIdx.x, row = b * 8 + hw * 2 + (lane >> 5);
  u32x4 v[3];
  v[0] = *(const u32x4*)(x + row * 640 + r32 * 8); v[1] = *(const u32x4*)(x + row * 640 + 256 + r32 * 8);
  v[2] = *(const u32x4*)(x + row * 640 + 512 + (r32 & 15) * 8);
  if (hw == 3 && b < 240) {
    const uint16_t* p = wqkv + (size_t)((b / 30) * 80 + (lane >> 3)) * 1920 + (b % 30) * 64 + (lane & 7) * 8;
    u32x4 t[10];
#pragma unroll
    for (int i = 0; i < 10; ++i) t[i] = *(const u32x4*)(p + i * 8 * 1920);
    float s[8];
#pragma unroll
    for (int e = 0; e < 4; ++e) {
      s[2 * e] = 0.f; s[2 * e + 1] = 0.f;
#pragma unroll
      for (int i = 0; i < 10; ++i) { s[2 * e] += bflo(t[i][e]); s[2 * e + 1] += bfhi(t[i][e]); }
    }
#pragma unroll
    for (int e = 0; e < 8; ++e) s[e] = rows4(s[e] + ror16<8>(s[e]));
    const int hi = (lane >> 3) & 1;
    const f32x4 o = hi ? f32x4{s[4], s[5], s[6], s[7]} : f32x4{s[0], s[1], s[2], s[3]};
    if (lane < 16) st16(c.r, WS_CSQ + ((b / 30) * 1920 + (b % 30) * 64 + (lane & 7) * 8 + hi * 4) * 4, __builtin_bit_cast(u32x4, o));
  }
  const float w2 = r32 < 16 ? 1.f : 0.f;
  float sum = 0.f, s2 = 0.f;
#pragma unroll
  for (int e = 0; e < 4; ++e) { sum += (bflo(v[0][e]) + bfhi(v[0][e])) + (bflo(v[1][e]) + bfhi(v[1][e])); s2 += bflo(v[2][e]) + bfhi(v[2][e]); }
  sum = sum32(sum + w2 * s2);
  const float mean = sum * (1.f / 640.f); float m2 = 0.f; s2 = 0.f;
#pragma unroll
  for (int e = 0; e < 4; ++e) {
    const float a0 = bflo(v[0][e]) - mean, a1 = bfhi(v[0][e]) - mean, a2 = bflo(v[1][e]) - mean, a3 = bfhi(v[1][e]) - mean;
    const float a4 = bflo(v[2][e]) - mean, a5 = bfhi(v[2][e]) - mean;
    m2 += a0 * a0 + a1 * a1 + a2 * a2 + a3 * a3; s2 += a4 * a4 + a5 * a5;
  }
  m2 = sum32(m2 + w2 * s2);
  if (r32 < 8) st8(c.r, WS_ST + (row >> 7) * 8192 + r32 * 1024 + (row & 127) * 8, u32x2{__float_as_uint(sum * 0.125f), __float_as_uint(m2 * 0.125f)});
}

template <bool SWAP>
DI void qkv_loop(const Ctx& c, int w, int layer, f32x16 (&acc)[2][2]) {
  const int lane = c.lane, i = lane & 15, g = lane >> 4, wm = w & 1, wn = w >> 1;
  lds_ptr pa[4];
#pragma unroll
  for (int s = 0; s < 4; ++s)
    pa[s] = c.lds + QKV_A + wm * 8192 + (g & 1) * 2048 +
            (layer == 0 ? (i >> 3) * 1024 + (i & 7) * 128 + (((2 * s + (g >> 1)) ^ ((i >> 1) & 7)) << 4) : (2 * s + (g >> 1)) * 256 + i * 16);
  const int rb = ((g >> 1) * 8 + (i >> 2)) * 256 + (i & 3) * 8, u = (wn * 4 + (g & 1)) ^ ((i >> 2) << 1);
  lds_ptr pb0 = c.lds + rb + (u << 5), pb1 = c.lds + rb + ((u ^ 2) << 5);
  bf16x8 x0[4], x1[4], w0[4], w1[4];
  auto rdx = [&](bf16x8* xn, int hs, int e) { xn[e] = ldsr16(pa[hs * 2 + (e >> 1)] + (e & 1) * 4096); };
  auto rdw = [&](bf16x8* wn_, int hs, int e) { wn_[e] = ldstr(((e & 1) ? pb1 : pb0) + (hs * 2 + (e >> 1)) * 4096, 1024); };
  auto step = [&](const bf16x8* xa, const bf16x8* wb, bf16x8* xn, bf16x8* wn_, int hs, bool ld) {
#pragma unroll
    for (int q = 0; q < 8; ++q) {
      if (ld) { if (q & 1) rdx(xn, hs, q >> 1); else rdw(wn_, hs, q >> 1); }
      const int s = q >> 2, tm = q & 1, tn = (q >> 1) & 1;
      acc[tm][tn] = SWAP ? mfma32(wb[s * 2 + tn], xa[s * 2 + tm], acc[tm][tn]) : mfma32(xa[s * 2 + tm], wb[s * 2 + tn], acc[tm][tn]);
      SBAR();
    }
  };
  SLOT(layer * 5);
  if (layer) STAMP(0);
  STAMP(1);
  BARRIER();
  STAMP(2); STAMPC(0); WRESET();
#pragma unroll
  for (int e = 0; e < 4; ++e) { rdw(w0, 0, e); rdx(x0, 0, e); }
  int sa = 0, sb = 0;
#pragma unroll 1
  for (int st = 0; st < 9; ++st) {
    step(x0, w0, x1, w1, 1, true);
    const int da = sa == QKV_NA - 1 ? -(QKV_NA - 1) * 16384 : 16384, db = sb == QKV_NB - 1 ? -(QKV_NB - 1) * 16384 : 16384;
    sa = sa == QKV_NA - 1 ? 0 : sa + 1; sb = sb == QKV_NB - 1 ? 0 : sb + 1;
    pa[0] += da; pa[1] += da; pa[2] += da; pa[3] += da; pb0 += db; pb1 += db;
    BARRIER_NW();
    step(x1, w1, x0, w0, 0, true);
  }
  step(x0, w0, x1, w1, 1, true);
  BARRIER();
  step(x1, w1, x0, w0, 0, false);
  STAMP(3); STAMPC(1);
}
DI void qkv_helper(const Ctx& c, int hw, int layer) {
  const int panel = qkv_panel(c), cb = qkv_cb(c), l = c.lane;
  BARRIER();
  if (layer == 0) {
    BARRIER(); BARRIER();
    publish(c, M_CNT2, 4, FL_P0, blockIdx.x);
    BARRIER(); BARRIER(); BARRIER();
    const int idx = hw < 2 ? (l < 8 ? panel * 16 + hw * 8 + l : -1) : (l < 16 ? (l & 7) * 30 + cb * 2 + (l >> 3) : -1);
    const u64 v = __hip_atomic_load((const GAS u64*)(c.ws + WS_FLAG + (FL_P0 * 256 + (idx < 0 ? 0 : idx)) * 128), __ATOMIC_RELAXED, __HIP_MEMORY_SCOPE_AGENT);
    BARRIER();
    if (!__all(idx < 0 || v == c.tgt)) poll(c, FL_P0, idx);
  }
  f32x2 p[8];
  if (hw < 2) {
#pragma unroll
    for (int i = 0; i < 8; ++i)
      p[i] = __builtin_bit_cast(f32x2, ld8(c.r, WS_ST + layer * 2 * STB + panel * 8192 + i * 1024 + (hw * 64 + l) * 8));
  }
  BARRIER();
  if (hw == 2) ln_cols<8>(c, 32, WS_CSQ + cb * 512, 7680);
  BARRIER();
  if (hw < 2) ln_rows(c, hw * 64 + l, p);
  if (layer) { BARRIER(); BARRIER(); BARRIER(); BARRIER(); BARRIER(); BARRIER(); }
  BARRIER(); BARRIER();
}
DI void qkv_compute(const Ctx& c, int w, int layer) {
  const int panel = qkv_panel(c), cb = qkv_cb(c);
  if (cb == 15) { if (layer == 0 && w >= 4) publish(c, M_CNT2, 4, FL_P0, blockIdx.x); return; }
  if (w >= 4) { qkv_helper(c, w - 4, layer); return; }
  const int lane = c.lane, r32 = lane & 31, h = lane >> 5, wm = w & 1, wn = w >> 1;
  const unsigned L = WS_L + layer * L_SZ;
  f32x16 acc[2][2] = {{f32x16{}, f32x16{}}, {f32x16{}, f32x16{}}};
  const LAS float* srs = (const LAS float*)(c.lds + M_SST) + wm * 64;
  const LAS float* scs = (const LAS float*)(c.lds + M_SCS) + wn * 64;
  if (cb < 10) {
    qkv_loop<true>(c, w, layer, acc);
    const float qs = cb < 5 ? 0.22360679774997896f * 1.4426950408889634f : 1.f;
    const unsigned i1 = L + (cb < 5 ? L_Q1 : L_K1), i2 = L + (cb < 5 ? L_Q2 : L_K2);
    const float rs[2] = {srs[r32] * qs, srs[32 + r32] * qs}, nm[2] = {srs[128 + r32] * qs, srs[160 + r32] * qs};
#pragma unroll
    for (int tp = 0; tp < 4; ++tp) {
      const int tn = tp >> 1, pr = tp & 1;
      const f32x4 c0 = *(const LAS f32x4*)(scs + tp * 16 + h * 4), c1 = *(const LAS f32x4*)(scs + tp * 16 + 8 + h * 4);
      const int o = (cb % 5) * 16 + wn * 8 + tp * 2 + h, hp = o / 5, pos = o - hp * 5;
      const unsigned ts = pos == 2 ? 512 : 1024;
      const unsigned a0 = (pos == 2 ? i2 + hp * 32768 : i1 + (2 * hp + (pos > 2)) * 65536 + (pos > 2 ? pos - 3 : pos) * 512) +
                          (panel * 4 + wm * 2) * ts + r32 * 16;
#pragma unroll
      for (int tm = 0; tm < 2; ++tm) {
        const f32x16 a = acc[tm][tn];
        const f32x4 u = f32x4{a[pr * 8], a[pr * 8 + 1], a[pr * 8 + 2], a[pr * 8 + 3]} * rs[tm] + c0 * nm[tm];
        const f32x4 v = f32x4{a[pr * 8 + 4], a[pr * 8 + 5], a[pr * 8 + 6], a[pr * 8 + 7]} * rs[tm] + c1 * nm[tm];
        st16(c.r, a0 + tm * ts, pair32(pk2(u[0], u[1]), pk2(u[2], u[3]), pk2(v[0], v[1]), pk2(v[2], v[3])));
      }
    }
  } else {
    qkv_loop<false>(c, w, layer, acc);
    unsigned a0[2]; float cs[2];
#pragma unroll
    for (int tn = 0; tn < 2; ++tn) {
      const int cv = (cb - 10) * 128 + wn * 64 + tn * 32 + r32, head = cv / 20, dim = cv - head * 20;
      const int d = (head & 1) ? (dim < 4 ? dim + 16 : dim - 4) : dim;
      a0[tn] = L + L_V + head * 81920 + (panel * 8 + wm * 4) * 640 + h * 320 + d * 16;
      cs[tn] = scs[tn * 32 + r32];
    }
#pragma unroll
    for (int tp = 0; tp < 4; ++tp) {
      const int tm = tp >> 1, pr = tp & 1;
      const f32x4 r0 = *(const LAS f32x4*)(srs + tp * 16 + h * 4), r1 = *(const LAS f32x4*)(srs + tp * 16 + 8 + h * 4);
      const f32x4 n0 = *(const LAS f32x4*)(srs + 128 + tp * 16 + h * 4), n1 = *(const LAS f32x4*)(srs + 128 + tp * 16 + 8 + h * 4);
#pragma unroll
      for (int tn = 0; tn < 2; ++tn) {
        const f32x16 a = acc[tm][tn];
        const f32x4 u = f32x4{a[pr * 8], a[pr * 8 + 1], a[pr * 8 + 2], a[pr * 8 + 3]} * r0 + n0 * cs[tn];
        const f32x4 v = f32x4{a[pr * 8 + 4], a[pr * 8 + 5], a[pr * 8 + 6], a[pr * 8 + 7]} * r1 + n1 * cs[tn];
        st16(c.r, a0[tn] + tp * 640, pair32(pk2(u[0], u[1]), pk2(u[2], u[3]), pk2(v[0], v[1]), pk2(v[2], v[3])));
      }
    }
  }
  SLOT(layer * 5);
  STAMP(4);
  publish(c, M_CNT4, 4, FL_QKV + layer * 5, panel * 16 + cb);
  STAMP(5);
}
typedef Ring<8, QKV_NB, 10, 2> QkvB;
DI void qkv_b_init(const Ctx& c, QkvB& rg, int half, const uint16_t* wqkv) {
  const int cb = qkv_cb(c) == 15 ? 14 : qkv_cb(c);
#pragma unroll
  for (int p = 0; p < 8; ++p) {
    const int row = (half * 8 + p) * 4 + (c.lane >> 4);
    rg.voff[p] = row * 3840 + cb * 256 + (((c.lane & 15) ^ ((row & 3) << 2)) << 4);
  }
  rg.init((const char*)wqkv, 64 * 3840, 0, half);
}
DI void qkv_a_run(const Ctx& c, int half, int layer, const uint16_t* x) {
  const int panel = qkv_panel(c);
  Ring<8, QKV_NA, 10, 2> rg;
#pragma unroll
  for (int p = 0; p < 8; ++p) {
    const int mt = panel * 8 + half * 4 + (p >> 1), r16 = (p & 1) * 8 + (c.lane >> 3);
    rg.voff[p] = layer == 0 ? (mt * 16 + r16) * 1280 + (((c.lane & 7) ^ ((r16 >> 1) & 7)) << 4) : WS_X2 + mt * 20480 + (p & 1) * 1024 + c.lane * 16;
  }
  rg.init(layer == 0 ? (const char*)x : c.ws, layer == 0 ? 128 : 2048, QKV_A, half);
  rg.fill();
  if (layer == 0) BARRIER();
  rg.run();
}

DI f32x16 qk8(s16x4 k2, s16x4 q2) { return __builtin_amdgcn_mfma_f32_32x32x8bf16_1k(k2, q2, f32x16{}, 0, 0, 0); }
DI f32x16 qk16(bf16x8 k1, bf16x8 q1, f32x16 s) { return __builtin_amdgcn_mfma_f32_32x32x16_bf16(k1, q1, s, 0, 0, 0); }
DI f32x16 pv_lo(bf16x8 v, f32x16 p, f32x16 o) {
  return __builtin_amdgcn_smfmac_f32_32x32x32_bf16(v, __builtin_bit_cast(bf16x16, (f32x8)__builtin_shufflevector(p, p, 0, 1, 2, 3, 4, 5, 6, 7)), o, 0xDDDD, 0, 0);
}
DI f32x16 pv_hi(bf16x8 v, f32x16 p, f32x16 o) {
  return __builtin_amdgcn_smfmac_f32_32x32x32_bf16(v, __builtin_bit_cast(bf16x16, (f32x8)__builtin_shufflevector(p, p, 8, 9, 10, 11, 12, 13, 14, 15)), o, 0xDDDD, 0, 0);
}
DI float swap32(float v, int h) {
  auto r = __builtin_amdgcn_permlane32_swap(__float_as_uint(v), __float_as_uint(v), false, false);
  return __uint_as_float(h ? r[0] : r[1]);
}
#define EXP4(X, i) do { X[i] = __builtin_amdgcn_exp2f(X[i]); X[i + 1] = __builtin_amdgcn_exp2f(X[i + 1]); X[i + 2] = __builtin_amdgcn_exp2f(X[i + 2]); \
                        X[i + 3] = __builtin_amdgcn_exp2f(X[i + 3]); PIN(X); SBAR(); } while (0)
DI void att_compute(const Ctx& c, int w, int layer) {
  const int lane = c.lane, head = att_head(c), qb = att_qb(c);
  const int qp = w & 3, kp = w >> 2, r32 = lane & 31, h = lane >> 5;
  lds_ptr lds = c.lds;
  const unsigned L = WS_L + layer * L_SZ;
  SLOT(layer * 5 + 1);
  STAMP(0);
  for (int s = w * 64 + lane; s < 672; s += 512) {
    const unsigned v = s % 20 == 0 ? 0x3f803f80u : 0u;
    *(LAS u32x4*)(lds + AT_CONST + s * 16) = u32x4{v, v, v, v};
  }
  lds_ptr pk1 = lds + kp * 8192 + lane * 16;
  lds_ptr pk2_ = h ? lds + AT_CONST + 16 : lds + 16384 + kp * 4096 + r32 * 16 + (head & 1) * 8;
  lds_ptr pv = (r32 >= 20 ? lds + AT_CONST : lds + 24576 + kp * 10240) + h * 320 + r32 * 16;
  const int vmask = r32 >= 20 ? 0 : -1, kmask = h ? 0 : -1;
  f32x16 o0 = f32x16{}, o1 = f32x16{};
  f32x16 s0, s1, s2, s3 = f32x16{};
  STAMP(1);
  BARRIER();
  STAMP(2); STAMPC(0); WRESET();
  const bf16x8 q1a = ldsr16(lds + 2 * AT_SLOT + qp * 2048 + lane * 16), q1b = ldsr16(lds + 2 * AT_SLOT + qp * 2048 + 1024 + lane * 16);
  lds_ptr pq2 = h ? lds + AT_CONST + 16 : lds + 2 * AT_SLOT + 8192 + qp * 1024 + r32 * 16 + (head & 1) * 8;
  const s16x4 q2a = *(const LAS s16x4*)pq2, q2b = *(const LAS s16x4*)(pq2 + 512);
  bf16x8 k1 = ldsr16(pk1), vlo = ldsr16(pv), vhi = bf16x8{};
  s16x4 k2 = *(const LAS s16x4*)pk2_;
  s0 = qk16(k1, q1a, qk8(k2, q2a));
#define PH_EVEN(SC, SN, SP, KLOAD, VOFF) do { \
    SN = qk8(k2, q2b); SBAR(); EXP4(SC, 0); SN = qk16(k1, q1b, SN); SBAR(); KLOAD; SBAR(); EXP4(SC, 4); \
    o1 = pv_hi(vhi, SP, o1); SBAR(); vhi = ldsr16(pv + (VOFF) + 640); SBAR(); EXP4(SC, 8); o0 = pv_lo(vlo, SC, o0); SBAR(); EXP4(SC, 12); } while (0)
#define PH_ODD(SC, SN, SP, VLOAD) do { \
    SN = qk8(k2, q2a); SBAR(); EXP4(SC, 0); SN = qk16(k1, q1a, SN); SBAR(); EXP4(SC, 4); \
    o0 = pv_hi(vhi, SP, o0); SBAR(); EXP4(SC, 8); o1 = pv_lo(vlo, SC, o1); SBAR(); VLOAD; SBAR(); EXP4(SC, 12); } while (0)
#define KLD(t) do { k1 = ldsr16(pk1 + (t) * 1024); k2 = *(const LAS s16x4*)(pk2_ + (t) * 512); } while (0)
#define TWO(t) PH_EVEN(s0, s1, s3, KLD((t) + 1), (t) * 1280); PH_ODD(s1, s2, s0, vlo = ldsr16(pv + ((t) + 1) * 1280));
  for (int grp = 0; grp < 4; ++grp) {
    TWO(0) PH_EVEN(s2, s3, s1, KLD(2), 1280); PH_ODD(s3, s0, s2, vlo = ldsr16(pv + 2 * 1280));
    TWO(2) PH_EVEN(s2, s3, s1, KLD(4), 3 * 1280); PH_ODD(s3, s0, s2, vlo = ldsr16(pv + 4 * 1280));
    TWO(4) PH_EVEN(s2, s3, s1, KLD(6), 5 * 1280); PH_ODD(s3, s0, s2, vlo = ldsr16(pv + 6 * 1280));
    TWO(6)
    const int d = grp == 2 ? -2 * AT_SLOT : grp == 3 ? 0 : AT_SLOT;
    PH_EVEN(s2, s3, s1, pk1 += d; pk2_ += (d & kmask); if (grp != 3) BARRIER(); KLD(0), 7 * 1280);
    PH_ODD(s3, s0, s2, pv += (d & vmask); vlo = ldsr16(pv));
  }
  o1 = pv_hi(vhi, s3, o1);
  STAMP(3); STAMPC(1);
  BARRIER();
  const f32x16 give = kp ? o0 : o1, keep = kp ? o1 : o0;
#pragma unroll
  for (int e = 0; e < 3; ++e)
    *(LAS f32x4*)(lds + AT_MERGE + (w * 3 + e) * 1024 + lane * 16) = f32x4{give[4 * e], give[4 * e + 1], give[4 * e + 2], give[4 * e + 3]};
  BARRIER();
  f32x4 a[3];
#pragma unroll
  for (int e = 0; e < 3; ++e)
    a[e] = f32x4{keep[4 * e], keep[4 * e + 1], keep[4 * e + 2], keep[4 * e + 3]} + *(const LAS f32x4*)(lds + AT_MERGE + ((w ^ 4) * 3 + e) * 1024 + lane * 16);
  const int qt = qb * 8 + qp * 2 + kp;
  float l = h ? a[2][0] : 0.f;
  l += swap32(l, h);
  if (__builtin_expect(__any(!(l >= 5.421e-20f && l <= 1.8447e19f)), 0)) {
    const unsigned o2 = (head >> 1) * 32768 + r32 * 16 + (head & 1) * 8;
    const bf16x8 fq1 = __builtin_bit_cast(bf16x8, ld16(c.r, L + L_Q1 + head * 65536 + qt * 1024 + lane * 16));
    u32x2 fq2 = ld8(c.r, L + L_Q2 + o2 + qt * 512);
    if (h) fq2 = u32x2{0u, 0u};
    const unsigned vi = L + L_V + head * 81920 + h * 320 + (r32 < 20 ? r32 : 0) * 16;
    f32x16 oo = f32x16{};
    float m = -INFINITY;
    const u32x4 one8 = {0x3f803f80u, 0x3f803f80u, 0x3f803f80u, 0x3f803f80u};
    for (int t = 0; t < 64; ++t) {
      const u32x4 fk1 = ld16(c.r, L + L_K1 + head * 65536 + t * 1024 + lane * 16);
      const u32x2 fk2 = ld8(c.r, L + L_K2 + o2 + t * 512);
      u32x4 v0 = ld16(c.r, vi + t * 1280), v1 = ld16(c.r, vi + t * 1280 + 640);
      if (r32 >= 20) { v0 = one8; v1 = one8; }
      f32x16 s = qk16(__builtin_bit_cast(bf16x8, fk1), fq1, qk8(__builtin_bit_cast(s16x4, fk2), __builtin_bit_cast(s16x4, fq2)));
      float tm = s[0];
#pragma unroll
      for (int r = 1; r < 16; ++r) tm = fmaxf(tm, s[r]);
      tm = fmaxf(tm, swap32(tm, h));
      const float mn = fmaxf(m, tm), al = __builtin_amdgcn_exp2f(m - mn);
      m = mn;
#pragma unroll
      for (int r = 0; r < 16; ++r) { s[r] = __builtin_amdgcn_exp2f(s[r] - mn); oo[r] *= al; }
      oo = pv_hi(__builtin_bit_cast(bf16x8, v1), s, pv_lo(__builtin_bit_cast(bf16x8, v0), s, oo));
    }
#pragma unroll
    for (int e = 0; e < 3; ++e) a[e] = f32x4{oo[4 * e], oo[4 * e + 1], oo[4 * e + 2], oo[4 * e + 3]};
    l = h ? a[2][0] : 0.f;
    l += swap32(l, h);
  }
  const float inv = 1.f / l;
  const unsigned A0 = pk2(a[0][0] * inv, a[0][1] * inv), A1 = pk2(a[0][2] * inv, a[0][3] * inv);
  const unsigned B0 = pk2(a[1][0] * inv, a[1][1] * inv), B1 = pk2(a[1][2] * inv, a[1][3] * inv);
  const u32x2 C = {pk2(a[2][0] * inv, a[2][1] * inv), pk2(a[2][2] * inv, a[2][3] * inv)};
  const int tok = qt * 32 + r32;
  const unsigned ob = L + L_O + (tok >> 4) * 20480 + (tok & 15) * 16 + (head >> 1) * 1280;
  st16(c.r, ob + ((head & 1) * 3 + h) * 256, pair32(A0, A1, B0, B1));
  if (!h) st8(c.r, ob + 512 + (head & 1) * 8, C);
  STAMP(4);
  publish(c, M_CNT, 8, FL_ATT + layer * 5, qb * 32 + head, true);
  STAMP(5);
}
DI void retile_unit(const Ctx& c, lds_ptr scr, const u32x4 (&v)[4], unsigned dst, unsigned tile_stride, float (&cs)[4]) {
#pragma unroll
  for (int p = 0; p < 4; ++p) *(LAS u32x4*)(scr + p * 1024 + c.lane * 16) = v[p];
  const int i = c.lane & 15, g = c.lane >> 4;
#pragma unroll
  for (int t = 0; t < 4; ++t) {
    const u32x4 f = __builtin_bit_cast(u32x4, ldstr(scr + (g * 8 + (i >> 2)) * 128 + t * 32 + (i & 3) * 8, 512));
    st16(c.r, dst + t * tile_stride + c.lane * 16, f);
#pragma unroll
    for (int e = 0; e < 4; ++e) cs[t] += bflo(f[e]) + bfhi(f[e]);
  }
}
DI void retile_load(const Ctx& c, int J, u32x4 (&v)[5][4], const uint16_t* wo, const uint16_t* wg, const uint16_t* wu, const uint16_t* wd) {
  const int e = J < 216 ? J : J < 256 ? J - 216 : J - 256;
  const int strip = J < 216 ? (e >> 2) % 27 : e % 10, k0 = J < 216 ? (e & 3) * 5 : min((e / 10) * 5, 49), ld = J < 216 ? 1728 : 640;
  const uint16_t* p = (J < 108 ? wg : J < 216 ? wu : J < 256 ? wo : wd) + (size_t)(k0 * 32 + (c.lane >> 3)) * ld + strip * 64 + (c.lane & 7) * 8;
#pragma unroll
  for (int u = 0; u < 5; ++u)
#pragma unroll
    for (int q = 0; q < 4; ++q) v[u][q] = *(const u32x4*)(p + (u * 32 + q * 8) * ld);
}
DI void retile_store(const Ctx& c, int J, lds_ptr scr, const u32x4 (&v)[5][4]) {
  const int e = J < 216 ? J : J < 256 ? J - 216 : J - 256, mat = J >= 108;
  const int strip = J < 216 ? (e >> 2) % 27 : e % 10, k0 = J < 216 ? (e & 3) * 5 : min((e / 10) * 5, 49);
  const unsigned ts = J < 216 ? 40960 : J < 256 ? 20480 : 55296;
  const unsigned dst = (J < 216 ? WS_WGU + mat * 20480 : J < 256 ? WS_WO : WS_WD) + strip * 4 * ts + k0 * 1024;
  float cs[4] = {0.f, 0.f, 0.f, 0.f};
#pragma unroll
  for (int u = 0; u < 5; ++u) retile_unit(c, scr, v[u], dst + u * 1024, ts, cs);
#pragma unroll
  for (int t = 0; t < 4; ++t) {
    const float tot = rows4(cs[t]);
    if (J < 216 && c.lane < 16) st4(c.r, WS_CSG + ((e & 3) * 3456 + (strip * 8 + t * 2 + mat) * 16 + c.lane) * 4, __float_as_uint(tot));
  }
}
DI void att_load(const Ctx& c, int lw, int layer, const uint16_t* wo, const uint16_t* wg, const uint16_t* wu, const uint16_t* wd) {
  const int head = att_head(c), qb = att_qb(c), lane = c.lane;
  const char* Lp = c.ws + WS_L + layer * L_SZ;
  if (lw == 0) {
    const int o0 = (head >> 1) * 5 + (head & 1) * 2, v0 = head * 20;
    const int sel = lane >> 4, cb = sel == 0 ? 5 + (o0 >> 4) : sel == 1 ? 5 + ((o0 + 2) >> 4) : sel == 2 ? 10 + (v0 >> 7) : 10 + ((v0 + 19) >> 7);
    poll(c, FL_QKV + layer * 5, lane < 4 ? (qb * 2 + (lane & 1)) * 16 + ((o0 + (lane & 2)) >> 4) : -1);
    poll(c, FL_QKV + layer * 5, (lane & 15) * 16 + cb);
    sig_set(c, layer * 5 + 2);
  } else sig_wait(c, layer * 5 + 2);
  const unsigned vo = lane * 16;
  auto issue = [&](int grp, int slot) {
#pragma unroll 1
    for (int q = 0; q < 11; ++q) {
      const int p = lw * 11 + q;
      const unsigned src = p < 16 ? L_K1 + head * 65536 + grp * 16384 + p * 1024
                         : p < 24 ? L_K2 + (head >> 1) * 32768 + grp * 8192 + (p - 16) * 1024 : L_V + head * 81920 + grp * 20480 + (p - 24) * 1024;
      glds1(slot * AT_SLOT + p * 1024, Lp + src, vo);
    }
  };
#pragma unroll 1
  for (int q = 0; q < 3; ++q) {
    const int p = lw * 3 + q;
    glds1(2 * AT_SLOT + p * 1024,
          Lp + (p < 8 ? L_Q1 + head * 65536 + qb * 8192 + p * 1024 : L_Q2 + (head >> 1) * 32768 + qb * 4096 + (p - 8) * 1024), vo);
  }
  issue(0, 0);
  wait_vm<0>(); BARRIER();
  issue(1, 1);
  const int J = (lw - 1) * 256 + blockIdx.x;
  const bool rt = layer == 0 && lw > 0 && J < 366;
  u32x4 v[5][4];
  if (rt) { retile_load(c, J, v, wo, wg, wu, wd); wait_vm<20>(); } else wait_vm<0>();
  BARRIER();
  issue(2, 2);
  wait_vm<0>(); BARRIER();
  issue(3, 0);
  if (rt) retile_store(c, J, c.lds + AT_SCR + (lw - 1) * 4096, v);
  if (layer == 0 && (lw == 1 || lw == 2)) publish(c, M_CNT3, 2, FL_RT, blockIdx.x);
  wait_vm<0>(); BARRIER();
  if (layer == 0 && lw >= 2) {
#pragma unroll 1
    for (int q = 0; q < 4; ++q) poll(c, FL_RT, q * 64 + lane);
  }
  BARRIER(); BARRIER();
}

template <int NS, int NB, int WIN>
DI void proj_compute(const Ctx& c, int w, int slot_id, unsigned a_off, const char* res_rm, unsigned res_off, unsigned out_off, unsigned st_off,
                     uint16_t* out, int arr) {
  constexpr bool RES = NS <= NB;
  const int panel = pr_panel(c), cb = pr_cb(c);
  SLOT(slot_id);
  STAMP(0);
  if (w >= 4) {
#pragma unroll 1
    for (int k = 0; k <= (RES ? 1 : NS); ++k) BARRIER();
    return;
  }
  const int lane = c.lane, i = lane & 15, g = lane >> 4, mt = panel * 4 + w;
  u32x2 res[5];
#pragma unroll
  for (int t = 0; t < 5; ++t)
    res[t] = res_rm ? *(const u32x2*)(res_rm + (mt * 16 + i) * 1280 + cb * 160 + t * 32 + g * 8)
                    : ld8(c.r, res_off + (mt * 80 + cb * 10 + t * 2 + (g >> 1)) * 256 + i * 16 + (g & 1) * 8);
  f32x4 acc[5];
#pragma unroll
  for (int t = 0; t < 5; ++t) { acc[t] = f32x4{bflo(res[t][0]), bfhi(res[t][0]), bflo(res[t][1]), bfhi(res[t][1])}; PIN(acc[t]); }
  lds_ptr pb = c.lds + lane * 16;
  unsigned av = a_off + mt * (NS * 2048) + lane * 16;
  u32x4 a[WIN];
  bf16x8 f[3][5];
  int sb = 0;
  STAMP(1);
  BARRIER();
  STAMP(2); STAMPC(0); WRESET();
  constexpr int AH = RES ? 12 : WIN;
#pragma unroll
  for (int k = 0; k < AH; ++k) a[k] = ld16(c.r, av + k * 1024);
#pragma unroll
  for (int t = 0; t < 5; ++t) { f[0][t] = ldsr16(pb + t * 2048); f[1][t] = ldsr16(pb + t * 2048 + 1024); }
  SBAR();
  auto window = [&](bool last) {
#pragma unroll
    for (int k = 0; k < WIN; ++k) {
      const bool ld = !(last && k + 2 >= WIN);
      if ((k & 1) == 0) {
        if (RES) pb += 10240;
        else { pb += sb == NB - 1 ? -(NB - 1) * 10240 : 10240; sb = sb == NB - 1 ? 0 : sb + 1; }
        if (!ld) BARRIER(); else if (!RES) BARRIER_NW();
      }
#pragma unroll
      for (int t = 0; t < 5; ++t) {
        if (ld) f[(k + 2) % 3][t] = ldsr16(pb + t * 2048 + (k & 1) * 1024);
        acc[t] = mfma16(f[k % 3][t], __builtin_bit_cast(bf16x8, a[k]), acc[t]);
        SBAR();
      }
      if (!last) { a[k] = ld16(c.r, av + (WIN + k) * 1024); SBAR(); }
      else if (k + AH < WIN) { a[k + AH] = ld16(c.r, av + (k + AH) * 1024); SBAR(); }
    }
    av += WIN * 1024;
  };
  static_assert(2 * NS % WIN == 0 && (2 * NS == WIN || WIN % 6 == 0), "window");
#pragma unroll 1
  for (int b = 1; b < 2 * NS / WIN; ++b) window(false);
  window(true);
  STAMP(3); STAMPC(1);
  u32x2 o[5];
#pragma unroll
  for (int t = 0; t < 5; ++t) {
    o[t][0] = pk2(acc[t][0], acc[t][1]); o[t][1] = pk2(acc[t][2], acc[t][3]);
  }
  const u32x4 p0 = pair16(o[0][0], o[0][1], o[1][0], o[1][1]), p1 = pair16(o[2][0], o[2][1], o[3][0], o[3][1]),
              p2 = pair16(o[4][0], o[4][1], o[4][0], o[4][1]);
  const int ch = (g & 1) * 2 + (g >> 1);
  if (out) {
    char* po = (char*)out + (mt * 16 + i) * 1280 + cb * 160;
    *(u32x4*)(po + ch * 16) = p0; *(u32x4*)(po + 64 + ch * 16) = p1;
    if (!(g & 1)) *(u32x4*)(po + 128 + (g >> 1) * 16) = p2;
    STAMP(4);
    return;
  }
  const unsigned xo = out_off + (mt * 80 + cb * 10) * 256 + i * 16;
  st16(c.r, xo + ch * 256, p0); st16(c.r, xo + 1024 + ch * 256, p1);
  if (!(g & 1)) st16(c.r, xo + 2048 + (g >> 1) * 256, p2);
  const f32x4 s4 = (acc[0] + acc[1]) + (acc[2] + acc[3]) + acc[4];
  const float sum = rows4((s4[0] + s4[1]) + (s4[2] + s4[3])), mean = sum * (1.f / 80.f);
  f32x4 q4 = {0.f, 0.f, 0.f, 0.f};
#pragma unroll
  for (int t = 0; t < 5; ++t) { const f32x4 d = acc[t] - mean; q4 += d * d; }
  const float m2 = rows4((q4[0] + q4[1]) + (q4[2] + q4[3]));
  if (g == 0) st8(c.r, st_off + (mt >> 3) * 8192 + cb * 1024 + ((mt & 7) * 16 + i) * 8, u32x2{__float_as_uint(sum), __float_as_uint(m2)});
  STAMP(4);
  publish(c, M_CNT4, 4, arr, panel * 8 + cb, true);
  STAMP(5);
}
template <int NS, int NB> DI void proj_b_run(const Ctx& c, int half, unsigned w_off) {
  Ring<5, NB, NS, 2> rg;
#pragma unroll
  for (int p = 0; p < 5; ++p) { const int q = half * 5 + p; rg.voff[p] = w_off + (pr_cb(c) * 5 + (q >> 1)) * (NS * 2048) + (q & 1) * 1024 + c.lane * 16; }
  rg.init(c.ws, 2048, 0, half);
  rg.fill_paced();
  if constexpr (NS <= NB) { wait_vm<0>(); BARRIER_L(); BARRIER_L(); } else rg.run();
}

template <int NF>
DI void gu_consumer(const Ctx& c, int layer, int f0, int mq) {
  const int panel = gu_panel(c), ft0 = (gu_cb(c) * 27) >> 2;
  const int lane = c.lane, r32 = lane & 31, h = lane >> 5;
  f32x16 acc[NF];
#pragma unroll
  for (int t = 0; t < NF; ++t) acc[t] = f32x16{};
  const int lo = (r32 >> 4) * 1024 + h * 256 + (r32 & 15) * 16;
  lds_ptr pa = c.lds + GU_A + mq * 2048 + lo, pb = c.lds + f0 * 2048 + lo;
  bf16x8 x0[2], x1[2], wa[NF], wb0[NF], wb1[NF];
  auto step = [&](const bf16x8* xa, const bf16x8* wb, bf16x8* xn, bf16x8* wn_, bool ld) {
#pragma unroll
    for (int q = 0; q < 2 * NF; ++q) {
      if (ld) {
        if (q == 0) { xn[0] = ldsr16(pa); xn[1] = ldsr16(pa + 512); }
        if (q >= 1 && q <= NF) wn_[q - 1] = ldsr16(pb + (q - 1) * 2048 + 512);
        if (q >= NF) wa[q - NF] = ldsr16(pb + (q - NF) * 2048);
      }
      if (q < NF) acc[q] = mfma32(wa[q], xa[0], acc[q]); else acc[q - NF] = mfma32(wb[q - NF], xa[1], acc[q - NF]);
      SBAR();
    }
  };
  static_assert(GU_NA == GU_NB, "one slot counter");
  int sl = 0;
  auto adv = [&]() {
    pa += sl == GU_NA - 1 ? -(GU_NA - 1) * 8192 : 8192; pb += sl == GU_NB - 1 ? -(GU_NB - 1) * 14336 : 14336;
    sl = sl == GU_NA - 1 ? 0 : sl + 1;
  };
  SLOT(layer * 5 + 3);
  STAMP(0);
  STAMP(1);
  BARRIER();
  STAMP(2); STAMPC(0); WRESET();
  x0[0] = ldsr16(pa); x0[1] = ldsr16(pa + 512);
#pragma unroll
  for (int t = 0; t < NF; ++t) { wa[t] = ldsr16(pb + t * 2048); wb0[t] = ldsr16(pb + t * 2048 + 512); }
#pragma unroll 1
  for (int st = 0; st < 9; ++st) {
    adv(); BARRIER_NW(); step(x0, wb0, x1, wb1, true);
    adv(); BARRIER_NW(); step(x1, wb1, x0, wb0, true);
  }
  adv(); BARRIER_NW(); step(x0, wb0, x1, wb1, true);
  BARRIER(); step(x1, wb1, x0, wb0, false);
  STAMP(3); STAMPC(1);
  const LAS float* srs = (const LAS float*)(c.lds + M_SST) + mq * 32 + r32;
  const float rs = srs[0], nm = srs[128];
  const LAS float* scs = (const LAS float*)(c.lds + M_SCS) + f0 * 32 + h * 4;
  const unsigned ho = WS_L + layer * L_SZ + L_H + ((panel * 8 + mq * 2 + (r32 >> 4)) * 216 + (ft0 + f0) * 2 + h) * 256 + (r32 & 15) * 16;
#pragma unroll
  for (int f = 0; f < NF; ++f) {
    unsigned p[2][2];
#pragma unroll
    for (int e = 0; e < 2; ++e) {
      const f32x4 cg = *(const LAS f32x4*)(scs + f * 32 + e * 8), cu = *(const LAS f32x4*)(scs + f * 32 + 16 + e * 8);
      const f32x16 a = acc[f];
      f32x4 gt = f32x4{a[e * 4], a[e * 4 + 1], a[e * 4 + 2], a[e * 4 + 3]} * rs + cg * nm;
      const f32x4 up = f32x4{a[8 + e * 4], a[9 + e * 4], a[10 + e * 4], a[11 + e * 4]} * rs + cu * nm;
      gt = __builtin_elementwise_max(gt, f32x4{0.f, 0.f, 0.f, 0.f}) * up;
      p[e][0] = pk2(gt[0], gt[1]); p[e][1] = pk2(gt[2], gt[3]);
    }
    st16(c.r, ho + f * 512, pair32(p[0][0], p[0][1], p[1][0], p[1][1]));
  }
  STAMP(4);
  publish(c, M_CNT, 8, FL_GU + layer * 5, panel * 16 + gu_cb(c), true);
  STAMP(5);
}
typedef Ring<7, GU_NB, 20, 2> GuB;
DI void gu_b_init(const Ctx& c, GuB& rg, int half) {
  const int ft0 = (gu_cb(c) * 27) >> 2;
#pragma unroll
  for (int p = 0; p < 7; ++p) rg.voff[p] = WS_WGU + (ft0 * 2 + half * 7 + p) * 20480 + c.lane * 16;
  rg.init(c.ws, 1024, 0, half);
}
DI void gu_b_run(const Ctx& c, int half, int layer) {
  GuB rg;
  gu_b_init(c, rg, half);
  rg.fill_paced();
  if (half == 0) ln_cols<4>(c, 56, WS_CSG + ((gu_cb(c) * 27) >> 2) * 128, 13824);
  wait_vm<(GU_NB - 1) * 7>(); BARRIER_L(); WRESET();
  f32x2 p[8];
#pragma unroll
  for (int i = 0; i < 8; ++i) p[i] = ld8_async(WS_ST + (layer * 2 + 1) * STB + gu_panel(c) * 8192 + i * 1024 + (half * 64 + c.lane) * 8, c.ws);
  wait_vm<(GU_NB - 2) * 7 + 8>(); BARRIER_L();
  asm volatile("s_waitcnt vmcnt(0)" : "+v"(p[0]), "+v"(p[1]), "+v"(p[2]), "+v"(p[3]), "+v"(p[4]), "+v"(p[5]), "+v"(p[6]), "+v"(p[7])::"memory");
  ln_rows(c, half * 64 + c.lane, p);
  rg.steady();
  rg.template range<GU_NB - 3, 0>(); BARRIER_L();
}
DI void gu_a_run(const Ctx& c, int half, unsigned a_off) {
  Ring<4, GU_NA, 20, 2> rg;
#pragma unroll
  for (int p = 0; p < 4; ++p) rg.voff[p] = a_off + (gu_panel(c) * 8 + half * 4 + p) * 20480 + c.lane * 16;
  rg.init(c.ws, 1024, GU_A, half);
  rg.fill();
  rg.run();
}

DI void gate(const Ctx& c, int half, int arr, int idx, unsigned sig) {
  if (half == 0) { poll(c, arr, idx); sig_set(c, sig); } else sig_wait(c, sig);
}

__global__ __launch_bounds__(768) void fused_kernel(const uint16_t* __restrict__ x, const uint16_t* __restrict__ wqkv, const uint16_t* __restrict__ wo,
                                                    const uint16_t* __restrict__ wg, const uint16_t* __restrict__ wu,
                                                    const uint16_t* __restrict__ wd, uint16_t* __restrict__ out, char* __restrict__ ws) {
  extern __shared__ __attribute__((aligned(16))) char smem[];
  Ctx c;
  c.ws = ws; c.lds = (lds_ptr)smem; c.lane = threadIdx.x & 63; c.xcd = blockIdx.x & 7; c.j = blockIdx.x >> 3;
  c.r = __builtin_amdgcn_make_buffer_rsrc(ws, 0, WS_END, 0x00020000);
  const int w = __builtin_amdgcn_readfirstlane(threadIdx.x >> 6);
  { SLOT(0); STAMP(0); }
  if (w == 0) SPANST(0);
  c.tgt = *(const u64*)(ws + WS_EPOCH) + 1;
  if (threadIdx.x < 16) *(LAS unsigned*)(c.lds + M_CNT + threadIdx.x * 4) = 0u;
  const bool qkv_on = qkv_cb(c) != 15;
  const Ctx c0 = c;
  WDUMP(15);
  if (w >= 8) __builtin_amdgcn_s_setprio(3);
  if (w >= 10) {
    const int half = w - 10;
    QkvB rq;
    qkv_b_init(c, rq, half, wqkv);
    if (qkv_on) rq.fill();
    BARRIER();
#pragma unroll 1
    for (int layer = 0; layer < 2; ++layer) {
      if (qkv_on) rq.run();
      WDUMP(layer * 5);
      att_load(fresh(c0), opq(half + 2), opq(layer), wo, wg, wu, wd);
      WDUMP(layer * 5 + 1);
      sig_wait(c0, layer * 24 + 8, M_ISS);
      proj_b_run<10, WO_NB>(fresh(c0), opq(half), WS_WO);
      WDUMP(layer * 5 + 2);
      sig_wait(c0, layer * 24 + 12, M_ISS);
      gu_b_run(fresh(c0), opq(half), opq(layer));
      WDUMP(layer * 5 + 3);
      sig_wait(c0, layer * 24 + 20, M_ISS);
      proj_b_run<27, DN_NB>(fresh(c0), opq(half), WS_WD);
      WDUMP(layer * 5 + 4);
      if (layer == 0 && qkv_on) { sig_wait(c0, 24, M_ISS); qkv_b_init(fresh(c0), rq, opq(half), wqkv); rq.fill_paced(); }
    }
  } else if (w >= 8) {
    const int half = w - 8;
    if (!qkv_on) BARRIER();
#pragma unroll 1
    for (int layer = 0; layer < 2; ++layer) {
      const unsigned L = WS_L + layer * L_SZ;
      const Ctx c = fresh(c0);
      const int l = c.lane;
      if (qkv_on) {
        if (layer) gate(c, half, FL_DN, l < 16 ? qkv_panel(c) * 16 + l : -1, 6);
        qkv_a_run(fresh(c0), opq(half), opq(layer), x);
      }
      WDUMP(layer * 5);
      att_load(fresh(c0), opq(half), opq(layer), wo, wg, wu, wd);
      WDUMP(layer * 5 + 1);
      if (half == 0) poll(c, FL_ATT + layer * 5, l < 32 ? (pr_panel(c) >> 2) * 32 + l : -1);
      BARRIER_L(); BARRIER_L();
      WDUMP(layer * 5 + 2);
      gate(c, half, FL_WO + layer * 5, l < 16 ? gu_panel(c) * 16 + l : -1, layer * 5 + 4);
      gu_a_run(fresh(c0), opq(half), L + L_X1);
      WDUMP(layer * 5 + 3);
      if (half == 0) poll(c, FL_GU + layer * 5, l < 16 ? (pr_panel(c) >> 1) * 16 + l : -1);
      if (layer == 1 && half == 0 && blockIdx.x == 0 && l == 0) *(u64*)(ws + WS_EPOCH) = c.tgt;
#pragma unroll 1
      for (int k = 0; k <= 27; ++k) BARRIER_L();
      WDUMP(layer * 5 + 4);
    }
  } else {
    BARRIER();
    if (w >= 4) phase0(c, w - 4, x, wqkv);
#pragma unroll 1
    for (int layer = 0; layer < 2; ++layer) {
      const unsigned L = WS_L + layer * L_SZ;
      qkv_compute(fresh(c0), opq(w), opq(layer));
      WDUMP(layer * 5);
      att_compute(fresh(c0), opq(w), opq(layer));
      WDUMP(layer * 5 + 1);
      proj_compute<10, WO_NB, 20>(fresh(c0), opq(w), layer * 5 + 2, L + L_O, layer == 0 ? (const char*)x : nullptr, WS_X2, L + L_X1,
                                  WS_ST + (layer * 2 + 1) * STB, nullptr, FL_WO + layer * 5);
      WDUMP(layer * 5 + 2);
      if (w < 4) gu_consumer<4>(fresh(c0), opq(layer), 0, opq(w)); else gu_consumer<3>(fresh(c0), opq(layer), 4, opq(w - 4));
      WDUMP(layer * 5 + 3);
      proj_compute<27, DN_NB, 18>(fresh(c0), opq(w), layer * 5 + 4, L + L_H, nullptr, L + L_X1, WS_X2, WS_ST + 2 * STB, layer ? out : nullptr,
                                  FL_DN + layer * 5);
      WDUMP(layer * 5 + 4);
    }
  }
}

extern "C" void kernel_launch(void* const* d_in, const int* in_sizes, int n_in, void* d_out, int out_size, void* d_ws, size_t ws_size,
                              hipStream_t stream) {
  (void)hipFuncSetAttribute((const void*)fused_kernel, hipFuncAttributeMaxDynamicSharedMemorySize, LDS_BYTES);
  fused_kernel<<<256, 768, LDS_BYTES, stream>>>((const uint16_t*)d_in[0], (const uint16_t*)d_in[1], (const uint16_t*)d_in[2],
                                                (const uint16_t*)d_in[4], (const uint16_t*)d_in[5], (const uint16_t*)d_in[6],
                                                (uint16_t*)d_out, (char*)d_ws);
}
```

```cpp
#include <hip/hip_runtime.h>
#include <hip/hip_bf16.h>
#include <stdint.h>

typedef __attribute__((ext_vector_type(8))) __bf16 bf16x8;
typedef __attribute__((ext_vector_type(16))) __bf16 bf16x16;
typedef __attribute__((ext_vector_type(4))) short s16x4;
typedef __attribute__((ext_vector_type(8))) short s16x8;
typedef __attribute__((ext_vector_type(2))) __bf16 bf16x2;
typedef __attribute__((ext_vector_type(2))) float f32x2;
typedef __attribute__((ext_vector_type(4))) float f32x4;
typedef __attribute__((ext_vector_type(8))) float f32x8;
typedef __attribute__((ext_vector_type(16))) float f32x16;
typedef __attribute__((ext_vector_type(2))) unsigned u32x2;
typedef __attribute__((ext_vector_type(4))) unsigned u32x4;
typedef unsigned long long u64;
#define LAS __attribute__((address_space(3)))
#define GAS __attribute__((address_space(1)))
typedef LAS char* lds_ptr;
typedef __amdgpu_buffer_rsrc_t rsrc_t;
#define DI __device__ __forceinline__

constexpr unsigned XB = 2048u * 640 * 2;
constexpr unsigned WS_EPOCH = 0;
constexpr unsigned WS_FLAG = 4096;
constexpr int FL_P0 = 0, FL_RT = 1, FL_QKV = 2, FL_ATT = 3, FL_WO = 4, FL_GU = 5, FL_DN = 6;
constexpr unsigned WS_WO  = WS_FLAG + 12u * 32768;
constexpr unsigned WS_WGU = WS_WO + 640u * 640 * 2;
constexpr unsigned WS_WD  = WS_WGU + 3456u * 640 * 2;
constexpr unsigned WS_CSQ = WS_WD + 640u * 1728 * 2;
constexpr unsigned WS_CSG = WS_CSQ + 8u * 1920 * 4;
constexpr unsigned WS_ST  = WS_CSG + 4u * 3456 * 4;
constexpr unsigned STB = 2048u * 64;
constexpr unsigned WS_X2  = WS_ST + 4 * STB;
constexpr unsigned WS_L   = WS_X2 + XB;
constexpr unsigned L_Q1 = 0, L_Q2 = L_Q1 + 32u * 65536, L_K1 = L_Q2 + 16u * 32768, L_K2 = L_K1 + 32u * 65536, L_V = L_K2 + 16u * 32768,
                   L_O = L_V + XB, L_X1 = L_O + XB, L_H = L_X1 + XB, L_SZ = L_H + 2048u * 1728 * 2;
constexpr unsigned WS_END = WS_L + 2 * L_SZ;

constexpr int LDS_BYTES = 163840, MISC = 159744, M_SST = MISC, M_SCS = MISC + 1024, M_CNT = MISC + 2048, M_CNT2 = M_CNT + 8, M_CNT3 = M_CNT + 16, M_CNT4 = M_CNT + 24, M_SIG = M_CNT + 32, M_ISS = M_CNT + 40;
constexpr int QKV_NB = 5, QKV_NA = 4, QKV_A = QKV_NB * 16384;
constexpr int WO_NB = 10, DN_NB = 15;
constexpr int GU_NB = 7, GU_NA = 7, GU_A = GU_NB * 14336;
constexpr int AT_SLOT = 45056, AT_CONST = 3 * AT_SLOT, AT_SCR = AT_CONST + 10752, AT_MERGE = 3 * AT_SLOT;
static_assert(QKV_A + QKV_NA * 16384 <= MISC && DN_NB * 10240 <= MISC && GU_A + GU_NA * 8192 <= MISC, "lds");
static_assert(WO_NB * 10240 <= AT_MERGE && AT_MERGE + 24576 <= MISC && AT_SCR + 3 * 4096 <= MISC, "lds");

DI unsigned pk2(float a, float b) { f32x2 v = {a, b}; bf16x2 r = __builtin_convertvector(v, bf16x2); return __builtin_bit_cast(unsigned, r); }
DI float bflo(unsigned u) { return __uint_as_float(u << 16); }
DI float bfhi(unsigned u) { return __uint_as_float(u & 0xffff0000u); }
DI f32x4 mfma16(bf16x8 a, bf16x8 b, f32x4 c) { return __builtin_amdgcn_mfma_f32_16x16x32_bf16(a, b, c, 0, 0, 0); }
DI f32x16 mfma32(bf16x8 a, bf16x8 b, f32x16 c) { return __builtin_amdgcn_mfma_f32_32x32x16_bf16(a, b, c, 0, 0, 0); }
DI bf16x8 ldsr16(lds_ptr p) { return *(const LAS bf16x8*)p; }
DI bf16x8 ldstr(lds_ptr p, int second) {
  const s16x4 a = __builtin_amdgcn_ds_read_tr16_b64_v4i16((LAS s16x4*)p), b = __builtin_amdgcn_ds_read_tr16_b64_v4i16((LAS s16x4*)(p + second));
  return __builtin_bit_cast(bf16x8, (s16x8)__builtin_shufflevector(a, b, 0, 1, 2, 3, 4, 5, 6, 7));
}
#define BARRIER() asm volatile("s_waitcnt lgkmcnt(0)\n\ts_barrier" ::: "memory")
template <int N> DI void wait_vm() { asm volatile("s_waitcnt vmcnt(%0)" ::"n"(N > 63 ? 63 : N) : "memory"); }
#define WDUMP(id)
#define WRESET()
#define BARRIER_L() BARRIER()
#define BARRIER_NW() asm volatile("s_barrier" ::: "memory")
#define SBAR() __builtin_amdgcn_sched_barrier(0)
#define PIN(x) asm volatile("" : "+v"(x))
#define GL_HEAD "s_mov_b32 m0, %0\n\ts_nop 3\n\t"
#define GL(v, off) "global_load_lds_dwordx4 %" #v ", %1 offset:" #off " sc1\n\t"
DI void glds1(unsigned m, const char* sb, unsigned v0) { asm volatile(GL_HEAD GL(2, 0)::"s"(m), "s"(sb), "v"(v0) : "memory"); }
DI void glds2(unsigned m, const char* sb, unsigned v0, unsigned v1) {
  asm volatile(GL_HEAD GL(2, 0) GL(3, 1024)::"s"(m), "s"(sb), "v"(v0), "v"(v1) : "memory");
}
DI void glds3(unsigned m, const char* sb, unsigned v0, unsigned v1, unsigned v2) {
  asm volatile(GL_HEAD GL(2, 0) GL(3, 1024) GL(4, 2048)::"s"(m), "s"(sb), "v"(v0), "v"(v1), "v"(v2) : "memory");
}
DI void glds4(unsigned m, const char* sb, unsigned v0, unsigned v1, unsigned v2, unsigned v3) {
  asm volatile(GL_HEAD GL(2, 0) GL(3, 1024) GL(4, 2048) GL(5, 3072)::"s"(m), "s"(sb), "v"(v0), "v"(v1), "v"(v2), "v"(v3) : "memory");
}
DI void st16(rsrc_t r, unsigned off, u32x4 v) { __builtin_amdgcn_raw_buffer_store_b128(v, r, off, 0, 16); }
DI void st8(rsrc_t r, unsigned off, u32x2 v) { __builtin_amdgcn_raw_buffer_store_b64(v, r, off, 0, 16); }
DI void st4(rsrc_t r, unsigned off, unsigned v) { __builtin_amdgcn_raw_buffer_store_b32(v, r, off, 0, 16); }
DI u32x4 ld16(rsrc_t r, unsigned off) { return __builtin_amdgcn_raw_buffer_load_b128(r, off, 0, 16); }
DI u32x2 ld8(rsrc_t r, unsigned off) { return __builtin_amdgcn_raw_buffer_load_b64(r, off, 0, 16); }
DI f32x2 ld8_async(unsigned voff, const char* sbase) {
  f32x2 v; asm volatile("s_nop 4\n\tglobal_load_dwordx2 %0, %1, %2 sc1" : "=v"(v) : "v"(voff), "s"(sbase) : "memory"); return v;
}
DI u32x4 pair32(unsigned a0, unsigned a1, unsigned b0, unsigned b1) {
  auto x = __builtin_amdgcn_permlane32_swap(a0, b0, false, false);
  auto y = __builtin_amdgcn_permlane32_swap(a1, b1, false, false);
  return u32x4{x[0], y[0], x[1], y[1]};
}
template <int N> DI float ror16(float v) { return __int_as_float(__builtin_amdgcn_update_dpp(0, __float_as_int(v), 0x120 + N, 0xf, 0xf, false)); }
DI float sum32(float v) {
  v += ror16<8>(v); v += ror16<4>(v); v += ror16<2>(v); v += ror16<1>(v);
  auto a = __builtin_amdgcn_permlane16_swap(__float_as_uint(v), __float_as_uint(v), false, false);
  return __uint_as_float(a[0]) + __uint_as_float(a[1]);
}
DI u32x4 pair16(unsigned a0, unsigned a1, unsigned b0, unsigned b1) {
  auto x = __builtin_amdgcn_permlane16_swap(a0, b0, false, false);
  auto y = __builtin_amdgcn_permlane16_swap(a1, b1, false, false);
  return u32x4{x[0], y[0], x[1], y[1]};
}

DI float rows4(float v) {
  auto a = __builtin_amdgcn_permlane16_swap(__float_as_uint(v), __float_as_uint(v), false, false);
  v = __uint_as_float(a[0]) + __uint_as_float(a[1]);
  auto b = __builtin_amdgcn_permlane32_swap(__float_as_uint(v), __float_as_uint(v), false, false);
  return __uint_as_float(b[0]) + __uint_as_float(b[1]);
}

#define STAMP(id)
#define STAMPC(id)
#define SLOT(n)

#define PACE 9
#define GATE_DRAIN 0
#define SPANST(id)
struct Ctx {
  char* ws; rsrc_t r; lds_ptr lds; u64 tgt; int lane, xcd, j;
};
DI Ctx fresh(const Ctx& c0) { Ctx c = c0; asm volatile("" : "+v"(c.lane), "+s"(c.xcd), "+s"(c.j), "+s"(c.ws)); return c; }
DI int opq(int v) { asm volatile("" : "+s"(v)); return v; }
constexpr int SPIN_CAP = 1 << 18;

DI void publish(const Ctx& c, int cnt_off, unsigned n, int arr, int idx, bool mark = false) {
  if (!GATE_DRAIN && mark && c.lane == 0) __hip_atomic_fetch_add((LAS unsigned*)(c.lds + M_ISS), 1u, __ATOMIC_RELAXED, __HIP_MEMORY_SCOPE_WORKGROUP);
  wait_vm<0>();
  if (GATE_DRAIN && mark && c.lane == 0) __hip_atomic_fetch_add((LAS unsigned*)(c.lds + M_ISS), 1u, __ATOMIC_RELAXED, __HIP_MEMORY_SCOPE_WORKGROUP);
  if (c.lane == 0) {
    const unsigned old = __hip_atomic_fetch_add((LAS unsigned*)(c.lds + cnt_off), 1u, __ATOMIC_RELAXED, __HIP_MEMORY_SCOPE_WORKGROUP);
    if (old % n == n - 1)
      __hip_atomic_store((GAS u64*)(c.ws + WS_FLAG + (arr * 256 + idx) * 128), c.tgt, __ATOMIC_RELAXED, __HIP_MEMORY_SCOPE_AGENT);
  }
}
DI void poll(const Ctx& c, int arr, int idx) {
  const GAS u64* p = (const GAS u64*)(c.ws + WS_FLAG + (arr * 256 + (idx < 0 ? 0 : idx)) * 128);
#pragma unroll 1
  for (int spin = 0; spin < SPIN_CAP; ++spin) {
    const u64 v = __hip_atomic_load(p, __ATOMIC_RELAXED, __HIP_MEMORY_SCOPE_AGENT);
    if (__all(idx < 0 || v == c.tgt)) break;
    __builtin_amdgcn_s_sleep(1);
  }
  asm volatile("" ::: "memory");
}
DI void sig_set(const Ctx& c, unsigned v) { if (c.lane == 0) __hip_atomic_store((LAS unsigned*)(c.lds + M_SIG), v, __ATOMIC_RELAXED, __HIP_MEMORY_SCOPE_WORKGROUP); }
DI void sig_wait(const Ctx& c, unsigned v, int off = M_SIG) {
#pragma unroll 1
  for (int spin = 0; spin < SPIN_CAP * 8; ++spin) {
    if (__builtin_amdgcn_readfirstlane(__hip_atomic_load((LAS unsigned*)(c.lds + off), __ATOMIC_RELAXED, __HIP_MEMORY_SCOPE_WORKGROUP)) >= v) break;
    __builtin_amdgcn_s_sleep(1);
  }
  asm volatile("" ::: "memory");
}

template <int PP, int NSLOT, int NS, int LAG = 1>
struct Ring {
  unsigned voff[PP];
  const char* base;
  unsigned adv, l0;
  int slot;
  DI void init(const char* b, unsigned a, unsigned lds0, int half) {
    base = b - 4096; adv = a; l0 = lds0 + half * PP * 1024; slot = 0;
#pragma unroll
    for (int p = 0; p < PP; ++p) voff[p] += 4096 - (p & 3) * 1024;
  }
  DI void issue() {
    const unsigned m = l0 + slot * (2 * PP * 1024);
    glds4(m, base, voff[0], voff[1], voff[2], voff[3]);
    if constexpr (PP == 5) glds1(m + 4096, base, voff[4]);
    if constexpr (PP == 7) glds3(m + 4096, base, voff[4], voff[5], voff[6]);
    if constexpr (PP == 8) glds4(m + 4096, base, voff[4], voff[5], voff[6], voff[7]);
    base += adv; slot = slot == NSLOT - 1 ? 0 : slot + 1;
  }
  template <int N = NSLOT> DI void fill() {
#pragma unroll 1
    for (int s = 0; s < N; ++s) issue();
  }
  DI void fill_paced() {
#pragma unroll 1
    for (int s = 0; s < NSLOT; ++s) { issue(); wait_vm<PACE>(); }
  }
  template <int R, int E> DI void range() { if constexpr (R >= E) { wait_vm<R * PP>(); BARRIER_L(); range<R - 1, E>(); } }
  template <int D0 = NSLOT> DI void head() {
    wait_vm<(D0 - 1) * PP>(); BARRIER_L(); WRESET();
    if constexpr (D0 < NSLOT) fill<NSLOT - D0>();
    range<NSLOT - 2, NSLOT - LAG>();
    steady();
  }
  DI void steady() {
#pragma unroll 1
    for (int k = LAG; k < NS - NSLOT + LAG; ++k) { wait_vm<(NSLOT - 1 - LAG) * PP>(); BARRIER_L(); issue(); }
  }
  template <int D0 = NSLOT> DI void run() { head<D0>(); range<NSLOT - 1 - LAG, 0>(); BARRIER_L(); }
};

DI void ln_rows(const Ctx& c, int row, const f32x2 (&p)[8]) {
  f32x2 t = p[0];
#pragma unroll
  for (int i = 1; i < 8; ++i) t += p[i];
  const float m8 = t[0] * 0.125f; float q = 0.f;
#pragma unroll
  for (int i = 0; i < 8; ++i) { const float a = p[i][0] - m8; q += a * a; }
  const float rstd = __builtin_amdgcn_rsqf((t[1] + q * (1.f / 80.f)) * (1.f / 640.f) + 1e-5f);
  *(LAS float*)(c.lds + M_SST + row * 4) = rstd;
  *(LAS float*)(c.lds + M_SST + 512 + row * 4) = -t[0] * (1.f / 640.f) * rstd;
}
template <int NP> DI void ln_cols(const Ctx& c, int n4, unsigned off, unsigned pitch) {
  u32x4 q[NP];
#pragma unroll
  for (int i = 0; i < NP; ++i) q[i] = ld16(c.r, off + i * pitch + (c.lane < n4 ? c.lane : 0) * 16);
  f32x4 cs = __builtin_bit_cast(f32x4, q[0]);
#pragma unroll
  for (int i = 1; i < NP; ++i) cs += __builtin_bit_cast(f32x4, q[i]);
  if (c.lane < n4) *(LAS f32x4*)(c.lds + M_SCS + c.lane * 16) = cs;
}

DI int qkv_panel(const Ctx& c) { return (c.xcd & 3) * 4 + (c.j & 3); }
DI int qkv_cb(const Ctx& c) { return (c.xcd >> 2) * 8 + (c.j >> 2); }
DI int att_head(const Ctx& c) { return c.xcd * 4 + (c.j & 3); }
DI int att_qb(const Ctx& c) { return c.j >> 2; }
DI int pr_panel(const Ctx& c) { return (c.xcd & 3) * 8 + (c.j & 7); }
DI int pr_cb(const Ctx& c) { return (c.xcd >> 2) * 4 + (c.j >> 3); }
DI int gu_panel(const Ctx& c) { return (c.xcd & 1) * 8 + (c.j & 7); }
DI int gu_cb(const Ctx& c) { return (c.xcd >> 1) * 4 + (c.j >> 3); }

DI void phase0(const Ctx& c, int hw, const uint16_t* x, const uint16_t* wqkv) {
  const int lane = c.lane, r32 = lane & 31, b = blockIdx.x, row = b * 8 + hw * 2 + (lane >> 5);
  u32x4 v[3];
  v[0] = *(const u32x4*)(x + row * 640 + r32 * 8); v[1] = *(const u32x4*)(x + row * 640 + 256 + r32 * 8);
  v[2] = *(const u32x4*)(x + row * 640 + 512 + (r32 & 15) * 8);
  if (hw == 3 && b < 240) {
    const uint16_t* p = wqkv + (size_t)((b / 30) * 80 + (lane >> 3)) * 1920 + (b % 30) * 64 + (lane & 7) * 8;
    u32x4 t[10];
#pragma unroll
    for (int i = 0; i < 10; ++i) t[i] = *(const u32x4*)(p + i * 8 * 1920);
    float s[8];
#pragma unroll
    for (int e = 0; e < 4; ++e) {
      s[2 * e] = 0.f; s[2 * e + 1] = 0.f;
#pragma unroll
      for (int i = 0; i < 10; ++i) { s[2 * e] += bflo(t[i][e]); s[2 * e + 1] += bfhi(t[i][e]); }
    }
#pragma unroll
    for (int e = 0; e < 8; ++e) s[e] = rows4(s[e] + ror16<8>(s[e]));
    const int hi = (lane >> 3) & 1;
    const f32x4 o = hi ? f32x4{s[4], s[5], s[6], s[7]} : f32x4{s[0], s[1], s[2], s[3]};
    if (lane < 16) st16(c.r, WS_CSQ + ((b / 30) * 1920 + (b % 30) * 64 + (lane & 7) * 8 + hi * 4) * 4, __builtin_bit_cast(u32x4, o));
  }
  const float w2 = r32 < 16 ? 1.f : 0.f;
  float sum = 0.f, s2 = 0.f;
#pragma unroll
  for (int e = 0; e < 4; ++e) { sum += (bflo(v[0][e]) + bfhi(v[0][e])) + (bflo(v[1][e]) + bfhi(v[1][e])); s2 += bflo(v[2][e]) + bfhi(v[2][e]); }
  sum = sum32(sum + w2 * s2);
  const float mean = sum * (1.f / 640.f); float m2 = 0.f; s2 = 0.f;
#pragma unroll
  for (int e = 0; e < 4; ++e) {
    const float a0 = bflo(v[0][e]) - mean, a1 = bfhi(v[0][e]) - mean, a2 = bflo(v[1][e]) - mean, a3 = bfhi(v[1][e]) - mean;
    const float a4 = bflo(v[2][e]) - mean, a5 = bfhi(v[2][e]) - mean;
    m2 += a0 * a0 + a1 * a1 + a2 * a2 + a3 * a3; s2 += a4 * a4 + a5 * a5;
  }
  m2 = sum32(m2 + w2 * s2);
  if (r32 < 8) st8(c.r, WS_ST + (row >> 7) * 8192 + r32 * 1024 + (row & 127) * 8, u32x2{__float_as_uint(sum * 0.125f), __float_as_uint(m2 * 0.125f)});
}

template <bool SWAP>
DI void qkv_loop(const Ctx& c, int w, int layer, f32x16 (&acc)[2][2]) {
  const int lane = c.lane, i = lane & 15, g = lane >> 4, wm = w & 1, wn = w >> 1;
  lds_ptr pa[4];
#pragma unroll
  for (int s = 0; s < 4; ++s)
    pa[s] = c.lds + QKV_A + wm * 8192 + (g & 1) * 2048 +
            (layer == 0 ? (i >> 3) * 1024 + (i & 7) * 128 + (((2 * s + (g >> 1)) ^ ((i >> 1) & 7)) << 4) : (2 * s + (g >> 1)) * 256 + i * 16);
  const int rb = ((g >> 1) * 8 + (i >> 2)) * 256 + (i & 3) * 8, u = (wn * 4 + (g & 1)) ^ ((i >> 2) << 1);
  lds_ptr pb0 = c.lds + rb + (u << 5), pb1 = c.lds + rb + ((u ^ 2) << 5);
  bf16x8 x0[4], x1[4], w0[4], w1[4];
  auto rdx = [&](bf16x8* xn, int hs, int e) { xn[e] = ldsr16(pa[hs * 2 + (e >> 1)] + (e & 1) * 4096); };
  auto rdw = [&](bf16x8* wn_, int hs, int e) { wn_[e] = ldstr(((e & 1) ? pb1 : pb0) + (hs * 2 + (e >> 1)) * 4096, 1024); };
  auto step = [&](const bf16x8* xa, const bf16x8* wb, bf16x8* xn, bf16x8* wn_, int hs, bool ld) {
#pragma unroll
    for (int q = 0; q < 8; ++q) {
      if (ld) { if (q & 1) rdx(xn, hs, q >> 1); else rdw(wn_, hs, q >> 1); }
      const int s = q >> 2, tm = q & 1, tn = (q >> 1) & 1;
      acc[tm][tn] = SWAP ? mfma32(wb[s * 2 + tn], xa[s * 2 + tm], acc[tm][tn]) : mfma32(xa[s * 2 + tm], wb[s * 2 + tn], acc[tm][tn]);
      SBAR();
    }
  };
  SLOT(layer * 5);
  if (layer) STAMP(0);
  STAMP(1);
  BARRIER();
  STAMP(2); STAMPC(0); WRESET();
#pragma unroll
  for (int e = 0; e < 4; ++e) { rdw(w0, 0, e); rdx(x0, 0, e); }
  int sa = 0, sb = 0;
#pragma unroll 1
  for (int st = 0; st < 9; ++st) {
    step(x0, w0, x1, w1, 1, true);
    const int da = sa == QKV_NA - 1 ? -(QKV_NA - 1) * 16384 : 16384, db = sb == QKV_NB - 1 ? -(QKV_NB - 1) * 16384 : 16384;
    sa = sa == QKV_NA - 1 ? 0 : sa + 1; sb = sb == QKV_NB - 1 ? 0 : sb + 1;
    pa[0] += da; pa[1] += da; pa[2] += da; pa[3] += da; pb0 += db; pb1 += db;
    BARRIER_NW();
    step(x1, w1, x0, w0, 0, true);
  }
  step(x0, w0, x1, w1, 1, true);
  BARRIER();
  step(x1, w1, x0, w0, 0, false);
  STAMP(3); STAMPC(1);
}
DI void qkv_helper(const Ctx& c, int hw, int layer) {
  const int panel = qkv_panel(c), cb = qkv_cb(c), l = c.lane;
  BARRIER();
  if (layer == 0) {
    BARRIER(); BARRIER();
    publish(c, M_CNT2, 4, FL_P0, blockIdx.x);
    BARRIER(); BARRIER(); BARRIER();
    const int idx = hw < 2 ? (l < 8 ? panel * 16 + hw * 8 + l : -1) : (l < 16 ? (l & 7) * 30 + cb * 2 + (l >> 3) : -1);
    const u64 v = __hip_atomic_load((const GAS u64*)(c.ws + WS_FLAG + (FL_P0 * 256 + (idx < 0 ? 0 : idx)) * 128), __ATOMIC_RELAXED, __HIP_MEMORY_SCOPE_AGENT);
    BARRIER();
    if (!__all(idx < 0 || v == c.tgt)) poll(c, FL_P0, idx);
  }
  f32x2 p[8];
  if (hw < 2) {
#pragma unroll
    for (int i = 0; i < 8; ++i)
      p[i] = __builtin_bit_cast(f32x2, ld8(c.r, WS_ST + layer * 2 * STB + panel * 8192 + i * 1024 + (hw * 64 + l) * 8));
  }
  BARRIER();
  if (hw == 2) ln_cols<8>(c, 32, WS_CSQ + cb * 512, 7680);
  BARRIER();
  if (hw < 2) ln_rows(c, hw * 64 + l, p);
  if (layer) { BARRIER(); BARRIER(); BARRIER(); BARRIER(); BARRIER(); BARRIER(); }
  BARRIER(); BARRIER();
}
DI void qkv_compute(const Ctx& c, int w, int layer) {
  const int panel = qkv_panel(c), cb = qkv_cb(c);
  if (cb == 15) { if (layer == 0 && w >= 4) publish(c, M_CNT2, 4, FL_P0, blockIdx.x); return; }
  if (w >= 4) { qkv_helper(c, w - 4, layer); return; }
  const int lane = c.lane, r32 = lane & 31, h = lane >> 5, wm = w & 1, wn = w >> 1;
  const unsigned L = WS_L + layer * L_SZ;
  f32x16 acc[2][2] = {{f32x16{}, f32x16{}}, {f32x16{}, f32x16{}}};
  const LAS float* srs = (const LAS float*)(c.lds + M_SST) + wm * 64;
  const LAS float* scs = (const LAS float*)(c.lds + M_SCS) + wn * 64;
  if (cb < 10) {
    qkv_loop<true>(c, w, layer, acc);
    const float qs = cb < 5 ? 0.22360679774997896f * 1.4426950408889634f : 1.f;
    const unsigned i1 = L + (cb < 5 ? L_Q1 : L_K1), i2 = L + (cb < 5 ? L_Q2 : L_K2);
    const float rs[2] = {srs[r32] * qs, srs[32 + r32] * qs}, nm[2] = {srs[128 + r32] * qs, srs[160 + r32] * qs};
#pragma unroll
    for (int tp = 0; tp < 4; ++tp) {
      const int tn = tp >> 1, pr = tp & 1;
      const f32x4 c0 = *(const LAS f32x4*)(scs + tp * 16 + h * 4), c1 = *(const LAS f32x4*)(scs + tp * 16 + 8 + h * 4);
      const int o = (cb % 5) * 16 + wn * 8 + tp * 2 + h, hp = o / 5, pos = o - hp * 5;
      const unsigned ts = pos == 2 ? 512 : 1024;
      const unsigned a0 = (pos == 2 ? i2 + hp * 32768 : i1 + (2 * hp + (pos > 2)) * 65536 + (pos > 2 ? pos - 3 : pos) * 512) +
                          (panel * 4 + wm * 2) * ts + r32 * 16;
#pragma unroll
      for (int tm = 0; tm < 2; ++tm) {
        const f32x16 a = acc[tm][tn];
        const f32x4 u = f32x4{a[pr * 8], a[pr * 8 + 1], a[pr * 8 + 2], a[pr * 8 + 3]} * rs[tm] + c0 * nm[tm];
        const f32x4 v = f32x4{a[pr * 8 + 4], a[pr * 8 + 5], a[pr * 8 + 6], a[pr * 8 + 7]} * rs[tm] + c1 * nm[tm];
        st16(c.r, a0 + tm * ts, pair32(pk2(u[0], u[1]), pk2(u[2], u[3]), pk2(v[0], v[1]), pk2(v[2], v[3])));
      }
    }
  } else {
    qkv_loop<false>(c, w, layer, acc);
    unsigned a0[2]; float cs[2];
#pragma unroll
    for (int tn = 0; tn < 2; ++tn) {
      const int cv = (cb - 10) * 128 + wn * 64 + tn * 32 + r32, head = cv / 20, dim = cv - head * 20;
      const int d = (head & 1) ? (dim < 4 ? dim + 16 : dim - 4) : dim;
      a0[tn] = L + L_V + head * 81920 + (panel * 8 + wm * 4) * 640 + h * 320 + d * 16;
      cs[tn] = scs[tn * 32 + r32];
    }
#pragma unroll
    for (int tp = 0; tp < 4; ++tp) {
      const int tm = tp >> 1, pr = tp & 1;
      const f32x4 r0 = *(const LAS f32x4*)(srs + tp * 16 + h * 4), r1 = *(const LAS f32x4*)(srs + tp * 16 + 8 + h * 4);
      const f32x4 n0 = *(const LAS f32x4*)(srs + 128 + tp * 16 + h * 4), n1 = *(const LAS f32x4*)(srs + 128 + tp * 16 + 8 + h * 4);
#pragma unroll
      for (int tn = 0; tn < 2; ++tn) {
        const f32x16 a = acc[tm][tn];
        const f32x4 u = f32x4{a[pr * 8], a[pr * 8 + 1], a[pr * 8 + 2], a[pr * 8 + 3]} * r0 + n0 * cs[tn];
        const f32x4 v = f32x4{a[pr * 8 + 4], a[pr * 8 + 5], a[pr * 8 + 6], a[pr * 8 + 7]} * r1 + n1 * cs[tn];
        st16(c.r, a0[tn] + tp * 640, pair32(pk2(u[0], u[1]), pk2(u[2], u[3]), pk2(v[0], v[1]), pk2(v[2], v[3])));
      }
    }
  }
  SLOT(layer * 5);
  STAMP(4);
  publish(c, M_CNT4, 4, FL_QKV + layer * 5, panel * 16 + cb);
  STAMP(5);
}
typedef Ring<8, QKV_NB, 10, 2> QkvB;
DI void qkv_b_init(const Ctx& c, QkvB& rg, int half, const uint16_t* wqkv) {
  const int cb = qkv_cb(c) == 15 ? 14 : qkv_cb(c);
#pragma unroll
  for (int p = 0; p < 8; ++p) {
    const int row = (half * 8 + p) * 4 + (c.lane >> 4);
    rg.voff[p] = row * 3840 + cb * 256 + (((c.lane & 15) ^ ((row & 3) << 2)) << 4);
  }
  rg.init((const char*)wqkv, 64 * 3840, 0, half);
}
DI void qkv_a_run(const Ctx& c, int half, int layer, const uint16_t* x) {
  const int panel = qkv_panel(c);
  Ring<8, QKV_NA, 10, 2> rg;
#pragma unroll
  for (int p = 0; p < 8; ++p) {
    const int mt = panel * 8 + half * 4 + (p >> 1), r16 = (p & 1) * 8 + (c.lane >> 3);
    rg.voff[p] = layer == 0 ? (mt * 16 + r16) * 1280 + (((c.lane & 7) ^ ((r16 >> 1) & 7)) << 4) : WS_X2 + mt * 20480 + (p & 1) * 1024 + c.lane * 16;
  }
  rg.init(layer == 0 ? (const char*)x : c.ws, layer == 0 ? 128 : 2048, QKV_A, half);
  rg.fill();
  if (layer == 0) BARRIER();
  rg.run();
}

DI f32x16 qk8(s16x4 k2, s16x4 q2) { return __builtin_amdgcn_mfma_f32_32x32x8bf16_1k(k2, q2, f32x16{}, 0, 0, 0); }
DI f32x16 qk16(bf16x8 k1, bf16x8 q1, f32x16 s) { return __builtin_amdgcn_mfma_f32_32x32x16_bf16(k1, q1, s, 0, 0, 0); }
DI f32x16 pv_lo(bf16x8 v, f32x16 p, f32x16 o) {
  return __builtin_amdgcn_smfmac_f32_32x32x32_bf16(v, __builtin_bit_cast(bf16x16, (f32x8)__builtin_shufflevector(p, p, 0, 1, 2, 3, 4, 5, 6, 7)), o, 0xDDDD, 0, 0);
}
DI f32x16 pv_hi(bf16x8 v, f32x16 p, f32x16 o) {
  return __builtin_amdgcn_smfmac_f32_32x32x32_bf16(v, __builtin_bit_cast(bf16x16, (f32x8)__builtin_shufflevector(p, p, 8, 9, 10, 11, 12, 13, 14, 15)), o, 0xDDDD, 0, 0);
}
DI float swap32(float v, int h) {
  auto r = __builtin_amdgcn_permlane32_swap(__float_as_uint(v), __float_as_uint(v), false, false);
  return __uint_as_float(h ? r[0] : r[1]);
}
#define EXP4(X, i) do { X[i] = __builtin_amdgcn_exp2f(X[i]); X[i + 1] = __builtin_amdgcn_exp2f(X[i + 1]); X[i + 2] = __builtin_amdgcn_exp2f(X[i + 2]); \
                        X[i + 3] = __builtin_amdgcn_exp2f(X[i + 3]); PIN(X); SBAR(); } while (0)
DI void att_compute(const Ctx& c, int w, int layer) {
  const int lane = c.lane, head = att_head(c), qb = att_qb(c);
  const int qp = w & 3, kp = w >> 2, r32 = lane & 31, h = lane >> 5;
  lds_ptr lds = c.lds;
  const unsigned L = WS_L + layer * L_SZ;
  SLOT(layer * 5 + 1);
  STAMP(0);
  for (int s = w * 64 + lane; s < 672; s += 512) {
    const unsigned v = s % 20 == 0 ? 0x3f803f80u : 0u;
    *(LAS u32x4*)(lds + AT_CONST + s * 16) = u32x4{v, v, v, v};
  }
  lds_ptr pk1 = lds + kp * 8192 + lane * 16;
  lds_ptr pk2_ = h ? lds + AT_CONST + 16 : lds + 16384 + kp * 4096 + r32 * 16 + (head & 1) * 8;
  lds_ptr pv = (r32 >= 20 ? lds + AT_CONST : lds + 24576 + kp * 10240) + h * 320 + r32 * 16;
  const int vmask = r32 >= 20 ? 0 : -1, kmask = h ? 0 : -1;
  f32x16 o0 = f32x16{}, o1 = f32x16{};
  f32x16 s0, s1, s2, s3 = f32x16{};
  STAMP(1);
  BARRIER();
  STAMP(2); STAMPC(0); WRESET();
  const bf16x8 q1a = ldsr16(lds + 2 * AT_SLOT + qp * 2048 + lane * 16), q1b = ldsr16(lds + 2 * AT_SLOT + qp * 2048 + 1024 + lane * 16);
  lds_ptr pq2 = h ? lds + AT_CONST + 16 : lds + 2 * AT_SLOT + 8192 + qp * 1024 + r32 * 16 + (head & 1) * 8;
  const s16x4 q2a = *(const LAS s16x4*)pq2, q2b = *(const LAS s16x4*)(pq2 + 512);
  bf16x8 k1 = ldsr16(pk1), vlo = ldsr16(pv), vhi = bf16x8{};
  s16x4 k2 = *(const LAS s16x4*)pk2_;
  s0 = qk16(k1, q1a, qk8(k2, q2a));
#define PH_EVEN(SC, SN, SP, KLOAD, VOFF) do { \
    SN = qk8(k2, q2b); SBAR(); EXP4(SC, 0); SN = qk16(k1, q1b, SN); SBAR(); KLOAD; SBAR(); EXP4(SC, 4); \
    o1 = pv_hi(vhi, SP, o1); SBAR(); vhi = ldsr16(pv + (VOFF) + 640); SBAR(); EXP4(SC, 8); o0 = pv_lo(vlo, SC, o0); SBAR(); EXP4(SC, 12); } while (0)
#define PH_ODD(SC, SN, SP, VLOAD) do { \
    SN = qk8(k2, q2a); SBAR(); EXP4(SC, 0); SN = qk16(k1, q1a, SN); SBAR(); EXP4(SC, 4); \
    o0 = pv_hi(vhi, SP, o0); SBAR(); EXP4(SC, 8); o1 = pv_lo(vlo, SC, o1); SBAR(); VLOAD; SBAR(); EXP4(SC, 12); } while (0)
#define KLD(t) do { k1 = ldsr16(pk1 + (t) * 1024); k2 = *(const LAS s16x4*)(pk2_ + (t) * 512); } while (0)
#define TWO(t) PH_EVEN(s0, s1, s3, KLD((t) + 1), (t) * 1280); PH_ODD(s1, s2, s0, vlo = ldsr16(pv + ((t) + 1) * 1280));
  for (int grp = 0; grp < 4; ++grp) {
    TWO(0) PH_EVEN(s2, s3, s1, KLD(2), 1280); PH_ODD(s3, s0, s2, vlo = ldsr16(pv + 2 * 1280));
    TWO(2) PH_EVEN(s2, s3, s1, KLD(4), 3 * 1280); PH_ODD(s3, s0, s2, vlo = ldsr16(pv + 4 * 1280));
    TWO(4) PH_EVEN(s2, s3, s1, KLD(6), 5 * 1280); PH_ODD(s3, s0, s2, vlo = ldsr16(pv + 6 * 1280));
    TWO(6)
    const int d = grp == 2 ? -2 * AT_SLOT : grp == 3 ? 0 : AT_SLOT;
    PH_EVEN(s2, s3, s1, pk1 += d; pk2_ += (d & kmask); if (grp != 3) BARRIER(); KLD(0), 7 * 1280);
    PH_ODD(s3, s0, s2, pv += (d & vmask); vlo = ldsr16(pv));
  }
  o1 = pv_hi(vhi, s3, o1);
  STAMP(3); STAMPC(1);
  BARRIER();
  const f32x16 give = kp ? o0 : o1, keep = kp ? o1 : o0;
#pragma unroll
  for (int e = 0; e < 3; ++e)
    *(LAS f32x4*)(lds + AT_MERGE + (w * 3 + e) * 1024 + lane * 16) = f32x4{give[4 * e], give[4 * e + 1], give[4 * e + 2], give[4 * e + 3]};
  BARRIER();
  f32x4 a[3];
#pragma unroll
  for (int e = 0; e < 3; ++e)
    a[e] = f32x4{keep[4 * e], keep[4 * e + 1], keep[4 * e + 2], keep[4 * e + 3]} + *(const LAS f32x4*)(lds + AT_MERGE + ((w ^ 4) * 3 + e) * 1024 + lane * 16);
  const int qt = qb * 8 + qp * 2 + kp;
  float l = h ? a[2][0] : 0.f;
  l += swap32(l, h);
  if (__builtin_expect(__any(!(l >= 5.421e-20f && l <= 1.8447e19f)), 0)) {
    const unsigned o2 = (head >> 1) * 32768 + r32 * 16 + (head & 1) * 8;
    const bf16x8 fq1 = __builtin_bit_cast(bf16x8, ld16(c.r, L + L_Q1 + head * 65536 + qt * 1024 + lane * 16));
    u32x2 fq2 = ld8(c.r, L + L_Q2 + o2 + qt * 512);
    if (h) fq2 = u32x2{0u, 0u};
    const unsigned vi = L + L_V + head * 81920 + h * 320 + (r32 < 20 ? r32 : 0) * 16;
    f32x16 oo = f32x16{};
    float m = -INFINITY;
    const u32x4 one8 = {0x3f803f80u, 0x3f803f80u, 0x3f803f80u, 0x3f803f80u};
    for (int t = 0; t < 64; ++t) {
      const u32x4 fk1 = ld16(c.r, L + L_K1 + head * 65536 + t * 1024 + lane * 16);
      const u32x2 fk2 = ld8(c.r, L + L_K2 + o2 + t * 512);
      u32x4 v0 = ld16(c.r, vi + t * 1280), v1 = ld16(c.r, vi + t * 1280 + 640);
      if (r32 >= 20) { v0 = one8; v1 = one8; }
      f32x16 s = qk16(__builtin_bit_cast(bf16x8, fk1), fq1, qk8(__builtin_bit_cast(s16x4, fk2), __builtin_bit_cast(s16x4, fq2)));
      float tm = s[0];
#pragma unroll
      for (int r = 1; r < 16; ++r) tm = fmaxf(tm, s[r]);
      tm = fmaxf(tm, swap32(tm, h));
      const float mn = fmaxf(m, tm), al = __builtin_amdgcn_exp2f(m - mn);
      m = mn;
#pragma unroll
      for (int r = 0; r < 16; ++r) { s[r] = __builtin_amdgcn_exp2f(s[r] - mn); oo[r] *= al; }
      oo = pv_hi(__builtin_bit_cast(bf16x8, v1), s, pv_lo(__builtin_bit_cast(bf16x8, v0), s, oo));
    }
#pragma unroll
    for (int e = 0; e < 3; ++e) a[e] = f32x4{oo[4 * e], oo[4 * e + 1], oo[4 * e + 2], oo[4 * e + 3]};
    l = h ? a[2][0] : 0.f;
    l += swap32(l, h);
  }
  const float inv = 1.f / l;
  const unsigned A0 = pk2(a[0][0] * inv, a[0][1] * inv), A1 = pk2(a[0][2] * inv, a[0][3] * inv);
  const unsigned B0 = pk2(a[1][0] * inv, a[1][1] * inv), B1 = pk2(a[1][2] * inv, a[1][3] * inv);
  const u32x2 C = {pk2(a[2][0] * inv, a[2][1] * inv), pk2(a[2][2] * inv, a[2][3] * inv)};
  const int tok = qt * 32 + r32;
  const unsigned ob = L + L_O + (tok >> 4) * 20480 + (tok & 15) * 16 + (head >> 1) * 1280;
  st16(c.r, ob + ((head & 1) * 3 + h) * 256, pair32(A0, A1, B0, B1));
  if (!h) st8(c.r, ob + 512 + (head & 1) * 8, C);
  STAMP(4);
  publish(c, M_CNT, 8, FL_ATT + layer * 5, qb * 32 + head, true);
  STAMP(5);
}
DI void retile_unit(const Ctx& c, lds_ptr scr, const u32x4 (&v)[4], unsigned dst, unsigned tile_stride, float (&cs)[4]) {
#pragma unroll
  for (int p = 0; p < 4; ++p) *(LAS u32x4*)(scr + p * 1024 + c.lane * 16) = v[p];
  const int i = c.lane & 15, g = c.lane >> 4;
#pragma unroll
  for (int t = 0; t < 4; ++t) {
    const u32x4 f = __builtin_bit_cast(u32x4, ldstr(scr + (g * 8 + (i >> 2)) * 128 + t * 32 + (i & 3) * 8, 512));
    st16(c.r, dst + t * tile_stride + c.lane * 16, f);
#pragma unroll
    for (int e = 0; e < 4; ++e) cs[t] += bflo(f[e]) + bfhi(f[e]);
  }
}
DI void retile_load(const Ctx& c, int J, u32x4 (&v)[5][4], const uint16_t* wo, const uint16_t* wg, const uint16_t* wu, const uint16_t* wd) {
  const int e = J < 216 ? J : J < 256 ? J - 216 : J - 256;
  const int strip = J < 216 ? (e >> 2) % 27 : e % 10, k0 = J < 216 ? (e & 3) * 5 : min((e / 10) * 5, 49), ld = J < 216 ? 1728 : 640;
  const uint16_t* p = (J < 108 ? wg : J < 216 ? wu : J < 256 ? wo : wd) + (size_t)(k0 * 32 + (c.lane >> 3)) * ld + strip * 64 + (c.lane & 7) * 8;
#pragma unroll
  for (int u = 0; u < 5; ++u)
#pragma unroll
    for (int q = 0; q < 4; ++q) v[u][q] = *(const u32x4*)(p + (u * 32 + q * 8) * ld);
}
DI void retile_store(const Ctx& c, int J, lds_ptr scr, const u32x4 (&v)[5][4]) {
  const int e = J < 216 ? J : J < 256 ? J - 216 : J - 256, mat = J >= 108;
  const int strip = J < 216 ? (e >> 2) % 27 : e % 10, k0 = J < 216 ? (e & 3) * 5 : min((e / 10) * 5, 49);
  const unsigned ts = J < 216 ? 40960 : J < 256 ? 20480 : 55296;
  const unsigned dst = (J < 216 ? WS_WGU + mat * 20480 : J < 256 ? WS_WO : WS_WD) + strip * 4 * ts + k0 * 1024;
  float cs[4] = {0.f, 0.f, 0.f, 0.f};
#pragma unroll
  for (int u = 0; u < 5; ++u) retile_unit(c, scr, v[u], dst + u * 1024, ts, cs);
#pragma unroll
  for (int t = 0; t < 4; ++t) {
    const float tot = rows4(cs[t]);
    if (J < 216 && c.lane < 16) st4(c.r, WS_CSG + ((e & 3) * 3456 + (strip * 8 + t * 2 + mat) * 16 + c.lane) * 4, __float_as_uint(tot));
  }
}
DI void att_load(const Ctx& c, int lw, int layer, const uint16_t* wo, const uint16_t* wg, const uint16_t* wu, const uint16_t* wd) {
  const int head = att_head(c), qb = att_qb(c), lane = c.lane;
  const char* Lp = c.ws + WS_L + layer * L_SZ;
  if (lw == 0) {
    const int o0 = (head >> 1) * 5 + (head & 1) * 2, v0 = head * 20;
    const int sel = lane >> 4, cb = sel == 0 ? 5 + (o0 >> 4) : sel == 1 ? 5 + ((o0 + 2) >> 4) : sel == 2 ? 10 + (v0 >> 7) : 10 + ((v0 + 19) >> 7);
    poll(c, FL_QKV + layer * 5, lane < 4 ? (qb * 2 + (lane & 1)) * 16 + ((o0 + (lane & 2)) >> 4) : -1);
    poll(c, FL_QKV + layer * 5, (lane & 15) * 16 + cb);
    sig_set(c, layer * 5 + 2);
  } else sig_wait(c, layer * 5 + 2);
  const unsigned vo = lane * 16;
  auto issue = [&](int grp, int slot) {
#pragma unroll 1
    for (int q = 0; q < 11; ++q) {
      const int p = lw * 11 + q;
      const unsigned src = p < 16 ? L_K1 + head * 65536 + grp * 16384 + p * 1024
                         : p < 24 ? L_K2 + (head >> 1) * 32768 + grp * 8192 + (p - 16) * 1024 : L_V + head * 81920 + grp * 20480 + (p - 24) * 1024;
      glds1(slot * AT_SLOT + p * 1024, Lp + src, vo);
    }
  };
#pragma unroll 1
  for (int q = 0; q < 3; ++q) {
    const int p = lw * 3 + q;
    glds1(2 * AT_SLOT + p * 1024,
          Lp + (p < 8 ? L_Q1 + head * 65536 + qb * 8192 + p * 1024 : L_Q2 + (head >> 1) * 32768 + qb * 4096 + (p - 8) * 1024), vo);
  }
  issue(0, 0);
  wait_vm<0>(); BARRIER();
  issue(1, 1);
  const int J = (lw - 1) * 256 + blockIdx.x;
  const bool rt = layer == 0 && lw > 0 && J < 366;
  u32x4 v[5][4];
  if (rt) { retile_load(c, J, v, wo, wg, wu, wd); wait_vm<20>(); } else wait_vm<0>();
  BARRIER();
  issue(2, 2);
  wait_vm<0>(); BARRIER();
  issue(3, 0);
  if (rt) retile_store(c, J, c.lds + AT_SCR + (lw - 1) * 4096, v);
  if (layer == 0 && (lw == 1 || lw == 2)) publish(c, M_CNT3, 2, FL_RT, blockIdx.x);
  wait_vm<0>(); BARRIER();
  if (layer == 0 && lw >= 2) {
#pragma unroll 1
    for (int q = 0; q < 4; ++q) poll(c, FL_RT, q * 64 + lane);
  }
  BARRIER(); BARRIER();
}

template <int NS, int NB, int WIN>
DI void proj_compute(const Ctx& c, int w, int slot_id, unsigned a_off, const char* res_rm, unsigned res_off, unsigned out_off, unsigned st_off,
                     uint16_t* out, int arr) {
  constexpr bool RES = NS <= NB;
  const int panel = pr_panel(c), cb = pr_cb(c);
  SLOT(slot_id);
  STAMP(0);
  if (w >= 4) {
#pragma unroll 1
    for (int k = 0; k <= (RES ? 1 : NS); ++k) BARRIER();
    return;
  }
  const int lane = c.lane, i = lane & 15, g = lane >> 4, mt = panel * 4 + w;
  u32x2 res[5];
#pragma unroll
  for (int t = 0; t < 5; ++t)
    res[t] = res_rm ? *(const u32x2*)(res_rm + (mt * 16 + i) * 1280 + cb * 160 + t * 32 + g * 8)
                    : ld8(c.r, res_off + (mt * 80 + cb * 10 + t * 2 + (g >> 1)) * 256 + i * 16 + (g & 1) * 8);
  f32x4 acc[5];
#pragma unroll
  for (int t = 0; t < 5; ++t) { acc[t] = f32x4{bflo(res[t][0]), bfhi(res[t][0]), bflo(res[t][1]), bfhi(res[t][1])}; PIN(acc[t]); }
  lds_ptr pb = c.lds + lane * 16;
  unsigned av = a_off + mt * (NS * 2048) + lane * 16;
  u32x4 a[WIN];
  bf16x8 f[3][5];
  int sb = 0;
  STAMP(1);
  BARRIER();
  STAMP(2); STAMPC(0); WRESET();
  constexpr int AH = RES ? 12 : WIN;
#pragma unroll
  for (int k = 0; k < AH; ++k) a[k] = ld16(c.r, av + k * 1024);
#pragma unroll
  for (int t = 0; t < 5; ++t) { f[0][t] = ldsr16(pb + t * 2048); f[1][t] = ldsr16(pb + t * 2048 + 1024); }
  SBAR();
  auto window = [&](bool last) {
#pragma unroll
    for (int k = 0; k < WIN; ++k) {
      const bool ld = !(last && k + 2 >= WIN);
      if ((k & 1) == 0) {
        if (RES) pb += 10240;
        else { pb += sb == NB - 1 ? -(NB - 1) * 10240 : 10240; sb = sb == NB - 1 ? 0 : sb + 1; }
        if (!ld) BARRIER(); else if (!RES) BARRIER_NW();
      }
#pragma unroll
      for (int t = 0; t < 5; ++t) {
        if (ld) f[(k + 2) % 3][t] = ldsr16(pb + t * 2048 + (k & 1) * 1024);
        acc[t] = mfma16(f[k % 3][t], __builtin_bit_cast(bf16x8, a[k]), acc[t]);
        SBAR();
      }
      if (!last) { a[k] = ld16(c.r, av + (WIN + k) * 1024); SBAR(); }
      else if (k + AH < WIN) { a[k + AH] = ld16(c.r, av + (k + AH) * 1024); SBAR(); }
    }
    av += WIN * 1024;
  };
  static_assert(2 * NS % WIN == 0 && (2 * NS == WIN || WIN % 6 == 0), "window");
#pragma unroll 1
  for (int b = 1; b < 2 * NS / WIN; ++b) window(false);
  window(true);
  STAMP(3); STAMPC(1);
  u32x2 o[5];
#pragma unroll
  for (int t = 0; t < 5; ++t) {
    o[t][0] = pk2(acc[t][0], acc[t][1]); o[t][1] = pk2(acc[t][2], acc[t][3]);
  }
  const u32x4 p0 = pair16(o[0][0], o[0][1], o[1][0], o[1][1]), p1 = pair16(o[2][0], o[2][1], o[3][0], o[3][1]),
              p2 = pair16(o[4][0], o[4][1], o[4][0], o[4][1]);
  const int ch = (g & 1) * 2 + (g >> 1);
  if (out) {
    char* po = (char*)out + (mt * 16 + i) * 1280 + cb * 160;
    *(u32x4*)(po + ch * 16) = p0; *(u32x4*)(po + 64 + ch * 16) = p1;
    if (!(g & 1)) *(u32x4*)(po + 128 + (g >> 1) * 16) = p2;
    STAMP(4);
    return;
  }
  const unsigned xo = out_off + (mt * 80 + cb * 10) * 256 + i * 16;
  st16(c.r, xo + ch * 256, p0); st16(c.r, xo + 1024 + ch * 256, p1);
  if (!(g & 1)) st16(c.r, xo + 2048 + (g >> 1) * 256, p2);
  const f32x4 s4 = (acc[0] + acc[1]) + (acc[2] + acc[3]) + acc[4];
  const float sum = rows4((s4[0] + s4[1]) + (s4[2] + s4[3])), mean = sum * (1.f / 80.f);
  f32x4 q4 = {0.f, 0.f, 0.f, 0.f};
#pragma unroll
  for (int t = 0; t < 5; ++t) { const f32x4 d = acc[t] - mean; q4 += d * d; }
  const float m2 = rows4((q4[0] + q4[1]) + (q4[2] + q4[3]));
  if (g == 0) st8(c.r, st_off + (mt >> 3) * 8192 + cb * 1024 + ((mt & 7) * 16 + i) * 8, u32x2{__float_as_uint(sum), __float_as_uint(m2)});
  STAMP(4);
  publish(c, M_CNT4, 4, arr, panel * 8 + cb, true);
  STAMP(5);
}
template <int NS, int NB> DI void proj_b_run(const Ctx& c, int half, unsigned w_off) {
  Ring<5, NB, NS, 2> rg;
#pragma unroll
  for (int p = 0; p < 5; ++p) { const int q = half * 5 + p; rg.voff[p] = w_off + (pr_cb(c) * 5 + (q >> 1)) * (NS * 2048) + (q & 1) * 1024 + c.lane * 16; }
  rg.init(c.ws, 2048, 0, half);
  rg.fill_paced();
  if constexpr (NS <= NB) { wait_vm<0>(); BARRIER_L(); BARRIER_L(); } else rg.run();
}

template <int NF>
DI void gu_consumer(const Ctx& c, int layer, int f0, int mq) {
  const int panel = gu_panel(c), ft0 = (gu_cb(c) * 27) >> 2;
  const int lane = c.lane, r32 = lane & 31, h = lane >> 5;
  f32x16 acc[NF];
#pragma unroll
  for (int t = 0; t < NF; ++t) acc[t] = f32x16{};
  const int lo = (r32 >> 4) * 1024 + h * 256 + (r32 & 15) * 16;
  lds_ptr pa = c.lds + GU_A + mq * 2048 + lo, pb = c.lds + f0 * 2048 + lo;
  bf16x8 x0[2], x1[2], wa[NF], wb0[NF], wb1[NF];
  auto step = [&](const bf16x8* xa, const bf16x8* wb, bf16x8* xn, bf16x8* wn_, bool ld) {
#pragma unroll
    for (int q = 0; q < 2 * NF; ++q) {
      if (ld) {
        if (q == 0) { xn[0] = ldsr16(pa); xn[1] = ldsr16(pa + 512); }
        if (q >= 1 && q <= NF) wn_[q - 1] = ldsr16(pb + (q - 1) * 2048 + 512);
        if (q >= NF) wa[q - NF] = ldsr16(pb + (q - NF) * 2048);
      }
      if (q < NF) acc[q] = mfma32(wa[q], xa[0], acc[q]); else acc[q - NF] = mfma32(wb[q - NF], xa[1], acc[q - NF]);
      SBAR();
    }
  };
  static_assert(GU_NA == GU_NB, "one slot counter");
  int sl = 0;
  auto adv = [&]() {
    pa += sl == GU_NA - 1 ? -(GU_NA - 1) * 8192 : 8192; pb += sl == GU_NB - 1 ? -(GU_NB - 1) * 14336 : 14336;
    sl = sl == GU_NA - 1 ? 0 : sl + 1;
  };
  SLOT(layer * 5 + 3);
  STAMP(0);
  STAMP(1);
  BARRIER();
  STAMP(2); STAMPC(0); WRESET();
  x0[0] = ldsr16(pa); x0[1] = ldsr16(pa + 512);
#pragma unroll
  for (int t = 0; t < NF; ++t) { wa[t] = ldsr16(pb + t * 2048); wb0[t] = ldsr16(pb + t * 2048 + 512); }
#pragma unroll 1
  for (int st = 0; st < 9; ++st) {
    adv(); BARRIER_NW(); step(x0, wb0, x1, wb1, true);
    adv(); BARRIER_NW(); step(x1, wb1, x0, wb0, true);
  }
  adv(); BARRIER_NW(); step(x0, wb0, x1, wb1, true);
  BARRIER(); step(x1, wb1, x0, wb0, false);
  STAMP(3); STAMPC(1);
  const LAS float* srs = (const LAS float*)(c.lds + M_SST) + mq * 32 + r32;
  const float rs = srs[0], nm = srs[128];
  const LAS float* scs = (const LAS float*)(c.lds + M_SCS) + f0 * 32 + h * 4;
  const unsigned ho = WS_L + layer * L_SZ + L_H + ((panel * 8 + mq * 2 + (r32 >> 4)) * 216 + (ft0 + f0) * 2 + h) * 256 + (r32 & 15) * 16;
#pragma unroll
  for (int f = 0; f < NF; ++f) {
    unsigned p[2][2];
#pragma unroll
    for (int e = 0; e < 2; ++e) {
      const f32x4 cg = *(const LAS f32x4*)(scs + f * 32 + e * 8), cu = *(const LAS f32x4*)(scs + f * 32 + 16 + e * 8);
      const f32x16 a = acc[f];
      f32x4 gt = f32x4{a[e * 4], a[e * 4 + 1], a[e * 4 + 2], a[e * 4 + 3]} * rs + cg * nm;
      const f32x4 up = f32x4{a[8 + e * 4], a[9 + e * 4], a[10 + e * 4], a[11 + e * 4]} * rs + cu * nm;
      gt = __builtin_elementwise_max(gt, f32x4{0.f, 0.f, 0.f, 0.f}) * up;
      p[e][0] = pk2(gt[0], gt[1]); p[e][1] = pk2(gt[2], gt[3]);
    }
    st16(c.r, ho + f * 512, pair32(p[0][0], p[0][1], p[1][0], p[1][1]));
  }
  STAMP(4);
  publish(c, M_CNT, 8, FL_GU + layer * 5, panel * 16 + gu_cb(c), true);
  STAMP(5);
}
typedef Ring<7, GU_NB, 20, 2> GuB;
DI void gu_b_init(const Ctx& c, GuB& rg, int half) {
  const int ft0 = (gu_cb(c) * 27) >> 2;
#pragma unroll
  for (int p = 0; p < 7; ++p) rg.voff[p] = WS_WGU + (ft0 * 2 + half * 7 + p) * 20480 + c.lane * 16;
  rg.init(c.ws, 1024, 0, half);
}
DI void gu_b_run(const Ctx& c, int half, int layer) {
  GuB rg;
  gu_b_init(c, rg, half);
  rg.fill_paced();
  if (half == 0) ln_cols<4>(c, 56, WS_CSG + ((gu_cb(c) * 27) >> 2) * 128, 13824);
  wait_vm<(GU_NB - 1) * 7>(); BARRIER_L(); WRESET();
  f32x2 p[8];
#pragma unroll
  for (int i = 0; i < 8; ++i) p[i] = ld8_async(WS_ST + (layer * 2 + 1) * STB + gu_panel(c) * 8192 + i * 1024 + (half * 64 + c.lane) * 8, c.ws);
  wait_vm<(GU_NB - 2) * 7 + 8>(); BARRIER_L();
  asm volatile("s_waitcnt vmcnt(0)" : "+v"(p[0]), "+v"(p[1]), "+v"(p[2]), "+v"(p[3]), "+v"(p[4]), "+v"(p[5]), "+v"(p[6]), "+v"(p[7])::"memory");
  ln_rows(c, half * 64 + c.lane, p);
  rg.steady();
  rg.template range<GU_NB - 3, 0>(); BARRIER_L();
}
DI void gu_a_run(const Ctx& c, int half, unsigned a_off) {
  Ring<4, GU_NA, 20, 2> rg;
#pragma unroll
  for (int p = 0; p < 4; ++p) rg.voff[p] = a_off + (gu_panel(c) * 8 + half * 4 + p) * 20480 + c.lane * 16;
  rg.init(c.ws, 1024, GU_A, half);
  rg.fill();
  rg.run();
}

DI void gate(const Ctx& c, int half, int arr, int idx, unsigned sig) {
  if (half == 0) { poll(c, arr, idx); sig_set(c, sig); } else sig_wait(c, sig);
}

__global__ __launch_bounds__(768) void fused_kernel(const uint16_t* __restrict__ x, const uint16_t* __restrict__ wqkv, const uint16_t* __restrict__ wo,
                                                    const uint16_t* __restrict__ wg, const uint16_t* __restrict__ wu,
                                                    const uint16_t* __restrict__ wd, uint16_t* __restrict__ out, char* __restrict__ ws) {
  extern __shared__ __attribute__((aligned(16))) char smem[];
  Ctx c;
  c.ws = ws; c.lds = (lds_ptr)smem; c.lane = threadIdx.x & 63; c.xcd = blockIdx.x & 7; c.j = blockIdx.x >> 3;
  c.r = __builtin_amdgcn_make_buffer_rsrc(ws, 0, WS_END, 0x00020000);
  const int w = __builtin_amdgcn_readfirstlane(threadIdx.x >> 6);
  { SLOT(0); STAMP(0); }
  SPANST(0);
  c.tgt = *(const u64*)(ws + WS_EPOCH) + 1;
  if (threadIdx.x < 16) *(LAS unsigned*)(c.lds + M_CNT + threadIdx.x * 4) = 0u;
  const bool qkv_on = qkv_cb(c) != 15;
  const Ctx c0 = c;
  WDUMP(15);
  if (w >= 8) __builtin_amdgcn_s_setprio(3);
  if (w >= 10) {
    const int half = w - 10;
    QkvB rq;
    qkv_b_init(c, rq, half, wqkv);
    if (qkv_on) rq.fill();
    BARRIER();
#pragma unroll 1
    for (int layer = 0; layer < 2; ++layer) {
      if (qkv_on) rq.run();
      WDUMP(layer * 5);
      att_load(fresh(c0), opq(half + 2), opq(layer), wo, wg, wu, wd);
      WDUMP(layer * 5 + 1);
      sig_wait(c0, layer * 24 + 8, M_ISS);
      proj_b_run<10, WO_NB>(fresh(c0), opq(half), WS_WO);
      WDUMP(layer * 5 + 2);
      sig_wait(c0, layer * 24 + 12, M_ISS);
      gu_b_run(fresh(c0), opq(half), opq(layer));
      WDUMP(layer * 5 + 3);
      sig_wait(c0, layer * 24 + 20, M_ISS);
      proj_b_run<27, DN_NB>(fresh(c0), opq(half), WS_WD);
      WDUMP(layer * 5 + 4);
      if (layer == 0 && qkv_on) { sig_wait(c0, 24, M_ISS); qkv_b_init(fresh(c0), rq, opq(half), wqkv); rq.fill_paced(); }
    }
  } else if (w >= 8) {
    const int half = w - 8;
    if (!qkv_on) BARRIER();
#pragma unroll 1
    for (int layer = 0; layer < 2; ++layer) {
      const unsigned L = WS_L + layer * L_SZ;
      const Ctx c = fresh(c0);
      const int l = c.lane;
      if (qkv_on) {
        if (layer) gate(c, half, FL_DN, l < 16 ? qkv_panel(c) * 16 + l : -1, 6);
        qkv_a_run(fresh(c0), opq(half), opq(layer), x);
      }
      WDUMP(layer * 5);
      att_load(fresh(c0), opq(half), opq(layer), wo, wg, wu, wd);
      WDUMP(layer * 5 + 1);
      if (half == 0) poll(c, FL_ATT + layer * 5, l < 32 ? (pr_panel(c) >> 2) * 32 + l : -1);
      BARRIER_L(); BARRIER_L();
      WDUMP(layer * 5 + 2);
      gate(c, half, FL_WO + layer * 5, l < 16 ? gu_panel(c) * 16 + l : -1, layer * 5 + 4);
      gu_a_run(fresh(c0), opq(half), L + L_X1);
      WDUMP(layer * 5 + 3);
      if (half == 0) poll(c, FL_GU + layer * 5, l < 16 ? (pr_panel(c) >> 1) * 16 + l : -1);
      if (layer == 1 && half == 0 && blockIdx.x == 0 && l == 0) *(u64*)(ws + WS_EPOCH) = c.tgt;
#pragma unroll 1
      for (int k = 0; k <= 27; ++k) BARRIER_L();
      WDUMP(layer * 5 + 4);
    }
  } else {
    BARRIER();
    if (w < 2) {
      const char* pc; constexpr unsigned len = 51 * 1024;
      asm volatile("s_getpc_b64 %0" : "=s"(pc));
      const unsigned o = (c.j * 2 + w) * 1024 + c.lane * 16;
      if (o + 16 <= len) { const u32x4 t = *(const GAS u32x4*)(pc + o); asm volatile("" ::"v"(t)); }
    }
    if (w >= 4) phase0(c, w - 4, x, wqkv);
#pragma unroll 1
    for (int layer = 0; layer < 2; ++layer) {
      const unsigned L = WS_L + layer * L_SZ;
      qkv_compute(fresh(c0), opq(w), opq(layer));
      WDUMP(layer * 5);
      att_compute(fresh(c0), opq(w), opq(layer));
      WDUMP(layer * 5 + 1);
      proj_compute<10, WO_NB, 20>(fresh(c0), opq(w), layer * 5 + 2, L + L_O, layer == 0 ? (const char*)x : nullptr, WS_X2, L + L_X1,
                                  WS_ST + (layer * 2 + 1) * STB, nullptr, FL_WO + layer * 5);
      WDUMP(layer * 5 + 2);
      if (w < 4) gu_consumer<4>(fresh(c0), opq(layer), 0, opq(w)); else gu_consumer<3>(fresh(c0), opq(layer), 4, opq(w - 4));
      WDUMP(layer * 5 + 3);
      proj_compute<27, DN_NB, 18>(fresh(c0), opq(w), layer * 5 + 4, L + L_H, nullptr, L + L_X1, WS_X2, WS_ST + 2 * STB, layer ? out : nullptr,
                                  FL_DN + layer * 5);
      WDUMP(layer * 5 + 4);
    }
  }
}

extern "C" void kernel_launch(void* const* d_in, const int* in_sizes, int n_in, void* d_out, int out_size, void* d_ws, size_t ws_size,
                              hipStream_t stream) {
  (void)hipFuncSetAttribute((const void*)fused_kernel, hipFuncAttributeMaxDynamicSharedMemorySize, LDS_BYTES);
  fused_kernel<<<256, 768, LDS_BYTES, stream>>>((const uint16_t*)d_in[0], (const uint16_t*)d_in[1], (const uint16_t*)d_in[2],
                                                (const uint16_t*)d_in[4], (const uint16_t*)d_in[5], (const uint16_t*)d_in[6],
                                                (uint16_t*)d_out, (char*)d_ws);
}
```

```cpp
#include <hip/hip_runtime.h>
#include <hip/hip_bf16.h>
#include <stdint.h>

typedef __attribute__((ext_vector_type(8))) __bf16 bf16x8;
typedef __attribute__((ext_vector_type(16))) __bf16 bf16x16;
typedef __attribute__((ext_vector_type(4))) short s16x4;
typedef __attribute__((ext_vector_type(8))) short s16x8;
typedef __attribute__((ext_vector_type(2))) __bf16 bf16x2;
typedef __attribute__((ext_vector_type(2))) float f32x2;
typedef __attribute__((ext_vector_type(4))) float f32x4;
typedef __attribute__((ext_vector_type(8))) float f32x8;
typedef __attribute__((ext_vector_type(16))) float f32x16;
typedef __attribute__((ext_vector_type(2))) unsigned u32x2;
typedef __attribute__((ext_vector_type(4))) unsigned u32x4;
typedef unsigned long long u64;
#define LAS __attribute__((address_space(3)))
#define GAS __attribute__((address_space(1)))
typedef LAS char* lds_ptr;
typedef __amdgpu_buffer_rsrc_t rsrc_t;
#define DI __device__ __forceinline__

constexpr unsigned XB = 2048u * 640 * 2;
constexpr unsigned WS_EPOCH = 0;
constexpr unsigned WS_FLAG = 4096;
constexpr int FL_P0 = 0, FL_RT = 1, FL_QKV = 2, FL_ATT = 3, FL_WO = 4, FL_GU = 5, FL_DN = 6, FL_RT2 = 12;
constexpr unsigned WS_WO  = WS_FLAG + 13u * 256 * 9216;
constexpr unsigned WS_WGU = WS_WO + 640u * 640 * 2;
constexpr unsigned WS_WD  = WS_WGU + 3456u * 640 * 2;
constexpr unsigned WS_CSQ = WS_WD + 640u * 1728 * 2;
constexpr unsigned WS_CSG = WS_CSQ + 8u * 1920 * 4;
constexpr unsigned WS_ST  = WS_CSG + 4u * 3456 * 4;
constexpr unsigned STB = 2048u * 64;
constexpr unsigned WS_X2  = WS_ST + 4 * STB;
constexpr unsigned WS_L   = WS_X2 + XB;
constexpr unsigned L_Q1 = 0, L_Q2 = L_Q1 + 32u * 65536, L_K1 = L_Q2 + 16u * 32768, L_K2 = L_K1 + 32u * 65536, L_V = L_K2 + 16u * 32768,
                   L_O = L_V + XB, L_X1 = L_O + XB, L_H = L_X1 + XB, L_SZ = L_H + 2048u * 1728 * 2;
constexpr unsigned WS_END = WS_L + 2 * L_SZ;

constexpr int LDS_BYTES = 163840, MISC = 159744, M_SST = MISC, M_SCS = MISC + 1024, M_CNT = MISC + 2048, M_CNT2 = M_CNT + 8, M_CNT3 = M_CNT + 16, M_CNT4 = M_CNT + 24, M_SIG = M_CNT + 32, M_ISS = M_CNT + 40;
constexpr int QKV_NB = 5, QKV_NA = 4, QKV_A = QKV_NB * 16384;
constexpr int WO_NB = 10, DN_NB = 15;
constexpr int GU_NB = 7, GU_NA = 7, GU_A = GU_NB * 14336;
constexpr int AT_SLOT = 45056, AT_CONST = 3 * AT_SLOT, AT_SCR = AT_CONST + 10752, AT_MERGE = 3 * AT_SLOT;
static_assert(QKV_A + QKV_NA * 16384 <= MISC && DN_NB * 10240 <= MISC && GU_A + GU_NA * 8192 <= MISC, "lds");
static_assert(WO_NB * 10240 <= AT_MERGE && AT_MERGE + 24576 <= MISC && AT_SCR + 3 * 4096 <= MISC, "lds");

DI unsigned pk2(float a, float b) { f32x2 v = {a, b}; bf16x2 r = __builtin_convertvector(v, bf16x2); return __builtin_bit_cast(unsigned, r); }
DI float bflo(unsigned u) { return __uint_as_float(u << 16); }
DI float bfhi(unsigned u) { return __uint_as_float(u & 0xffff0000u); }
DI f32x4 mfma16(bf16x8 a, bf16x8 b, f32x4 c) { return __builtin_amdgcn_mfma_f32_16x16x32_bf16(a, b, c, 0, 0, 0); }
DI f32x16 mfma32(bf16x8 a, bf16x8 b, f32x16 c) { return __builtin_amdgcn_mfma_f32_32x32x16_bf16(a, b, c, 0, 0, 0); }
DI bf16x8 ldsr16(lds_ptr p) { return *(const LAS bf16x8*)p; }
DI bf16x8 ldstr(lds_ptr p, int second) {
  const s16x4 a = __builtin_amdgcn_ds_read_tr16_b64_v4i16((LAS s16x4*)p), b = __builtin_amdgcn_ds_read_tr16_b64_v4i16((LAS s16x4*)(p + second));
  return __builtin_bit_cast(bf16x8, (s16x8)__builtin_shufflevector(a, b, 0, 1, 2, 3, 4, 5, 6, 7));
}
#define BARRIER() asm volatile("s_waitcnt lgkmcnt(0)\n\ts_barrier" ::: "memory")
template <int N> DI void wait_vm() { asm volatile("s_waitcnt vmcnt(%0)" ::"n"(N > 63 ? 63 : N) : "memory"); }
#define WDUMP(id)
#define WRESET()
#define BARRIER_L() BARRIER()
#define BARRIER_NW() asm volatile("s_barrier" ::: "memory")
#define SBAR() __builtin_amdgcn_sched_barrier(0)
#define PIN(x) asm volatile("" : "+v"(x))
#define GL_HEAD "s_mov_b32 m0, %0\n\ts_nop 3\n\t"
#define GL(v, off) "global_load_lds_dwordx4 %" #v ", %1 offset:" #off " sc1\n\t"
DI void glds1(unsigned m, const char* sb, unsigned v0) { asm volatile(GL_HEAD GL(2, 0)::"s"(m), "s"(sb), "v"(v0) : "memory"); }
DI void glds2(unsigned m, const char* sb, unsigned v0, unsigned v1) {
  asm volatile(GL_HEAD GL(2, 0) GL(3, 1024)::"s"(m), "s"(sb), "v"(v0), "v"(v1) : "memory");
}
DI void glds3(unsigned m, const char* sb, unsigned v0, unsigned v1, unsigned v2) {
  asm volatile(GL_HEAD GL(2, 0) GL(3, 1024) GL(4, 2048)::"s"(m), "s"(sb), "v"(v0), "v"(v1), "v"(v2) : "memory");
}
DI void glds4(unsigned m, const char* sb, unsigned v0, unsigned v1, unsigned v2, unsigned v3) {
  asm volatile(GL_HEAD GL(2, 0) GL(3, 1024) GL(4, 2048) GL(5, 3072)::"s"(m), "s"(sb), "v"(v0), "v"(v1), "v"(v2), "v"(v3) : "memory");
}
DI void st16(rsrc_t r, unsigned off, u32x4 v) { __builtin_amdgcn_raw_buffer_store_b128(v, r, off, 0, 16); }
DI void st8(rsrc_t r, unsigned off, u32x2 v) { __builtin_amdgcn_raw_buffer_store_b64(v, r, off, 0, 16); }
DI void st4(rsrc_t r, unsigned off, unsigned v) { __builtin_amdgcn_raw_buffer_store_b32(v, r, off, 0, 16); }
DI u32x4 ld16(rsrc_t r, unsigned off) { return __builtin_amdgcn_raw_buffer_load_b128(r, off, 0, 16); }
DI u32x2 ld8(rsrc_t r, unsigned off) { return __builtin_amdgcn_raw_buffer_load_b64(r, off, 0, 16); }
DI f32x2 ld8_async(unsigned voff, const char* sbase) {
  f32x2 v; asm volatile("s_nop 4\n\tglobal_load_dwordx2 %0, %1, %2 sc1" : "=v"(v) : "v"(voff), "s"(sbase) : "memory"); return v;
}
DI u32x4 pair32(unsigned a0, unsigned a1, unsigned b0, unsigned b1) {
  auto x = __builtin_amdgcn_permlane32_swap(a0, b0, false, false);
  auto y = __builtin_amdgcn_permlane32_swap(a1, b1, false, false);
  return u32x4{x[0], y[0], x[1], y[1]};
}
template <int N> DI float ror16(float v) { return __int_as_float(__builtin_amdgcn_update_dpp(0, __float_as_int(v), 0x120 + N, 0xf, 0xf, false)); }
DI float sum32(float v) {
  v += ror16<8>(v); v += ror16<4>(v); v += ror16<2>(v); v += ror16<1>(v);
  auto a = __builtin_amdgcn_permlane16_swap(__float_as_uint(v), __float_as_uint(v), false, false);
  return __uint_as_float(a[0]) + __uint_as_float(a[1]);
}
DI u32x4 pair16(unsigned a0, unsigned a1, unsigned b0, unsigned b1) {
  auto x = __builtin_amdgcn_permlane16_swap(a0, b0, false, false);
  auto y = __builtin_amdgcn_permlane16_swap(a1, b1, false, false);
  return u32x4{x[0], y[0], x[1], y[1]};
}

DI float rows4(float v) {
  auto a = __builtin_amdgcn_permlane16_swap(__float_as_uint(v), __float_as_uint(v), false, false);
  v = __uint_as_float(a[0]) + __uint_as_float(a[1]);
  auto b = __builtin_amdgcn_permlane32_swap(__float_as_uint(v), __float_as_uint(v), false, false);
  return __uint_as_float(b[0]) + __uint_as_float(b[1]);
}

#define STAMP(id)
#define STAMPC(id)
#define STAMPL(sl, id)
#define SLOT(n)

#define WARM_WO 1
#define WARM_GU 1
#define WARM_DN 0
#define POLL_DEPTH 1
#define POLL_GATE 1
#define POLL_SLEEP 1
#define DN_D0 10
#define A_D0 2
#define ATT_PRIO 3
#define ATT_ORD 0
#define PACE 9
#define SPANST(id)
struct Ctx {
  char* ws; rsrc_t r; lds_ptr lds; u64 tgt; int lane, xcd, j;
};
DI Ctx fresh(const Ctx& c0) { Ctx c = c0; asm volatile("" : "+v"(c.lane), "+s"(c.xcd), "+s"(c.j), "+s"(c.ws)); return c; }
DI int opq(int v) { asm volatile("" : "+s"(v)); return v; }
constexpr int SPIN_CAP = 1 << 18;

#define PAIR 0
DI unsigned box(int arr, int unit, int slot) { return WS_FLAG + (arr * 256 + unit) * 9216 + slot * 128; }
DI unsigned flg(int arr, int unit) { return WS_FLAG + (arr * 256 + unit) * 128; }
DI void publish(const Ctx& c, int cnt_off, unsigned n, unsigned to, bool mark = false) {
  if (mark && c.lane == 0) __hip_atomic_fetch_add((LAS unsigned*)(c.lds + M_ISS), 1u, __ATOMIC_RELAXED, __HIP_MEMORY_SCOPE_WORKGROUP);
  wait_vm<0>();
  unsigned old = 0;
  if (c.lane == 0) old = __hip_atomic_fetch_add((LAS unsigned*)(c.lds + cnt_off), 1u, __ATOMIC_RELAXED, __HIP_MEMORY_SCOPE_WORKGROUP);
  if (__builtin_amdgcn_readfirstlane(old) % n == n - 1 && to != ~0u)
    __hip_atomic_store((GAS u64*)(c.ws + to), c.tgt, __ATOMIC_RELAXED, __HIP_MEMORY_SCOPE_AGENT);
}
template <int N> struct Poll {
  u64 v[POLL_DEPTH][N];
  DI void run(const Ctx& c, unsigned i0, unsigned i1 = ~0u) {
    const unsigned f0 = __builtin_amdgcn_readfirstlane(i0), f1 = __builtin_amdgcn_readfirstlane(i1);
    const unsigned o0 = i0 == ~0u ? f0 : i0, o1 = i1 == ~0u ? f1 : i1;
    auto ld = [&](int r) {
      asm volatile("global_load_dwordx2 %0, %1, %2 sc1" : "=v"(v[r][0]) : "v"(o0), "s"(c.ws) : "memory");
      if constexpr (N == 2) asm volatile("global_load_dwordx2 %0, %1, %2 sc1" : "=v"(v[r][N - 1]) : "v"(o1), "s"(c.ws) : "memory");
      __builtin_amdgcn_s_sleep(POLL_SLEEP);
    };
    auto ok = [&](int r) {
      if constexpr (N == 2) asm volatile("s_waitcnt vmcnt(%2)" : "+v"(v[r][0]), "+v"(v[r][N - 1]) : "n"((POLL_DEPTH - 1) * N) : "memory");
      else asm volatile("s_waitcnt vmcnt(%1)" : "+v"(v[r][0]) : "n"(POLL_DEPTH - 1) : "memory");
      return __all((i0 == ~0u || v[r][0] == c.tgt) && (N == 1 || i1 == ~0u || v[r][N - 1] == c.tgt));
    };
    asm volatile("s_nop 4" ::: "memory");
    ld(0); if (POLL_DEPTH > 1) ld(1);
#pragma unroll 1
    for (int spin = 0; spin < SPIN_CAP; ++spin) {
      if (ok(0)) break; ld(0);
      if (POLL_DEPTH > 1) { if (ok(1)) break; ld(1); }
    }
  }
  DI void keep() {
#pragma unroll
    for (int r = 0; r < POLL_DEPTH; ++r) { asm volatile("" : "+v"(v[r][0])); if constexpr (N == 2) asm volatile("" : "+v"(v[r][N - 1])); }
  }
};
DI void poll(const Ctx& c, unsigned slot) { Poll<1> p; p.run(c, slot); wait_vm<0>(); p.keep(); }
DI void sig_set(const Ctx& c, unsigned v) { if (c.lane == 0) __hip_atomic_store((LAS unsigned*)(c.lds + M_SIG), v, __ATOMIC_RELAXED, __HIP_MEMORY_SCOPE_WORKGROUP); }
DI void sig_wait(const Ctx& c, unsigned v, int off = M_SIG) {
#pragma unroll 1
  for (int spin = 0; spin < SPIN_CAP * 16; ++spin) {
    if (__builtin_amdgcn_readfirstlane(__hip_atomic_load((LAS unsigned*)(c.lds + off), __ATOMIC_RELAXED, __HIP_MEMORY_SCOPE_WORKGROUP)) >= v) break;
    if (off != M_SIG) __builtin_amdgcn_s_sleep(1);
  }
  asm volatile("" ::: "memory");
}

DI void gate(const Ctx& c, Poll<1>& p, int half, unsigned slot, unsigned sig, unsigned iss) {
  if (half == 0) { if (POLL_GATE) sig_wait(c, iss, M_ISS); p.run(c, slot); sig_set(c, sig); STAMPL(sig - 1, 6); } else sig_wait(c, sig);
}

template <int PP, int NSLOT, int NS, int LAG = 1>
struct Ring {
  unsigned voff[PP];
  const char* base;
  unsigned adv, l0;
  int slot;
  DI void init(const char* b, unsigned a, unsigned lds0, int half) {
    base = b - 4096; adv = a; l0 = lds0 + half * PP * 1024; slot = 0;
#pragma unroll
    for (int p = 0; p < PP; ++p) voff[p] += 4096 - (p & 3) * 1024;
  }
  DI void issue() {
    const unsigned m = l0 + slot * (2 * PP * 1024);
    glds4(m, base, voff[0], voff[1], voff[2], voff[3]);
    if constexpr (PP == 5) glds1(m + 4096, base, voff[4]);
    if constexpr (PP == 7) glds3(m + 4096, base, voff[4], voff[5], voff[6]);
    if constexpr (PP == 8) glds4(m + 4096, base, voff[4], voff[5], voff[6], voff[7]);
    base += adv; slot = slot == NSLOT - 1 ? 0 : slot + 1;
  }
  template <int N = NSLOT> DI void fill() {
#pragma unroll 1
    for (int s = 0; s < N; ++s) issue();
  }
  template <int N = NSLOT> DI void fill_paced() {
#pragma unroll 1
    for (int s = 0; s < N; ++s) { issue(); wait_vm<PACE>(); }
  }
  template <int R, int E> DI void range() { if constexpr (R >= E) { wait_vm<R * PP>(); BARRIER_L(); range<R - 1, E>(); } }
  template <int D0 = NSLOT> DI void head() {
    wait_vm<(D0 - 1) * PP>(); BARRIER_L(); WRESET();
    if constexpr (D0 < NSLOT) fill<NSLOT - D0>();
    range<NSLOT - 2, NSLOT - LAG>();
    steady();
  }
  DI void steady() {
#pragma unroll 1
    for (int k = LAG; k < NS - NSLOT + LAG; ++k) { wait_vm<(NSLOT - 1 - LAG) * PP>(); BARRIER_L(); issue(); }
  }
  template <int D0 = NSLOT> DI void run() { head<D0>(); range<NSLOT - 1 - LAG, 0>(); BARRIER_L(); }
};

DI void ln_rows(const Ctx& c, int row, const f32x2 (&p)[8]) {
  f32x2 t = p[0];
#pragma unroll
  for (int i = 1; i < 8; ++i) t += p[i];
  const float m8 = t[0] * 0.125f; float q = 0.f;
#pragma unroll
  for (int i = 0; i < 8; ++i) { const float a = p[i][0] - m8; q += a * a; }
  const float rstd = __builtin_amdgcn_rsqf((t[1] + q * (1.f / 80.f)) * (1.f / 640.f) + 1e-5f);
  *(LAS float*)(c.lds + M_SST + row * 4) = rstd;
  *(LAS float*)(c.lds + M_SST + 512 + row * 4) = -t[0] * (1.f / 640.f);
}
template <int NP> DI void ln_cols(const Ctx& c, int n4, unsigned off, unsigned pitch) {
  u32x4 q[NP];
#pragma unroll
  for (int i = 0; i < NP; ++i) q[i] = ld16(c.r, off + i * pitch + (c.lane < n4 ? c.lane : 0) * 16);
  f32x4 cs = __builtin_bit_cast(f32x4, q[0]);
#pragma unroll
  for (int i = 1; i < NP; ++i) cs += __builtin_bit_cast(f32x4, q[i]);
  if (c.lane < n4) *(LAS f32x4*)(c.lds + M_SCS + c.lane * 16) = cs;
}

#define MAP 0
DI int qkv_panel(const Ctx& c) { return (c.xcd & 3) * 4 + (c.j & 3); }
DI int qkv_cb(const Ctx& c) { return (c.xcd >> 2) * 8 + (c.j >> 2); }
DI int pr_panel(const Ctx& c) { return (c.xcd & 3) * 8 + (c.j & 7); }
DI int pr_cb(const Ctx& c) { return (c.xcd >> 2) * 4 + (c.j >> 3); }
DI int gu_panel(const Ctx& c) { return (c.xcd & 1) * 8 + (c.j & 7); }
DI int gu_cb(const Ctx& c) { return (c.xcd >> 1) * 4 + (c.j >> 3); }
DI int att_head(const Ctx& c) { return c.xcd * 4 + (c.j & 3); }
DI int att_qb(const Ctx& c) { return c.j >> 2; }

struct Phase0 {
  u32x4 v[3], t[10];
  DI void load(const Ctx& c, int hw, const uint16_t* x, const uint16_t* wqkv) {
    const int lane = c.lane, r32 = lane & 31, b = blockIdx.x, row = b * 8 + hw * 2 + (lane >> 5);
    v[0] = *(const u32x4*)(x + row * 640 + r32 * 8); v[1] = *(const u32x4*)(x + row * 640 + 256 + r32 * 8);
    v[2] = *(const u32x4*)(x + row * 640 + 512 + (r32 & 15) * 8);
    if (hw == 3 && b < 240) {
      const rsrc_t r = __builtin_amdgcn_make_buffer_rsrc((void*)wqkv, 0, 640 * 3840, 0x00020000);
#pragma unroll
      for (int i = 0; i < 10; ++i)
        t[i] = __builtin_amdgcn_raw_buffer_load_b128(r, (lane >> 3) * 3840 + (lane & 7) * 16, ((b / 30) * 80 + i * 8) * 3840 + (b % 30) * 128, 0);
    }
  }
  DI void rows(const Ctx& c, int hw) {
    const int lane = c.lane, r32 = lane & 31, row = blockIdx.x * 8 + hw * 2 + (lane >> 5);
    const float w2 = r32 < 16 ? 1.f : 0.f;
    float sum = 0.f, s2 = 0.f;
#pragma unroll
    for (int e = 0; e < 4; ++e) { sum += (bflo(v[0][e]) + bfhi(v[0][e])) + (bflo(v[1][e]) + bfhi(v[1][e])); s2 += bflo(v[2][e]) + bfhi(v[2][e]); }
    sum = sum32(sum + w2 * s2);
    const float mean = sum * (1.f / 640.f); float m2 = 0.f; s2 = 0.f;
#pragma unroll
    for (int e = 0; e < 4; ++e) {
      const float a0 = bflo(v[0][e]) - mean, a1 = bfhi(v[0][e]) - mean, a2 = bflo(v[1][e]) - mean, a3 = bfhi(v[1][e]) - mean;
      const float a4 = bflo(v[2][e]) - mean, a5 = bfhi(v[2][e]) - mean;
      m2 += a0 * a0 + a1 * a1 + a2 * a2 + a3 * a3; s2 += a4 * a4 + a5 * a5;
    }
    m2 = sum32(m2 + w2 * s2);
    if (r32 < 8) st8(c.r, WS_ST + (row >> 7) * 8192 + r32 * 1024 + (row & 127) * 8, u32x2{__float_as_uint(sum * 0.125f), __float_as_uint(m2 * 0.125f)});
  }
  float s[8];
  DI void cols1(int hw) {
    if (hw == 3 && blockIdx.x < 240) {
      unsigned ul = 0x00003f80u, uh = 0x3f800000u;
      asm volatile("" : "+v"(ul), "+v"(uh));
      const bf16x2 lo = __builtin_bit_cast(bf16x2, ul), hi = __builtin_bit_cast(bf16x2, uh);
#pragma unroll
      for (int e = 0; e < 4; ++e) {
        s[2 * e] = 0.f; s[2 * e + 1] = 0.f;
#pragma unroll
        for (int i = 0; i < 10; ++i) {
          const unsigned u = t[i][e];
          s[2 * e] = __builtin_amdgcn_fdot2_f32_bf16(__builtin_bit_cast(bf16x2, u), lo, s[2 * e], false);
          s[2 * e + 1] = __builtin_amdgcn_fdot2_f32_bf16(__builtin_bit_cast(bf16x2, u), hi, s[2 * e + 1], false);
        }
      }
#pragma unroll
      for (int e = 0; e < 8; ++e) PIN(s[e]);
    }
  }
  DI void cols2(const Ctx& c, int hw) {
    const int lane = c.lane, b = blockIdx.x;
    if (hw == 3 && b < 240) {
#pragma unroll
      for (int e = 0; e < 8; ++e) s[e] = rows4(s[e] + ror16<8>(s[e]));
      const int hi = (lane >> 3) & 1;
      const f32x4 o = hi ? f32x4{s[4], s[5], s[6], s[7]} : f32x4{s[0], s[1], s[2], s[3]};
      if (lane < 16) st16(c.r, WS_CSQ + ((b / 30) * 1920 + (b % 30) * 64 + (lane & 7) * 8 + hi * 4) * 4, __builtin_bit_cast(u32x4, o));
    }
  }
};

template <bool SWAP, class PRE>
DI void qkv_loop(const Ctx& c, int w, int layer, f32x16 (&acc)[2][2], PRE pre) {
  const int lane = c.lane, i = lane & 15, g = lane >> 4, wm = w & 1, wn = w >> 1;
  lds_ptr pa[4];
#pragma unroll
  for (int s = 0; s < 4; ++s)
    pa[s] = c.lds + QKV_A + wm * 8192 + (g & 1) * 2048 +
            (layer == 0 ? (i >> 3) * 1024 + (i & 7) * 128 + (((2 * s + (g >> 1)) ^ ((i >> 1) & 7)) << 4) : (2 * s + (g >> 1)) * 256 + i * 16);
  const int rb = ((g >> 1) * 8 + (i >> 2)) * 256 + (i & 3) * 8, u = (wn * 4 + (g & 1)) ^ ((i >> 2) << 1);
  lds_ptr pb0 = c.lds + rb + (u << 5), pb1 = c.lds + rb + ((u ^ 2) << 5);
  bf16x8 x0[4], x1[4], w0[4], w1[4];
  auto rdx = [&](bf16x8* xn, int hs, int e) { xn[e] = ldsr16(pa[hs * 2 + (e >> 1)] + (e & 1) * 4096); };
  auto rdw = [&](bf16x8* wn_, int hs, int e) { wn_[e] = ldstr(((e & 1) ? pb1 : pb0) + (hs * 2 + (e >> 1)) * 4096, 1024); };
  auto step = [&](const bf16x8* xa, const bf16x8* wb, bf16x8* xn, bf16x8* wn_, int hs, bool ld) {
#pragma unroll
    for (int q = 0; q < 8; ++q) {
      if (ld) { if (q & 1) rdx(xn, hs, q >> 1); else rdw(wn_, hs, q >> 1); }
      const int s = q >> 2, tm = q & 1, tn = (q >> 1) & 1;
      acc[tm][tn] = SWAP ? mfma32(wb[s * 2 + tn], xa[s * 2 + tm], acc[tm][tn]) : mfma32(xa[s * 2 + tm], wb[s * 2 + tn], acc[tm][tn]);
      SBAR();
    }
  };
  SLOT(layer * 5);
  if (layer) STAMP(0);
  STAMP(1);
  BARRIER();
  STAMP(2); STAMPC(0); WRESET();
#pragma unroll
  for (int e = 0; e < 4; ++e) { rdw(w0, 0, e); rdx(x0, 0, e); }
  int sa = 0, sb = 0;
#pragma unroll 1
  for (int st = 0; st < 9; ++st) {
    step(x0, w0, x1, w1, 1, true);
    const int da = sa == QKV_NA - 1 ? -(QKV_NA - 1) * 16384 : 16384, db = sb == QKV_NB - 1 ? -(QKV_NB - 1) * 16384 : 16384;
    sa = sa == QKV_NA - 1 ? 0 : sa + 1; sb = sb == QKV_NB - 1 ? 0 : sb + 1;
    pa[0] += da; pa[1] += da; pa[2] += da; pa[3] += da; pb0 += db; pb1 += db;
    BARRIER_NW();
    step(x1, w1, x0, w0, 0, true);
  }
  step(x0, w0, x1, w1, 1, true);
  BARRIER();
  pre(); SBAR();
  step(x1, w1, x0, w0, 0, false);
  STAMP(3); STAMPC(1);
}
DI unsigned p0_to(const Ctx& c) {
  const int b = blockIdx.x, l = c.lane, strip = b % 30;
  return l < 15 ? box(FL_P0, (b >> 4) * 16 + l, b & 15)
                : l >= 16 && l < 32 && b < 240 ? box(FL_P0, (l - 16) * 16 + (strip >> 1), 16 + (b / 30) * 2 + (strip & 1)) : ~0u;
}
DI void qkv_helper(const Ctx& c, int hw, int layer, const uint16_t* x, const uint16_t* wqkv) {
  const int panel = qkv_panel(c), cb = qkv_cb(c), l = c.lane;
  const bool on = cb != 15;
  if (layer == 0) {
    Phase0 p0;
    p0.load(c, hw, x, wqkv);
    if (on) BARRIER();
    p0.cols1(hw);
    if (on && hw == 3) BARRIER();
    p0.rows(c, hw);
    if (on && hw != 3) BARRIER();
    p0.cols2(c, hw);
    if (on) { BARRIER(); BARRIER(); }
    publish(c, M_CNT2, 4, PAIR ? p0_to(c) : c.lane == 0 ? flg(FL_P0, blockIdx.x) : ~0u);
    if (!on) return;
    BARRIER(); BARRIER();
    const unsigned at = PAIR ? box(FL_P0, panel * 16 + cb, hw < 2 ? hw * 8 + (l & 7) : 16 + (l & 15)) : flg(FL_P0, hw < 2 ? panel * 16 + hw * 8 + (l & 7) : (l & 7) * 30 + cb * 2 + ((l >> 3) & 1));
    const u64 v = __hip_atomic_load((const GAS u64*)(c.ws + at), __ATOMIC_RELAXED, __HIP_MEMORY_SCOPE_AGENT);
    BARRIER();
    if (!__all(v == c.tgt)) poll(c, at);
  } else {
    if (!on) return;
    BARRIER();
  }
  f32x2 p[8];
  if (hw < 2) {
#pragma unroll
    for (int i = 0; i < 8; ++i)
      p[i] = __builtin_bit_cast(f32x2, ld8(c.r, WS_ST + layer * 2 * STB + panel * 8192 + i * 1024 + (hw * 64 + l) * 8));
  }
  BARRIER();
  if (hw == 2) ln_cols<8>(c, 32, WS_CSQ + cb * 512, 7680);
  BARRIER();
  if (hw < 2) ln_rows(c, hw * 64 + l, p);
  if (layer) { BARRIER(); BARRIER(); BARRIER(); BARRIER(); BARRIER(); BARRIER(); }
  BARRIER(); BARRIER();
}
DI void qkv_compute(const Ctx& c, int w, int layer, const uint16_t* x, const uint16_t* wqkv) {
  const int panel = qkv_panel(c), cb = qkv_cb(c);
  if (w >= 4) { qkv_helper(c, w - 4, layer, x, wqkv); return; }
  if (cb == 15) return;
  const int lane = c.lane, r32 = lane & 31, h = lane >> 5, wm = w & 1, wn = w >> 1;
  const unsigned L = WS_L + layer * L_SZ;
  f32x16 acc[2][2] = {{f32x16{}, f32x16{}}, {f32x16{}, f32x16{}}};
  const LAS float* srs = (const LAS float*)(c.lds + M_SST) + wm * 64;
  const LAS float* scs = (const LAS float*)(c.lds + M_SCS) + wn * 64;
  if (cb < 10) {
    float rs[2], nm[2]; f32x4 cs[8];
    qkv_loop<true>(c, w, layer, acc, [&]() {
      rs[0] = srs[r32]; rs[1] = srs[32 + r32]; nm[0] = srs[128 + r32]; nm[1] = srs[160 + r32];
#pragma unroll
      for (int e = 0; e < 8; ++e) cs[e] = *(const LAS f32x4*)(scs + e * 8 + h * 4);
    });
    const float qs = cb < 5 ? 0.22360679774997896f * 1.4426950408889634f : 1.f;
    const unsigned i1 = L + (cb < 5 ? L_Q1 : L_K1), i2 = L + (cb < 5 ? L_Q2 : L_K2);
    rs[0] *= qs; rs[1] *= qs;
#pragma unroll
    for (int tp = 0; tp < 4; ++tp) {
      const int tn = tp >> 1, pr = tp & 1;
      const int o = (cb % 5) * 16 + wn * 8 + tp * 2 + h, hp = o / 5, pos = o - hp * 5;
      const unsigned ts = pos == 2 ? 512 : 1024;
      const unsigned a0 = (pos == 2 ? i2 + hp * 32768 : i1 + (2 * hp + (pos > 2)) * 65536 + (pos > 2 ? pos - 3 : pos) * 512) +
                          (panel * 4 + wm * 2) * ts + r32 * 16;
#pragma unroll
      for (int tm = 0; tm < 2; ++tm) {
        const f32x16 a = acc[tm][tn];
        const f32x4 u = (f32x4{a[pr * 8], a[pr * 8 + 1], a[pr * 8 + 2], a[pr * 8 + 3]} + cs[tp * 2] * nm[tm]) * rs[tm];
        const f32x4 v = (f32x4{a[pr * 8 + 4], a[pr * 8 + 5], a[pr * 8 + 6], a[pr * 8 + 7]} + cs[tp * 2 + 1] * nm[tm]) * rs[tm];
        st16(c.r, a0 + tm * ts, pair32(pk2(u[0], u[1]), pk2(u[2], u[3]), pk2(v[0], v[1]), pk2(v[2], v[3])));
      }
    }
  } else {
    float cs[2]; f32x4 rn[2][4];
    auto rd = [&](int tp) {
#pragma unroll
      for (int e = 0; e < 4; ++e) rn[tp & 1][e] = *(const LAS f32x4*)(srs + (e >> 1) * 128 + tp * 16 + (e & 1) * 8 + h * 4);
    };
    qkv_loop<false>(c, w, layer, acc, [&]() { cs[0] = scs[r32]; cs[1] = scs[32 + r32]; rd(0); rd(1); });
    unsigned a0[2];
#pragma unroll
    for (int tn = 0; tn < 2; ++tn) {
      const int cv = (cb - 10) * 128 + wn * 64 + tn * 32 + r32, head = cv / 20, dim = cv - head * 20;
      const int d = (head & 1) ? (dim < 4 ? dim + 16 : dim - 4) : dim;
      a0[tn] = L + L_V + head * 81920 + (panel * 8 + wm * 4) * 640 + h * 320 + d * 16;
    }
#pragma unroll
    for (int tp = 0; tp < 4; ++tp) {
      const int tm = tp >> 1, pr = tp & 1;
      u32x4 o[2];
#pragma unroll
      for (int tn = 0; tn < 2; ++tn) {
        const f32x16 a = acc[tm][tn];
        const f32x4 u = (f32x4{a[pr * 8], a[pr * 8 + 1], a[pr * 8 + 2], a[pr * 8 + 3]} + rn[pr][2] * cs[tn]) * rn[pr][0];
        const f32x4 v = (f32x4{a[pr * 8 + 4], a[pr * 8 + 5], a[pr * 8 + 6], a[pr * 8 + 7]} + rn[pr][3] * cs[tn]) * rn[pr][1];
        o[tn] = pair32(pk2(u[0], u[1]), pk2(u[2], u[3]), pk2(v[0], v[1]), pk2(v[2], v[3]));
      }
      if (tp < 2) rd(tp + 2);
      st16(c.r, a0[0] + tp * 640, o[0]); st16(c.r, a0[1] + tp * 640, o[1]);
      SBAR();
    }
  }
  SLOT(layer * 5);
  STAMP(4);
  {
    const int c5 = cb % 5, hd = (cb < 10 ? (c5 * 16 / 5) * 2 : c5 * 128 / 20) + (lane >> 3), qb = cb < 5 ? panel >> 1 : lane & 7;
    const int o0 = (hd >> 1) * 5 + (hd & 1) * 2, first = cb < 10 ? o0 >> 4 : hd * 20 >> 7, last = cb < 10 ? (o0 + 2) >> 4 : (hd * 20 + 19) >> 7;
    const int slot = (cb < 5 ? 64 + (panel & 1) * 2 : panel * 4 + (cb < 10 ? 0 : 2)) + (first != c5);
    const bool on = hd < 32 && (first == c5 || last == c5) && (cb >= 5 || (lane & 7) == 0);
    publish(c, M_CNT4, 4, PAIR ? (on ? box(FL_QKV + layer * 5, qb * 32 + hd, slot) : ~0u) : lane == 0 ? flg(FL_QKV + layer * 5, panel * 16 + cb) : ~0u, true);
  }
  STAMP(5);
}
typedef Ring<8, QKV_NB, 10, 2> QkvB;
DI void qkv_b_run(const Ctx& c, int half, int layer, const uint16_t* wqkv) {
  QkvB rg;
  const int cb = qkv_cb(c);
#pragma unroll
  for (int p = 0; p < 8; ++p) {
    const int row = (half * 8 + p) * 4 + (c.lane >> 4);
    rg.voff[p] = row * 3840 + cb * 256 + (((c.lane & 15) ^ ((row & 3) << 2)) << 4);
  }
  rg.init((const char*)wqkv, 64 * 3840, 0, half);
  if (layer) { sig_wait(c, 28, M_ISS); rg.fill_paced(); wait_vm<(QKV_NB - 1) * 8>(); BARRIER_L(); }
  else { rg.template fill<2>(); wait_vm<8>(); BARRIER_L(); rg.template fill<QKV_NB - 2>(); }
  WRESET();
  rg.template range<QKV_NB - 2, QKV_NB - 2>(); rg.steady(); rg.template range<QKV_NB - 3, 0>(); BARRIER_L();
}
DI void qkv_a_run(const Ctx& c, int half, int layer, const uint16_t* x) {
  const int panel = qkv_panel(c);
  Ring<8, QKV_NA, 10, 2> rg;
#pragma unroll
  for (int p = 0; p < 8; ++p) {
    const int mt = panel * 8 + half * 4 + (p >> 1), r16 = (p & 1) * 8 + (c.lane >> 3);
    rg.voff[p] = layer == 0 ? (mt * 16 + r16) * 1280 + (((c.lane & 7) ^ ((r16 >> 1) & 7)) << 4) : WS_X2 + mt * 20480 + (p & 1) * 1024 + c.lane * 16;
  }
  rg.init(layer == 0 ? (const char*)x : c.ws, layer == 0 ? 128 : 2048, QKV_A, half);
  Poll<1> pl;
  if (layer) gate(c, pl, half, PAIR ? box(FL_DN, panel * 16 + qkv_cb(c), c.lane & 15) : flg(FL_DN, panel * 16 + (c.lane & 15)), 6, 28);
  rg.template fill<A_D0>();
  rg.template run<A_D0>();
  pl.keep();
}

DI f32x16 qk8(s16x4 k2, s16x4 q2) {
 return __builtin_amdgcn_mfma_f32_32x32x8bf16_1k(k2, q2, f32x16{}, 0, 0, 0); }
DI f32x16 qk16(bf16x8 k1, bf16x8 q1, f32x16 s) { return __builtin_amdgcn_mfma_f32_32x32x16_bf16(k1, q1, s, 0, 0, 0); }
DI f32x16 pv_lo(bf16x8 v, f32x16 p, f32x16 o) {
  return __builtin_amdgcn_smfmac_f32_32x32x32_bf16(v, __builtin_bit_cast(bf16x16, (f32x8)__builtin_shufflevector(p, p, 0, 1, 2, 3, 4, 5, 6, 7)), o, 0xDDDD, 0, 0);
}
DI f32x16 pv_hi(bf16x8 v, f32x16 p, f32x16 o) {
  return __builtin_amdgcn_smfmac_f32_32x32x32_bf16(v, __builtin_bit_cast(bf16x16, (f32x8)__builtin_shufflevector(p, p, 8, 9, 10, 11, 12, 13, 14, 15)), o, 0xDDDD, 0, 0);
}
DI float swap32(float v, int h) {
  auto r = __builtin_amdgcn_permlane32_swap(__float_as_uint(v), __float_as_uint(v), false, false);
  return __uint_as_float(h ? r[0] : r[1]);
}
#define EXP4(X, i) do { X[i] = __builtin_amdgcn_exp2f(X[i]); X[i + 1] = __builtin_amdgcn_exp2f(X[i + 1]); X[i + 2] = __builtin_amdgcn_exp2f(X[i + 2]); \
                        X[i + 3] = __builtin_amdgcn_exp2f(X[i + 3]); PIN(X); SBAR(); } while (0)
DI void att_compute(const Ctx& c, int w, int layer) {
  const int lane = c.lane, head = att_head(c), qb = att_qb(c);
  const int qp = w & 3, kp = w >> 2, r32 = lane & 31, h = lane >> 5;
  lds_ptr lds = c.lds;
  const unsigned L = WS_L + layer * L_SZ;
  SLOT(layer * 5 + 1);
  STAMP(0);
  for (int s = w * 64 + lane; s < 672; s += 512) {
    const unsigned v = s % 20 == 0 ? 0x3f803f80u : 0u;
    *(LAS u32x4*)(lds + AT_CONST + s * 16) = u32x4{v, v, v, v};
  }
  lds_ptr pk1 = lds + kp * 8192 + lane * 16;
  lds_ptr pk2_ = h ? lds + AT_CONST + 16 : lds + 16384 + kp * 4096 + r32 * 16 + (head & 1) * 8;
  lds_ptr pv = (r32 >= 20 ? lds + AT_CONST : lds + 24576 + kp * 10240) + h * 320 + r32 * 16;
  const int vmask = r32 >= 20 ? 0 : -1, kmask = h ? 0 : -1;
  f32x16 o0 = f32x16{}, o1 = f32x16{};
  f32x16 s0, s1, s2, s3 = f32x16{};
  STAMP(1);
  BARRIER();
  STAMP(2); STAMPC(0); WRESET();
  const bf16x8 q1a = ldsr16(lds + 2 * AT_SLOT + qp * 2048 + lane * 16), q1b = ldsr16(lds + 2 * AT_SLOT + qp * 2048 + 1024 + lane * 16);
  lds_ptr pq2 = h ? lds + AT_CONST + 16 : lds + 2 * AT_SLOT + 8192 + qp * 1024 + r32 * 16 + (head & 1) * 8;
  const s16x4 q2a = *(const LAS s16x4*)pq2, q2b = *(const LAS s16x4*)(pq2 + 512);
  bf16x8 k1 = ldsr16(pk1), vlo = ldsr16(pv), vhi = bf16x8{};
  s16x4 k2 = *(const LAS s16x4*)pk2_;
  s0 = qk16(k1, q1a, qk8(k2, q2a));
#define PH_EVEN(SC, SN, SP, KLOAD, VOFF) do { \
    SN = qk8(k2, q2b); SBAR(); EXP4(SC, 0); SN = qk16(k1, q1b, SN); SBAR(); KLOAD; SBAR(); EXP4(SC, 4); \
    o1 = pv_hi(vhi, SP, o1); SBAR(); vhi = ldsr16(pv + (VOFF) + 640); SBAR(); EXP4(SC, 8); o0 = pv_lo(vlo, SC, o0); SBAR(); EXP4(SC, 12); } while (0)
#define PH_ODD(SC, SN, SP, VLOAD) do { \
    SN = qk8(k2, q2a); SBAR(); EXP4(SC, 0); SN = qk16(k1, q1a, SN); SBAR(); EXP4(SC, 4); \
    o0 = pv_hi(vhi, SP, o0); SBAR(); EXP4(SC, 8); o1 = pv_lo(vlo, SC, o1); SBAR(); VLOAD; SBAR(); EXP4(SC, 12); } while (0)
#define KLD(t) do { k1 = ldsr16(pk1 + (t) * 1024); k2 = *(const LAS s16x4*)(pk2_ + (t) * 512); } while (0)
#define PRI(v) do { asm volatile("s_cmp_eq_u32 %0, 0\n\ts_cbranch_scc1 1f\n\ts_setprio " #v "\n1:" :: "s"(kp) : "scc"); SBAR(); } while (0)
#define HI(n) if (ATT_PRIO == (n)) PRI(1);
#define LO(n) if (ATT_PRIO == (n)) PRI(0);
#define TWO(t) HI(3) HI(4) HI(5) PH_EVEN(s0, s1, s3, KLD((t) + 1), (t) * 1280); PH_ODD(s1, s2, s0, vlo = ldsr16(pv + ((t) + 1) * 1280)); LO(4)
  for (int grp = 0; grp < 4; ++grp) {
    TWO(0) PH_EVEN(s2, s3, s1, if (grp == 0) BARRIER_NW(); KLD(2), 1280); LO(3) PH_ODD(s3, s0, s2, vlo = ldsr16(pv + 2 * 1280));
    TWO(2) PH_EVEN(s2, s3, s1, KLD(4), 3 * 1280); LO(3) LO(5) PH_ODD(s3, s0, s2, vlo = ldsr16(pv + 4 * 1280));
    TWO(4) PH_EVEN(s2, s3, s1, KLD(6), 5 * 1280); LO(3) PH_ODD(s3, s0, s2, vlo = ldsr16(pv + 6 * 1280));
    TWO(6)
    const int d = grp == 2 ? -2 * AT_SLOT : grp == 3 ? 0 : AT_SLOT;
    PH_EVEN(s2, s3, s1, pk1 += d; pk2_ += (d & kmask); if (grp != 3) BARRIER(); KLD(0), 7 * 1280);
    LO(3) LO(5) PH_ODD(s3, s0, s2, pv += (d & vmask); vlo = ldsr16(pv));
  }
  o1 = pv_hi(vhi, s3, o1);
  STAMP(3); STAMPC(1);
  __builtin_amdgcn_s_setprio(0);
  BARRIER();
  const f32x16 give = kp ? o0 : o1, keep = kp ? o1 : o0;
#pragma unroll
  for (int e = 0; e < 3; ++e)
    *(LAS f32x4*)(lds + AT_MERGE + (w * 3 + e) * 1024 + lane * 16) = f32x4{give[4 * e], give[4 * e + 1], give[4 * e + 2], give[4 * e + 3]};
  BARRIER();
  f32x4 a[3];
#pragma unroll
  for (int e = 0; e < 3; ++e)
    a[e] = f32x4{keep[4 * e], keep[4 * e + 1], keep[4 * e + 2], keep[4 * e + 3]} + *(const LAS f32x4*)(lds + AT_MERGE + ((w ^ 4) * 3 + e) * 1024 + lane * 16);
  const int qt = qb * 8 + qp * 2 + kp;
  float l = h ? a[2][0] : 0.f;
  l += swap32(l, h);
  if (__builtin_expect(__any(!(l >= 5.421e-20f && l <= 1.8447e19f)), 0)) {
    const unsigned o2 = (head >> 1) * 32768 + r32 * 16 + (head & 1) * 8;
    const bf16x8 fq1 = __builtin_bit_cast(bf16x8, ld16(c.r, L + L_Q1 + head * 65536 + qt * 1024 + lane * 16));
    u32x2 fq2 = ld8(c.r, L + L_Q2 + o2 + qt * 512);
    if (h) fq2 = u32x2{0u, 0u};
    const unsigned vi = L + L_V + head * 81920 + h * 320 + (r32 < 20 ? r32 : 0) * 16;
    f32x16 oo = f32x16{};
    float m = -INFINITY;
    const u32x4 one8 = {0x3f803f80u, 0x3f803f80u, 0x3f803f80u, 0x3f803f80u};
    for (int t = 0; t < 64; ++t) {
      const u32x4 fk1 = ld16(c.r, L + L_K1 + head * 65536 + t * 1024 + lane * 16);
      const u32x2 fk2 = ld8(c.r, L + L_K2 + o2 + t * 512);
      u32x4 v0 = ld16(c.r, vi + t * 1280), v1 = ld16(c.r, vi + t * 1280 + 640);
      if (r32 >= 20) { v0 = one8; v1 = one8; }
      f32x16 s = qk16(__builtin_bit_cast(bf16x8, fk1), fq1, qk8(__builtin_bit_cast(s16x4, fk2), __builtin_bit_cast(s16x4, fq2)));
      float tm = s[0];
#pragma unroll
      for (int r = 1; r < 16; ++r) tm = fmaxf(tm, s[r]);
      tm = fmaxf(tm, swap32(tm, h));
      const float mn = fmaxf(m, tm), al = __builtin_amdgcn_exp2f(m - mn);
      m = mn;
#pragma unroll
      for (int r = 0; r < 16; ++r) { s[r] = __builtin_amdgcn_exp2f(s[r] - mn); oo[r] *= al; }
      oo = pv_hi(__builtin_bit_cast(bf16x8, v1), s, pv_lo(__builtin_bit_cast(bf16x8, v0), s, oo));
    }
#pragma unroll
    for (int e = 0; e < 3; ++e) a[e] = f32x4{oo[4 * e], oo[4 * e + 1], oo[4 * e + 2], oo[4 * e + 3]};
    l = h ? a[2][0] : 0.f;
    l += swap32(l, h);
  }
  const float inv = 1.f / l;
  const unsigned A0 = pk2(a[0][0] * inv, a[0][1] * inv), A1 = pk2(a[0][2] * inv, a[0][3] * inv);
  const unsigned B0 = pk2(a[1][0] * inv, a[1][1] * inv), B1 = pk2(a[1][2] * inv, a[1][3] * inv);
  const u32x2 C = {pk2(a[2][0] * inv, a[2][1] * inv), pk2(a[2][2] * inv, a[2][3] * inv)};
  const int tok = qt * 32 + r32;
  const unsigned ob = L + L_O + (tok >> 4) * 20480 + (tok & 15) * 16 + (head >> 1) * 1280;
  st16(c.r, ob + ((head & 1) * 3 + h) * 256, pair32(A0, A1, B0, B1));
  if (!h) st8(c.r, ob + 512 + (head & 1) * 8, C);
  STAMP(4);
  publish(c, M_CNT, 8, PAIR ? (lane < 32 ? box(FL_ATT + layer * 5, qb * 32 + lane, head) : ~0u) : lane == 0 ? flg(FL_ATT + layer * 5, qb * 32 + head) : ~0u, true);
  STAMP(5);
}
DI void retile_load(const Ctx& c, int J, u32x4 (&v)[5][4], const uint16_t* wo, const uint16_t* wg, const uint16_t* wu, const uint16_t* wd) {
  const int e = J < 216 ? J : J < 256 ? J - 216 : J - 256;
  const int strip = J < 216 ? (e >> 2) % 27 : e % 10, k0 = J < 216 ? (e & 3) * 5 : min((e / 10) * 5, 49), ld = J < 216 ? 3456 : 1280;
  const rsrc_t r = __builtin_amdgcn_make_buffer_rsrc((void*)(J < 108 ? wg : J < 216 ? wu : J < 256 ? wo : wd), 0,
                                                     J < 216 ? 640 * 3456 : J < 256 ? 640 * 1280 : 1728 * 1280, 0x00020000);
  const unsigned vo = (c.lane >> 3) * ld + (c.lane & 7) * 16, so = k0 * 32 * ld + strip * 128;
#pragma unroll
  for (int u = 0; u < 5; ++u)
#pragma unroll
    for (int q = 0; q < 4; ++q) v[u][q] = __builtin_amdgcn_raw_buffer_load_b128(r, vo, so + (u * 32 + q * 8) * ld, 0);
}
DI void retile_store(const Ctx& c, int J, lds_ptr scr, const u32x4 (&v)[5][4]) {
  const int e = J < 216 ? J : J < 256 ? J - 216 : J - 256, mat = J >= 108;
  const int strip = J < 216 ? (e >> 2) % 27 : e % 10, k0 = J < 216 ? (e & 3) * 5 : min((e / 10) * 5, 49);
  const unsigned ts = J < 216 ? 40960 : J < 256 ? 20480 : 55296;
  const unsigned dst = (J < 216 ? WS_WGU + mat * 20480 : J < 256 ? WS_WO : WS_WD) + strip * 4 * ts + k0 * 1024;
  const int i = c.lane & 15, g = c.lane >> 4;
  const bf16x8 ones = __builtin_bit_cast(bf16x8, u32x4{0x3f803f80u, 0x3f803f80u, 0x3f803f80u, 0x3f803f80u});
  f32x4 cs[4] = {f32x4{}, f32x4{}, f32x4{}, f32x4{}};
#pragma unroll
  for (int u = 0; u < 5; ++u) {
#pragma unroll
    for (int p = 0; p < 4; ++p) *(LAS u32x4*)(scr + p * 1024 + c.lane * 16) = v[u][p];
#pragma unroll
    for (int t = 0; t < 4; ++t) {
      const bf16x8 f = ldstr(scr + (g * 8 + (i >> 2)) * 128 + t * 32 + (i & 3) * 8, 512);
      __builtin_amdgcn_raw_buffer_store_b128(__builtin_bit_cast(u32x4, f), c.r, c.lane * 16, dst + u * 1024 + t * ts, 16);
      cs[t] = mfma16(f, ones, cs[t]);
    }
  }
  if (J < 216 && i == 0) {
#pragma unroll
    for (int t = 0; t < 4; ++t)
      __builtin_amdgcn_raw_buffer_store_b128(__builtin_bit_cast(u32x4, cs[t]), c.r, g * 16, WS_CSG + ((e & 3) * 3456 + (strip * 8 + t * 2 + mat) * 16) * 4, 16);
  }
}
DI int early_piece(int e) { return e < 4 ? (e & 1) + (e >> 1) * 8 : e < 6 ? 16 + (e - 4) * 4 : e < 9 ? 18 + e : 25 + e; }
DI int late_piece(int e) { return e < 6 ? 2 + e : e < 12 ? 4 + e : e < 15 ? 5 + e : e < 18 ? 6 + e : e < 25 ? 9 + e : 12 + e; }
DI void att_load(const Ctx& c, int lw, int layer, unsigned iss, const uint16_t* wo, const uint16_t* wg, const uint16_t* wu, const uint16_t* wd) {
  const int head = att_head(c), qb = att_qb(c), lane = c.lane;
  const char* Lp = c.ws + WS_L + layer * L_SZ;
  const unsigned vo = lane * 16;
  auto src_of = [&](int p) {
    return p < 16 ? L_K1 + head * 65536 + p * 1024 : p < 24 ? L_K2 + (head >> 1) * 32768 + (p - 16) * 1024 : L_V + head * 81920 + (p - 24) * 1024;
  };
  unsigned v0[14], m0[11], vg[11], sg[11];
#pragma unroll
  for (int q = 0; q < 11; ++q) {
    const int p = q < 3 ? early_piece(lw * 3 + q) : late_piece(lw * 8 + q - 3), pg = lw * 11 + q;
    v0[q] = src_of(p) + vo; m0[q] = p * 1024;
    vg[q] = src_of(pg) + vo; sg[q] = pg < 16 ? 16384 : pg < 24 ? 8192 : 20480;
    asm volatile("" : "+v"(v0[q]), "+s"(m0[q]));
  }
#pragma unroll
  for (int q = 0; q < 3; ++q) {
    const int p = lw * 3 + q;
    v0[11 + q] = (p < 8 ? L_Q1 + head * 65536 + qb * 8192 + p * 1024 : L_Q2 + (head >> 1) * 32768 + qb * 4096 + (p - 8) * 1024) + vo - q * 1024;
    PIN(v0[11 + q]);
  }
  auto issue = [&](int slot) {
#pragma unroll
    for (int q = 0; q < 11; ++q) { vg[q] += sg[q]; glds1(slot * AT_SLOT + (lw * 11 + q) * 1024, Lp, vg[q]); }
  };
  Poll<2> pl;
  if (lw == 0) {
    const int o0 = (head >> 1) * 5 + (head & 1) * 2, v0 = head * 20;
    const bool two_k = o0 >> 4 != (o0 + 2) >> 4, two_v = v0 >> 7 != (v0 + 19) >> 7;
    const unsigned bx = box(FL_QKV + layer * 5, qb * 32 + head, lane);
    if (POLL_GATE) sig_wait(c, iss, M_ISS);
    const int sel = lane >> 4, cb = sel == 0 ? 5 + (o0 >> 4) : sel == 1 ? 5 + ((o0 + 2) >> 4) : sel == 2 ? 10 + (v0 >> 7) : 10 + ((v0 + 19) >> 7);
    if (PAIR) pl.run(c, !(lane & 1) || ((lane & 2) ? two_v : two_k) ? bx : ~0u, lane < 4 && (!(lane & 1) || two_k) ? bx + 64 * 128 : ~0u);
    else pl.run(c, flg(FL_QKV + layer * 5, (lane & 15) * 16 + cb), flg(FL_QKV + layer * 5, (qb * 2 + (lane & 1)) * 16 + ((o0 + (lane & 2)) >> 4)));
    sig_set(c, layer * 5 + 2);
    STAMPL(layer * 5 + 1, 6);
  } else sig_wait(c, layer * 5 + 2);
  glds3(2 * AT_SLOT + lw * 3072, Lp, v0[11], v0[12], v0[13]);
#pragma unroll
  for (int q = 0; q < 3; ++q) glds1(m0[q], Lp, v0[q]);
  wait_vm<0>(); BARRIER();
#pragma unroll
  for (int q = 3; q < 11; ++q) glds1(m0[q], Lp, v0[q]);
  pl.keep();
  wait_vm<0>(); BARRIER();
  issue(1);
  auto warm = [&]() {
    const int e = c.j & 7;
    const unsigned n1 = WARM_WO ? 13 : 0, n2 = n1 + (WARM_GU ? 35 : 0), n3 = n2 + (WARM_DN ? 34 : 0);
#pragma unroll 1
    for (unsigned q = lw; q < n3; q += 4) {
      const unsigned src = q < n1 ? WS_WO + pr_cb(c) * 102400 + min(e * 13 + q, 99u) * 1024
                         : q < n2 ? WS_WGU + ((gu_cb(c) * 27) >> 2) * 40960 + (e * 35 + q - n1) * 1024
                                  : WS_WD + pr_cb(c) * 276480 + min(e * 34 + q - n2, 269u) * 1024;
      glds1(AT_SCR + lw * 1024, c.ws + src, vo);
      wait_vm<4>();
    }
    wait_vm<0>();
  };
  const int J = (lw - 1) * 256 + blockIdx.x;
  const bool rt = layer == 0 && lw > 0 && J < 366;
  u32x4 v[5][4];
  if (rt) { retile_load(c, J, v, wo, wg, wu, wd); wait_vm<20>(); } else wait_vm<0>();
  BARRIER();
  issue(2);
  wait_vm<0>(); BARRIER();
  issue(0);
  if (rt) retile_store(c, J, c.lds + AT_SCR + (lw - 1) * 4096, v);
  if (layer == 0 && (lw == 1 || lw == 2)) publish(c, M_CNT3, 2, lane < 8 ? box(FL_RT, lane * 4 + (blockIdx.x >> 6), blockIdx.x & 63) : ~0u);
  wait_vm<0>(); BARRIER();
  if (layer == 0) {
    if (lw == 0) {
      if (c.j == 0) {
#pragma unroll 1
        for (int q = 0; q < 2; ++q) { pl.run(c, box(FL_RT, c.xcd * 4 + q * 2, lane), box(FL_RT, c.xcd * 4 + q * 2 + 1, lane)); wait_vm<0>(); pl.keep(); }
        if (lane < 32) __hip_atomic_store((GAS u64*)(c.ws + box(FL_RT2, lane * 8 + c.xcd, 0)), c.tgt, __ATOMIC_RELAXED, __HIP_MEMORY_SCOPE_AGENT);
      }
      poll(c, box(FL_RT2, blockIdx.x, 0));
      sig_set(c, 3);
    } else sig_wait(c, 3);
  }
  warm();
  BARRIER(); BARRIER();
}

template <int NS, int NB, int WIN>
DI void proj_compute(const Ctx& c, int w, int slot_id, unsigned a_off, const char* res_rm, unsigned res_off, unsigned out_off, unsigned st_off,
                     uint16_t* out, int arr) {
  constexpr bool RES = NS <= NB;
  const int panel = pr_panel(c), cb = pr_cb(c);
  SLOT(slot_id);
  STAMP(0);
  if (w >= 4) {
#pragma unroll 1
    for (int k = 0; k <= (RES ? 1 : NS); ++k) BARRIER();
    return;
  }
  const int lane = c.lane, i = lane & 15, g = lane >> 4, mt = panel * 4 + w;
  u32x2 res[5];
#pragma unroll
  for (int t = 0; t < 5; ++t)
    res[t] = res_rm ? *(const u32x2*)(res_rm + (mt * 16 + i) * 1280 + cb * 160 + t * 32 + g * 8)
                    : ld8(c.r, res_off + (mt * 80 + cb * 10 + t * 2 + (g >> 1)) * 256 + i * 16 + (g & 1) * 8);
  f32x4 acc[5];
#pragma unroll
  for (int t = 0; t < 5; ++t) { acc[t] = f32x4{bflo(res[t][0]), bfhi(res[t][0]), bflo(res[t][1]), bfhi(res[t][1])}; PIN(acc[t]); }
  lds_ptr pb = c.lds + lane * 16;
  unsigned av = a_off + mt * (NS * 2048) + lane * 16;
  u32x4 a[WIN];
  bf16x8 f[3][5];
  int sb = 0;
  STAMP(1);
  BARRIER();
  STAMP(2); STAMPC(0); WRESET();
  constexpr int AH = RES ? 12 : WIN;
#pragma unroll
  for (int k = 0; k < AH; ++k) a[k] = ld16(c.r, av + k * 1024);
#pragma unroll
  for (int t = 0; t < 5; ++t) { f[0][t] = ldsr16(pb + t * 2048); f[1][t] = ldsr16(pb + t * 2048 + 1024); }
  SBAR();
  auto window = [&](bool last) {
#pragma unroll
    for (int k = 0; k < WIN; ++k) {
      const bool ld = !(last && k + 2 >= WIN);
      if ((k & 1) == 0) {
        if (RES) pb += 10240;
        else { pb += sb == NB - 1 ? -(NB - 1) * 10240 : 10240; sb = sb == NB - 1 ? 0 : sb + 1; }
        if (!ld) BARRIER(); else if (!RES) BARRIER_NW();
      }
#pragma unroll
      for (int t = 0; t < 5; ++t) {
        if (ld) f[(k + 2) % 3][t] = ldsr16(pb + t * 2048 + (k & 1) * 1024);
        acc[t] = mfma16(f[k % 3][t], __builtin_bit_cast(bf16x8, a[k]), acc[t]);
        SBAR();
      }
      if (!last) { a[k] = ld16(c.r, av + (WIN + k) * 1024); SBAR(); }
      else if (k + AH < WIN) { a[k + AH] = ld16(c.r, av + (k + AH) * 1024); SBAR(); }
    }
    av += WIN * 1024;
  };
  static_assert(2 * NS % WIN == 0 && (2 * NS == WIN || WIN % 6 == 0), "window");
#pragma unroll 1
  for (int b = 1; b < 2 * NS / WIN; ++b) window(false);
  window(true);
  STAMP(3); STAMPC(1);
  u32x2 o[5];
#pragma unroll
  for (int t = 0; t < 5; ++t) {
    o[t][0] = pk2(acc[t][0], acc[t][1]); o[t][1] = pk2(acc[t][2], acc[t][3]);
  }
  const u32x4 p0 = pair16(o[0][0], o[0][1], o[1][0], o[1][1]), p1 = pair16(o[2][0], o[2][1], o[3][0], o[3][1]),
              p2 = pair16(o[4][0], o[4][1], o[4][0], o[4][1]);
  const int ch = (g & 1) * 2 + (g >> 1);
  if (out) {
    char* po = (char*)out + (mt * 16 + i) * 1280 + cb * 160;
    *(u32x4*)(po + ch * 16) = p0; *(u32x4*)(po + 64 + ch * 16) = p1;
    if (!(g & 1)) *(u32x4*)(po + 128 + (g >> 1) * 16) = p2;
    STAMP(4);
    return;
  }
  const unsigned xo = out_off + (mt * 80 + cb * 10) * 256 + i * 16;
  st16(c.r, xo + ch * 256, p0); st16(c.r, xo + 1024 + ch * 256, p1);
  if (!(g & 1)) st16(c.r, xo + 2048 + (g >> 1) * 256, p2);
  const f32x4 s4 = (acc[0] + acc[1]) + (acc[2] + acc[3]) + acc[4];
  const float sum = rows4((s4[0] + s4[1]) + (s4[2] + s4[3])), mean = sum * (1.f / 80.f);
  f32x4 q4 = {0.f, 0.f, 0.f, 0.f};
#pragma unroll
  for (int t = 0; t < 5; ++t) { const f32x4 d = acc[t] - mean; q4 += d * d; }
  const float m2 = rows4((q4[0] + q4[1]) + (q4[2] + q4[3]));
  if (g == 0) st8(c.r, st_off + (mt >> 3) * 8192 + cb * 1024 + ((mt & 7) * 16 + i) * 8, u32x2{__float_as_uint(sum), __float_as_uint(m2)});
  STAMP(4);
  publish(c, M_CNT4, 4, PAIR ? (lane < 16 ? box(arr, (panel >> 1) * 16 + lane, (panel & 1) * 8 + cb) : ~0u) : lane == 0 ? flg(arr, panel * 8 + cb) : ~0u, true);
  STAMP(5);
}
template <int NS, int NB> DI void proj_b_run(const Ctx& c, int half, unsigned w_off) {
  Ring<5, NB, NS, 2> rg;
#pragma unroll
  for (int p = 0; p < 5; ++p) { const int q = half * 5 + p; rg.voff[p] = w_off + (pr_cb(c) * 5 + (q >> 1)) * (NS * 2048) + (q & 1) * 1024 + c.lane * 16; }
  rg.init(c.ws, 2048, 0, half);
  rg.template fill_paced<(NB < DN_D0 ? NB : DN_D0)>();
  if constexpr (NS <= NB) { wait_vm<0>(); BARRIER_L(); BARRIER_L(); } else rg.template run<DN_D0>();
}

template <int NF>
DI void gu_consumer(const Ctx& c, int layer, int f0, int mq) {
  const int panel = gu_panel(c), ft0 = (gu_cb(c) * 27) >> 2;
  const int lane = c.lane, r32 = lane & 31, h = lane >> 5;
  f32x16 acc[NF];
#pragma unroll
  for (int t = 0; t < NF; ++t) acc[t] = f32x16{};
  const int lo = (r32 >> 4) * 1024 + h * 256 + (r32 & 15) * 16;
  lds_ptr pa = c.lds + GU_A + mq * 2048 + lo, pb = c.lds + f0 * 2048 + lo;
  bf16x8 x0[2], x1[2], wa[NF], wb0[NF], wb1[NF];
  auto step = [&](const bf16x8* xa, const bf16x8* wb, bf16x8* xn, bf16x8* wn_, bool ld) {
#pragma unroll
    for (int q = 0; q < 2 * NF; ++q) {
      if (ld) {
        if (q == 0) { xn[0] = ldsr16(pa); xn[1] = ldsr16(pa + 512); }
        if (q >= 1 && q <= NF) wn_[q - 1] = ldsr16(pb + (q - 1) * 2048 + 512);
        if (q >= NF) wa[q - NF] = ldsr16(pb + (q - NF) * 2048);
      }
      if (q < NF) acc[q] = mfma32(wa[q], xa[0], acc[q]); else acc[q - NF] = mfma32(wb[q - NF], xa[1], acc[q - NF]);
      SBAR();
    }
  };
  static_assert(GU_NA == GU_NB, "one slot counter");
  int sl = 0;
  auto adv = [&]() {
    pa += sl == GU_NA - 1 ? -(GU_NA - 1) * 8192 : 8192; pb += sl == GU_NB - 1 ? -(GU_NB - 1) * 14336 : 14336;
    sl = sl == GU_NA - 1 ? 0 : sl + 1;
  };
  SLOT(layer * 5 + 3);
  STAMP(0);
  STAMP(1);
  BARRIER();
  STAMP(2); STAMPC(0); WRESET();
  x0[0] = ldsr16(pa); x0[1] = ldsr16(pa + 512);
#pragma unroll
  for (int t = 0; t < NF; ++t) { wa[t] = ldsr16(pb + t * 2048); wb0[t] = ldsr16(pb + t * 2048 + 512); }
#pragma unroll 1
  for (int st = 0; st < 9; ++st) {
    adv(); BARRIER_NW(); step(x0, wb0, x1, wb1, true);
    adv(); BARRIER_NW(); step(x1, wb1, x0, wb0, true);
  }
  adv(); BARRIER_NW(); step(x0, wb0, x1, wb1, true);
  BARRIER();
  const LAS float* srs = (const LAS float*)(c.lds + M_SST) + mq * 32 + r32;
  const LAS float* scs = (const LAS float*)(c.lds + M_SCS) + f0 * 32 + h * 4;
  float rs = srs[0], nm = srs[128];
  f32x4 cs[2][4];
#pragma unroll
  for (int e = 0; e < 8; ++e) cs[e >> 2][e & 3] = *(const LAS f32x4*)(scs + e * 8);
  SBAR();
  step(x1, wb1, x0, wb0, false);
  STAMP(3); STAMPC(1);
  rs *= rs;
  const unsigned ho = WS_L + layer * L_SZ + L_H + ((panel * 8 + mq * 2 + (r32 >> 4)) * 216 + (ft0 + f0) * 2 + h) * 256 + (r32 & 15) * 16;
#pragma unroll
  for (int f = 0; f < NF; ++f) {
    unsigned p[2][2];
#pragma unroll
    for (int e = 0; e < 2; ++e) {
      const f32x16 a = acc[f];
      f32x4 gt = f32x4{a[e * 4], a[e * 4 + 1], a[e * 4 + 2], a[e * 4 + 3]} + cs[f & 1][e] * nm;
      const f32x4 up = (f32x4{a[8 + e * 4], a[9 + e * 4], a[10 + e * 4], a[11 + e * 4]} + cs[f & 1][2 + e] * nm) * rs;
      gt = __builtin_elementwise_max(gt, f32x4{0.f, 0.f, 0.f, 0.f}) * up;
      p[e][0] = pk2(gt[0], gt[1]); p[e][1] = pk2(gt[2], gt[3]);
    }
    if (f + 2 < NF) {
#pragma unroll
      for (int e = 0; e < 4; ++e) cs[f & 1][e] = *(const LAS f32x4*)(scs + (f + 2) * 32 + e * 8);
    }
    st16(c.r, ho + f * 512, pair32(p[0][0], p[0][1], p[1][0], p[1][1]));
    SBAR();
  }
  STAMP(4);
  publish(c, M_CNT, 8, PAIR ? (lane < 16 ? box(FL_GU + layer * 5, panel * 16 + lane, gu_cb(c)) : ~0u) : lane == 0 ? flg(FL_GU + layer * 5, panel * 16 + gu_cb(c)) : ~0u, true);
  STAMP(5);
}
typedef Ring<7, GU_NB, 20, 2> GuB;
DI void gu_b_init(const Ctx& c, GuB& rg, int half) {
  const int ft0 = (gu_cb(c) * 27) >> 2;
#pragma unroll
  for (int p = 0; p < 7; ++p) rg.voff[p] = WS_WGU + (ft0 * 2 + half * 7 + p) * 20480 + c.lane * 16;
  rg.init(c.ws, 1024, 0, half);
}
DI void gu_b_run(const Ctx& c, int half, int layer) {
  GuB rg;
  gu_b_init(c, rg, half);
  rg.fill_paced();
  if (half == 0) ln_cols<4>(c, 56, WS_CSG + ((gu_cb(c) * 27) >> 2) * 128, 13824);
  wait_vm<(GU_NB - 1) * 7>(); BARRIER_L(); WRESET();
  f32x2 p[8];
#pragma unroll
  for (int i = 0; i < 8; ++i) p[i] = ld8_async(WS_ST + (layer * 2 + 1) * STB + gu_panel(c) * 8192 + i * 1024 + (half * 64 + c.lane) * 8, c.ws);
  wait_vm<(GU_NB - 2) * 7 + 8>(); BARRIER_L();
  asm volatile("s_waitcnt vmcnt(0)" : "+v"(p[0]), "+v"(p[1]), "+v"(p[2]), "+v"(p[3]), "+v"(p[4]), "+v"(p[5]), "+v"(p[6]), "+v"(p[7])::"memory");
  ln_rows(c, half * 64 + c.lane, p);
  rg.steady();
  rg.template range<GU_NB - 3, 0>(); BARRIER_L();
}
DI void gu_a_run(const Ctx& c, int half, int layer, unsigned iss) {
  const unsigned a_off = WS_L + layer * L_SZ + L_X1;
  Ring<4, GU_NA, 20, 2> rg;
#pragma unroll
  for (int p = 0; p < 4; ++p) rg.voff[p] = a_off + (gu_panel(c) * 8 + half * 4 + p) * 20480 + c.lane * 16;
  rg.init(c.ws, 1024, GU_A, half);
  Poll<1> pl;
  gate(c, pl, half, PAIR ? box(FL_WO + layer * 5, gu_panel(c) * 16 + gu_cb(c), c.lane & 15) : flg(FL_WO + layer * 5, gu_panel(c) * 16 + (c.lane & 15)), layer * 5 + 4, iss);
  rg.template fill<A_D0 + 1>();
  rg.template head<A_D0 + 1>();
  rg.template range<GU_NA - 3, 3>();
  rg.template range<2, 0>();
  BARRIER_L();
  pl.keep();
}

__global__ __launch_bounds__(768) void fused_kernel(const uint16_t* __restrict__ x, const uint16_t* __restrict__ wqkv, const uint16_t* __restrict__ wo,
                                                    const uint16_t* __restrict__ wg, const uint16_t* __restrict__ wu,
                                                    const uint16_t* __restrict__ wd, uint16_t* __restrict__ out, char* __restrict__ ws) {
  extern __shared__ __attribute__((aligned(16))) char smem[];
  Ctx c;
  c.ws = ws; c.lds = (lds_ptr)smem; c.lane = threadIdx.x & 63; c.xcd = blockIdx.x & 7; c.j = blockIdx.x >> 3;
  c.r = __builtin_amdgcn_make_buffer_rsrc(ws, 0, WS_END, 0x00020000);
  const int w = __builtin_amdgcn_readfirstlane(threadIdx.x >> 6);
  SPANST(0);
  c.tgt = *(const u64*)(ws + WS_EPOCH) + 1;
  if (threadIdx.x < 16) *(LAS unsigned*)(c.lds + M_CNT + threadIdx.x * 4) = 0u;
  const bool qkv_on = qkv_cb(c) != 15;
  const Ctx c0 = c;
  WDUMP(15);
  if (w >= 8) __builtin_amdgcn_s_setprio(3);
  if (w >= 8) {
    const int half = w & 1;
    const bool wl = w >= 10;
    if (!qkv_on) BARRIER();
#pragma unroll 1
    for (int layer = 0; layer < 2; ++layer) {
      const unsigned iss = layer * 24 + (qkv_on ? (layer + 1) * 4 : 0);
      if (qkv_on) {
        if (wl) qkv_b_run(fresh(c0), opq(half), opq(layer), wqkv); else qkv_a_run(fresh(c0), opq(half), opq(layer), x);
      }
      WDUMP(layer * 5);
      att_load(fresh(c0), opq(w - 8), opq(layer), iss, wo, wg, wu, wd);
      WDUMP(layer * 5 + 1);
      const Ctx c = fresh(c0);
      const int l = c.lane;
      Poll<1> pl;
      if (wl) {
        sig_wait(c0, iss + 8, M_ISS);
        proj_b_run<10, WO_NB>(fresh(c0), opq(half), WS_WO);
        WDUMP(layer * 5 + 2);
        sig_wait(c0, iss + 12, M_ISS);
        gu_b_run(fresh(c0), opq(half), opq(layer));
        WDUMP(layer * 5 + 3);
        sig_wait(c0, iss + 20, M_ISS);
        proj_b_run<27, DN_NB>(fresh(c0), opq(half), WS_WD);
      } else {
        if (half == 0) {
          if (POLL_GATE) sig_wait(c, iss + 8, M_ISS);
          pl.run(c, PAIR ? box(FL_ATT + layer * 5, pr_panel(c) * 8 + pr_cb(c), l & 31) : flg(FL_ATT + layer * 5, (pr_panel(c) >> 2) * 32 + (l & 31)));
          STAMPL(layer * 5 + 2, 6);
        }
        BARRIER_L(); wait_vm<0>(); pl.keep(); BARRIER_L();
        WDUMP(layer * 5 + 2);
        gu_a_run(fresh(c0), opq(half), opq(layer), iss + 12);
        WDUMP(layer * 5 + 3);
        if (half == 0) {
          if (POLL_GATE) sig_wait(c, iss + 20, M_ISS);
          pl.run(c, PAIR ? box(FL_GU + layer * 5, pr_panel(c) * 8 + pr_cb(c), l & 15) : flg(FL_GU + layer * 5, (pr_panel(c) >> 1) * 16 + (l & 15)));
          STAMPL(layer * 5 + 4, 6);
        }
        BARRIER_L(); wait_vm<0>(); pl.keep();
        if (layer == 1 && half == 0 && blockIdx.x == 0 && l == 0) *(u64*)(ws + WS_EPOCH) = c.tgt;
#pragma unroll 1
        for (int k = 1; k <= 27; ++k) BARRIER_L();
      }
      WDUMP(layer * 5 + 4);
    }
  } else {
    if (!qkv_on) BARRIER();
    if (w < 2) {
      const char* pc; constexpr unsigned len = 51 * 1024;
      asm volatile("s_getpc_b64 %0" : "=s"(pc));
      const unsigned o = (c.j * 2 + w) * 1024 + c.lane * 16;
      if (o + 16 <= len) { const u32x4 t = *(const GAS u32x4*)(pc + o); asm volatile("" ::"v"(t)); }
    }
#pragma unroll 1
    for (int layer = 0; layer < 2; ++layer) {
      const unsigned L = WS_L + layer * L_SZ;
      qkv_compute(fresh(c0), opq(w), opq(layer), x, wqkv);
      WDUMP(layer * 5);
      att_compute(fresh(c0), opq(w), opq(layer));
      WDUMP(layer * 5 + 1);
      proj_compute<10, WO_NB, 20>(fresh(c0), opq(w), layer * 5 + 2, L + L_O, layer == 0 ? (const char*)x : nullptr, WS_X2, L + L_X1,
                                  WS_ST + (layer * 2 + 1) * STB, nullptr, FL_WO + layer * 5);
      WDUMP(layer * 5 + 2);
      if (w < 4) gu_consumer<4>(fresh(c0), opq(layer), 0, opq(w)); else gu_consumer<3>(fresh(c0), opq(layer), 4, opq(w - 4));
      WDUMP(layer * 5 + 3);
      proj_compute<27, DN_NB, 18>(fresh(c0), opq(w), layer * 5 + 4, L + L_H, nullptr, L + L_X1, WS_X2, WS_ST + 2 * STB, layer ? out : nullptr,
                                  FL_DN + layer * 5);
      WDUMP(layer * 5 + 4);
    }
  }
}

extern "C" void kernel_launch(void* const* d_in, const int* in_sizes, int n_in, void* d_out, int out_size, void* d_ws, size_t ws_size,
                              hipStream_t stream) {
  (void)hipFuncSetAttribute((const void*)fused_kernel, hipFuncAttributeMaxDynamicSharedMemorySize, LDS_BYTES);
  fused_kernel<<<256, 768, LDS_BYTES, stream>>>((const uint16_t*)d_in[0], (const uint16_t*)d_in[1], (const uint16_t*)d_in[2],
                                                (const uint16_t*)d_in[4], (const uint16_t*)d_in[5], (const uint16_t*)d_in[6],
                                                (uint16_t*)d_out, (char*)d_ws);
}
```

```cpp
#include <hip/hip_runtime.h>
#include <hip/hip_bf16.h>
#include <stdint.h>

typedef __attribute__((ext_vector_type(8))) __bf16 bf16x8;
typedef __attribute__((ext_vector_type(16))) __bf16 bf16x16;
typedef __attribute__((ext_vector_type(4))) short s16x4;
typedef __attribute__((ext_vector_type(8))) short s16x8;
typedef __attribute__((ext_vector_type(2))) __bf16 bf16x2;
typedef __attribute__((ext_vector_type(2))) float f32x2;
typedef __attribute__((ext_vector_type(4))) float f32x4;
typedef __attribute__((ext_vector_type(8))) float f32x8;
typedef __attribute__((ext_vector_type(16))) float f32x16;
typedef __attribute__((ext_vector_type(2))) unsigned u32x2;
typedef __attribute__((ext_vector_type(4))) unsigned u32x4;
typedef unsigned long long u64;
#define LAS __attribute__((address_space(3)))
#define GAS __attribute__((address_space(1)))
typedef LAS char* lds_ptr;
typedef __amdgpu_buffer_rsrc_t rsrc_t;
#define DI __device__ __forceinline__

constexpr unsigned XB = 2048u * 640 * 2;
constexpr unsigned WS_EPOCH = 0;
constexpr unsigned WS_FLAG = 4096;
constexpr int FL_P0 = 0, FL_RT = 1, FL_QKV = 2, FL_ATT = 3, FL_WO = 4, FL_GU = 5, FL_DN = 6, FL_RT2 = 12;
constexpr unsigned WS_WO  = WS_FLAG + 13u * 32768;
constexpr unsigned WS_WGU = WS_WO + 640u * 640 * 2;
constexpr unsigned WS_WD  = WS_WGU + 3456u * 640 * 2;
constexpr unsigned WS_CSQ = WS_WD + 640u * 1728 * 2;
constexpr unsigned WS_CSG = WS_CSQ + 8u * 1920 * 4;
constexpr unsigned WS_ST  = WS_CSG + 4u * 3456 * 4;
constexpr unsigned STB = 2048u * 64;
constexpr unsigned WS_X2  = WS_ST + 4 * STB;
constexpr unsigned WS_L   = WS_X2 + XB;
constexpr unsigned L_Q1 = 0, L_Q2 = L_Q1 + 32u * 65536, L_K1 = L_Q2 + 16u * 32768, L_K2 = L_K1 + 32u * 65536, L_V = L_K2 + 16u * 32768,
                   L_O = L_V + XB, L_X1 = L_O + XB, L_H = L_X1 + XB, L_SZ = L_H + 2048u * 1728 * 2;
constexpr unsigned WS_END = WS_L + 2 * L_SZ;

constexpr int LDS_BYTES = 163840, MISC = 159744, M_SST = MISC, M_SCS = MISC + 1024, M_CNT = MISC + 2048, M_CNT2 = M_CNT + 8, M_CNT3 = M_CNT + 16, M_CNT4 = M_CNT + 24, M_SIG = M_CNT + 32, M_ISS = M_CNT + 40;
constexpr int QKV_NB = 5, QKV_NA = 4, QKV_A = QKV_NB * 16384;
constexpr int WO_NB = 10, DN_NB = 15;
constexpr int GU_NB = 7, GU_NA = 7, GU_A = GU_NB * 14336;
constexpr int AT_SLOT = 45056, AT_CONST = 3 * AT_SLOT, AT_SCR = AT_CONST + 10752, AT_MERGE = 3 * AT_SLOT;
static_assert(QKV_A + QKV_NA * 16384 <= MISC && DN_NB * 10240 <= MISC && GU_A + GU_NA * 8192 <= MISC, "lds");
static_assert(WO_NB * 10240 <= AT_MERGE && AT_MERGE + 24576 <= MISC && AT_SCR + 3 * 4096 <= MISC, "lds");

DI unsigned pk2(float a, float b) { f32x2 v = {a, b}; bf16x2 r = __builtin_convertvector(v, bf16x2); return __builtin_bit_cast(unsigned, r); }
DI float bflo(unsigned u) { return __uint_as_float(u << 16); }
DI float bfhi(unsigned u) { return __uint_as_float(u & 0xffff0000u); }
DI f32x4 mfma16(bf16x8 a, bf16x8 b, f32x4 c) { return __builtin_amdgcn_mfma_f32_16x16x32_bf16(a, b, c, 0, 0, 0); }
DI f32x16 mfma32(bf16x8 a, bf16x8 b, f32x16 c) { return __builtin_amdgcn_mfma_f32_32x32x16_bf16(a, b, c, 0, 0, 0); }
DI bf16x8 ldsr16(lds_ptr p) { return *(const LAS bf16x8*)p; }
DI bf16x8 ldstr(lds_ptr p, int second) {
  const s16x4 a = __builtin_amdgcn_ds_read_tr16_b64_v4i16((LAS s16x4*)p), b = __builtin_amdgcn_ds_read_tr16_b64_v4i16((LAS s16x4*)(p + second));
  return __builtin_bit_cast(bf16x8, (s16x8)__builtin_shufflevector(a, b, 0, 1, 2, 3, 4, 5, 6, 7));
}
#define BARRIER() asm volatile("s_waitcnt lgkmcnt(0)\n\ts_barrier" ::: "memory")
template <int N> DI void wait_vm() { asm volatile("s_waitcnt vmcnt(%0)" ::"n"(N > 63 ? 63 : N) : "memory"); }
#define WDUMP(id)
#define WRESET()
#define BARRIER_L() BARRIER()
#define BARRIER_NW() asm volatile("s_barrier" ::: "memory")
#define SBAR() __builtin_amdgcn_sched_barrier(0)
#define PIN(x) asm volatile("" : "+v"(x))
#define GL_HEAD "s_mov_b32 m0, %0\n\ts_nop 3\n\t"
#define GL(v, off) "global_load_lds_dwordx4 %" #v ", %1 offset:" #off " sc1\n\t"
DI void glds1(unsigned m, const char* sb, unsigned v0) { asm volatile(GL_HEAD GL(2, 0)::"s"(m), "s"(sb), "v"(v0) : "memory"); }
DI void glds2(unsigned m, const char* sb, unsigned v0, unsigned v1) {
  asm volatile(GL_HEAD GL(2, 0) GL(3, 1024)::"s"(m), "s"(sb), "v"(v0), "v"(v1) : "memory");
}
DI void glds3(unsigned m, const char* sb, unsigned v0, unsigned v1, unsigned v2) {
  asm volatile(GL_HEAD GL(2, 0) GL(3, 1024) GL(4, 2048)::"s"(m), "s"(sb), "v"(v0), "v"(v1), "v"(v2) : "memory");
}
DI void glds4(unsigned m, const char* sb, unsigned v0, unsigned v1, unsigned v2, unsigned v3) {
  asm volatile(GL_HEAD GL(2, 0) GL(3, 1024) GL(4, 2048) GL(5, 3072)::"s"(m), "s"(sb), "v"(v0), "v"(v1), "v"(v2), "v"(v3) : "memory");
}
DI void st16(rsrc_t r, unsigned off, u32x4 v) { __builtin_amdgcn_raw_buffer_store_b128(v, r, off, 0, 16); }
DI void st8(rsrc_t r, unsigned off, u32x2 v) { __builtin_amdgcn_raw_buffer_store_b64(v, r, off, 0, 16); }
DI void st4(rsrc_t r, unsigned off, unsigned v) { __builtin_amdgcn_raw_buffer_store_b32(v, r, off, 0, 16); }
DI u32x4 ld16(rsrc_t r, unsigned off) { return __builtin_amdgcn_raw_buffer_load_b128(r, off, 0, 16); }
DI u32x2 ld8(rsrc_t r, unsigned off) { return __builtin_amdgcn_raw_buffer_load_b64(r, off, 0, 16); }
DI f32x2 ld8_async(unsigned voff, const char* sbase) {
  f32x2 v; asm volatile("s_nop 4\n\tglobal_load_dwordx2 %0, %1, %2 sc1" : "=v"(v) : "v"(voff), "s"(sbase) : "memory"); return v;
}
DI u32x4 pair32(unsigned a0, unsigned a1, unsigned b0, unsigned b1) {
  auto x = __builtin_amdgcn_permlane32_swap(a0, b0, false, false);
  auto y = __builtin_amdgcn_permlane32_swap(a1, b1, false, false);
  return u32x4{x[0], y[0], x[1], y[1]};
}
template <int N> DI float ror16(float v) { return __int_as_float(__builtin_amdgcn_update_dpp(0, __float_as_int(v), 0x120 + N, 0xf, 0xf, false)); }
DI float sum32(float v) {
  v += ror16<8>(v); v += ror16<4>(v); v += ror16<2>(v); v += ror16<1>(v);
  auto a = __builtin_amdgcn_permlane16_swap(__float_as_uint(v), __float_as_uint(v), false, false);
  return __uint_as_float(a[0]) + __uint_as_float(a[1]);
}
DI u32x4 pair16(unsigned a0, unsigned a1, unsigned b0, unsigned b1) {
  auto x = __builtin_amdgcn_permlane16_swap(a0, b0, false, false);
  auto y = __builtin_amdgcn_permlane16_swap(a1, b1, false, false);
  return u32x4{x[0], y[0], x[1], y[1]};
}

DI float rows4(float v) {
  auto a = __builtin_amdgcn_permlane16_swap(__float_as_uint(v), __float_as_uint(v), false, false);
  v = __uint_as_float(a[0]) + __uint_as_float(a[1]);
  auto b = __builtin_amdgcn_permlane32_swap(__float_as_uint(v), __float_as_uint(v), false, false);
  return __uint_as_float(b[0]) + __uint_as_float(b[1]);
}

#define STAMP(id)
#define STAMPC(id)
#define STAMPL(sl, id)
#define SLOT(n)

#define WARM_WO 1
#define WARM_GU 1
#define WARM_DN 0
#define POLL_DEPTH 1
#define POLL_GATE 1
#define POLL_SLEEP 1
#define DN_D0 10
#define A_D0 2
#define ATT_PRIO 3
#define ATT_ORD 0
#define PACE 9
#define SPANST(id)
struct Ctx {
  char* ws; rsrc_t r; lds_ptr lds; u64 tgt; int lane, xcd, j;
};
DI Ctx fresh(const Ctx& c0) { Ctx c = c0; asm volatile("" : "+v"(c.lane), "+s"(c.xcd), "+s"(c.j), "+s"(c.ws)); return c; }
DI int opq(int v) { asm volatile("" : "+s"(v)); return v; }
constexpr int SPIN_CAP = 1 << 18;

DI unsigned flg(int arr, int unit) { return WS_FLAG + (arr * 256 + unit) * 128; }
DI void publish(const Ctx& c, int cnt_off, unsigned n, unsigned to, bool mark = false) {
  if (mark && c.lane == 0) __hip_atomic_fetch_add((LAS unsigned*)(c.lds + M_ISS), 1u, __ATOMIC_RELAXED, __HIP_MEMORY_SCOPE_WORKGROUP);
  wait_vm<0>();
  if (c.lane == 0) {
    const unsigned old = __hip_atomic_fetch_add((LAS unsigned*)(c.lds + cnt_off), 1u, __ATOMIC_RELAXED, __HIP_MEMORY_SCOPE_WORKGROUP);
    if (old % n == n - 1) __hip_atomic_store((GAS u64*)(c.ws + to), c.tgt, __ATOMIC_RELAXED, __HIP_MEMORY_SCOPE_AGENT);
  }
}
template <int N> struct Poll {
  u64 v[POLL_DEPTH][N];
  DI void run(const Ctx& c, unsigned o0, unsigned o1 = 0) {

    auto ld = [&](int r) {
      asm volatile("global_load_dwordx2 %0, %1, %2 sc1" : "=v"(v[r][0]) : "v"(o0), "s"(c.ws) : "memory");
      if constexpr (N == 2) asm volatile("global_load_dwordx2 %0, %1, %2 sc1" : "=v"(v[r][N - 1]) : "v"(o1), "s"(c.ws) : "memory");
      __builtin_amdgcn_s_sleep(POLL_SLEEP);
    };
    auto ok = [&](int r) {
      if constexpr (N == 2) asm volatile("s_waitcnt vmcnt(%2)" : "+v"(v[r][0]), "+v"(v[r][N - 1]) : "n"((POLL_DEPTH - 1) * N) : "memory");
      else asm volatile("s_waitcnt vmcnt(%1)" : "+v"(v[r][0]) : "n"(POLL_DEPTH - 1) : "memory");
      return __all(v[r][0] == c.tgt && v[r][N - 1] == c.tgt);
    };
    asm volatile("s_nop 4" ::: "memory");
    ld(0); if (POLL_DEPTH > 1) ld(1);
#pragma unroll 1
    for (int spin = 0; spin < SPIN_CAP; ++spin) {
      if (ok(0)) break; ld(0);
      if (POLL_DEPTH > 1) { if (ok(1)) break; ld(1); }
    }
  }
  DI void keep() {
#pragma unroll
    for (int r = 0; r < POLL_DEPTH; ++r) { asm volatile("" : "+v"(v[r][0])); if constexpr (N == 2) asm volatile("" : "+v"(v[r][N - 1])); }
  }
};
DI void poll(const Ctx& c, unsigned slot) { Poll<1> p; p.run(c, slot); wait_vm<0>(); p.keep(); }
DI void sig_set(const Ctx& c, unsigned v) { if (c.lane == 0) __hip_atomic_store((LAS unsigned*)(c.lds + M_SIG), v, __ATOMIC_RELAXED, __HIP_MEMORY_SCOPE_WORKGROUP); }
DI void sig_wait(const Ctx& c, unsigned v, int off = M_SIG) {
#pragma unroll 1
  for (int spin = 0; spin < SPIN_CAP * 16; ++spin) {
    if (__builtin_amdgcn_readfirstlane(__hip_atomic_load((LAS unsigned*)(c.lds + off), __ATOMIC_RELAXED, __HIP_MEMORY_SCOPE_WORKGROUP)) >= v) break;
    if (off != M_SIG) __builtin_amdgcn_s_sleep(1);
  }
  asm volatile("" ::: "memory");
}

DI void gate(const Ctx& c, Poll<1>& p, int half, unsigned slot, unsigned sig, unsigned iss) {
  if (half == 0) { if (POLL_GATE) sig_wait(c, iss, M_ISS); p.run(c, slot); sig_set(c, sig); STAMPL(sig - 1, 6); } else sig_wait(c, sig);
}

template <int PP, int NSLOT, int NS, int LAG = 1>
struct Ring {
  unsigned voff[PP];
  const char* base;
  unsigned adv, l0;
  int slot;
  DI void init(const char* b, unsigned a, unsigned lds0, int half) {
    base = b - 4096; adv = a; l0 = lds0 + half * PP * 1024; slot = 0;
#pragma unroll
    for (int p = 0; p < PP; ++p) voff[p] += 4096 - (p & 3) * 1024;
  }
  DI void issue() {
    const unsigned m = l0 + slot * (2 * PP * 1024);
    glds4(m, base, voff[0], voff[1], voff[2], voff[3]);
    if constexpr (PP == 5) glds1(m + 4096, base, voff[4]);
    if constexpr (PP == 7) glds3(m + 4096, base, voff[4], voff[5], voff[6]);
    if constexpr (PP == 8) glds4(m + 4096, base, voff[4], voff[5], voff[6], voff[7]);
    base += adv; slot = slot == NSLOT - 1 ? 0 : slot + 1;
  }
  template <int N = NSLOT> DI void fill() {
#pragma unroll 1
    for (int s = 0; s < N; ++s) issue();
  }
  template <int N = NSLOT> DI void fill_paced() {
#pragma unroll 1
    for (int s = 0; s < N; ++s) { issue(); wait_vm<PACE>(); }
  }
  template <int R, int E> DI void range() { if constexpr (R >= E) { wait_vm<R * PP>(); BARRIER_L(); range<R - 1, E>(); } }
  template <int D0 = NSLOT> DI void head() {
    wait_vm<(D0 - 1) * PP>(); BARRIER_L(); WRESET();
    if constexpr (D0 < NSLOT) fill<NSLOT - D0>();
    range<NSLOT - 2, NSLOT - LAG>();
    steady();
  }
  DI void steady() {
#pragma unroll 1
    for (int k = LAG; k < NS - NSLOT + LAG; ++k) { wait_vm<(NSLOT - 1 - LAG) * PP>(); BARRIER_L(); issue(); }
  }
  template <int D0 = NSLOT> DI void run() { head<D0>(); range<NSLOT - 1 - LAG, 0>(); BARRIER_L(); }
};

DI void ln_rows(const Ctx& c, int row, const f32x2 (&p)[8]) {
  f32x2 t = p[0];
#pragma unroll
  for (int i = 1; i < 8; ++i) t += p[i];
  const float m8 = t[0] * 0.125f; float q = 0.f;
#pragma unroll
  for (int i = 0; i < 8; ++i) { const float a = p[i][0] - m8; q += a * a; }
  const float rstd = __builtin_amdgcn_rsqf((t[1] + q * (1.f / 80.f)) * (1.f / 640.f) + 1e-5f);
  *(LAS float*)(c.lds + M_SST + row * 4) = rstd;
  *(LAS float*)(c.lds + M_SST + 512 + row * 4) = -t[0] * (1.f / 640.f);
}
template <int NP> DI void ln_cols(const Ctx& c, int n4, unsigned off, unsigned pitch) {
  u32x4 q[NP];
#pragma unroll
  for (int i = 0; i < NP; ++i) q[i] = ld16(c.r, off + i * pitch + (c.lane < n4 ? c.lane : 0) * 16);
  f32x4 cs = __builtin_bit_cast(f32x4, q[0]);
#pragma unroll
  for (int i = 1; i < NP; ++i) cs += __builtin_bit_cast(f32x4, q[i]);
  if (c.lane < n4) *(LAS f32x4*)(c.lds + M_SCS + c.lane * 16) = cs;
}

#define MAP 0
DI int qkv_panel(const Ctx& c) { return (c.xcd & 3) * 4 + (c.j & 3); }
DI int qkv_cb(const Ctx& c) { return (c.xcd >> 2) * 8 + (c.j >> 2); }
DI int pr_panel(const Ctx& c) { return (c.xcd & 3) * 8 + (c.j & 7); }
DI int pr_cb(const Ctx& c) { return (c.xcd >> 2) * 4 + (c.j >> 3); }
DI int gu_panel(const Ctx& c) { return (c.xcd & 1) * 8 + (c.j & 7); }
DI int gu_cb(const Ctx& c) { return (c.xcd >> 1) * 4 + (c.j >> 3); }
DI int att_head(const Ctx& c) { return c.xcd * 4 + (c.j & 3); }
DI int att_qb(const Ctx& c) { return c.j >> 2; }

struct Phase0 {
  u32x4 v[3], t[10];
  DI void load(const Ctx& c, int hw, const uint16_t* x, const uint16_t* wqkv) {
    const int lane = c.lane, r32 = lane & 31, b = blockIdx.x, row = b * 8 + hw * 2 + (lane >> 5);
    v[0] = *(const u32x4*)(x + row * 640 + r32 * 8); v[1] = *(const u32x4*)(x + row * 640 + 256 + r32 * 8);
    v[2] = *(const u32x4*)(x + row * 640 + 512 + (r32 & 15) * 8);
    if (hw == 3 && b < 240) {
      const rsrc_t r = __builtin_amdgcn_make_buffer_rsrc((void*)wqkv, 0, 640 * 3840, 0x00020000);
#pragma unroll
      for (int i = 0; i < 10; ++i)
        t[i] = __builtin_amdgcn_raw_buffer_load_b128(r, (lane >> 3) * 3840 + (lane & 7) * 16, ((b / 30) * 80 + i * 8) * 3840 + (b % 30) * 128, 0);
    }
  }
  DI void rows(const Ctx& c, int hw) {
    const int lane = c.lane, r32 = lane & 31, row = blockIdx.x * 8 + hw * 2 + (lane >> 5);
    const float w2 = r32 < 16 ? 1.f : 0.f;
    float sum = 0.f, s2 = 0.f;
#pragma unroll
    for (int e = 0; e < 4; ++e) { sum += (bflo(v[0][e]) + bfhi(v[0][e])) + (bflo(v[1][e]) + bfhi(v[1][e])); s2 += bflo(v[2][e]) + bfhi(v[2][e]); }
    sum = sum32(sum + w2 * s2);
    const float mean = sum * (1.f / 640.f); float m2 = 0.f; s2 = 0.f;
#pragma unroll
    for (int e = 0; e < 4; ++e) {
      const float a0 = bflo(v[0][e]) - mean, a1 = bfhi(v[0][e]) - mean, a2 = bflo(v[1][e]) - mean, a3 = bfhi(v[1][e]) - mean;
      const float a4 = bflo(v[2][e]) - mean, a5 = bfhi(v[2][e]) - mean;
      m2 += a0 * a0 + a1 * a1 + a2 * a2 + a3 * a3; s2 += a4 * a4 + a5 * a5;
    }
    m2 = sum32(m2 + w2 * s2);
    if (r32 < 8) st8(c.r, WS_ST + (row >> 7) * 8192 + r32 * 1024 + (row & 127) * 8, u32x2{__float_as_uint(sum * 0.125f), __float_as_uint(m2 * 0.125f)});
  }
  float s[8];
  DI void cols1(int hw) {
    if (hw == 3 && blockIdx.x < 240) {
      unsigned ul = 0x00003f80u, uh = 0x3f800000u;
      asm volatile("" : "+v"(ul), "+v"(uh));
      const bf16x2 lo = __builtin_bit_cast(bf16x2, ul), hi = __builtin_bit_cast(bf16x2, uh);
#pragma unroll
      for (int e = 0; e < 4; ++e) {
        s[2 * e] = 0.f; s[2 * e + 1] = 0.f;
#pragma unroll
        for (int i = 0; i < 10; ++i) {
          const unsigned u = t[i][e];
          s[2 * e] = __builtin_amdgcn_fdot2_f32_bf16(__builtin_bit_cast(bf16x2, u), lo, s[2 * e], false);
          s[2 * e + 1] = __builtin_amdgcn_fdot2_f32_bf16(__builtin_bit_cast(bf16x2, u), hi, s[2 * e + 1], false);
        }
      }
#pragma unroll
      for (int e = 0; e < 8; ++e) PIN(s[e]);
    }
  }
  DI void cols2(const Ctx& c, int hw) {
    const int lane = c.lane, b = blockIdx.x;
    if (hw == 3 && b < 240) {
#pragma unroll
      for (int e = 0; e < 8; ++e) s[e] = rows4(s[e] + ror16<8>(s[e]));
      const int hi = (lane >> 3) & 1;
      const f32x4 o = hi ? f32x4{s[4], s[5], s[6], s[7]} : f32x4{s[0], s[1], s[2], s[3]};
      if (lane < 16) st16(c.r, WS_CSQ + ((b / 30) * 1920 + (b % 30) * 64 + (lane & 7) * 8 + hi * 4) * 4, __builtin_bit_cast(u32x4, o));
    }
  }
};

template <bool SWAP, class PRE>
DI void qkv_loop(const Ctx& c, int w, int layer, f32x16 (&acc)[2][2], PRE pre) {
  const int lane = c.lane, i = lane & 15, g = lane >> 4, wm = w & 1, wn = w >> 1;
  lds_ptr pa[4];
#pragma unroll
  for (int s = 0; s < 4; ++s)
    pa[s] = c.lds + QKV_A + wm * 8192 + (g & 1) * 2048 +
            (layer == 0 ? (i >> 3) * 1024 + (i & 7) * 128 + (((2 * s + (g >> 1)) ^ ((i >> 1) & 7)) << 4) : (2 * s + (g >> 1)) * 256 + i * 16);
  const int rb = ((g >> 1) * 8 + (i >> 2)) * 256 + (i & 3) * 8, u = (wn * 4 + (g & 1)) ^ ((i >> 2) << 1);
  lds_ptr pb0 = c.lds + rb + (u << 5), pb1 = c.lds + rb + ((u ^ 2) << 5);
  bf16x8 x0[4], x1[4], w0[4], w1[4];
  auto rdx = [&](bf16x8* xn, int hs, int e) { xn[e] = ldsr16(pa[hs * 2 + (e >> 1)] + (e & 1) * 4096); };
  auto rdw = [&](bf16x8* wn_, int hs, int e) { wn_[e] = ldstr(((e & 1) ? pb1 : pb0) + (hs * 2 + (e >> 1)) * 4096, 1024); };
  auto step = [&](const bf16x8* xa, const bf16x8* wb, bf16x8* xn, bf16x8* wn_, int hs, bool ld) {
#pragma unroll
    for (int q = 0; q < 8; ++q) {
      if (ld) { if (q & 1) rdx(xn, hs, q >> 1); else rdw(wn_, hs, q >> 1); }
      const int s = q >> 2, tm = q & 1, tn = (q >> 1) & 1;
      acc[tm][tn] = SWAP ? mfma32(wb[s * 2 + tn], xa[s * 2 + tm], acc[tm][tn]) : mfma32(xa[s * 2 + tm], wb[s * 2 + tn], acc[tm][tn]);
      SBAR();
    }
  };
  SLOT(layer * 5);
  if (layer) STAMP(0);
  STAMP(1);
  BARRIER();
  STAMP(2); STAMPC(0); WRESET();
#pragma unroll
  for (int e = 0; e < 4; ++e) { rdw(w0, 0, e); rdx(x0, 0, e); }
  int sa = 0, sb = 0;
#pragma unroll 1
  for (int st = 0; st < 9; ++st) {
    step(x0, w0, x1, w1, 1, true);
    const int da = sa == QKV_NA - 1 ? -(QKV_NA - 1) * 16384 : 16384, db = sb == QKV_NB - 1 ? -(QKV_NB - 1) * 16384 : 16384;
    sa = sa == QKV_NA - 1 ? 0 : sa + 1; sb = sb == QKV_NB - 1 ? 0 : sb + 1;
    pa[0] += da; pa[1] += da; pa[2] += da; pa[3] += da; pb0 += db; pb1 += db;
    BARRIER_NW();
    step(x1, w1, x0, w0, 0, true);
  }
  step(x0, w0, x1, w1, 1, true);
  BARRIER();
  pre(); SBAR();
  step(x1, w1, x0, w0, 0, false);
  STAMP(3); STAMPC(1);
}
DI void qkv_helper(const Ctx& c, int hw, int layer, const uint16_t* x, const uint16_t* wqkv) {
  const int panel = qkv_panel(c), cb = qkv_cb(c), l = c.lane;
  const bool on = cb != 15;
  if (layer == 0) {
    Phase0 p0;
    p0.load(c, hw, x, wqkv);
    if (on) BARRIER();
    p0.cols1(hw);
    if (on && hw == 3) BARRIER();
    p0.rows(c, hw);
    if (on && hw != 3) BARRIER();
    p0.cols2(c, hw);
    if (on) { BARRIER(); BARRIER(); }
    publish(c, M_CNT2, 4, flg(FL_P0, blockIdx.x));
    if (!on) return;
    BARRIER(); BARRIER();
    const unsigned at = flg(FL_P0, hw < 2 ? panel * 16 + hw * 8 + (l & 7) : (l & 7) * 30 + cb * 2 + ((l >> 3) & 1));
    const u64 v = __hip_atomic_load((const GAS u64*)(c.ws + at), __ATOMIC_RELAXED, __HIP_MEMORY_SCOPE_AGENT);
    BARRIER();
    if (!__all(v == c.tgt)) poll(c, at);
  } else {
    if (!on) return;
    BARRIER();
  }
  f32x2 p[8];
  if (hw < 2) {
#pragma unroll
    for (int i = 0; i < 8; ++i)
      p[i] = __builtin_bit_cast(f32x2, ld8(c.r, WS_ST + layer * 2 * STB + panel * 8192 + i * 1024 + (hw * 64 + l) * 8));
  }
  BARRIER();
  if (hw == 2) ln_cols<8>(c, 32, WS_CSQ + cb * 512, 7680);
  BARRIER();
  if (hw < 2) ln_rows(c, hw * 64 + l, p);
  if (layer) { BARRIER(); BARRIER(); BARRIER(); BARRIER(); BARRIER(); BARRIER(); }
  BARRIER(); BARRIER();
}
DI void qkv_compute(const Ctx& c, int w, int layer, const uint16_t* x, const uint16_t* wqkv) {
  const int panel = qkv_panel(c), cb = qkv_cb(c);
  if (w >= 4) { qkv_helper(c, w - 4, layer, x, wqkv); return; }
  if (cb == 15) return;
  const int lane = c.lane, r32 = lane & 31, h = lane >> 5, wm = w & 1, wn = w >> 1;
  const unsigned L = WS_L + layer * L_SZ;
  f32x16 acc[2][2] = {{f32x16{}, f32x16{}}, {f32x16{}, f32x16{}}};
  const LAS float* srs = (const LAS float*)(c.lds + M_SST) + wm * 64;
  const LAS float* scs = (const LAS float*)(c.lds + M_SCS) + wn * 64;
  if (cb < 10) {
    float rs[2], nm[2]; f32x4 cs[8];
    qkv_loop<true>(c, w, layer, acc, [&]() {
      rs[0] = srs[r32]; rs[1] = srs[32 + r32]; nm[0] = srs[128 + r32]; nm[1] = srs[160 + r32];
#pragma unroll
      for (int e = 0; e < 8; ++e) cs[e] = *(const LAS f32x4*)(scs + e * 8 + h * 4);
    });
    const float qs = cb < 5 ? 0.22360679774997896f * 1.4426950408889634f : 1.f;
    const unsigned i1 = L + (cb < 5 ? L_Q1 : L_K1), i2 = L + (cb < 5 ? L_Q2 : L_K2);
    rs[0] *= qs; rs[1] *= qs;
#pragma unroll
    for (int tp = 0; tp < 4; ++tp) {
      const int tn = tp >> 1, pr = tp & 1;
      const int o = (cb % 5) * 16 + wn * 8 + tp * 2 + h, hp = o / 5, pos = o - hp * 5;
      const unsigned ts = pos == 2 ? 512 : 1024;
      const unsigned a0 = (pos == 2 ? i2 + hp * 32768 : i1 + (2 * hp + (pos > 2)) * 65536 + (pos > 2 ? pos - 3 : pos) * 512) +
                          (panel * 4 + wm * 2) * ts + r32 * 16;
#pragma unroll
      for (int tm = 0; tm < 2; ++tm) {
        const f32x16 a = acc[tm][tn];
        const f32x4 u = (f32x4{a[pr * 8], a[pr * 8 + 1], a[pr * 8 + 2], a[pr * 8 + 3]} + cs[tp * 2] * nm[tm]) * rs[tm];
        const f32x4 v = (f32x4{a[pr * 8 + 4], a[pr * 8 + 5], a[pr * 8 + 6], a[pr * 8 + 7]} + cs[tp * 2 + 1] * nm[tm]) * rs[tm];
        st16(c.r, a0 + tm * ts, pair32(pk2(u[0], u[1]), pk2(u[2], u[3]), pk2(v[0], v[1]), pk2(v[2], v[3])));
      }
    }
  } else {
    float cs[2]; f32x4 rn[2][4];
    auto rd = [&](int tp) {
#pragma unroll
      for (int e = 0; e < 4; ++e) rn[tp & 1][e] = *(const LAS f32x4*)(srs + (e >> 1) * 128 + tp * 16 + (e & 1) * 8 + h * 4);
    };
    qkv_loop<false>(c, w, layer, acc, [&]() { cs[0] = scs[r32]; cs[1] = scs[32 + r32]; rd(0); rd(1); });
    unsigned a0[2];
#pragma unroll
    for (int tn = 0; tn < 2; ++tn) {
      const int cv = (cb - 10) * 128 + wn * 64 + tn * 32 + r32, head = cv / 20, dim = cv - head * 20;
      const int d = (head & 1) ? (dim < 4 ? dim + 16 : dim - 4) : dim;
      a0[tn] = L + L_V + head * 81920 + (panel * 8 + wm * 4) * 640 + h * 320 + d * 16;
    }
#pragma unroll
    for (int tp = 0; tp < 4; ++tp) {
      const int tm = tp >> 1, pr = tp & 1;
      u32x4 o[2];
#pragma unroll
      for (int tn = 0; tn < 2; ++tn) {
        const f32x16 a = acc[tm][tn];
        const f32x4 u = (f32x4{a[pr * 8], a[pr * 8 + 1], a[pr * 8 + 2], a[pr * 8 + 3]} + rn[pr][2] * cs[tn]) * rn[pr][0];
        const f32x4 v = (f32x4{a[pr * 8 + 4], a[pr * 8 + 5], a[pr * 8 + 6], a[pr * 8 + 7]} + rn[pr][3] * cs[tn]) * rn[pr][1];
        o[tn] = pair32(pk2(u[0], u[1]), pk2(u[2], u[3]), pk2(v[0], v[1]), pk2(v[2], v[3]));
      }
      if (tp < 2) rd(tp + 2);
      st16(c.r, a0[0] + tp * 640, o[0]); st16(c.r, a0[1] + tp * 640, o[1]);
      SBAR();
    }
  }
  SLOT(layer * 5);
  STAMP(4);
  publish(c, M_CNT4, 4, flg(FL_QKV + layer * 5, panel * 16 + cb), true);
  STAMP(5);
}
typedef Ring<8, QKV_NB, 10, 2> QkvB;
DI void qkv_b_run(const Ctx& c, int half, int layer, const uint16_t* wqkv) {
  QkvB rg;
  const int cb = qkv_cb(c);
#pragma unroll
  for (int p = 0; p < 8; ++p) {
    const int row = (half * 8 + p) * 4 + (c.lane >> 4);
    rg.voff[p] = row * 3840 + cb * 256 + (((c.lane & 15) ^ ((row & 3) << 2)) << 4);
  }
  rg.init((const char*)wqkv, 64 * 3840, 0, half);
  if (layer) { sig_wait(c, 28, M_ISS); rg.fill_paced(); wait_vm<(QKV_NB - 1) * 8>(); BARRIER_L(); }
  else { rg.template fill<2>(); wait_vm<8>(); BARRIER_L(); rg.template fill<QKV_NB - 2>(); }
  WRESET();
  rg.template range<QKV_NB - 2, QKV_NB - 2>(); rg.steady(); rg.template range<QKV_NB - 3, 0>(); BARRIER_L();
}
DI void qkv_a_run(const Ctx& c, int half, int layer, const uint16_t* x) {
  const int panel = qkv_panel(c);
  Ring<8, QKV_NA, 10, 2> rg;
#pragma unroll
  for (int p = 0; p < 8; ++p) {
    const int mt = panel * 8 + half * 4 + (p >> 1), r16 = (p & 1) * 8 + (c.lane >> 3);
    rg.voff[p] = layer == 0 ? (mt * 16 + r16) * 1280 + (((c.lane & 7) ^ ((r16 >> 1) & 7)) << 4) : WS_X2 + mt * 20480 + (p & 1) * 1024 + c.lane * 16;
  }
  rg.init(layer == 0 ? (const char*)x : c.ws, layer == 0 ? 128 : 2048, QKV_A, half);
  Poll<1> pl;
  if (layer) gate(c, pl, half, flg(FL_DN, panel * 16 + (c.lane & 15)), 6, 28);
  rg.template fill<A_D0>();
  rg.template run<A_D0>();
  pl.keep();
}

DI f32x16 qk8(s16x4 k2, s16x4 q2) {
 return __builtin_amdgcn_mfma_f32_32x32x8bf16_1k(k2, q2, f32x16{}, 0, 0, 0); }
DI f32x16 qk16(bf16x8 k1, bf16x8 q1, f32x16 s) { return __builtin_amdgcn_mfma_f32_32x32x16_bf16(k1, q1, s, 0, 0, 0); }
DI f32x16 pv_lo(bf16x8 v, f32x16 p, f32x16 o) {
  return __builtin_amdgcn_smfmac_f32_32x32x32_bf16(v, __builtin_bit_cast(bf16x16, (f32x8)__builtin_shufflevector(p, p, 0, 1, 2, 3, 4, 5, 6, 7)), o, 0xDDDD, 0, 0);
}
DI f32x16 pv_hi(bf16x8 v, f32x16 p, f32x16 o) {
  return __builtin_amdgcn_smfmac_f32_32x32x32_bf16(v, __builtin_bit_cast(bf16x16, (f32x8)__builtin_shufflevector(p, p, 8, 9, 10, 11, 12, 13, 14, 15)), o, 0xDDDD, 0, 0);
}
DI float swap32(float v, int h) {
  auto r = __builtin_amdgcn_permlane32_swap(__float_as_uint(v), __float_as_uint(v), false, false);
  return __uint_as_float(h ? r[0] : r[1]);
}
#define EXP4(X, i) do { X[i] = __builtin_amdgcn_exp2f(X[i]); X[i + 1] = __builtin_amdgcn_exp2f(X[i + 1]); X[i + 2] = __builtin_amdgcn_exp2f(X[i + 2]); \
                        X[i + 3] = __builtin_amdgcn_exp2f(X[i + 3]); PIN(X); SBAR(); } while (0)
DI void att_compute(const Ctx& c, int w, int layer) {
  const int lane = c.lane, head = att_head(c), qb = att_qb(c);
  const int qp = w & 3, kp = w >> 2, r32 = lane & 31, h = lane >> 5;
  lds_ptr lds = c.lds;
  const unsigned L = WS_L + layer * L_SZ;
  SLOT(layer * 5 + 1);
  STAMP(0);
  for (int s = w * 64 + lane; s < 672; s += 512) {
    const unsigned v = s % 20 == 0 ? 0x3f803f80u : 0u;
    *(LAS u32x4*)(lds + AT_CONST + s * 16) = u32x4{v, v, v, v};
  }
  lds_ptr pk1 = lds + kp * 8192 + lane * 16;
  lds_ptr pk2_ = h ? lds + AT_CONST + 16 : lds + 16384 + kp * 4096 + r32 * 16 + (head & 1) * 8;
  lds_ptr pv = (r32 >= 20 ? lds + AT_CONST : lds + 24576 + kp * 10240) + h * 320 + r32 * 16;
  const int vmask = r32 >= 20 ? 0 : -1, kmask = h ? 0 : -1;
  f32x16 o0 = f32x16{}, o1 = f32x16{};
  f32x16 s0, s1, s2, s3 = f32x16{};
  STAMP(1);
  BARRIER();
  STAMP(2); STAMPC(0); WRESET();
  const bf16x8 q1a = ldsr16(lds + 2 * AT_SLOT + qp * 2048 + lane * 16), q1b = ldsr16(lds + 2 * AT_SLOT + qp * 2048 + 1024 + lane * 16);
  lds_ptr pq2 = h ? lds + AT_CONST + 16 : lds + 2 * AT_SLOT + 8192 + qp * 1024 + r32 * 16 + (head & 1) * 8;
  const s16x4 q2a = *(const LAS s16x4*)pq2, q2b = *(const LAS s16x4*)(pq2 + 512);
  bf16x8 k1 = ldsr16(pk1), vlo = ldsr16(pv), vhi = bf16x8{};
  s16x4 k2 = *(const LAS s16x4*)pk2_;
  s0 = qk16(k1, q1a, qk8(k2, q2a));
#define PH_EVEN(SC, SN, SP, KLOAD, VOFF) do { \
    SN = qk8(k2, q2b); SBAR(); EXP4(SC, 0); SN = qk16(k1, q1b, SN); SBAR(); KLOAD; SBAR(); EXP4(SC, 4); \
    o1 = pv_hi(vhi, SP, o1); SBAR(); vhi = ldsr16(pv + (VOFF) + 640); SBAR(); EXP4(SC, 8); o0 = pv_lo(vlo, SC, o0); SBAR(); EXP4(SC, 12); } while (0)
#define PH_ODD(SC, SN, SP, VLOAD) do { \
    SN = qk8(k2, q2a); SBAR(); EXP4(SC, 0); SN = qk16(k1, q1a, SN); SBAR(); EXP4(SC, 4); \
    o0 = pv_hi(vhi, SP, o0); SBAR(); EXP4(SC, 8); o1 = pv_lo(vlo, SC, o1); SBAR(); VLOAD; SBAR(); EXP4(SC, 12); } while (0)
#define KLD(t) do { k1 = ldsr16(pk1 + (t) * 1024); k2 = *(const LAS s16x4*)(pk2_ + (t) * 512); } while (0)
#define PRI(v) do { asm volatile("s_cmp_eq_u32 %0, 0\n\ts_cbranch_scc1 1f\n\ts_setprio " #v "\n1:" :: "s"(kp) : "scc"); SBAR(); } while (0)
#define HI(n) if (ATT_PRIO == (n)) PRI(1);
#define LO(n) if (ATT_PRIO == (n)) PRI(0);
#define TWO(t) HI(3) HI(4) HI(5) PH_EVEN(s0, s1, s3, KLD((t) + 1), (t) * 1280); PH_ODD(s1, s2, s0, vlo = ldsr16(pv + ((t) + 1) * 1280)); LO(4)
  for (int grp = 0; grp < 4; ++grp) {
    TWO(0) PH_EVEN(s2, s3, s1, if (grp == 0) BARRIER_NW(); KLD(2), 1280); LO(3) PH_ODD(s3, s0, s2, vlo = ldsr16(pv + 2 * 1280));
    TWO(2) PH_EVEN(s2, s3, s1, KLD(4), 3 * 1280); LO(3) LO(5) PH_ODD(s3, s0, s2, vlo = ldsr16(pv + 4 * 1280));
    TWO(4) PH_EVEN(s2, s3, s1, KLD(6), 5 * 1280); LO(3) PH_ODD(s3, s0, s2, vlo = ldsr16(pv + 6 * 1280));
    TWO(6)
    const int d = grp == 2 ? -2 * AT_SLOT : grp == 3 ? 0 : AT_SLOT;
    PH_EVEN(s2, s3, s1, pk1 += d; pk2_ += (d & kmask); if (grp != 3) BARRIER(); KLD(0), 7 * 1280);
    LO(3) LO(5) PH_ODD(s3, s0, s2, pv += (d & vmask); vlo = ldsr16(pv));
  }
  o1 = pv_hi(vhi, s3, o1);
  STAMP(3); STAMPC(1);
  __builtin_amdgcn_s_setprio(0);
  BARRIER();
  const f32x16 give = kp ? o0 : o1, keep = kp ? o1 : o0;
#pragma unroll
  for (int e = 0; e < 3; ++e)
    *(LAS f32x4*)(lds + AT_MERGE + (w * 3 + e) * 1024 + lane * 16) = f32x4{give[4 * e], give[4 * e + 1], give[4 * e + 2], give[4 * e + 3]};
  BARRIER();
  f32x4 a[3];
#pragma unroll
  for (int e = 0; e < 3; ++e)
    a[e] = f32x4{keep[4 * e], keep[4 * e + 1], keep[4 * e + 2], keep[4 * e + 3]} + *(const LAS f32x4*)(lds + AT_MERGE + ((w ^ 4) * 3 + e) * 1024 + lane * 16);
  const int qt = qb * 8 + qp * 2 + kp;
  float l = h ? a[2][0] : 0.f;
  l += swap32(l, h);
  if (__builtin_expect(__any(!(l >= 5.421e-20f && l <= 1.8447e19f)), 0)) {
    const unsigned o2 = (head >> 1) * 32768 + r32 * 16 + (head & 1) * 8;
    const bf16x8 fq1 = __builtin_bit_cast(bf16x8, ld16(c.r, L + L_Q1 + head * 65536 + qt * 1024 + lane * 16));
    u32x2 fq2 = ld8(c.r, L + L_Q2 + o2 + qt * 512);
    if (h) fq2 = u32x2{0u, 0u};
    const unsigned vi = L + L_V + head * 81920 + h * 320 + (r32 < 20 ? r32 : 0) * 16;
    f32x16 oo = f32x16{};
    float m = -INFINITY;
    const u32x4 one8 = {0x3f803f80u, 0x3f803f80u, 0x3f803f80u, 0x3f803f80u};
    for (int t = 0; t < 64; ++t) {
      const u32x4 fk1 = ld16(c.r, L + L_K1 + head * 65536 + t * 1024 + lane * 16);
      const u32x2 fk2 = ld8(c.r, L + L_K2 + o2 + t * 512);
      u32x4 v0 = ld16(c.r, vi + t * 1280), v1 = ld16(c.r, vi + t * 1280 + 640);
      if (r32 >= 20) { v0 = one8; v1 = one8; }
      f32x16 s = qk16(__builtin_bit_cast(bf16x8, fk1), fq1, qk8(__builtin_bit_cast(s16x4, fk2), __builtin_bit_cast(s16x4, fq2)));
      float tm = s[0];
#pragma unroll
      for (int r = 1; r < 16; ++r) tm = fmaxf(tm, s[r]);
      tm = fmaxf(tm, swap32(tm, h));
      const float mn = fmaxf(m, tm), al = __builtin_amdgcn_exp2f(m - mn);
      m = mn;
#pragma unroll
      for (int r = 0; r < 16; ++r) { s[r] = __builtin_amdgcn_exp2f(s[r] - mn); oo[r] *= al; }
      oo = pv_hi(__builtin_bit_cast(bf16x8, v1), s, pv_lo(__builtin_bit_cast(bf16x8, v0), s, oo));
    }
#pragma unroll
    for (int e = 0; e < 3; ++e) a[e] = f32x4{oo[4 * e], oo[4 * e + 1], oo[4 * e + 2], oo[4 * e + 3]};
    l = h ? a[2][0] : 0.f;
    l += swap32(l, h);
  }
  const float inv = 1.f / l;
  const unsigned A0 = pk2(a[0][0] * inv, a[0][1] * inv), A1 = pk2(a[0][2] * inv, a[0][3] * inv);
  const unsigned B0 = pk2(a[1][0] * inv, a[1][1] * inv), B1 = pk2(a[1][2] * inv, a[1][3] * inv);
  const u32x2 C = {pk2(a[2][0] * inv, a[2][1] * inv), pk2(a[2][2] * inv, a[2][3] * inv)};
  const int tok = qt * 32 + r32;
  const unsigned ob = L + L_O + (tok >> 4) * 20480 + (tok & 15) * 16 + (head >> 1) * 1280;
  st16(c.r, ob + ((head & 1) * 3 + h) * 256, pair32(A0, A1, B0, B1));
  if (!h) st8(c.r, ob + 512 + (head & 1) * 8, C);
  STAMP(4);
  publish(c, M_CNT, 8, flg(FL_ATT + layer * 5, qb * 32 + head), true);
  STAMP(5);
}
DI void retile_load(const Ctx& c, int J, u32x4 (&v)[5][4], const uint16_t* wo, const uint16_t* wg, const uint16_t* wu, const uint16_t* wd) {
  const int e = J < 216 ? J : J < 256 ? J - 216 : J - 256;
  const int strip = J < 216 ? (e >> 2) % 27 : e % 10, k0 = J < 216 ? (e & 3) * 5 : min((e / 10) * 5, 49), ld = J < 216 ? 3456 : 1280;
  const rsrc_t r = __builtin_amdgcn_make_buffer_rsrc((void*)(J < 108 ? wg : J < 216 ? wu : J < 256 ? wo : wd), 0,
                                                     J < 216 ? 640 * 3456 : J < 256 ? 640 * 1280 : 1728 * 1280, 0x00020000);
  const unsigned vo = (c.lane >> 3) * ld + (c.lane & 7) * 16, so = k0 * 32 * ld + strip * 128;
#pragma unroll
  for (int u = 0; u < 5; ++u)
#pragma unroll
    for (int q = 0; q < 4; ++q) v[u][q] = __builtin_amdgcn_raw_buffer_load_b128(r, vo, so + (u * 32 + q * 8) * ld, 0);
}
DI void retile_store(const Ctx& c, int J, lds_ptr scr, const u32x4 (&v)[5][4]) {
  const int e = J < 216 ? J : J < 256 ? J - 216 : J - 256, mat = J >= 108;
  const int strip = J < 216 ? (e >> 2) % 27 : e % 10, k0 = J < 216 ? (e & 3) * 5 : min((e / 10) * 5, 49);
  const unsigned ts = J < 216 ? 40960 : J < 256 ? 20480 : 55296;
  const unsigned dst = (J < 216 ? WS_WGU + mat * 20480 : J < 256 ? WS_WO : WS_WD) + strip * 4 * ts + k0 * 1024;
  const int i = c.lane & 15, g = c.lane >> 4;
  const bf16x8 ones = __builtin_bit_cast(bf16x8, u32x4{0x3f803f80u, 0x3f803f80u, 0x3f803f80u, 0x3f803f80u});
  f32x4 cs[4] = {f32x4{}, f32x4{}, f32x4{}, f32x4{}};
#pragma unroll
  for (int u = 0; u < 5; ++u) {
#pragma unroll
    for (int p = 0; p < 4; ++p) *(LAS u32x4*)(scr + p * 1024 + c.lane * 16) = v[u][p];
#pragma unroll
    for (int t = 0; t < 4; ++t) {
      const bf16x8 f = ldstr(scr + (g * 8 + (i >> 2)) * 128 + t * 32 + (i & 3) * 8, 512);
      __builtin_amdgcn_raw_buffer_store_b128(__builtin_bit_cast(u32x4, f), c.r, c.lane * 16, dst + u * 1024 + t * ts, 16);
      cs[t] = mfma16(f, ones, cs[t]);
    }
  }
  if (J < 216 && i == 0) {
#pragma unroll
    for (int t = 0; t < 4; ++t)
      __builtin_amdgcn_raw_buffer_store_b128(__builtin_bit_cast(u32x4, cs[t]), c.r, g * 16, WS_CSG + ((e & 3) * 3456 + (strip * 8 + t * 2 + mat) * 16) * 4, 16);
  }
}
DI int early_piece(int e) { return e < 4 ? (e & 1) + (e >> 1) * 8 : e < 6 ? 16 + (e - 4) * 4 : e < 9 ? 18 + e : 25 + e; }
DI int late_piece(int e) { return e < 6 ? 2 + e : e < 12 ? 4 + e : e < 15 ? 5 + e : e < 18 ? 6 + e : e < 25 ? 9 + e : 12 + e; }
DI void att_load(const Ctx& c, int lw, int layer, unsigned iss, const uint16_t* wo, const uint16_t* wg, const uint16_t* wu, const uint16_t* wd) {
  const int head = att_head(c), qb = att_qb(c), lane = c.lane;
  const char* Lp = c.ws + WS_L + layer * L_SZ;
  const unsigned vo = lane * 16;
  auto src_of = [&](int p) {
    return p < 16 ? L_K1 + head * 65536 + p * 1024 : p < 24 ? L_K2 + (head >> 1) * 32768 + (p - 16) * 1024 : L_V + head * 81920 + (p - 24) * 1024;
  };
  unsigned v0[14], m0[11], vg[11], sg[11];
#pragma unroll
  for (int q = 0; q < 11; ++q) {
    const int p = q < 3 ? early_piece(lw * 3 + q) : late_piece(lw * 8 + q - 3), pg = lw * 11 + q;
    v0[q] = src_of(p) + vo; m0[q] = p * 1024;
    vg[q] = src_of(pg) + vo; sg[q] = pg < 16 ? 16384 : pg < 24 ? 8192 : 20480;
    asm volatile("" : "+v"(v0[q]), "+s"(m0[q]));
  }
#pragma unroll
  for (int q = 0; q < 3; ++q) {
    const int p = lw * 3 + q;
    v0[11 + q] = (p < 8 ? L_Q1 + head * 65536 + qb * 8192 + p * 1024 : L_Q2 + (head >> 1) * 32768 + qb * 4096 + (p - 8) * 1024) + vo - q * 1024;
    PIN(v0[11 + q]);
  }
  auto issue = [&](int slot) {
#pragma unroll
    for (int q = 0; q < 11; ++q) { vg[q] += sg[q]; glds1(slot * AT_SLOT + (lw * 11 + q) * 1024, Lp, vg[q]); }
  };
  Poll<2> pl;
  if (lw == 0) {
    const int o0 = (head >> 1) * 5 + (head & 1) * 2, v0 = head * 20;
    if (POLL_GATE) sig_wait(c, iss, M_ISS);
    const int sel = lane >> 4, cb = sel == 0 ? 5 + (o0 >> 4) : sel == 1 ? 5 + ((o0 + 2) >> 4) : sel == 2 ? 10 + (v0 >> 7) : 10 + ((v0 + 19) >> 7);
    pl.run(c, flg(FL_QKV + layer * 5, (lane & 15) * 16 + cb), flg(FL_QKV + layer * 5, (qb * 2 + (lane & 1)) * 16 + ((o0 + (lane & 2)) >> 4)));
    sig_set(c, layer * 5 + 2);
    STAMPL(layer * 5 + 1, 6);
  } else sig_wait(c, layer * 5 + 2);
  glds3(2 * AT_SLOT + lw * 3072, Lp, v0[11], v0[12], v0[13]);
#pragma unroll
  for (int q = 0; q < 3; ++q) glds1(m0[q], Lp, v0[q]);
  wait_vm<0>(); BARRIER();
#pragma unroll
  for (int q = 3; q < 11; ++q) glds1(m0[q], Lp, v0[q]);
  pl.keep();
  wait_vm<0>(); BARRIER();
  issue(1);
  auto warm = [&]() {
    const int e = c.j & 7;
    const unsigned n1 = WARM_WO ? 13 : 0, n2 = n1 + (WARM_GU ? 35 : 0), n3 = n2 + (WARM_DN ? 34 : 0);
#pragma unroll 1
    for (unsigned q = lw; q < n3; q += 4) {
      const unsigned src = q < n1 ? WS_WO + pr_cb(c) * 102400 + min(e * 13 + q, 99u) * 1024
                         : q < n2 ? WS_WGU + ((gu_cb(c) * 27) >> 2) * 40960 + (e * 35 + q - n1) * 1024
                                  : WS_WD + pr_cb(c) * 276480 + min(e * 34 + q - n2, 269u) * 1024;
      glds1(AT_SCR + lw * 1024, c.ws + src, vo);
      wait_vm<4>();
    }
    wait_vm<0>();
  };
  const int J = (lw - 1) * 256 + blockIdx.x;
  const bool rt = layer == 0 && lw > 0 && J < 366;
  u32x4 v[5][4];
  if (rt) { retile_load(c, J, v, wo, wg, wu, wd); wait_vm<20>(); } else wait_vm<0>();
  BARRIER();
  issue(2);
  wait_vm<0>(); BARRIER();
  issue(0);
  if (rt) retile_store(c, J, c.lds + AT_SCR + (lw - 1) * 4096, v);
  if (layer == 0 && (lw == 1 || lw == 2)) publish(c, M_CNT3, 2, flg(FL_RT, blockIdx.x));
  wait_vm<0>(); BARRIER();
  if (layer == 0) {
    if (lw == 0) {
      if (c.j == 0) {
#pragma unroll 1
        for (int q = 0; q < 2; ++q) { pl.run(c, flg(FL_RT, q * 128 + lane), flg(FL_RT, q * 128 + 64 + lane)); wait_vm<0>(); pl.keep(); }
        if (lane < 32) __hip_atomic_store((GAS u64*)(c.ws + flg(FL_RT2, lane * 8 + c.xcd)), c.tgt, __ATOMIC_RELAXED, __HIP_MEMORY_SCOPE_AGENT);
      }
      poll(c, flg(FL_RT2, blockIdx.x));
      sig_set(c, 3);
    } else sig_wait(c, 3);
  }
  warm();
  BARRIER(); BARRIER();
}

template <int NS, int NB, int WIN>
DI void proj_compute(const Ctx& c, int w, int slot_id, unsigned a_off, const char* res_rm, unsigned res_off, unsigned out_off, unsigned st_off,
                     uint16_t* out, int arr) {
  constexpr bool RES = NS <= NB;
  const int panel = pr_panel(c), cb = pr_cb(c);
  SLOT(slot_id);
  STAMP(0);
  if (w >= 4) {
#pragma unroll 1
    for (int k = 0; k <= (RES ? 1 : NS); ++k) BARRIER();
    return;
  }
  const int lane = c.lane, i = lane & 15, g = lane >> 4, mt = panel * 4 + w;
  u32x2 res[5];
#pragma unroll
  for (int t = 0; t < 5; ++t)
    res[t] = res_rm ? *(const u32x2*)(res_rm + (mt * 16 + i) * 1280 + cb * 160 + t * 32 + g * 8)
                    : ld8(c.r, res_off + (mt * 80 + cb * 10 + t * 2 + (g >> 1)) * 256 + i * 16 + (g & 1) * 8);
  f32x4 acc[5];
#pragma unroll
  for (int t = 0; t < 5; ++t) { acc[t] = f32x4{bflo(res[t][0]), bfhi(res[t][0]), bflo(res[t][1]), bfhi(res[t][1])}; PIN(acc[t]); }
  lds_ptr pb = c.lds + lane * 16;
  unsigned av = a_off + mt * (NS * 2048) + lane * 16;
  u32x4 a[WIN];
  bf16x8 f[3][5];
  int sb = 0;
  STAMP(1);
  BARRIER();
  STAMP(2); STAMPC(0); WRESET();
  constexpr int AH = RES ? 12 : WIN;
#pragma unroll
  for (int k = 0; k < AH; ++k) a[k] = ld16(c.r, av + k * 1024);
#pragma unroll
  for (int t = 0; t < 5; ++t) { f[0][t] = ldsr16(pb + t * 2048); f[1][t] = ldsr16(pb + t * 2048 + 1024); }
  SBAR();
  auto window = [&](bool last) {
#pragma unroll
    for (int k = 0; k < WIN; ++k) {
      const bool ld = !(last && k + 2 >= WIN);
      if ((k & 1) == 0) {
        if (RES) pb += 10240;
        else { pb += sb == NB - 1 ? -(NB - 1) * 10240 : 10240; sb = sb == NB - 1 ? 0 : sb + 1; }
        if (!ld) BARRIER(); else if (!RES) BARRIER_NW();
      }
#pragma unroll
      for (int t = 0; t < 5; ++t) {
        if (ld) f[(k + 2) % 3][t] = ldsr16(pb + t * 2048 + (k & 1) * 1024);
        acc[t] = mfma16(f[k % 3][t], __builtin_bit_cast(bf16x8, a[k]), acc[t]);
        SBAR();
      }
      if (!last) { a[k] = ld16(c.r, av + (WIN + k) * 1024); SBAR(); }
      else if (k + AH < WIN) { a[k + AH] = ld16(c.r, av + (k + AH) * 1024); SBAR(); }
    }
    av += WIN * 1024;
  };
  static_assert(2 * NS % WIN == 0 && (2 * NS == WIN || WIN % 6 == 0), "window");
#pragma unroll 1
  for (int b = 1; b < 2 * NS / WIN; ++b) window(false);
  window(true);
  STAMP(3); STAMPC(1);
  u32x2 o[5];
#pragma unroll
  for (int t = 0; t < 5; ++t) {
    o[t][0] = pk2(acc[t][0], acc[t][1]); o[t][1] = pk2(acc[t][2], acc[t][3]);
  }
  const u32x4 p0 = pair16(o[0][0], o[0][1], o[1][0], o[1][1]), p1 = pair16(o[2][0], o[2][1], o[3][0], o[3][1]),
              p2 = pair16(o[4][0], o[4][1], o[4][0], o[4][1]);
  const int ch = (g & 1) * 2 + (g >> 1);
  if (out) {
    char* po = (char*)out + (mt * 16 + i) * 1280 + cb * 160;
    *(u32x4*)(po + ch * 16) = p0; *(u32x4*)(po + 64 + ch * 16) = p1;
    if (!(g & 1)) *(u32x4*)(po + 128 + (g >> 1) * 16) = p2;
    STAMP(4);
    return;
  }
  const unsigned xo = out_off + (mt * 80 + cb * 10) * 256 + i * 16;
  st16(c.r, xo + ch * 256, p0); st16(c.r, xo + 1024 + ch * 256, p1);
  if (!(g & 1)) st16(c.r, xo + 2048 + (g >> 1) * 256, p2);
  const f32x4 s4 = (acc[0] + acc[1]) + (acc[2] + acc[3]) + acc[4];
  const float sum = rows4((s4[0] + s4[1]) + (s4[2] + s4[3])), mean = sum * (1.f / 80.f);
  f32x4 q4 = {0.f, 0.f, 0.f, 0.f};
#pragma unroll
  for (int t = 0; t < 5; ++t) { const f32x4 d = acc[t] - mean; q4 += d * d; }
  const float m2 = rows4((q4[0] + q4[1]) + (q4[2] + q4[3]));
  if (g == 0) st8(c.r, st_off + (mt >> 3) * 8192 + cb * 1024 + ((mt & 7) * 16 + i) * 8, u32x2{__float_as_uint(sum), __float_as_uint(m2)});
  STAMP(4);
  publish(c, M_CNT4, 4, flg(arr, panel * 8 + cb), true);
  STAMP(5);
}
template <int NS, int NB> DI void proj_b_run(const Ctx& c, int half, unsigned w_off) {
  Ring<5, NB, NS, 2> rg;
#pragma unroll
  for (int p = 0; p < 5; ++p) { const int q = half * 5 + p; rg.voff[p] = w_off + (pr_cb(c) * 5 + (q >> 1)) * (NS * 2048) + (q & 1) * 1024 + c.lane * 16; }
  rg.init(c.ws, 2048, 0, half);
  rg.template fill_paced<(NB < DN_D0 ? NB : DN_D0)>();
  if constexpr (NS <= NB) { wait_vm<0>(); BARRIER_L(); BARRIER_L(); } else rg.template run<DN_D0>();
}

template <int NF>
DI void gu_consumer(const Ctx& c, int layer, int f0, int mq) {
  const int panel = gu_panel(c), ft0 = (gu_cb(c) * 27) >> 2;
  const int lane = c.lane, r32 = lane & 31, h = lane >> 5;
  f32x16 acc[NF];
#pragma unroll
  for (int t = 0; t < NF; ++t) acc[t] = f32x16{};
  const int lo = (r32 >> 4) * 1024 + h * 256 + (r32 & 15) * 16;
  lds_ptr pa = c.lds + GU_A + mq * 2048 + lo, pb = c.lds + f0 * 2048 + lo;
  bf16x8 x0[2], x1[2], wa[NF], wb0[NF], wb1[NF];
  auto step = [&](const bf16x8* xa, const bf16x8* wb, bf16x8* xn, bf16x8* wn_, bool ld) {
#pragma unroll
    for (int q = 0; q < 2 * NF; ++q) {
      if (ld) {
        if (q == 0) { xn[0] = ldsr16(pa); xn[1] = ldsr16(pa + 512); }
        if (q >= 1 && q <= NF) wn_[q - 1] = ldsr16(pb + (q - 1) * 2048 + 512);
        if (q >= NF) wa[q - NF] = ldsr16(pb + (q - NF) * 2048);
      }
      if (q < NF) acc[q] = mfma32(wa[q], xa[0], acc[q]); else acc[q - NF] = mfma32(wb[q - NF], xa[1], acc[q - NF]);
      SBAR();
    }
  };
  static_assert(GU_NA == GU_NB, "one slot counter");
  int sl = 0;
  auto adv = [&]() {
    pa += sl == GU_NA - 1 ? -(GU_NA - 1) * 8192 : 8192; pb += sl == GU_NB - 1 ? -(GU_NB - 1) * 14336 : 14336;
    sl = sl == GU_NA - 1 ? 0 : sl + 1;
  };
  SLOT(layer * 5 + 3);
  STAMP(0);
  STAMP(1);
  BARRIER();
  STAMP(2); STAMPC(0); WRESET();
  x0[0] = ldsr16(pa); x0[1] = ldsr16(pa + 512);
#pragma unroll
  for (int t = 0; t < NF; ++t) { wa[t] = ldsr16(pb + t * 2048); wb0[t] = ldsr16(pb + t * 2048 + 512); }
#pragma unroll 1
  for (int st = 0; st < 9; ++st) {
    adv(); BARRIER_NW(); step(x0, wb0, x1, wb1, true);
    adv(); BARRIER_NW(); step(x1, wb1, x0, wb0, true);
  }
  adv(); BARRIER_NW(); step(x0, wb0, x1, wb1, true);
  BARRIER();
  const LAS float* srs = (const LAS float*)(c.lds + M_SST) + mq * 32 + r32;
  const LAS float* scs = (const LAS float*)(c.lds + M_SCS) + f0 * 32 + h * 4;
  float rs = srs[0], nm = srs[128];
  f32x4 cs[2][4];
#pragma unroll
  for (int e = 0; e < 8; ++e) cs[e >> 2][e & 3] = *(const LAS f32x4*)(scs + e * 8);
  SBAR();
  step(x1, wb1, x0, wb0, false);
  STAMP(3); STAMPC(1);
  rs *= rs;
  const unsigned ho = WS_L + layer * L_SZ + L_H + ((panel * 8 + mq * 2 + (r32 >> 4)) * 216 + (ft0 + f0) * 2 + h) * 256 + (r32 & 15) * 16;
#pragma unroll
  for (int f = 0; f < NF; ++f) {
    unsigned p[2][2];
#pragma unroll
    for (int e = 0; e < 2; ++e) {
      const f32x16 a = acc[f];
      f32x4 gt = f32x4{a[e * 4], a[e * 4 + 1], a[e * 4 + 2], a[e * 4 + 3]} + cs[f & 1][e] * nm;
      const f32x4 up = (f32x4{a[8 + e * 4], a[9 + e * 4], a[10 + e * 4], a[11 + e * 4]} + cs[f & 1][2 + e] * nm) * rs;
      gt = __builtin_elementwise_max(gt, f32x4{0.f, 0.f, 0.f, 0.f}) * up;
      p[e][0] = pk2(gt[0], gt[1]); p[e][1] = pk2(gt[2], gt[3]);
    }
    if (f + 2 < NF) {
#pragma unroll
      for (int e = 0; e < 4; ++e) cs[f & 1][e] = *(const LAS f32x4*)(scs + (f + 2) * 32 + e * 8);
    }
    st16(c.r, ho + f * 512, pair32(p[0][0], p[0][1], p[1][0], p[1][1]));
    SBAR();
  }
  STAMP(4);
  publish(c, M_CNT, 8, flg(FL_GU + layer * 5, panel * 16 + gu_cb(c)), true);
  STAMP(5);
}
typedef Ring<7, GU_NB, 20, 2> GuB;
DI void gu_b_init(const Ctx& c, GuB& rg, int half) {
  const int ft0 = (gu_cb(c) * 27) >> 2;
#pragma unroll
  for (int p = 0; p < 7; ++p) rg.voff[p] = WS_WGU + (ft0 * 2 + half * 7 + p) * 20480 + c.lane * 16;
  rg.init(c.ws, 1024, 0, half);
}
DI void gu_b_run(const Ctx& c, int half, int layer) {
  GuB rg;
  gu_b_init(c, rg, half);
  rg.fill_paced();
  if (half == 0) ln_cols<4>(c, 56, WS_CSG + ((gu_cb(c) * 27) >> 2) * 128, 13824);
  wait_vm<(GU_NB - 1) * 7>(); BARRIER_L(); WRESET();
  f32x2 p[8];
#pragma unroll
  for (int i = 0; i < 8; ++i) p[i] = ld8_async(WS_ST + (layer * 2 + 1) * STB + gu_panel(c) * 8192 + i * 1024 + (half * 64 + c.lane) * 8, c.ws);
  wait_vm<(GU_NB - 2) * 7 + 8>(); BARRIER_L();
  asm volatile("s_waitcnt vmcnt(0)" : "+v"(p[0]), "+v"(p[1]), "+v"(p[2]), "+v"(p[3]), "+v"(p[4]), "+v"(p[5]), "+v"(p[6]), "+v"(p[7])::"memory");
  ln_rows(c, half * 64 + c.lane, p);
  rg.steady();
  rg.template range<GU_NB - 3, 0>(); BARRIER_L();
}
DI void gu_a_run(const Ctx& c, int half, int layer, unsigned iss) {
  const unsigned a_off = WS_L + layer * L_SZ + L_X1;
  Ring<4, GU_NA, 20, 2> rg;
#pragma unroll
  for (int p = 0; p < 4; ++p) rg.voff[p] = a_off + (gu_panel(c) * 8 + half * 4 + p) * 20480 + c.lane * 16;
  rg.init(c.ws, 1024, GU_A, half);
  Poll<1> pl;
  gate(c, pl, half, flg(FL_WO + layer * 5, gu_panel(c) * 16 + (c.lane & 15)), layer * 5 + 4, iss);
  rg.template fill<A_D0 + 1>();
  rg.template head<A_D0 + 1>();
  rg.template range<GU_NA - 3, 3>();
  rg.template range<2, 0>();
  BARRIER_L();
  pl.keep();
}

__global__ __launch_bounds__(768) void fused_kernel(const uint16_t* __restrict__ x, const uint16_t* __restrict__ wqkv, const uint16_t* __restrict__ wo,
                                                    const uint16_t* __restrict__ wg, const uint16_t* __restrict__ wu,
                                                    const uint16_t* __restrict__ wd, uint16_t* __restrict__ out, char* __restrict__ ws) {
  extern __shared__ __attribute__((aligned(16))) char smem[];
  Ctx c;
  c.ws = ws; c.lds = (lds_ptr)smem; c.lane = threadIdx.x & 63; c.xcd = blockIdx.x & 7; c.j = blockIdx.x >> 3;
  c.r = __builtin_amdgcn_make_buffer_rsrc(ws, 0, WS_END, 0x00020000);
  const int w = __builtin_amdgcn_readfirstlane(threadIdx.x >> 6);
  SPANST(0);
  c.tgt = *(const u64*)(ws + WS_EPOCH) + 1;
  if (threadIdx.x < 16) *(LAS unsigned*)(c.lds + M_CNT + threadIdx.x * 4) = 0u;
  const bool qkv_on = qkv_cb(c) != 15;
  const Ctx c0 = c;
  WDUMP(15);
  if (w >= 8) __builtin_amdgcn_s_setprio(3);
  if (w >= 8) {
    const int half = w & 1;
    const bool wl = w >= 10;
    if (!qkv_on) BARRIER();
#pragma unroll 1
    for (int layer = 0; layer < 2; ++layer) {
      const unsigned iss = layer * 24 + (qkv_on ? (layer + 1) * 4 : 0);
      if (qkv_on) {
        if (wl) qkv_b_run(fresh(c0), opq(half), opq(layer), wqkv); else qkv_a_run(fresh(c0), opq(half), opq(layer), x);
      }
      WDUMP(layer * 5);
      att_load(fresh(c0), opq(w - 8), opq(layer), iss, wo, wg, wu, wd);
      WDUMP(layer * 5 + 1);
      const Ctx c = fresh(c0);
      const int l = c.lane;
      Poll<1> pl;
      if (wl) {
        sig_wait(c0, iss + 8, M_ISS);
        proj_b_run<10, WO_NB>(fresh(c0), opq(half), WS_WO);
        WDUMP(layer * 5 + 2);
        sig_wait(c0, iss + 12, M_ISS);
        gu_b_run(fresh(c0), opq(half), opq(layer));
        WDUMP(layer * 5 + 3);
        sig_wait(c0, iss + 20, M_ISS);
        proj_b_run<27, DN_NB>(fresh(c0), opq(half), WS_WD);
      } else {
        if (half == 0) {
          if (POLL_GATE) sig_wait(c, iss + 8, M_ISS);
          pl.run(c, flg(FL_ATT + layer * 5, (pr_panel(c) >> 2) * 32 + (l & 31)));
          STAMPL(layer * 5 + 2, 6);
        }
        BARRIER_L(); wait_vm<0>(); pl.keep(); BARRIER_L();
        WDUMP(layer * 5 + 2);
        gu_a_run(fresh(c0), opq(half), opq(layer), iss + 12);
        WDUMP(layer * 5 + 3);
        if (half == 0) {
          if (POLL_GATE) sig_wait(c, iss + 20, M_ISS);
          pl.run(c, flg(FL_GU + layer * 5, (pr_panel(c) >> 1) * 16 + (l & 15)));
          STAMPL(layer * 5 + 4, 6);
        }
        BARRIER_L(); wait_vm<0>(); pl.keep();
        if (layer == 1 && half == 0 && blockIdx.x == 0 && l == 0) *(u64*)(ws + WS_EPOCH) = c.tgt;
#pragma unroll 1
        for (int k = 1; k <= 27; ++k) BARRIER_L();
      }
      WDUMP(layer * 5 + 4);
    }
  } else {
    if (!qkv_on) BARRIER();
#define TLB_PF 524288
    if (w == 2 || w == 3) {
      const unsigned o = (w - 2) * 64 + c.lane;
      const unsigned t = o * TLB_PF < WS_END ? __builtin_amdgcn_raw_buffer_load_b32(c.r, o * TLB_PF + 2048, 0, 16) : out[(o & 1) * 1048576];
      asm volatile("" ::"v"(t));
    }
    if (w < 2) {
      const char* pc; constexpr unsigned len = 48 * 1024;
      asm volatile("s_getpc_b64 %0" : "=s"(pc));
      const unsigned o = (c.j * 2 + w) * 1024 + c.lane * 16;
      if (o + 16 <= len) { const u32x4 t = *(const GAS u32x4*)(pc + o); asm volatile("" ::"v"(t)); }
    }
#pragma unroll 1
    for (int layer = 0; layer < 2; ++layer) {
      const unsigned L = WS_L + layer * L_SZ;
      qkv_compute(fresh(c0), opq(w), opq(layer), x, wqkv);
      WDUMP(layer * 5);
      att_compute(fresh(c0), opq(w), opq(layer));
      WDUMP(layer * 5 + 1);
      proj_compute<10, WO_NB, 20>(fresh(c0), opq(w), layer * 5 + 2, L + L_O, layer == 0 ? (const char*)x : nullptr, WS_X2, L + L_X1,
                                  WS_ST + (layer * 2 + 1) * STB, nullptr, FL_WO + layer * 5);
      WDUMP(layer * 5 + 2);
      if (w < 4) gu_consumer<4>(fresh(c0), opq(layer), 0, opq(w)); else gu_consumer<3>(fresh(c0), opq(layer), 4, opq(w - 4));
      WDUMP(layer * 5 + 3);
      proj_compute<27, DN_NB, 18>(fresh(c0), opq(w), layer * 5 + 4, L + L_H, nullptr, L + L_X1, WS_X2, WS_ST + 2 * STB, layer ? out : nullptr,
                                  FL_DN + layer * 5);
      WDUMP(layer * 5 + 4);
    }
  }
}

extern "C" void kernel_launch(void* const* d_in, const int* in_sizes, int n_in, void* d_out, int out_size, void* d_ws, size_t ws_size,
                              hipStream_t stream) {
  (void)hipFuncSetAttribute((const void*)fused_kernel, hipFuncAttributeMaxDynamicSharedMemorySize, LDS_BYTES);
  fused_kernel<<<256, 768, LDS_BYTES, stream>>>((const uint16_t*)d_in[0], (const uint16_t*)d_in[1], (const uint16_t*)d_in[2],
                                                (const uint16_t*)d_in[4], (const uint16_t*)d_in[5], (const uint16_t*)d_in[6],
                                                (uint16_t*)d_out, (char*)d_ws);
}
```

```cpp
#include <hip/hip_runtime.h>
#include <hip/hip_bf16.h>
#include <stdint.h>

typedef __attribute__((ext_vector_type(8))) __bf16 bf16x8;
typedef __attribute__((ext_vector_type(16))) __bf16 bf16x16;
typedef __attribute__((ext_vector_type(4))) short s16x4;
typedef __attribute__((ext_vector_type(8))) short s16x8;
typedef __attribute__((ext_vector_type(2))) __bf16 bf16x2;
typedef __attribute__((ext_vector_type(2))) float f32x2;
typedef __attribute__((ext_vector_type(4))) float f32x4;
typedef __attribute__((ext_vector_type(8))) float f32x8;
typedef __attribute__((ext_vector_type(16))) float f32x16;
typedef __attribute__((ext_vector_type(2))) unsigned u32x2;
typedef __attribute__((ext_vector_type(4))) unsigned u32x4;
typedef unsigned long long u64;
#define LAS __attribute__((address_space(3)))
#define GAS __attribute__((address_space(1)))
typedef LAS char* lds_ptr;
typedef __amdgpu_buffer_rsrc_t rsrc_t;
#define DI __device__ __forceinline__

constexpr unsigned XB = 2048u * 640 * 2;
constexpr unsigned WS_EPOCH = 0;
constexpr unsigned WS_FLAG = 4096;
constexpr int FL_P0 = 0, FL_RT = 1, FL_QKV = 2, FL_ATT = 3, FL_WO = 4, FL_GU = 5, FL_DN = 6, FL_RT2 = 12;
constexpr unsigned WS_WO  = WS_FLAG + 13u * 32768;
constexpr unsigned WS_WGU = WS_WO + 640u * 640 * 2;
constexpr unsigned WS_WD  = WS_WGU + 3456u * 640 * 2;
constexpr unsigned WS_CSQ = WS_WD + 640u * 1728 * 2;
constexpr unsigned WS_CSG = WS_CSQ + 8u * 1920 * 4;
constexpr unsigned WS_ST  = WS_CSG + 4u * 3456 * 4;
constexpr unsigned STB = 2048u * 64;
constexpr unsigned WS_X2  = WS_ST + 4 * STB;
constexpr unsigned WS_L   = WS_X2 + XB;
constexpr unsigned L_Q1 = 0, L_Q2 = L_Q1 + 32u * 65536, L_K1 = L_Q2 + 16u * 32768, L_K2 = L_K1 + 32u * 65536, L_V = L_K2 + 16u * 32768,
                   L_O = L_V + XB, L_X1 = L_O + XB, L_H = L_X1 + XB, L_SZ = L_H + 2048u * 1728 * 2;
constexpr unsigned WS_END = WS_L + 2 * L_SZ;

constexpr int LDS_BYTES = 163840, MISC = 159744, M_SST = MISC, M_SCS = MISC + 1024, M_CNT = MISC + 2048, M_CNT2 = M_CNT + 8, M_CNT3 = M_CNT + 16, M_CNT4 = M_CNT + 24, M_SIG = M_CNT + 32, M_ISS = M_CNT + 40;
constexpr int QKV_NB = 5, QKV_NA = 4, QKV_A = QKV_NB * 16384;
constexpr int WO_NB = 10, DN_NB = 15;
constexpr int GU_NB = 7, GU_NA = 7, GU_A = GU_NB * 14336;
constexpr int AT_SLOT = 45056, AT_CONST = 3 * AT_SLOT, AT_SCR = AT_CONST + 10752, AT_MERGE = 3 * AT_SLOT;
static_assert(QKV_A + QKV_NA * 16384 <= MISC && DN_NB * 10240 <= MISC && GU_A + GU_NA * 8192 <= MISC, "lds");
static_assert(WO_NB * 10240 <= AT_MERGE && AT_MERGE + 24576 <= MISC && AT_SCR + 3 * 4096 <= MISC, "lds");

DI unsigned pk2(float a, float b) { f32x2 v = {a, b}; bf16x2 r = __builtin_convertvector(v, bf16x2); return __builtin_bit_cast(unsigned, r); }
DI float bflo(unsigned u) { return __uint_as_float(u << 16); }
DI float bfhi(unsigned u) { return __uint_as_float(u & 0xffff0000u); }
DI f32x4 mfma16(bf16x8 a, bf16x8 b, f32x4 c) { return __builtin_amdgcn_mfma_f32_16x16x32_bf16(a, b, c, 0, 0, 0); }
DI f32x16 mfma32(bf16x8 a, bf16x8 b, f32x16 c) { return __builtin_amdgcn_mfma_f32_32x32x16_bf16(a, b, c, 0, 0, 0); }
DI bf16x8 ldsr16(lds_ptr p) { return *(const LAS bf16x8*)p; }
DI bf16x8 ldstr(lds_ptr p, int second) {
  const s16x4 a = __builtin_amdgcn_ds_read_tr16_b64_v4i16((LAS s16x4*)p), b = __builtin_amdgcn_ds_read_tr16_b64_v4i16((LAS s16x4*)(p + second));
  return __builtin_bit_cast(bf16x8, (s16x8)__builtin_shufflevector(a, b, 0, 1, 2, 3, 4, 5, 6, 7));
}
#define BARRIER() asm volatile("s_waitcnt lgkmcnt(0)\n\ts_barrier" ::: "memory")
template <int N> DI void wait_vm() { asm volatile("s_waitcnt vmcnt(%0)" ::"n"(N > 63 ? 63 : N) : "memory"); }
#define WDUMP(id)
#define WRESET()
#define BARRIER_L() BARRIER()
#define BARRIER_NW() asm volatile("s_barrier" ::: "memory")
#define SBAR() __builtin_amdgcn_sched_barrier(0)
#define PIN(x) asm volatile("" : "+v"(x))
#define GL_HEAD "s_mov_b32 m0, %0\n\ts_nop 3\n\t"
#define GL(v, off) "global_load_lds_dwordx4 %" #v ", %1 offset:" #off " sc1\n\t"
DI void glds1(unsigned m, const char* sb, unsigned v0) { asm volatile(GL_HEAD GL(2, 0)::"s"(m), "s"(sb), "v"(v0) : "memory"); }
DI void glds2(unsigned m, const char* sb, unsigned v0, unsigned v1) {
  asm volatile(GL_HEAD GL(2, 0) GL(3, 1024)::"s"(m), "s"(sb), "v"(v0), "v"(v1) : "memory");
}
DI void glds3(unsigned m, const char* sb, unsigned v0, unsigned v1, unsigned v2) {
  asm volatile(GL_HEAD GL(2, 0) GL(3, 1024) GL(4, 2048)::"s"(m), "s"(sb), "v"(v0), "v"(v1), "v"(v2) : "memory");
}
DI void glds4(unsigned m, const char* sb, unsigned v0, unsigned v1, unsigned v2, unsigned v3) {
  asm volatile(GL_HEAD GL(2, 0) GL(3, 1024) GL(4, 2048) GL(5, 3072)::"s"(m), "s"(sb), "v"(v0), "v"(v1), "v"(v2), "v"(v3) : "memory");
}
DI void st16(rsrc_t r, unsigned off, u32x4 v) { __builtin_amdgcn_raw_buffer_store_b128(v, r, off, 0, 16); }
DI void st8(rsrc_t r, unsigned off, u32x2 v) { __builtin_amdgcn_raw_buffer_store_b64(v, r, off, 0, 16); }
DI void st4(rsrc_t r, unsigned off, unsigned v) { __builtin_amdgcn_raw_buffer_store_b32(v, r, off, 0, 16); }
DI u32x4 ld16(rsrc_t r, unsigned off) { return __builtin_amdgcn_raw_buffer_load_b128(r, off, 0, 16); }
DI u32x2 ld8(rsrc_t r, unsigned off) { return __builtin_amdgcn_raw_buffer_load_b64(r, off, 0, 16); }
DI f32x2 ld8_async(unsigned voff, const char* sbase) {
  f32x2 v; asm volatile("s_nop 4\n\tglobal_load_dwordx2 %0, %1, %2 sc1" : "=v"(v) : "v"(voff), "s"(sbase) : "memory"); return v;
}
DI u32x4 pair32(unsigned a0, unsigned a1, unsigned b0, unsigned b1) {
  auto x = __builtin_amdgcn_permlane32_swap(a0, b0, false, false);
  auto y = __builtin_amdgcn_permlane32_swap(a1, b1, false, false);
  return u32x4{x[0], y[0], x[1], y[1]};
}
template <int N> DI float ror16(float v) { return __int_as_float(__builtin_amdgcn_update_dpp(0, __float_as_int(v), 0x120 + N, 0xf, 0xf, false)); }
DI float sum32(float v) {
  v += ror16<8>(v); v += ror16<4>(v); v += ror16<2>(v); v += ror16<1>(v);
  auto a = __builtin_amdgcn_permlane16_swap(__float_as_uint(v), __float_as_uint(v), false, false);
  return __uint_as_float(a[0]) + __uint_as_float(a[1]);
}
DI u32x4 pair16(unsigned a0, unsigned a1, unsigned b0, unsigned b1) {
  auto x = __builtin_amdgcn_permlane16_swap(a0, b0, false, false);
  auto y = __builtin_amdgcn_permlane16_swap(a1, b1, false, false);
  return u32x4{x[0], y[0], x[1], y[1]};
}

DI float rows4(float v) {
  auto a = __builtin_amdgcn_permlane16_swap(__float_as_uint(v), __float_as_uint(v), false, false);
  v = __uint_as_float(a[0]) + __uint_as_float(a[1]);
  auto b = __builtin_amdgcn_permlane32_swap(__float_as_uint(v), __float_as_uint(v), false, false);
  return __uint_as_float(b[0]) + __uint_as_float(b[1]);
}

#define STAMP(id)
#define STAMPC(id)
#define STAMPL(sl, id)
#define SLOT(n)

#define WARM_WO 1
#define WARM_GU 1
#define WARM_DN 0
#define POLL_DEPTH 1
#define POLL_GATE 1
#define POLL_SLEEP 1
#define DN_D0 10
#define A_D0 2
#define ATT_PRIO 3
#define ATT_ORD 0
#define PACE 9
#define SPANST(id)
struct Ctx {
  char* ws; rsrc_t r; lds_ptr lds; u64 tgt; int lane, xcd, j;
};
DI Ctx fresh(const Ctx& c0) {
  Ctx c = c0;
  asm volatile("v_mbcnt_lo_u32_b32 %0, -1, 0\n\tv_mbcnt_hi_u32_b32 %0, -1, %0" : "=v"(c.lane), "+s"(c.xcd), "+s"(c.j), "+s"(c.ws));
  return c;
}
DI int opq(int v) { asm volatile("" : "+s"(v)); return v; }
constexpr int SPIN_CAP = 1 << 18;

DI unsigned flg(int arr, int unit) { return WS_FLAG + (arr * 256 + unit) * 128; }
DI void publish(const Ctx& c, int cnt_off, unsigned n, unsigned to, bool mark = false) {
  if (mark && c.lane == 0) __hip_atomic_fetch_add((LAS unsigned*)(c.lds + M_ISS), 1u, __ATOMIC_RELAXED, __HIP_MEMORY_SCOPE_WORKGROUP);
  wait_vm<0>();
  if (c.lane == 0) {
    const unsigned old = __hip_atomic_fetch_add((LAS unsigned*)(c.lds + cnt_off), 1u, __ATOMIC_RELAXED, __HIP_MEMORY_SCOPE_WORKGROUP);
    if (old % n == n - 1) __hip_atomic_store((GAS u64*)(c.ws + to), c.tgt, __ATOMIC_RELAXED, __HIP_MEMORY_SCOPE_AGENT);
  }
}
template <int N> struct Poll {
  u64 v[POLL_DEPTH][N];
  DI void run(const Ctx& c, unsigned o0, unsigned o1 = 0) {

    auto ld = [&](int r) {
      asm volatile("global_load_dwordx2 %0, %1, %2 sc1" : "=v"(v[r][0]) : "v"(o0), "s"(c.ws) : "memory");
      if constexpr (N == 2) asm volatile("global_load_dwordx2 %0, %1, %2 sc1" : "=v"(v[r][N - 1]) : "v"(o1), "s"(c.ws) : "memory");
      __builtin_amdgcn_s_sleep(POLL_SLEEP);
    };
    auto ok = [&](int r) {
      if constexpr (N == 2) asm volatile("s_waitcnt vmcnt(%2)" : "+v"(v[r][0]), "+v"(v[r][N - 1]) : "n"((POLL_DEPTH - 1) * N) : "memory");
      else asm volatile("s_waitcnt vmcnt(%1)" : "+v"(v[r][0]) : "n"(POLL_DEPTH - 1) : "memory");
      return __all(v[r][0] == c.tgt && v[r][N - 1] == c.tgt);
    };
    asm volatile("s_nop 4" ::: "memory");
    ld(0); if (POLL_DEPTH > 1) ld(1);
#pragma unroll 1
    for (int spin = 0; spin < SPIN_CAP; ++spin) {
      if (ok(0)) break; ld(0);
      if (POLL_DEPTH > 1) { if (ok(1)) break; ld(1); }
    }
  }
  DI void keep() {
#pragma unroll
    for (int r = 0; r < POLL_DEPTH; ++r) { asm volatile("" : "+v"(v[r][0])); if constexpr (N == 2) asm volatile("" : "+v"(v[r][N - 1])); }
  }
};
DI void poll(const Ctx& c, unsigned slot) { Poll<1> p; p.run(c, slot); wait_vm<0>(); p.keep(); }
DI void sig_set(const Ctx& c, unsigned v) { if (c.lane == 0) __hip_atomic_store((LAS unsigned*)(c.lds + M_SIG), v, __ATOMIC_RELAXED, __HIP_MEMORY_SCOPE_WORKGROUP); }
DI void sig_wait(const Ctx& c, unsigned v, int off = M_SIG) {
#pragma unroll 1
  for (int spin = 0; spin < SPIN_CAP * 16; ++spin) {
    if (__builtin_amdgcn_readfirstlane(__hip_atomic_load((LAS unsigned*)(c.lds + off), __ATOMIC_RELAXED, __HIP_MEMORY_SCOPE_WORKGROUP)) >= v) break;
    if (off != M_SIG) __builtin_amdgcn_s_sleep(1);
  }
  asm volatile("" ::: "memory");
}

DI void gate(const Ctx& c, Poll<1>& p, int half, unsigned slot, unsigned sig, unsigned iss) {
  if (half == 0) { if (POLL_GATE) sig_wait(c, iss, M_ISS); p.run(c, slot); sig_set(c, sig); STAMPL(sig - 1, 6); } else sig_wait(c, sig);
}

template <int PP, int NSLOT, int NS, int LAG = 1>
struct Ring {
  unsigned voff[PP];
  const char* base;
  unsigned adv, l0;
  int slot;
  DI void init(const char* b, unsigned a, unsigned lds0, int half) {
    base = b - 4096; adv = a; l0 = lds0 + half * PP * 1024; slot = 0;
#pragma unroll
    for (int p = 0; p < PP; ++p) voff[p] += 4096 - (p & 3) * 1024;
  }
  DI void issue() {
    const unsigned m = l0 + slot * (2 * PP * 1024);
    glds4(m, base, voff[0], voff[1], voff[2], voff[3]);
    if constexpr (PP == 5) glds1(m + 4096, base, voff[4]);
    if constexpr (PP == 7) glds3(m + 4096, base, voff[4], voff[5], voff[6]);
    if constexpr (PP == 8) glds4(m + 4096, base, voff[4], voff[5], voff[6], voff[7]);
    base += adv; slot = slot == NSLOT - 1 ? 0 : slot + 1;
  }
  template <int N = NSLOT> DI void fill() {
#pragma unroll 1
    for (int s = 0; s < N; ++s) issue();
  }
  template <int N = NSLOT> DI void fill_paced() {
#pragma unroll 1
    for (int s = 0; s < N; ++s) { issue(); wait_vm<PACE>(); }
  }
  template <int R, int E> DI void range() { if constexpr (R >= E) { wait_vm<R * PP>(); BARRIER_L(); range<R - 1, E>(); } }
  template <int D0 = NSLOT> DI void head() {
    wait_vm<(D0 - 1) * PP>(); BARRIER_L(); WRESET();
    if constexpr (D0 < NSLOT) fill<NSLOT - D0>();
    range<NSLOT - 2, NSLOT - LAG>();
    steady();
  }
  DI void steady() {
#pragma unroll 1
    for (int k = LAG; k < NS - NSLOT + LAG; ++k) { wait_vm<(NSLOT - 1 - LAG) * PP>(); BARRIER_L(); issue(); }
  }
  template <int D0 = NSLOT> DI void run() { head<D0>(); range<NSLOT - 1 - LAG, 0>(); BARRIER_L(); }
};

DI void ln_rows(const Ctx& c, int row, const f32x2 (&p)[8]) {
  f32x2 t = p[0];
#pragma unroll
  for (int i = 1; i < 8; ++i) t += p[i];
  const float m8 = t[0] * 0.125f; float q = 0.f;
#pragma unroll
  for (int i = 0; i < 8; ++i) { const float a = p[i][0] - m8; q += a * a; }
  const float rstd = __builtin_amdgcn_rsqf((t[1] + q * (1.f / 80.f)) * (1.f / 640.f) + 1e-5f);
  *(LAS float*)(c.lds + M_SST + row * 4) = rstd;
  *(LAS float*)(c.lds + M_SST + 512 + row * 4) = -t[0] * (1.f / 640.f);
}
template <int NP> DI void ln_cols(const Ctx& c, int n4, unsigned off, unsigned pitch) {
  u32x4 q[NP];
#pragma unroll
  for (int i = 0; i < NP; ++i) q[i] = ld16(c.r, off + i * pitch + (c.lane < n4 ? c.lane : 0) * 16);
  f32x4 cs = __builtin_bit_cast(f32x4, q[0]);
#pragma unroll
  for (int i = 1; i < NP; ++i) cs += __builtin_bit_cast(f32x4, q[i]);
  if (c.lane < n4) *(LAS f32x4*)(c.lds + M_SCS + c.lane * 16) = cs;
}

#define MAP 0
DI int qkv_panel(const Ctx& c) { return (c.xcd & 3) * 4 + (c.j & 3); }
DI int qkv_cb(const Ctx& c) { return (c.xcd >> 2) * 8 + (c.j >> 2); }
DI int pr_panel(const Ctx& c) { return (c.xcd & 3) * 8 + (c.j & 7); }
DI int pr_cb(const Ctx& c) { return (c.xcd >> 2) * 4 + (c.j >> 3); }
DI int gu_panel(const Ctx& c) { return (c.xcd & 1) * 8 + (c.j & 7); }
DI int gu_cb(const Ctx& c) { return (c.xcd >> 1) * 4 + (c.j >> 3); }
DI int att_head(const Ctx& c) { return c.xcd * 4 + (c.j & 3); }
DI int att_qb(const Ctx& c) { return c.j >> 2; }

struct Phase0 {
  u32x4 v[3], t[10];
  DI void load(const Ctx& c, int hw, const uint16_t* x, const uint16_t* wqkv) {
    const int lane = c.lane, r32 = lane & 31, b = blockIdx.x, row = b * 8 + hw * 2 + (lane >> 5);
    v[0] = *(const u32x4*)(x + row * 640 + r32 * 8); v[1] = *(const u32x4*)(x + row * 640 + 256 + r32 * 8);
    v[2] = *(const u32x4*)(x + row * 640 + 512 + (r32 & 15) * 8);
    if (hw == 3 && b < 240) {
      const rsrc_t r = __builtin_amdgcn_make_buffer_rsrc((void*)wqkv, 0, 640 * 3840, 0x00020000);
#pragma unroll
      for (int i = 0; i < 10; ++i)
        t[i] = __builtin_amdgcn_raw_buffer_load_b128(r, (lane >> 3) * 3840 + (lane & 7) * 16, ((b / 30) * 80 + i * 8) * 3840 + (b % 30) * 128, 0);
    }
  }
  DI void rows(const Ctx& c, int hw) {
    const int lane = c.lane, r32 = lane & 31, row = blockIdx.x * 8 + hw * 2 + (lane >> 5);
    const float w2 = r32 < 16 ? 1.f : 0.f;
    float sum = 0.f, s2 = 0.f;
#pragma unroll
    for (int e = 0; e < 4; ++e) { sum += (bflo(v[0][e]) + bfhi(v[0][e])) + (bflo(v[1][e]) + bfhi(v[1][e])); s2 += bflo(v[2][e]) + bfhi(v[2][e]); }
    sum = sum32(sum + w2 * s2);
    const float mean = sum * (1.f / 640.f); float m2 = 0.f; s2 = 0.f;
#pragma unroll
    for (int e = 0; e < 4; ++e) {
      const float a0 = bflo(v[0][e]) - mean, a1 = bfhi(v[0][e]) - mean, a2 = bflo(v[1][e]) - mean, a3 = bfhi(v[1][e]) - mean;
      const float a4 = bflo(v[2][e]) - mean, a5 = bfhi(v[2][e]) - mean;
      m2 += a0 * a0 + a1 * a1 + a2 * a2 + a3 * a3; s2 += a4 * a4 + a5 * a5;
    }
    m2 = sum32(m2 + w2 * s2);
    if (r32 < 8) st8(c.r, WS_ST + (row >> 7) * 8192 + r32 * 1024 + (row & 127) * 8, u32x2{__float_as_uint(sum * 0.125f), __float_as_uint(m2 * 0.125f)});
  }
  float s[8];
  DI void cols1(int hw) {
    if (hw == 3 && blockIdx.x < 240) {
      unsigned ul = 0x00003f80u, uh = 0x3f800000u;
      asm volatile("" : "+v"(ul), "+v"(uh));
      const bf16x2 lo = __builtin_bit_cast(bf16x2, ul), hi = __builtin_bit_cast(bf16x2, uh);
#pragma unroll
      for (int e = 0; e < 4; ++e) {
        s[2 * e] = 0.f; s[2 * e + 1] = 0.f;
#pragma unroll
        for (int i = 0; i < 10; ++i) {
          const unsigned u = t[i][e];
          s[2 * e] = __builtin_amdgcn_fdot2_f32_bf16(__builtin_bit_cast(bf16x2, u), lo, s[2 * e], false);
          s[2 * e + 1] = __builtin_amdgcn_fdot2_f32_bf16(__builtin_bit_cast(bf16x2, u), hi, s[2 * e + 1], false);
        }
      }
#pragma unroll
      for (int e = 0; e < 8; ++e) PIN(s[e]);
    }
  }
  DI void cols2(const Ctx& c, int hw) {
    const int lane = c.lane, b = blockIdx.x;
    if (hw == 3 && b < 240) {
#pragma unroll
      for (int e = 0; e < 8; ++e) s[e] = rows4(s[e] + ror16<8>(s[e]));
      const int hi = (lane >> 3) & 1;
      const f32x4 o = hi ? f32x4{s[4], s[5], s[6], s[7]} : f32x4{s[0], s[1], s[2], s[3]};
      if (lane < 16) st16(c.r, WS_CSQ + ((b / 30) * 1920 + (b % 30) * 64 + (lane & 7) * 8 + hi * 4) * 4, __builtin_bit_cast(u32x4, o));
    }
  }
};

template <bool SWAP, class PRE>
DI void qkv_loop(const Ctx& c, int w, int layer, f32x16 (&acc)[2][2], PRE pre) {
  const int lane = c.lane, i = lane & 15, g = lane >> 4, wm = w & 1, wn = w >> 1;
  lds_ptr pa[4];
#pragma unroll
  for (int s = 0; s < 4; ++s)
    pa[s] = c.lds + QKV_A + wm * 8192 + (g & 1) * 2048 +
            (layer == 0 ? (i >> 3) * 1024 + (i & 7) * 128 + (((2 * s + (g >> 1)) ^ ((i >> 1) & 7)) << 4) : (2 * s + (g >> 1)) * 256 + i * 16);
  const int rb = ((g >> 1) * 8 + (i >> 2)) * 256 + (i & 3) * 8, u = (wn * 4 + (g & 1)) ^ ((i >> 2) << 1);
  lds_ptr pb0 = c.lds + rb + (u << 5), pb1 = c.lds + rb + ((u ^ 2) << 5);
  bf16x8 x0[4], x1[4], w0[4], w1[4];
  auto rdx = [&](bf16x8* xn, int hs, int e) { xn[e] = ldsr16(pa[hs * 2 + (e >> 1)] + (e & 1) * 4096); };
  auto rdw = [&](bf16x8* wn_, int hs, int e) { wn_[e] = ldstr(((e & 1) ? pb1 : pb0) + (hs * 2 + (e >> 1)) * 4096, 1024); };
  auto step = [&](const bf16x8* xa, const bf16x8* wb, bf16x8* xn, bf16x8* wn_, int hs, bool ld) {
#pragma unroll
    for (int q = 0; q < 8; ++q) {
      if (ld) { if (q & 1) rdx(xn, hs, q >> 1); else rdw(wn_, hs, q >> 1); }
      const int s = q >> 2, tm = q & 1, tn = (q >> 1) & 1;
      acc[tm][tn] = SWAP ? mfma32(wb[s * 2 + tn], xa[s * 2 + tm], acc[tm][tn]) : mfma32(xa[s * 2 + tm], wb[s * 2 + tn], acc[tm][tn]);
      SBAR();
    }
  };
  SLOT(layer * 5);
  if (layer) STAMP(0);
  STAMP(1);
  BARRIER();
  STAMP(2); STAMPC(0); WRESET();
#pragma unroll
  for (int e = 0; e < 4; ++e) { rdw(w0, 0, e); rdx(x0, 0, e); }
  int sa = 0, sb = 0;
#pragma unroll 1
  for (int st = 0; st < 9; ++st) {
    step(x0, w0, x1, w1, 1, true);
    const int da = sa == QKV_NA - 1 ? -(QKV_NA - 1) * 16384 : 16384, db = sb == QKV_NB - 1 ? -(QKV_NB - 1) * 16384 : 16384;
    sa = sa == QKV_NA - 1 ? 0 : sa + 1; sb = sb == QKV_NB - 1 ? 0 : sb + 1;
    pa[0] += da; pa[1] += da; pa[2] += da; pa[3] += da; pb0 += db; pb1 += db;
    BARRIER_NW();
    step(x1, w1, x0, w0, 0, true);
  }
  step(x0, w0, x1, w1, 1, true);
  BARRIER();
  pre(); SBAR();
  step(x1, w1, x0, w0, 0, false);
  STAMP(3); STAMPC(1);
}
DI void qkv_helper(const Ctx& c, int hw, int layer, const uint16_t* x, const uint16_t* wqkv) {
  const int panel = qkv_panel(c), cb = qkv_cb(c), l = c.lane;
  const bool on = cb != 15;
  if (layer == 0) {
    Phase0 p0;
    p0.load(c, hw, x, wqkv);
    if (on) BARRIER();
    p0.cols1(hw);
    if (on && hw == 3) BARRIER();
    p0.rows(c, hw);
    if (on && hw != 3) BARRIER();
    p0.cols2(c, hw);
    if (on) { BARRIER(); BARRIER(); }
    publish(c, M_CNT2, 4, flg(FL_P0, blockIdx.x));
    if (!on) return;
    BARRIER(); BARRIER();
    const unsigned at = flg(FL_P0, hw < 2 ? panel * 16 + hw * 8 + (l & 7) : (l & 7) * 30 + cb * 2 + ((l >> 3) & 1));
    const u64 v = __hip_atomic_load((const GAS u64*)(c.ws + at), __ATOMIC_RELAXED, __HIP_MEMORY_SCOPE_AGENT);
    BARRIER();
    if (!__all(v == c.tgt)) poll(c, at);
  } else {
    if (!on) return;
    BARRIER();
  }
  f32x2 p[8];
  if (hw < 2) {
#pragma unroll
    for (int i = 0; i < 8; ++i)
      p[i] = __builtin_bit_cast(f32x2, ld8(c.r, WS_ST + layer * 2 * STB + panel * 8192 + i * 1024 + (hw * 64 + l) * 8));
  }
  BARRIER();
  if (hw == 2) ln_cols<8>(c, 32, WS_CSQ + cb * 512, 7680);
  BARRIER();
  if (hw < 2) ln_rows(c, hw * 64 + l, p);
  if (layer) { BARRIER(); BARRIER(); BARRIER(); BARRIER(); BARRIER(); BARRIER(); }
  BARRIER(); BARRIER();
}
DI void qkv_compute(const Ctx& c, int w, int layer, const uint16_t* x, const uint16_t* wqkv) {
  const int panel = qkv_panel(c), cb = qkv_cb(c);
  if (w >= 4) { qkv_helper(c, w - 4, layer, x, wqkv); return; }
  if (cb == 15) return;
  const int lane = c.lane, r32 = lane & 31, h = lane >> 5, wm = w & 1, wn = w >> 1;
  const unsigned L = WS_L + layer * L_SZ;
  f32x16 acc[2][2] = {{f32x16{}, f32x16{}}, {f32x16{}, f32x16{}}};
  const LAS float* srs = (const LAS float*)(c.lds + M_SST) + wm * 64;
  const LAS float* scs = (const LAS float*)(c.lds + M_SCS) + wn * 64;
  if (cb < 10) {
    float rs[2], nm[2]; f32x4 cs[8];
    qkv_loop<true>(c, w, layer, acc, [&]() {
      rs[0] = srs[r32]; rs[1] = srs[32 + r32]; nm[0] = srs[128 + r32]; nm[1] = srs[160 + r32];
#pragma unroll
      for (int e = 0; e < 8; ++e) cs[e] = *(const LAS f32x4*)(scs + e * 8 + h * 4);
    });
    const float qs = cb < 5 ? 0.22360679774997896f * 1.4426950408889634f : 1.f;
    const unsigned i1 = L + (cb < 5 ? L_Q1 : L_K1), i2 = L + (cb < 5 ? L_Q2 : L_K2);
    rs[0] *= qs; rs[1] *= qs;
#pragma unroll
    for (int tp = 0; tp < 4; ++tp) {
      const int tn = tp >> 1, pr = tp & 1;
      const int o = (cb % 5) * 16 + wn * 8 + tp * 2 + h, hp = o / 5, pos = o - hp * 5;
      const unsigned ts = pos == 2 ? 512 : 1024;
      const unsigned a0 = (pos == 2 ? i2 + hp * 32768 : i1 + (2 * hp + (pos > 2)) * 65536 + (pos > 2 ? pos - 3 : pos) * 512) +
                          (panel * 4 + wm * 2) * ts + r32 * 16;
#pragma unroll
      for (int tm = 0; tm < 2; ++tm) {
        const f32x16 a = acc[tm][tn];
        const f32x4 u = (f32x4{a[pr * 8], a[pr * 8 + 1], a[pr * 8 + 2], a[pr * 8 + 3]} + cs[tp * 2] * nm[tm]) * rs[tm];
        const f32x4 v = (f32x4{a[pr * 8 + 4], a[pr * 8 + 5], a[pr * 8 + 6], a[pr * 8 + 7]} + cs[tp * 2 + 1] * nm[tm]) * rs[tm];
        st16(c.r, a0 + tm * ts, pair32(pk2(u[0], u[1]), pk2(u[2], u[3]), pk2(v[0], v[1]), pk2(v[2], v[3])));
      }
    }
  } else {
    float cs[2]; f32x4 rn[2][4];
    auto rd = [&](int tp) {
#pragma unroll
      for (int e = 0; e < 4; ++e) rn[tp & 1][e] = *(const LAS f32x4*)(srs + (e >> 1) * 128 + tp * 16 + (e & 1) * 8 + h * 4);
    };
    qkv_loop<false>(c, w, layer, acc, [&]() { cs[0] = scs[r32]; cs[1] = scs[32 + r32]; rd(0); rd(1); });
    unsigned a0[2];
#pragma unroll
    for (int tn = 0; tn < 2; ++tn) {
      const int cv = (cb - 10) * 128 + wn * 64 + tn * 32 + r32, head = cv / 20, dim = cv - head * 20;
      const int d = (head & 1) ? (dim < 4 ? dim + 16 : dim - 4) : dim;
      a0[tn] = L + L_V + head * 81920 + (panel * 8 + wm * 4) * 640 + h * 320 + d * 16;
    }
#pragma unroll
    for (int tp = 0; tp < 4; ++tp) {
      const int tm = tp >> 1, pr = tp & 1;
      u32x4 o[2];
#pragma unroll
      for (int tn = 0; tn < 2; ++tn) {
        const f32x16 a = acc[tm][tn];
        const f32x4 u = (f32x4{a[pr * 8], a[pr * 8 + 1], a[pr * 8 + 2], a[pr * 8 + 3]} + rn[pr][2] * cs[tn]) * rn[pr][0];
        const f32x4 v = (f32x4{a[pr * 8 + 4], a[pr * 8 + 5], a[pr * 8 + 6], a[pr * 8 + 7]} + rn[pr][3] * cs[tn]) * rn[pr][1];
        o[tn] = pair32(pk2(u[0], u[1]), pk2(u[2], u[3]), pk2(v[0], v[1]), pk2(v[2], v[3]));
      }
      if (tp < 2) rd(tp + 2);
      st16(c.r, a0[0] + tp * 640, o[0]); st16(c.r, a0[1] + tp * 640, o[1]);
      SBAR();
    }
  }
  SLOT(layer * 5);
  STAMP(4);
  publish(c, M_CNT4, 4, flg(FL_QKV + layer * 5, panel * 16 + cb), true);
  STAMP(5);
}
typedef Ring<8, QKV_NB, 10, 2> QkvB;
DI void qkv_b_run(const Ctx& c, int half, int layer, const uint16_t* wqkv) {
  QkvB rg;
  const int cb = qkv_cb(c);
#pragma unroll
  for (int p = 0; p < 8; ++p) {
    const int row = (half * 8 + p) * 4 + (c.lane >> 4);
    rg.voff[p] = row * 3840 + cb * 256 + (((c.lane & 15) ^ ((row & 3) << 2)) << 4);
  }
  rg.init((const char*)wqkv, 64 * 3840, 0, half);
  if (layer) { sig_wait(c, 28, M_ISS); rg.fill_paced(); wait_vm<(QKV_NB - 1) * 8>(); BARRIER_L(); }
  else { rg.template fill<2>(); wait_vm<8>(); BARRIER_L(); rg.template fill<QKV_NB - 2>(); }
  WRESET();
  rg.template range<QKV_NB - 2, QKV_NB - 2>(); rg.steady(); rg.template range<QKV_NB - 3, 0>(); BARRIER_L();
}
DI void qkv_a_run(const Ctx& c, int half, int layer, const uint16_t* x) {
  const int panel = qkv_panel(c);
  Ring<8, QKV_NA, 10, 2> rg;
#pragma unroll
  for (int p = 0; p < 8; ++p) {
    const int mt = panel * 8 + half * 4 + (p >> 1), r16 = (p & 1) * 8 + (c.lane >> 3);
    rg.voff[p] = layer == 0 ? (mt * 16 + r16) * 1280 + (((c.lane & 7) ^ ((r16 >> 1) & 7)) << 4) : WS_X2 + mt * 20480 + (p & 1) * 1024 + c.lane * 16;
  }
  rg.init(layer == 0 ? (const char*)x : c.ws, layer == 0 ? 128 : 2048, QKV_A, half);
  Poll<1> pl;
  if (layer) gate(c, pl, half, flg(FL_DN, panel * 16 + (c.lane & 15)), 6, 28);
  rg.template fill<A_D0>();
  rg.template run<A_D0>();
  pl.keep();
}

DI f32x16 qk8(s16x4 k2, s16x4 q2) {
 return __builtin_amdgcn_mfma_f32_32x32x8bf16_1k(k2, q2, f32x16{}, 0, 0, 0); }
DI f32x16 qk16(bf16x8 k1, bf16x8 q1, f32x16 s) { return __builtin_amdgcn_mfma_f32_32x32x16_bf16(k1, q1, s, 0, 0, 0); }
DI f32x16 pv_lo(bf16x8 v, f32x16 p, f32x16 o) {
  return __builtin_amdgcn_smfmac_f32_32x32x32_bf16(v, __builtin_bit_cast(bf16x16, (f32x8)__builtin_shufflevector(p, p, 0, 1, 2, 3, 4, 5, 6, 7)), o, 0xDDDD, 0, 0);
}
DI f32x16 pv_hi(bf16x8 v, f32x16 p, f32x16 o) {
  return __builtin_amdgcn_smfmac_f32_32x32x32_bf16(v, __builtin_bit_cast(bf16x16, (f32x8)__builtin_shufflevector(p, p, 8, 9, 10, 11, 12, 13, 14, 15)), o, 0xDDDD, 0, 0);
}
DI float swap32(float v, int h) {
  auto r = __builtin_amdgcn_permlane32_swap(__float_as_uint(v), __float_as_uint(v), false, false);
  return __uint_as_float(h ? r[0] : r[1]);
}
#define EXP4(X, i) do { X[i] = __builtin_amdgcn_exp2f(X[i]); X[i + 1] = __builtin_amdgcn_exp2f(X[i + 1]); X[i + 2] = __builtin_amdgcn_exp2f(X[i + 2]); \
                        X[i + 3] = __builtin_amdgcn_exp2f(X[i + 3]); PIN(X); SBAR(); } while (0)
DI void att_compute(const Ctx& c, int w, int layer) {
  const int lane = c.lane, head = att_head(c), qb = att_qb(c);
  const int qp = w & 3, kp = w >> 2, r32 = lane & 31, h = lane >> 5;
  lds_ptr lds = c.lds;
  const unsigned L = WS_L + layer * L_SZ;
  SLOT(layer * 5 + 1);
  STAMP(0);
  for (int s = w * 64 + lane; s < 672; s += 512) {
    const unsigned v = s % 20 == 0 ? 0x3f803f80u : 0u;
    *(LAS u32x4*)(lds + AT_CONST + s * 16) = u32x4{v, v, v, v};
  }
  lds_ptr pk1 = lds + kp * 8192 + lane * 16;
  lds_ptr pk2_ = h ? lds + AT_CONST + 16 : lds + 16384 + kp * 4096 + r32 * 16 + (head & 1) * 8;
  lds_ptr pv = (r32 >= 20 ? lds + AT_CONST : lds + 24576 + kp * 10240) + h * 320 + r32 * 16;
  const int vmask = r32 >= 20 ? 0 : -1, kmask = h ? 0 : -1;
  f32x16 o0 = f32x16{}, o1 = f32x16{};
  f32x16 s0, s1, s2, s3 = f32x16{};
  STAMP(1);
  BARRIER();
  STAMP(2); STAMPC(0); WRESET();
  const bf16x8 q1a = ldsr16(lds + 2 * AT_SLOT + qp * 2048 + lane * 16), q1b = ldsr16(lds + 2 * AT_SLOT + qp * 2048 + 1024 + lane * 16);
  lds_ptr pq2 = h ? lds + AT_CONST + 16 : lds + 2 * AT_SLOT + 8192 + qp * 1024 + r32 * 16 + (head & 1) * 8;
  const s16x4 q2a = *(const LAS s16x4*)pq2, q2b = *(const LAS s16x4*)(pq2 + 512);
  bf16x8 k1 = ldsr16(pk1), vlo = ldsr16(pv), vhi = bf16x8{};
  s16x4 k2 = *(const LAS s16x4*)pk2_;
  s0 = qk16(k1, q1a, qk8(k2, q2a));
#define PH_EVEN(SC, SN, SP, KLOAD, VOFF) do { \
    SN = qk8(k2, q2b); SBAR(); EXP4(SC, 0); SN = qk16(k1, q1b, SN); SBAR(); KLOAD; SBAR(); EXP4(SC, 4); \
    o1 = pv_hi(vhi, SP, o1); SBAR(); vhi = ldsr16(pv + (VOFF) + 640); SBAR(); EXP4(SC, 8); o0 = pv_lo(vlo, SC, o0); SBAR(); EXP4(SC, 12); } while (0)
#define PH_ODD(SC, SN, SP, VLOAD) do { \
    SN = qk8(k2, q2a); SBAR(); EXP4(SC, 0); SN = qk16(k1, q1a, SN); SBAR(); EXP4(SC, 4); \
    o0 = pv_hi(vhi, SP, o0); SBAR(); EXP4(SC, 8); o1 = pv_lo(vlo, SC, o1); SBAR(); VLOAD; SBAR(); EXP4(SC, 12); } while (0)
#define KLD(t) do { k1 = ldsr16(pk1 + (t) * 1024); k2 = *(const LAS s16x4*)(pk2_ + (t) * 512); } while (0)
#define PRI(v) do { asm volatile("s_cmp_eq_u32 %0, 0\n\ts_cbranch_scc1 1f\n\ts_setprio " #v "\n1:" :: "s"(kp) : "scc"); SBAR(); } while (0)
#define HI(n) if (ATT_PRIO == (n)) PRI(1);
#define LO(n) if (ATT_PRIO == (n)) PRI(0);
#define TWO(t) HI(3) HI(4) HI(5) PH_EVEN(s0, s1, s3, KLD((t) + 1), (t) * 1280); PH_ODD(s1, s2, s0, vlo = ldsr16(pv + ((t) + 1) * 1280)); LO(4)
  for (int grp = 0; grp < 4; ++grp) {
    TWO(0) PH_EVEN(s2, s3, s1, if (grp == 0) BARRIER_NW(); KLD(2), 1280); LO(3) PH_ODD(s3, s0, s2, vlo = ldsr16(pv + 2 * 1280));
    TWO(2) PH_EVEN(s2, s3, s1, KLD(4), 3 * 1280); LO(3) LO(5) PH_ODD(s3, s0, s2, vlo = ldsr16(pv + 4 * 1280));
    TWO(4) PH_EVEN(s2, s3, s1, KLD(6), 5 * 1280); LO(3) PH_ODD(s3, s0, s2, vlo = ldsr16(pv + 6 * 1280));
    TWO(6)
    const int d = grp == 2 ? -2 * AT_SLOT : grp == 3 ? 0 : AT_SLOT;
    PH_EVEN(s2, s3, s1, pk1 += d; pk2_ += (d & kmask); if (grp != 3) BARRIER(); KLD(0), 7 * 1280);
    LO(3) LO(5) PH_ODD(s3, s0, s2, pv += (d & vmask); vlo = ldsr16(pv));
  }
  o1 = pv_hi(vhi, s3, o1);
  STAMP(3); STAMPC(1);
  __builtin_amdgcn_s_setprio(0);
  BARRIER();
  const f32x16 give = kp ? o0 : o1, keep = kp ? o1 : o0;
#pragma unroll
  for (int e = 0; e < 3; ++e)
    *(LAS f32x4*)(lds + AT_MERGE + (w * 3 + e) * 1024 + lane * 16) = f32x4{give[4 * e], give[4 * e + 1], give[4 * e + 2], give[4 * e + 3]};
  BARRIER();
  f32x4 a[3];
#pragma unroll
  for (int e = 0; e < 3; ++e)
    a[e] = f32x4{keep[4 * e], keep[4 * e + 1], keep[4 * e + 2], keep[4 * e + 3]} + *(const LAS f32x4*)(lds + AT_MERGE + ((w ^ 4) * 3 + e) * 1024 + lane * 16);
  const int qt = qb * 8 + qp * 2 + kp;
  float l = h ? a[2][0] : 0.f;
  l += swap32(l, h);
  if (__builtin_expect(__any(!(l >= 5.421e-20f && l <= 1.8447e19f)), 0)) {
    const unsigned o2 = (head >> 1) * 32768 + r32 * 16 + (head & 1) * 8;
    const bf16x8 fq1 = __builtin_bit_cast(bf16x8, ld16(c.r, L + L_Q1 + head * 65536 + qt * 1024 + lane * 16));
    u32x2 fq2 = ld8(c.r, L + L_Q2 + o2 + qt * 512);
    if (h) fq2 = u32x2{0u, 0u};
    const unsigned vi = L + L_V + head * 81920 + h * 320 + (r32 < 20 ? r32 : 0) * 16;
    f32x16 oo = f32x16{};
    float m = -INFINITY;
    const u32x4 one8 = {0x3f803f80u, 0x3f803f80u, 0x3f803f80u, 0x3f803f80u};
    for (int t = 0; t < 64; ++t) {
      const u32x4 fk1 = ld16(c.r, L + L_K1 + head * 65536 + t * 1024 + lane * 16);
      const u32x2 fk2 = ld8(c.r, L + L_K2 + o2 + t * 512);
      u32x4 v0 = ld16(c.r, vi + t * 1280), v1 = ld16(c.r, vi + t * 1280 + 640);
      if (r32 >= 20) { v0 = one8; v1 = one8; }
      f32x16 s = qk16(__builtin_bit_cast(bf16x8, fk1), fq1, qk8(__builtin_bit_cast(s16x4, fk2), __builtin_bit_cast(s16x4, fq2)));
      float tm = s[0];
#pragma unroll
      for (int r = 1; r < 16; ++r) tm = fmaxf(tm, s[r]);
      tm = fmaxf(tm, swap32(tm, h));
      const float mn = fmaxf(m, tm), al = __builtin_amdgcn_exp2f(m - mn);
      m = mn;
#pragma unroll
      for (int r = 0; r < 16; ++r) { s[r] = __builtin_amdgcn_exp2f(s[r] - mn); oo[r] *= al; }
      oo = pv_hi(__builtin_bit_cast(bf16x8, v1), s, pv_lo(__builtin_bit_cast(bf16x8, v0), s, oo));
    }
#pragma unroll
    for (int e = 0; e < 3; ++e) a[e] = f32x4{oo[4 * e], oo[4 * e + 1], oo[4 * e + 2], oo[4 * e + 3]};
    l = h ? a[2][0] : 0.f;
    l += swap32(l, h);
  }
  const float inv = 1.f / l;
  const unsigned A0 = pk2(a[0][0] * inv, a[0][1] * inv), A1 = pk2(a[0][2] * inv, a[0][3] * inv);
  const unsigned B0 = pk2(a[1][0] * inv, a[1][1] * inv), B1 = pk2(a[1][2] * inv, a[1][3] * inv);
  const u32x2 C = {pk2(a[2][0] * inv, a[2][1] * inv), pk2(a[2][2] * inv, a[2][3] * inv)};
  const int tok = qt * 32 + r32;
  const unsigned ob = L + L_O + (tok >> 4) * 20480 + (tok & 15) * 16 + (head >> 1) * 1280;
  st16(c.r, ob + ((head & 1) * 3 + h) * 256, pair32(A0, A1, B0, B1));
  if (!h) st8(c.r, ob + 512 + (head & 1) * 8, C);
  STAMP(4);
  publish(c, M_CNT, 8, flg(FL_ATT + layer * 5, qb * 32 + head), true);
  STAMP(5);
}
DI void retile_load(const Ctx& c, int J, u32x4 (&v)[5][4], const uint16_t* wo, const uint16_t* wg, const uint16_t* wu, const uint16_t* wd) {
  const int e = J < 216 ? J : J < 256 ? J - 216 : J - 256;
  const int strip = J < 216 ? (e >> 2) % 27 : e % 10, k0 = J < 216 ? (e & 3) * 5 : min((e / 10) * 5, 49), ld = J < 216 ? 3456 : 1280;
  const rsrc_t r = __builtin_amdgcn_make_buffer_rsrc((void*)(J < 108 ? wg : J < 216 ? wu : J < 256 ? wo : wd), 0,
                                                     J < 216 ? 640 * 3456 : J < 256 ? 640 * 1280 : 1728 * 1280, 0x00020000);
  const unsigned vo = (c.lane >> 3) * ld + (c.lane & 7) * 16, so = k0 * 32 * ld + strip * 128;
#pragma unroll
  for (int u = 0; u < 5; ++u)
#pragma unroll
    for (int q = 0; q < 4; ++q) v[u][q] = __builtin_amdgcn_raw_buffer_load_b128(r, vo, so + (u * 32 + q * 8) * ld, 0);
}
DI void retile_store(const Ctx& c, int J, lds_ptr scr, const u32x4 (&v)[5][4]) {
  const int e = J < 216 ? J : J < 256 ? J - 216 : J - 256, mat = J >= 108;
  const int strip = J < 216 ? (e >> 2) % 27 : e % 10, k0 = J < 216 ? (e & 3) * 5 : min((e / 10) * 5, 49);
  const unsigned ts = J < 216 ? 40960 : J < 256 ? 20480 : 55296;
  const unsigned dst = (J < 216 ? WS_WGU + mat * 20480 : J < 256 ? WS_WO : WS_WD) + strip * 4 * ts + k0 * 1024;
  const int i = c.lane & 15, g = c.lane >> 4;
  const bf16x8 ones = __builtin_bit_cast(bf16x8, u32x4{0x3f803f80u, 0x3f803f80u, 0x3f803f80u, 0x3f803f80u});
  f32x4 cs[4] = {f32x4{}, f32x4{}, f32x4{}, f32x4{}};
#pragma unroll
  for (int u = 0; u < 5; ++u) {
#pragma unroll
    for (int p = 0; p < 4; ++p) *(LAS u32x4*)(scr + p * 1024 + c.lane * 16) = v[u][p];
#pragma unroll
    for (int t = 0; t < 4; ++t) {
      const bf16x8 f = ldstr(scr + (g * 8 + (i >> 2)) * 128 + t * 32 + (i & 3) * 8, 512);
      __builtin_amdgcn_raw_buffer_store_b128(__builtin_bit_cast(u32x4, f), c.r, c.lane * 16, dst + u * 1024 + t * ts, 16);
      cs[t] = mfma16(f, ones, cs[t]);
    }
  }
  if (J < 216 && i == 0) {
#pragma unroll
    for (int t = 0; t < 4; ++t)
      __builtin_amdgcn_raw_buffer_store_b128(__builtin_bit_cast(u32x4, cs[t]), c.r, g * 16, WS_CSG + ((e & 3) * 3456 + (strip * 8 + t * 2 + mat) * 16) * 4, 16);
  }
}
DI int early_piece(int e) { return e < 4 ? (e & 1) + (e >> 1) * 8 : e < 6 ? 16 + (e - 4) * 4 : e < 9 ? 18 + e : 25 + e; }
DI int late_piece(int e) { return e < 6 ? 2 + e : e < 12 ? 4 + e : e < 15 ? 5 + e : e < 18 ? 6 + e : e < 25 ? 9 + e : 12 + e; }
DI void att_load(const Ctx& c, int lw, int layer, unsigned iss, const uint16_t* wo, const uint16_t* wg, const uint16_t* wu, const uint16_t* wd) {
  const int head = att_head(c), qb = att_qb(c), lane = c.lane;
  const char* Lp = c.ws + WS_L + layer * L_SZ;
  const unsigned vo = lane * 16;
  auto src_of = [&](int p) {
    return p < 16 ? L_K1 + head * 65536 + p * 1024 : p < 24 ? L_K2 + (head >> 1) * 32768 + (p - 16) * 1024 : L_V + head * 81920 + (p - 24) * 1024;
  };
  unsigned v0[14], m0[11], vg[11], sg[11];
#pragma unroll
  for (int q = 0; q < 11; ++q) {
    const int p = q < 3 ? early_piece(lw * 3 + q) : late_piece(lw * 8 + q - 3), pg = lw * 11 + q;
    v0[q] = src_of(p) + vo; m0[q] = p * 1024;
    vg[q] = src_of(pg) + vo; sg[q] = pg < 16 ? 16384 : pg < 24 ? 8192 : 20480;
    asm volatile("" : "+v"(v0[q]), "+s"(m0[q]));
  }
#pragma unroll
  for (int q = 0; q < 3; ++q) {
    const int p = lw * 3 + q;
    v0[11 + q] = (p < 8 ? L_Q1 + head * 65536 + qb * 8192 + p * 1024 : L_Q2 + (head >> 1) * 32768 + qb * 4096 + (p - 8) * 1024) + vo - q * 1024;
    PIN(v0[11 + q]);
  }
  auto issue = [&](int slot) {
#pragma unroll
    for (int q = 0; q < 11; ++q) { vg[q] += sg[q]; glds1(slot * AT_SLOT + (lw * 11 + q) * 1024, Lp, vg[q]); }
  };
  Poll<2> pl;
  if (lw == 0) {
    const int o0 = (head >> 1) * 5 + (head & 1) * 2, v0 = head * 20;
    if (POLL_GATE) sig_wait(c, iss, M_ISS);
    const int sel = lane >> 4, cb = sel == 0 ? 5 + (o0 >> 4) : sel == 1 ? 5 + ((o0 + 2) >> 4) : sel == 2 ? 10 + (v0 >> 7) : 10 + ((v0 + 19) >> 7);
    pl.run(c, flg(FL_QKV + layer * 5, (lane & 15) * 16 + cb), flg(FL_QKV + layer * 5, (qb * 2 + (lane & 1)) * 16 + ((o0 + (lane & 2)) >> 4)));
    sig_set(c, layer * 5 + 2);
    STAMPL(layer * 5 + 1, 6);
  } else sig_wait(c, layer * 5 + 2);
  glds3(2 * AT_SLOT + lw * 3072, Lp, v0[11], v0[12], v0[13]);
#pragma unroll
  for (int q = 0; q < 3; ++q) glds1(m0[q], Lp, v0[q]);
  wait_vm<0>(); BARRIER();
#pragma unroll
  for (int q = 3; q < 11; ++q) glds1(m0[q], Lp, v0[q]);
  pl.keep();
  wait_vm<0>(); BARRIER();
  issue(1);
  auto warm = [&]() {
    const int e = c.j & 7;
    const unsigned n1 = WARM_WO ? 13 : 0, n2 = n1 + (WARM_GU ? 35 : 0), n3 = n2 + (WARM_DN ? 34 : 0);
#pragma unroll 1
    for (unsigned q = lw; q < n3; q += 4) {
      const unsigned src = q < n1 ? WS_WO + pr_cb(c) * 102400 + min(e * 13 + q, 99u) * 1024
                         : q < n2 ? WS_WGU + ((gu_cb(c) * 27) >> 2) * 40960 + (e * 35 + q - n1) * 1024
                                  : WS_WD + pr_cb(c) * 276480 + min(e * 34 + q - n2, 269u) * 1024;
      glds1(AT_SCR + lw * 1024, c.ws + src, vo);
      wait_vm<4>();
    }
    wait_vm<0>();
  };
  const int J = (lw - 1) * 256 + blockIdx.x;
  const bool rt = layer == 0 && lw > 0 && J < 366;
  u32x4 v[5][4];
  if (rt) { retile_load(c, J, v, wo, wg, wu, wd); wait_vm<20>(); } else wait_vm<0>();
  BARRIER();
  issue(2);
  wait_vm<0>(); BARRIER();
  issue(0);
  if (rt) retile_store(c, J, c.lds + AT_SCR + (lw - 1) * 4096, v);
  if (layer == 0 && (lw == 1 || lw == 2)) publish(c, M_CNT3, 2, flg(FL_RT, blockIdx.x));
  wait_vm<0>(); BARRIER();
  if (layer == 0) {
    if (lw == 0) {
      if (c.j == 0) {
#pragma unroll 1
        for (int q = 0; q < 2; ++q) { pl.run(c, flg(FL_RT, q * 128 + lane), flg(FL_RT, q * 128 + 64 + lane)); wait_vm<0>(); pl.keep(); }
        if (lane < 32) __hip_atomic_store((GAS u64*)(c.ws + flg(FL_RT2, lane * 8 + c.xcd)), c.tgt, __ATOMIC_RELAXED, __HIP_MEMORY_SCOPE_AGENT);
      }
      poll(c, flg(FL_RT2, blockIdx.x));
      sig_set(c, 3);
    } else sig_wait(c, 3);
  }
  warm();
  BARRIER(); BARRIER();
}

template <int NS, int NB, int WIN>
DI void proj_compute(const Ctx& c, int w, int slot_id, unsigned a_off, const char* res_rm, unsigned res_off, unsigned out_off, unsigned st_off,
                     uint16_t* out, int arr) {
  constexpr bool RES = NS <= NB;
  const int panel = pr_panel(c), cb = pr_cb(c);
  SLOT(slot_id);
  STAMP(0);
  if (w >= 4) {
#pragma unroll 1
    for (int k = 0; k <= (RES ? 1 : NS); ++k) BARRIER();
    return;
  }
  const int lane = c.lane, i = lane & 15, g = lane >> 4, mt = panel * 4 + w;
  u32x2 res[5];
#pragma unroll
  for (int t = 0; t < 5; ++t)
    res[t] = res_rm ? *(const u32x2*)(res_rm + (mt * 16 + i) * 1280 + cb * 160 + t * 32 + g * 8)
                    : ld8(c.r, res_off + (mt * 80 + cb * 10 + t * 2 + (g >> 1)) * 256 + i * 16 + (g & 1) * 8);
  f32x4 acc[5];
#pragma unroll
  for (int t = 0; t < 5; ++t) { acc[t] = f32x4{bflo(res[t][0]), bfhi(res[t][0]), bflo(res[t][1]), bfhi(res[t][1])}; PIN(acc[t]); }
  lds_ptr pb = c.lds + lane * 16;
  unsigned av = a_off + mt * (NS * 2048) + lane * 16;
  u32x4 a[WIN];
  bf16x8 f[3][5];
  int sb = 0;
  STAMP(1);
  BARRIER();
  STAMP(2); STAMPC(0); WRESET();
  constexpr int AH = RES ? 12 : WIN;
#pragma unroll
  for (int k = 0; k < AH; ++k) a[k] = ld16(c.r, av + k * 1024);
#pragma unroll
  for (int t = 0; t < 5; ++t) { f[0][t] = ldsr16(pb + t * 2048); f[1][t] = ldsr16(pb + t * 2048 + 1024); }
  SBAR();
  auto window = [&](bool last) {
#pragma unroll
    for (int k = 0; k < WIN; ++k) {
      const bool ld = !(last && k + 2 >= WIN);
      if ((k & 1) == 0) {
        if (RES) pb += 10240;
        else { pb += sb == NB - 1 ? -(NB - 1) * 10240 : 10240; sb = sb == NB - 1 ? 0 : sb + 1; }
        if (!ld) BARRIER(); else if (!RES) BARRIER_NW();
      }
#pragma unroll
      for (int t = 0; t < 5; ++t) {
        if (ld) f[(k + 2) % 3][t] = ldsr16(pb + t * 2048 + (k & 1) * 1024);
        acc[t] = mfma16(f[k % 3][t], __builtin_bit_cast(bf16x8, a[k]), acc[t]);
        SBAR();
      }
      if (!last) { a[k] = ld16(c.r, av + (WIN + k) * 1024); SBAR(); }
      else if (k + AH < WIN) { a[k + AH] = ld16(c.r, av + (k + AH) * 1024); SBAR(); }
    }
    av += WIN * 1024;
  };
  static_assert(2 * NS % WIN == 0 && (2 * NS == WIN || WIN % 6 == 0), "window");
#pragma unroll 1
  for (int b = 1; b < 2 * NS / WIN; ++b) window(false);
  window(true);
  STAMP(3); STAMPC(1);
  u32x2 o[5];
#pragma unroll
  for (int t = 0; t < 5; ++t) {
    o[t][0] = pk2(acc[t][0], acc[t][1]); o[t][1] = pk2(acc[t][2], acc[t][3]);
  }
  const u32x4 p0 = pair16(o[0][0], o[0][1], o[1][0], o[1][1]), p1 = pair16(o[2][0], o[2][1], o[3][0], o[3][1]),
              p2 = pair16(o[4][0], o[4][1], o[4][0], o[4][1]);
  const int ch = (g & 1) * 2 + (g >> 1);
  if (out) {
    char* po = (char*)out + (mt * 16 + i) * 1280 + cb * 160;
    *(u32x4*)(po + ch * 16) = p0; *(u32x4*)(po + 64 + ch * 16) = p1;
    if (!(g & 1)) *(u32x4*)(po + 128 + (g >> 1) * 16) = p2;
    STAMP(4);
    return;
  }
  const unsigned xo = out_off + (mt * 80 + cb * 10) * 256 + i * 16;
  st16(c.r, xo + ch * 256, p0); st16(c.r, xo + 1024 + ch * 256, p1);
  if (!(g & 1)) st16(c.r, xo + 2048 + (g >> 1) * 256, p2);
  const f32x4 s4 = (acc[0] + acc[1]) + (acc[2] + acc[3]) + acc[4];
  const float sum = rows4((s4[0] + s4[1]) + (s4[2] + s4[3])), mean = sum * (1.f / 80.f);
  f32x4 q4 = {0.f, 0.f, 0.f, 0.f};
#pragma unroll
  for (int t = 0; t < 5; ++t) { const f32x4 d = acc[t] - mean; q4 += d * d; }
  const float m2 = rows4((q4[0] + q4[1]) + (q4[2] + q4[3]));
  if (g == 0) st8(c.r, st_off + (mt >> 3) * 8192 + cb * 1024 + ((mt & 7) * 16 + i) * 8, u32x2{__float_as_uint(sum), __float_as_uint(m2)});
  STAMP(4);
  publish(c, M_CNT4, 4, flg(arr, panel * 8 + cb), true);
  STAMP(5);
}
template <int NS, int NB> DI void proj_b_run(const Ctx& c, int half, unsigned w_off) {
  Ring<5, NB, NS, 2> rg;
#pragma unroll
  for (int p = 0; p < 5; ++p) { const int q = half * 5 + p; rg.voff[p] = w_off + (pr_cb(c) * 5 + (q >> 1)) * (NS * 2048) + (q & 1) * 1024 + c.lane * 16; }
  rg.init(c.ws, 2048, 0, half);
  rg.template fill_paced<(NB < DN_D0 ? NB : DN_D0)>();
  if constexpr (NS <= NB) { wait_vm<0>(); BARRIER_L(); BARRIER_L(); } else rg.template run<DN_D0>();
}

template <int NF>
DI void gu_consumer(const Ctx& c, int layer, int f0, int mq) {
  const int panel = gu_panel(c), ft0 = (gu_cb(c) * 27) >> 2;
  const int lane = c.lane, r32 = lane & 31, h = lane >> 5;
  f32x16 acc[NF];
#pragma unroll
  for (int t = 0; t < NF; ++t) acc[t] = f32x16{};
  const int lo = (r32 >> 4) * 1024 + h * 256 + (r32 & 15) * 16;
  lds_ptr pa = c.lds + GU_A + mq * 2048 + lo, pb = c.lds + f0 * 2048 + lo;
  bf16x8 x0[2], x1[2], wa[NF], wb0[NF], wb1[NF];
  auto step = [&](const bf16x8* xa, const bf16x8* wb, bf16x8* xn, bf16x8* wn_, bool ld) {
#pragma unroll
    for (int q = 0; q < 2 * NF; ++q) {
      if (ld) {
        if (q == 0) { xn[0] = ldsr16(pa); xn[1] = ldsr16(pa + 512); }
        if (q >= 1 && q <= NF) wn_[q - 1] = ldsr16(pb + (q - 1) * 2048 + 512);
        if (q >= NF) wa[q - NF] = ldsr16(pb + (q - NF) * 2048);
      }
      if (q < NF) acc[q] = mfma32(wa[q], xa[0], acc[q]); else acc[q - NF] = mfma32(wb[q - NF], xa[1], acc[q - NF]);
      SBAR();
    }
  };
  static_assert(GU_NA == GU_NB, "one slot counter");
  int sl = 0;
  auto adv = [&]() {
    pa += sl == GU_NA - 1 ? -(GU_NA - 1) * 8192 : 8192; pb += sl == GU_NB - 1 ? -(GU_NB - 1) * 14336 : 14336;
    sl = sl == GU_NA - 1 ? 0 : sl + 1;
  };
  SLOT(layer * 5 + 3);
  STAMP(0);
  STAMP(1);
  BARRIER();
  STAMP(2); STAMPC(0); WRESET();
  x0[0] = ldsr16(pa); x0[1] = ldsr16(pa + 512);
#pragma unroll
  for (int t = 0; t < NF; ++t) { wa[t] = ldsr16(pb + t * 2048); wb0[t] = ldsr16(pb + t * 2048 + 512); }
#pragma unroll 1
  for (int st = 0; st < 9; ++st) {
    adv(); BARRIER_NW(); step(x0, wb0, x1, wb1, true);
    adv(); BARRIER_NW(); step(x1, wb1, x0, wb0, true);
  }
  adv(); BARRIER_NW(); step(x0, wb0, x1, wb1, true);
  BARRIER();
  const LAS float* srs = (const LAS float*)(c.lds + M_SST) + mq * 32 + r32;
  const LAS float* scs = (const LAS float*)(c.lds + M_SCS) + f0 * 32 + h * 4;
  float rs = srs[0], nm = srs[128];
  f32x4 cs[2][4];
#pragma unroll
  for (int e = 0; e < 8; ++e) cs[e >> 2][e & 3] = *(const LAS f32x4*)(scs + e * 8);
  SBAR();
  step(x1, wb1, x0, wb0, false);
  STAMP(3); STAMPC(1);
  rs *= rs;
  const unsigned ho = WS_L + layer * L_SZ + L_H + ((panel * 8 + mq * 2 + (r32 >> 4)) * 216 + (ft0 + f0) * 2 + h) * 256 + (r32 & 15) * 16;
#pragma unroll
  for (int f = 0; f < NF; ++f) {
    unsigned p[2][2];
#pragma unroll
    for (int e = 0; e < 2; ++e) {
      const f32x16 a = acc[f];
      f32x4 gt = f32x4{a[e * 4], a[e * 4 + 1], a[e * 4 + 2], a[e * 4 + 3]} + cs[f & 1][e] * nm;
      const f32x4 up = (f32x4{a[8 + e * 4], a[9 + e * 4], a[10 + e * 4], a[11 + e * 4]} + cs[f & 1][2 + e] * nm) * rs;
      gt = __builtin_elementwise_max(gt, f32x4{0.f, 0.f, 0.f, 0.f}) * up;
      p[e][0] = pk2(gt[0], gt[1]); p[e][1] = pk2(gt[2], gt[3]);
    }
    if (f + 2 < NF) {
#pragma unroll
      for (int e = 0; e < 4; ++e) cs[f & 1][e] = *(const LAS f32x4*)(scs + (f + 2) * 32 + e * 8);
    }
    st16(c.r, ho + f * 512, pair32(p[0][0], p[0][1], p[1][0], p[1][1]));
    SBAR();
  }
  STAMP(4);
  publish(c, M_CNT, 8, flg(FL_GU + layer * 5, panel * 16 + gu_cb(c)), true);
  STAMP(5);
}
typedef Ring<7, GU_NB, 20, 2> GuB;
DI void gu_b_init(const Ctx& c, GuB& rg, int half) {
  const int ft0 = (gu_cb(c) * 27) >> 2;
#pragma unroll
  for (int p = 0; p < 7; ++p) rg.voff[p] = WS_WGU + (ft0 * 2 + half * 7 + p) * 20480 + c.lane * 16;
  rg.init(c.ws, 1024, 0, half);
}
DI void gu_b_run(const Ctx& c, int half, int layer) {
  GuB rg;
  gu_b_init(c, rg, half);
  rg.fill_paced();
  if (half == 0) ln_cols<4>(c, 56, WS_CSG + ((gu_cb(c) * 27) >> 2) * 128, 13824);
  wait_vm<(GU_NB - 1) * 7>(); BARRIER_L(); WRESET();
  f32x2 p[8];
#pragma unroll
  for (int i = 0; i < 8; ++i) p[i] = ld8_async(WS_ST + (layer * 2 + 1) * STB + gu_panel(c) * 8192 + i * 1024 + (half * 64 + c.lane) * 8, c.ws);
  wait_vm<(GU_NB - 2) * 7 + 8>(); BARRIER_L();
  asm volatile("s_waitcnt vmcnt(0)" : "+v"(p[0]), "+v"(p[1]), "+v"(p[2]), "+v"(p[3]), "+v"(p[4]), "+v"(p[5]), "+v"(p[6]), "+v"(p[7])::"memory");
  ln_rows(c, half * 64 + c.lane, p);
  rg.steady();
  rg.template range<GU_NB - 3, 0>(); BARRIER_L();
}
DI void gu_a_run(const Ctx& c, int half, int layer, unsigned iss) {
  const unsigned a_off = WS_L + layer * L_SZ + L_X1;
  Ring<4, GU_NA, 20, 2> rg;
#pragma unroll
  for (int p = 0; p < 4; ++p) rg.voff[p] = a_off + (gu_panel(c) * 8 + half * 4 + p) * 20480 + c.lane * 16;
  rg.init(c.ws, 1024, GU_A, half);
  Poll<1> pl;
  gate(c, pl, half, flg(FL_WO + layer * 5, gu_panel(c) * 16 + (c.lane & 15)), layer * 5 + 4, iss);
  rg.template fill<A_D0 + 1>();
  rg.template head<A_D0 + 1>();
  rg.template range<GU_NA - 3, 3>();
  rg.template range<2, 0>();
  BARRIER_L();
  pl.keep();
}

__global__ __launch_bounds__(768) void fused_kernel(const uint16_t* __restrict__ x, const uint16_t* __restrict__ wqkv, const uint16_t* __restrict__ wo,
                                                    const uint16_t* __restrict__ wg, const uint16_t* __restrict__ wu,
                                                    const uint16_t* __restrict__ wd, uint16_t* __restrict__ out, char* __restrict__ ws) {
  extern __shared__ __attribute__((aligned(16))) char smem[];
  Ctx c;
  c.ws = ws; c.lds = (lds_ptr)smem; c.lane = threadIdx.x & 63; c.xcd = blockIdx.x & 7; c.j = blockIdx.x >> 3;
  c.r = __builtin_amdgcn_make_buffer_rsrc(ws, 0, WS_END, 0x00020000);
  const int w = __builtin_amdgcn_readfirstlane(threadIdx.x >> 6);
  SPANST(0);
  c.tgt = *(const u64*)(ws + WS_EPOCH) + 1;
  if (threadIdx.x < 16) *(LAS unsigned*)(c.lds + M_CNT + threadIdx.x * 4) = 0u;
  const bool qkv_on = qkv_cb(c) != 15;
  const Ctx c0 = c;
  WDUMP(15);
  if (w >= 8) __builtin_amdgcn_s_setprio(3);
  if (w >= 8) {
    const int half = w & 1;
    const bool wl = w >= 10;
    if (!qkv_on) BARRIER();
#pragma unroll 1
    for (int layer = 0; layer < 2; ++layer) {
      const unsigned iss = layer * 24 + (qkv_on ? (layer + 1) * 4 : 0);
      if (qkv_on) {
        if (wl) qkv_b_run(fresh(c0), opq(half), opq(layer), wqkv); else qkv_a_run(fresh(c0), opq(half), opq(layer), x);
      }
      WDUMP(layer * 5);
      att_load(fresh(c0), opq(w - 8), opq(layer), iss, wo, wg, wu, wd);
      WDUMP(layer * 5 + 1);
      const Ctx c = fresh(c0);
      const int l = c.lane;
      Poll<1> pl;
      if (wl) {
        sig_wait(c, iss + 8, M_ISS);
        proj_b_run<10, WO_NB>(fresh(c0), opq(half), WS_WO);
        WDUMP(layer * 5 + 2);
        sig_wait(c, iss + 12, M_ISS);
        gu_b_run(fresh(c0), opq(half), opq(layer));
        WDUMP(layer * 5 + 3);
        sig_wait(c, iss + 20, M_ISS);
        proj_b_run<27, DN_NB>(fresh(c0), opq(half), WS_WD);
      } else {
        if (half == 0) {
          if (POLL_GATE) sig_wait(c, iss + 8, M_ISS);
          pl.run(c, flg(FL_ATT + layer * 5, (pr_panel(c) >> 2) * 32 + (l & 31)));
          STAMPL(layer * 5 + 2, 6);
        }
        BARRIER_L(); wait_vm<0>(); pl.keep(); BARRIER_L();
        WDUMP(layer * 5 + 2);
        gu_a_run(fresh(c0), opq(half), opq(layer), iss + 12);
        WDUMP(layer * 5 + 3);
        if (half == 0) {
          if (POLL_GATE) sig_wait(c, iss + 20, M_ISS);
          pl.run(c, flg(FL_GU + layer * 5, (pr_panel(c) >> 1) * 16 + (l & 15)));
          STAMPL(layer * 5 + 4, 6);
        }
        BARRIER_L(); wait_vm<0>(); pl.keep();
        if (layer == 1 && half == 0 && blockIdx.x == 0 && l == 0) *(u64*)(ws + WS_EPOCH) = c.tgt;
#pragma unroll 1
        for (int k = 1; k <= 27; ++k) BARRIER_L();
      }
      WDUMP(layer * 5 + 4);
    }
  } else {
    if (!qkv_on) BARRIER();
#define TLB_PF 524288
    if (w == 2 || w == 3) {
      const unsigned o = (w - 2) * 64 + c.lane;
      const unsigned t = o * TLB_PF < WS_END ? __builtin_amdgcn_raw_buffer_load_b32(c.r, o * TLB_PF + 2048, 0, 16) : out[(o & 1) * 1048576];
      asm volatile("" ::"v"(t));
    }
    if (w < 2) {
      const char* pc; constexpr unsigned len = 48 * 1024;
      asm volatile("s_getpc_b64 %0" : "=s"(pc));
      const unsigned o = (c.j * 2 + w) * 1024 + c.lane * 16;
      if (o + 16 <= len) { const u32x4 t = *(const GAS u32x4*)(pc + o); asm volatile("" ::"v"(t)); }
    }
#pragma unroll 1
    for (int layer = 0; layer < 2; ++layer) {
      const unsigned L = WS_L + layer * L_SZ;
      qkv_compute(fresh(c0), opq(w), opq(layer), x, wqkv);
      WDUMP(layer * 5);
      att_compute(fresh(c0), opq(w), opq(layer));
      WDUMP(layer * 5 + 1);
      proj_compute<10, WO_NB, 20>(fresh(c0), opq(w), layer * 5 + 2, L + L_O, layer == 0 ? (const char*)x : nullptr, WS_X2, L + L_X1,
                                  WS_ST + (layer * 2 + 1) * STB, nullptr, FL_WO + layer * 5);
      WDUMP(layer * 5 + 2);
      if (w < 4) gu_consumer<4>(fresh(c0), opq(layer), 0, opq(w)); else gu_consumer<3>(fresh(c0), opq(layer), 4, opq(w - 4));
      WDUMP(layer * 5 + 3);
      proj_compute<27, DN_NB, 18>(fresh(c0), opq(w), layer * 5 + 4, L + L_H, nullptr, L + L_X1, WS_X2, WS_ST + 2 * STB, layer ? out : nullptr,
                                  FL_DN + layer * 5);
      WDUMP(layer * 5 + 4);
    }
  }
}

extern "C" void kernel_launch(void* const* d_in, const int* in_sizes, int n_in, void* d_out, int out_size, void* d_ws, size_t ws_size,
                              hipStream_t stream) {
  (void)hipFuncSetAttribute((const void*)fused_kernel, hipFuncAttributeMaxDynamicSharedMemorySize, LDS_BYTES);
  fused_kernel<<<256, 768, LDS_BYTES, stream>>>((const uint16_t*)d_in[0], (const uint16_t*)d_in[1], (const uint16_t*)d_in[2],
                                                (const uint16_t*)d_in[4], (const uint16_t*)d_in[5], (const uint16_t*)d_in[6],
                                                (uint16_t*)d_out, (char*)d_ws);
}
```

```cpp
#include <hip/hip_runtime.h>
#include <hip/hip_bf16.h>
#include <stdint.h>

typedef __attribute__((ext_vector_type(8))) __bf16 bf16x8;
typedef __attribute__((ext_vector_type(16))) __bf16 bf16x16;
typedef __attribute__((ext_vector_type(4))) short s16x4;
typedef __attribute__((ext_vector_type(8))) short s16x8;
typedef __attribute__((ext_vector_type(2))) __bf16 bf16x2;
typedef __attribute__((ext_vector_type(2))) float f32x2;
typedef __attribute__((ext_vector_type(4))) float f32x4;
typedef __attribute__((ext_vector_type(8))) float f32x8;
typedef __attribute__((ext_vector_type(16))) float f32x16;
typedef __attribute__((ext_vector_type(2))) unsigned u32x2;
typedef __attribute__((ext_vector_type(4))) unsigned u32x4;
typedef unsigned long long u64;
#define LAS __attribute__((address_space(3)))
#define GAS __attribute__((address_space(1)))
typedef LAS char* lds_ptr;
typedef __amdgpu_buffer_rsrc_t rsrc_t;
#define DI __device__ __forceinline__

constexpr unsigned XB = 2048u * 640 * 2;
constexpr unsigned WS_EPOCH = 0;
constexpr unsigned WS_FLAG = 4096;
constexpr int FL_P0 = 0, FL_RT = 1, FL_QKV = 2, FL_ATT = 3, FL_WO = 4, FL_GU = 5, FL_DN = 6, FL_RT2 = 12;
constexpr unsigned WS_WO  = WS_FLAG + 13u * 32768;
constexpr unsigned WS_WGU = WS_WO + 640u * 640 * 2;
constexpr unsigned WS_WD  = WS_WGU + 3456u * 640 * 2;
constexpr unsigned WS_CSQ = WS_WD + 640u * 1728 * 2;
constexpr unsigned WS_CSG = WS_CSQ + 8u * 1920 * 4;
constexpr unsigned WS_ST  = WS_CSG + 4u * 3456 * 4;
constexpr unsigned STB = 2048u * 64;
constexpr unsigned WS_X2  = WS_ST + 4 * STB;
constexpr unsigned WS_L   = WS_X2 + XB;
constexpr unsigned L_Q1 = 0, L_Q2 = L_Q1 + 32u * 65536, L_K1 = L_Q2 + 16u * 32768, L_K2 = L_K1 + 32u * 65536, L_V = L_K2 + 16u * 32768,
                   L_O = L_V + XB, L_X1 = L_O + XB, L_H = L_X1 + XB, L_SZ = L_H + 2048u * 1728 * 2;
constexpr unsigned WS_END = WS_L + 2 * L_SZ;

constexpr int LDS_BYTES = 163840, MISC = 159744, M_SST = MISC, M_SCS = MISC + 1024, M_CNT = MISC + 2048, M_CNT2 = M_CNT + 8, M_CNT3 = M_CNT + 16, M_CNT4 = M_CNT + 24, M_SIG = M_CNT + 32, M_ISS = M_CNT + 40;
constexpr int QKV_NB = 5, QKV_NA = 4, QKV_A = QKV_NB * 16384;
constexpr int WO_NB = 10, DN_NB = 15;
constexpr int GU_NB = 7, GU_NA = 7, GU_A = GU_NB * 14336;
constexpr int AT_SLOT = 45056, AT_CONST = 3 * AT_SLOT, AT_SCR = AT_CONST + 10752, AT_MERGE = 3 * AT_SLOT;
static_assert(QKV_A + QKV_NA * 16384 <= MISC && DN_NB * 10240 <= MISC && GU_A + GU_NA * 8192 <= MISC, "lds");
static_assert(WO_NB * 10240 <= AT_MERGE && AT_MERGE + 24576 <= MISC && AT_SCR + 3 * 4096 <= MISC, "lds");

DI unsigned pk2(float a, float b) { f32x2 v = {a, b}; bf16x2 r = __builtin_convertvector(v, bf16x2); return __builtin_bit_cast(unsigned, r); }
DI float bflo(unsigned u) { return __uint_as_float(u << 16); }
DI float bfhi(unsigned u) { return __uint_as_float(u & 0xffff0000u); }
DI f32x4 mfma16(bf16x8 a, bf16x8 b, f32x4 c) { return __builtin_amdgcn_mfma_f32_16x16x32_bf16(a, b, c, 0, 0, 0); }
DI f32x16 mfma32(bf16x8 a, bf16x8 b, f32x16 c) { return __builtin_amdgcn_mfma_f32_32x32x16_bf16(a, b, c, 0, 0, 0); }
DI bf16x8 ldsr16(lds_ptr p) { return *(const LAS bf16x8*)p; }
DI bf16x8 ldstr(lds_ptr p, int second) {
  const s16x4 a = __builtin_amdgcn_ds_read_tr16_b64_v4i16((LAS s16x4*)p), b = __builtin_amdgcn_ds_read_tr16_b64_v4i16((LAS s16x4*)(p + second));
  return __builtin_bit_cast(bf16x8, (s16x8)__builtin_shufflevector(a, b, 0, 1, 2, 3, 4, 5, 6, 7));
}
#define BARRIER() asm volatile("s_waitcnt lgkmcnt(0)\n\ts_barrier" ::: "memory")
template <int N> DI void wait_vm() { asm volatile("s_waitcnt vmcnt(%0)" ::"n"(N > 63 ? 63 : N) : "memory"); }
#define WDUMP(id)
#define WRESET()
#define BARRIER_L() BARRIER()
#define BARRIER_NW() asm volatile("s_barrier" ::: "memory")
#define SBAR() __builtin_amdgcn_sched_barrier(0)
#define PIN(x) asm volatile("" : "+v"(x))
#define GL_HEAD "s_mov_b32 m0, %0\n\ts_nop 3\n\t"
#define GL(v, off) "global_load_lds_dwordx4 %" #v ", %1 offset:" #off " sc1\n\t"
DI void glds1(unsigned m, const char* sb, unsigned v0) { asm volatile(GL_HEAD GL(2, 0)::"s"(m), "s"(sb), "v"(v0) : "memory"); }
DI void glds2(unsigned m, const char* sb, unsigned v0, unsigned v1) {
  asm volatile(GL_HEAD GL(2, 0) GL(3, 1024)::"s"(m), "s"(sb), "v"(v0), "v"(v1) : "memory");
}
DI void glds3(unsigned m, const char* sb, unsigned v0, unsigned v1, unsigned v2) {
  asm volatile(GL_HEAD GL(2, 0) GL(3, 1024) GL(4, 2048)::"s"(m), "s"(sb), "v"(v0), "v"(v1), "v"(v2) : "memory");
}
DI void glds4(unsigned m, const char* sb, unsigned v0, unsigned v1, unsigned v2, unsigned v3) {
  asm volatile(GL_HEAD GL(2, 0) GL(3, 1024) GL(4, 2048) GL(5, 3072)::"s"(m), "s"(sb), "v"(v0), "v"(v1), "v"(v2), "v"(v3) : "memory");
}
DI void st16(rsrc_t r, unsigned off, u32x4 v) { __builtin_amdgcn_raw_buffer_store_b128(v, r, off, 0, 16); }
DI void st8(rsrc_t r, unsigned off, u32x2 v) { __builtin_amdgcn_raw_buffer_store_b64(v, r, off, 0, 16); }
DI void st4(rsrc_t r, unsigned off, unsigned v) { __builtin_amdgcn_raw_buffer_store_b32(v, r, off, 0, 16); }
DI u32x4 ld16(rsrc_t r, unsigned off) { return __builtin_amdgcn_raw_buffer_load_b128(r, off, 0, 16); }
DI u32x2 ld8(rsrc_t r, unsigned off) { return __builtin_amdgcn_raw_buffer_load_b64(r, off, 0, 16); }
DI u32x4 ld16s(rsrc_t r, unsigned voff, unsigned soff) { return __builtin_amdgcn_raw_buffer_load_b128(r, voff, soff, 16); }
DI void ldsw16(lds_ptr p, u32x4 v) { *(LAS u32x4*)p = v; }
DI f32x2 ld8_async(unsigned voff, const char* sbase) {
  f32x2 v; asm volatile("s_nop 4\n\tglobal_load_dwordx2 %0, %1, %2 sc1" : "=v"(v) : "v"(voff), "s"(sbase) : "memory"); return v;
}
DI u32x4 pair32(unsigned a0, unsigned a1, unsigned b0, unsigned b1) {
  auto x = __builtin_amdgcn_permlane32_swap(a0, b0, false, false);
  auto y = __builtin_amdgcn_permlane32_swap(a1, b1, false, false);
  return u32x4{x[0], y[0], x[1], y[1]};
}
template <int N> DI float ror16(float v) { return __int_as_float(__builtin_amdgcn_update_dpp(0, __float_as_int(v), 0x120 + N, 0xf, 0xf, false)); }
DI float sum32(float v) {
  v += ror16<8>(v); v += ror16<4>(v); v += ror16<2>(v); v += ror16<1>(v);
  auto a = __builtin_amdgcn_permlane16_swap(__float_as_uint(v), __float_as_uint(v), false, false);
  return __uint_as_float(a[0]) + __uint_as_float(a[1]);
}
DI u32x4 pair16(unsigned a0, unsigned a1, unsigned b0, unsigned b1) {
  auto x = __builtin_amdgcn_permlane16_swap(a0, b0, false, false);
  auto y = __builtin_amdgcn_permlane16_swap(a1, b1, false, false);
  return u32x4{x[0], y[0], x[1], y[1]};
}

DI float rows4(float v) {
  auto a = __builtin_amdgcn_permlane16_swap(__float_as_uint(v), __float_as_uint(v), false, false);
  v = __uint_as_float(a[0]) + __uint_as_float(a[1]);
  auto b = __builtin_amdgcn_permlane32_swap(__float_as_uint(v), __float_as_uint(v), false, false);
  return __uint_as_float(b[0]) + __uint_as_float(b[1]);
}

#define STAMP(id)
#define STAMPC(id)
#define STAMPL(sl, id)
#define SLOT(n)

#define WARM_GU 1
#define POLL_DEPTH 1
#define POLL_GATE 1
#define POLL_SLEEP 1
#define ATT_PRIO 3
#define ATT_ORD 0
#define WO_AH 6
#define DN_AH 8
#define PACE 16
#define SPANST(id)
struct Ctx {
  char* ws; rsrc_t r; lds_ptr lds; u64 tgt; int lane, xcd, j;
};
DI Ctx fresh(const Ctx& c0) {
  Ctx c = c0;
  asm volatile("v_mbcnt_lo_u32_b32 %0, -1, 0\n\tv_mbcnt_hi_u32_b32 %0, -1, %0" : "=v"(c.lane), "+s"(c.xcd), "+s"(c.j), "+s"(c.ws));
  return c;
}
DI int opq(int v) { asm volatile("" : "+s"(v)); return v; }
constexpr int SPIN_CAP = 1 << 18;

DI u64 epoch(const char* ws) {
  u64 e; asm volatile("s_nop 4\n\ts_load_dwordx2 %0, %1, 0x0\n\ts_waitcnt lgkmcnt(0)" : "=s"(e) : "s"(ws + WS_EPOCH) : "memory"); return e + 1;
}
DI unsigned flg(int arr, int unit) { return WS_FLAG + (arr * 256 + unit) * 128; }
DI void publish(const Ctx& c, int cnt_off, unsigned n, unsigned to, bool mark = false) {
  if (mark && c.lane == 0) __hip_atomic_fetch_add((LAS unsigned*)(c.lds + M_ISS), 1u, __ATOMIC_RELAXED, __HIP_MEMORY_SCOPE_WORKGROUP);
  wait_vm<0>();
  if (c.lane == 0) {
    const unsigned old = __hip_atomic_fetch_add((LAS unsigned*)(c.lds + cnt_off), 1u, __ATOMIC_RELAXED, __HIP_MEMORY_SCOPE_WORKGROUP);
    if (old % n == n - 1) __hip_atomic_store((GAS u64*)(c.ws + to), c.tgt, __ATOMIC_RELAXED, __HIP_MEMORY_SCOPE_AGENT);
  }
}
template <int N> struct Poll {
  u64 v[POLL_DEPTH][N];
  DI void run(const Ctx& c, unsigned o0, unsigned o1 = 0) {

    auto ld = [&](int r) {
      asm volatile("global_load_dwordx2 %0, %1, %2 sc1" : "=v"(v[r][0]) : "v"(o0), "s"(c.ws) : "memory");
      if constexpr (N == 2) asm volatile("global_load_dwordx2 %0, %1, %2 sc1" : "=v"(v[r][N - 1]) : "v"(o1), "s"(c.ws) : "memory");
      __builtin_amdgcn_s_sleep(POLL_SLEEP);
    };
    auto ok = [&](int r) {
      if constexpr (N == 2) asm volatile("s_waitcnt vmcnt(%2)" : "+v"(v[r][0]), "+v"(v[r][N - 1]) : "n"((POLL_DEPTH - 1) * N) : "memory");
      else asm volatile("s_waitcnt vmcnt(%1)" : "+v"(v[r][0]) : "n"(POLL_DEPTH - 1) : "memory");
      return __all(v[r][0] == c.tgt && v[r][N - 1] == c.tgt);
    };
    asm volatile("s_nop 4" ::: "memory");
    ld(0); if (POLL_DEPTH > 1) ld(1);
#pragma unroll 1
    for (int spin = 0; spin < SPIN_CAP; ++spin) {
      if (ok(0)) break; ld(0);
      if (POLL_DEPTH > 1) { if (ok(1)) break; ld(1); }
    }
  }
  DI void keep() {
#pragma unroll
    for (int r = 0; r < POLL_DEPTH; ++r) { asm volatile("" : "+v"(v[r][0])); if constexpr (N == 2) asm volatile("" : "+v"(v[r][N - 1])); }
  }
};
DI void poll(const Ctx& c, unsigned slot) { Poll<1> p; p.run(c, slot); wait_vm<0>(); p.keep(); }
DI void sig_set(const Ctx& c, unsigned v) { if (c.lane == 0) __hip_atomic_store((LAS unsigned*)(c.lds + M_SIG), v, __ATOMIC_RELAXED, __HIP_MEMORY_SCOPE_WORKGROUP); }
DI void sig_wait(const Ctx& c, unsigned v, int off = M_SIG) {
#pragma unroll 1
  for (int spin = 0; spin < SPIN_CAP * 16; ++spin) {
    if (__builtin_amdgcn_readfirstlane(__hip_atomic_load((LAS unsigned*)(c.lds + off), __ATOMIC_RELAXED, __HIP_MEMORY_SCOPE_WORKGROUP)) >= v) break;
    if (off != M_SIG) __builtin_amdgcn_s_sleep(1);
  }
  asm volatile("" ::: "memory");
}

DI void gate(const Ctx& c, Poll<1>& p, int half, unsigned slot, unsigned sig, unsigned iss) {
  if (half == 0) { if (POLL_GATE) sig_wait(c, iss, M_ISS); p.run(c, slot); sig_set(c, sig); STAMPL(sig - 1, 6); } else sig_wait(c, sig);
}

template <int PP, int NSLOT, int NS, int LAG = 1>
struct Ring {
  unsigned voff[PP];
  const char* base;
  unsigned adv, l0;
  int slot;
  DI void init(const char* b, unsigned a, unsigned lds0, int half) {
    base = b - 4096; adv = a; l0 = lds0 + half * PP * 1024; slot = 0;
#pragma unroll
    for (int p = 0; p < PP; ++p) voff[p] += 4096 - (p & 3) * 1024;
  }
  DI void issue() {
    const unsigned m = l0 + slot * (2 * PP * 1024);
    glds4(m, base, voff[0], voff[1], voff[2], voff[3]);
    if constexpr (PP == 5) glds1(m + 4096, base, voff[4]);
    if constexpr (PP == 7) glds3(m + 4096, base, voff[4], voff[5], voff[6]);
    if constexpr (PP == 8) glds4(m + 4096, base, voff[4], voff[5], voff[6], voff[7]);
    base += adv; slot = slot == NSLOT - 1 ? 0 : slot + 1;
  }
  template <int N = NSLOT> DI void fill() {
#pragma unroll 1
    for (int s = 0; s < N; ++s) issue();
  }
  template <int NEED> DI void fill_ahead(const Ctx& c, unsigned sig) {
    int n = 0;
#pragma unroll 1
    while (n < NSLOT) {
      issue(); wait_vm<PACE>(); ++n;
      if (n * PP >= NEED * PP + PACE &&
          __builtin_amdgcn_readfirstlane(__hip_atomic_load((LAS unsigned*)(c.lds + M_SIG), __ATOMIC_RELAXED, __HIP_MEMORY_SCOPE_WORKGROUP)) >= sig) break;
    }
    if (n == NSLOT) wait_vm<(NSLOT - NEED) * PP>();
    BARRIER_L(); WRESET();
#pragma unroll 1
    for (; n < NSLOT; ++n) issue();
  }
  DI void rest() { range<NSLOT - 2, NSLOT - LAG>(); steady(); range<NSLOT - 1 - LAG, 0>(); BARRIER_L(); }
  template <int R, int E> DI void range() { if constexpr (R >= E) { wait_vm<R * PP>(); BARRIER_L(); range<R - 1, E>(); } }
  template <int D0 = NSLOT> DI void head() {
    wait_vm<(D0 - 1) * PP>(); BARRIER_L(); WRESET();
    if constexpr (D0 < NSLOT) fill<NSLOT - D0>();
    range<NSLOT - 2, NSLOT - LAG>();
    steady();
  }
  DI void steady() {
#pragma unroll 1
    for (int k = LAG; k < NS - NSLOT + LAG; ++k) { wait_vm<(NSLOT - 1 - LAG) * PP>(); BARRIER_L(); issue(); }
  }
  template <int D0 = NSLOT> DI void run() { head<D0>(); range<NSLOT - 1 - LAG, 0>(); BARRIER_L(); }
};

DI void ln_rows(const Ctx& c, int row, const f32x2 (&p)[8]) {
  f32x2 t = p[0];
#pragma unroll
  for (int i = 1; i < 8; ++i) t += p[i];
  const float m8 = t[0] * 0.125f; float q = 0.f;
#pragma unroll
  for (int i = 0; i < 8; ++i) { const float a = p[i][0] - m8; q += a * a; }
  const float rstd = __builtin_amdgcn_rsqf((t[1] + q * (1.f / 80.f)) * (1.f / 640.f) + 1e-5f);
  *(LAS float*)(c.lds + M_SST + row * 4) = rstd;
  *(LAS float*)(c.lds + M_SST + 512 + row * 4) = -t[0] * (1.f / 640.f);
}
template <int NP> DI void ln_cols_sum(const Ctx& c, int n4, const u32x4 (&q)[NP]) {
  f32x4 cs = __builtin_bit_cast(f32x4, q[0]);
#pragma unroll
  for (int i = 1; i < NP; ++i) cs += __builtin_bit_cast(f32x4, q[i]);
  if (c.lane < n4) *(LAS f32x4*)(c.lds + M_SCS + c.lane * 16) = cs;
}
template <int NP> DI void ln_cols(const Ctx& c, int n4, unsigned off, unsigned pitch) {
  u32x4 q[NP];
#pragma unroll
  for (int i = 0; i < NP; ++i) q[i] = ld16(c.r, off + i * pitch + (c.lane < n4 ? c.lane : 0) * 16);
  ln_cols_sum<NP>(c, n4, q);
}

#define MAP 0
DI int qkv_panel(const Ctx& c) { return (c.xcd & 3) * 4 + (c.j & 3); }
DI int qkv_cb(const Ctx& c) { return (c.xcd >> 2) * 8 + (c.j >> 2); }
DI int pr_panel(const Ctx& c) { return (c.xcd & 3) * 8 + (c.j & 7); }
DI int pr_cb(const Ctx& c) { return (c.xcd >> 2) * 4 + (c.j >> 3); }
DI int gu_panel(const Ctx& c) { return (c.xcd & 1) * 8 + (c.j & 7); }
DI int gu_cb(const Ctx& c) { return (c.xcd >> 1) * 4 + (c.j >> 3); }
DI int att_head(const Ctx& c) { return c.xcd * 4 + (c.j & 3); }
DI int att_qb(const Ctx& c) { return c.j >> 2; }

struct Phase0 {
  u32x4 v[3], t[10];
  DI void load(const Ctx& c, int hw, const uint16_t* x, const uint16_t* wqkv) {
    const int lane = c.lane, r32 = lane & 31, b = blockIdx.x, row = b * 8 + hw * 2 + (lane >> 5);
    v[0] = *(const u32x4*)(x + row * 640 + r32 * 8); v[1] = *(const u32x4*)(x + row * 640 + 256 + r32 * 8);
    v[2] = *(const u32x4*)(x + row * 640 + 512 + (r32 & 15) * 8);
    if (hw == 3 && b < 240) {
      const rsrc_t r = __builtin_amdgcn_make_buffer_rsrc((void*)wqkv, 0, 640 * 3840, 0x00020000);
#pragma unroll
      for (int i = 0; i < 10; ++i)
        t[i] = __builtin_amdgcn_raw_buffer_load_b128(r, (lane >> 3) * 3840 + (lane & 7) * 16, ((b / 30) * 80 + i * 8) * 3840 + (b % 30) * 128, 0);
    }
  }
  DI void rows(const Ctx& c, int hw) {
    const int lane = c.lane, r32 = lane & 31, row = blockIdx.x * 8 + hw * 2 + (lane >> 5);
    const float w2 = r32 < 16 ? 1.f : 0.f;
    float sum = 0.f, s2 = 0.f;
#pragma unroll
    for (int e = 0; e < 4; ++e) { sum += (bflo(v[0][e]) + bfhi(v[0][e])) + (bflo(v[1][e]) + bfhi(v[1][e])); s2 += bflo(v[2][e]) + bfhi(v[2][e]); }
    sum = sum32(sum + w2 * s2);
    const float mean = sum * (1.f / 640.f); float m2 = 0.f; s2 = 0.f;
#pragma unroll
    for (int e = 0; e < 4; ++e) {
      const float a0 = bflo(v[0][e]) - mean, a1 = bfhi(v[0][e]) - mean, a2 = bflo(v[1][e]) - mean, a3 = bfhi(v[1][e]) - mean;
      const float a4 = bflo(v[2][e]) - mean, a5 = bfhi(v[2][e]) - mean;
      m2 += a0 * a0 + a1 * a1 + a2 * a2 + a3 * a3; s2 += a4 * a4 + a5 * a5;
    }
    m2 = sum32(m2 + w2 * s2);
    if (r32 < 8) st8(c.r, WS_ST + (row >> 7) * 8192 + r32 * 1024 + (row & 127) * 8, u32x2{__float_as_uint(sum * 0.125f), __float_as_uint(m2 * 0.125f)});
  }
  float s[8];
  DI void cols1(int hw) {
    if (hw == 3 && blockIdx.x < 240) {
      unsigned ul = 0x00003f80u, uh = 0x3f800000u;
      asm volatile("" : "+v"(ul), "+v"(uh));
      const bf16x2 lo = __builtin_bit_cast(bf16x2, ul), hi = __builtin_bit_cast(bf16x2, uh);
#pragma unroll
      for (int e = 0; e < 4; ++e) {
        s[2 * e] = 0.f; s[2 * e + 1] = 0.f;
#pragma unroll
        for (int i = 0; i < 10; ++i) {
          const unsigned u = t[i][e];
          s[2 * e] = __builtin_amdgcn_fdot2_f32_bf16(__builtin_bit_cast(bf16x2, u), lo, s[2 * e], false);
          s[2 * e + 1] = __builtin_amdgcn_fdot2_f32_bf16(__builtin_bit_cast(bf16x2, u), hi, s[2 * e + 1], false);
        }
      }
#pragma unroll
      for (int e = 0; e < 8; ++e) PIN(s[e]);
    }
  }
  DI void cols2(const Ctx& c, int hw) {
    const int lane = c.lane, b = blockIdx.x;
    if (hw == 3 && b < 240) {
#pragma unroll
      for (int e = 0; e < 8; ++e) s[e] = rows4(s[e] + ror16<8>(s[e]));
      const int hi = (lane >> 3) & 1;
      const f32x4 o = hi ? f32x4{s[4], s[5], s[6], s[7]} : f32x4{s[0], s[1], s[2], s[3]};
      if (lane < 16) st16(c.r, WS_CSQ + ((b / 30) * 1920 + (b % 30) * 64 + (lane & 7) * 8 + hi * 4) * 4, __builtin_bit_cast(u32x4, o));
    }
  }
};

template <bool SWAP, class PRE>
DI void qkv_loop(const Ctx& c, int w, int layer, f32x16 (&acc)[2][2], PRE pre) {
  const int lane = c.lane, i = lane & 15, g = lane >> 4, wm = w & 1, wn = w >> 1;
  lds_ptr pa[4];
#pragma unroll
  for (int s = 0; s < 4; ++s)
    pa[s] = c.lds + QKV_A + wm * 8192 + (g & 1) * 2048 +
            (layer == 0 ? (i >> 3) * 1024 + (i & 7) * 128 + (((2 * s + (g >> 1)) ^ ((i >> 1) & 7)) << 4) : (2 * s + (g >> 1)) * 256 + i * 16);
  const int rb = ((g >> 1) * 8 + (i >> 2)) * 256 + (i & 3) * 8, u = (wn * 4 + (g & 1)) ^ ((i >> 2) << 1);
  lds_ptr pb0 = c.lds + rb + (u << 5), pb1 = c.lds + rb + ((u ^ 2) << 5);
  bf16x8 x0[4], x1[4], w0[4], w1[4];
  auto rdx = [&](bf16x8* xn, int hs, int e) { xn[e] = ldsr16(pa[hs * 2 + (e >> 1)] + (e & 1) * 4096); };
  auto rdw = [&](bf16x8* wn_, int hs, int e) { wn_[e] = ldstr(((e & 1) ? pb1 : pb0) + (hs * 2 + (e >> 1)) * 4096, 1024); };
  auto step = [&](const bf16x8* xa, const bf16x8* wb, bf16x8* xn, bf16x8* wn_, int hs, bool ld) {
#pragma unroll
    for (int q = 0; q < 8; ++q) {
      if (ld) { if (q & 1) rdx(xn, hs, q >> 1); else rdw(wn_, hs, q >> 1); }
      const int s = q >> 2, tm = q & 1, tn = (q >> 1) & 1;
      acc[tm][tn] = SWAP ? mfma32(wb[s * 2 + tn], xa[s * 2 + tm], acc[tm][tn]) : mfma32(xa[s * 2 + tm], wb[s * 2 + tn], acc[tm][tn]);
      SBAR();
    }
  };
  SLOT(layer * 5);
  if (layer) STAMP(0);
  STAMP(1);
  BARRIER();
  STAMP(2); STAMPC(0); WRESET();
#pragma unroll
  for (int e = 0; e < 4; ++e) { rdw(w0, 0, e); rdx(x0, 0, e); }
  int sa = 0, sb = 0;
#pragma unroll 1
  for (int st = 0; st < 9; ++st) {
    step(x0, w0, x1, w1, 1, true);
    const int da = sa == QKV_NA - 1 ? -(QKV_NA - 1) * 16384 : 16384, db = sb == QKV_NB - 1 ? -(QKV_NB - 1) * 16384 : 16384;
    sa = sa == QKV_NA - 1 ? 0 : sa + 1; sb = sb == QKV_NB - 1 ? 0 : sb + 1;
    pa[0] += da; pa[1] += da; pa[2] += da; pa[3] += da; pb0 += db; pb1 += db;
    BARRIER_NW();
    step(x1, w1, x0, w0, 0, true);
  }
  step(x0, w0, x1, w1, 1, true);
  BARRIER();
  pre(); SBAR();
  step(x1, w1, x0, w0, 0, false);
  STAMP(3); STAMPC(1);
}
DI unsigned qkv_w_off(const Ctx& c, int q) {
  const int row = q * 4 + (c.lane >> 4);
  return row * 3840 + qkv_cb(c) * 256 + (((c.lane & 15) ^ ((row & 3) << 2)) << 4);
}
DI void qkv_helper(Ctx c, u64& tgt, int hw, int layer, const uint16_t* x, const uint16_t* wqkv) {
  const int panel = qkv_panel(c), cb = qkv_cb(c), l = c.lane;
  const bool on = cb != 15;
  if (layer == 0) {
    Phase0 p0;
    p0.load(c, hw, x, wqkv);
    c.tgt = tgt = epoch(c.ws);
    if (on) BARRIER();
    p0.cols1(hw);
    if (on && hw == 3) BARRIER();
    p0.rows(c, hw);
    if (on && hw != 3) BARRIER();
    p0.cols2(c, hw);
    if (on) { BARRIER(); BARRIER(); }
    publish(c, M_CNT2, 4, flg(FL_P0, blockIdx.x));
    if (!on) return;
    BARRIER(); BARRIER();
    const unsigned at = flg(FL_P0, hw < 2 ? panel * 16 + hw * 8 + (l & 7) : (l & 7) * 30 + cb * 2 + ((l >> 3) & 1));
    const u64 v = __hip_atomic_load((const GAS u64*)(c.ws + at), __ATOMIC_RELAXED, __HIP_MEMORY_SCOPE_AGENT);
    BARRIER();
    if (!__all(v == c.tgt)) poll(c, at);
    if (hw < 2) {
      f32x2 p[8];
#pragma unroll
      for (int i = 0; i < 8; ++i) p[i] = __builtin_bit_cast(f32x2, ld8(c.r, WS_ST + panel * 8192 + i * 1024 + (hw * 64 + l) * 8));
      BARRIER(); BARRIER();
      ln_rows(c, hw * 64 + l, p);
    } else if (hw == 2) {
      u32x4 q[8];
#pragma unroll
      for (int i = 0; i < 8; ++i) q[i] = ld16(c.r, WS_CSQ + cb * 512 + i * 7680 + (l & 31) * 16);
      BARRIER(); BARRIER();
      ln_cols_sum<8>(c, 32, q);
    } else { BARRIER(); BARRIER(); }
    BARRIER(); BARRIER();
  } else if (on) {
    if (hw == 2) ln_cols<8>(c, 32, WS_CSQ + cb * 512, 7680);
    BARRIER(); BARRIER(); BARRIER(); BARRIER(); BARRIER(); BARRIER();
    if (hw < 2) {
      f32x2 p[8];
#pragma unroll
      for (int i = 0; i < 8; ++i) p[i] = __builtin_bit_cast(f32x2, ld8(c.r, WS_ST + 2 * STB + panel * 8192 + i * 1024 + (hw * 64 + l) * 8));
      BARRIER(); BARRIER();
      ln_rows(c, hw * 64 + l, p);
    } else { BARRIER(); BARRIER(); }
    BARRIER(); BARRIER(); BARRIER();
  }
}
DI void qkv_compute(Ctx c, u64& tgt, int w, int layer, const uint16_t* x, const uint16_t* wqkv) {
  const int panel = qkv_panel(c), cb = qkv_cb(c);
  if (w >= 4) { qkv_helper(c, tgt, w - 4, layer, x, wqkv); return; }
  if (layer == 0) c.tgt = tgt = epoch(c.ws);
  if (cb == 15) return;
  const int lane = c.lane, r32 = lane & 31, h = lane >> 5, wm = w & 1, wn = w >> 1;
  const unsigned L = WS_L + layer * L_SZ;
  f32x16 acc[2][2] = {{f32x16{}, f32x16{}}, {f32x16{}, f32x16{}}};
  const LAS float* srs = (const LAS float*)(c.lds + M_SST) + wm * 64;
  const LAS float* scs = (const LAS float*)(c.lds + M_SCS) + wn * 64;
  if (cb < 10) {
    float rs[2], nm[2]; f32x4 cs[8];
    qkv_loop<true>(c, w, layer, acc, [&]() {
      rs[0] = srs[r32]; rs[1] = srs[32 + r32]; nm[0] = srs[128 + r32]; nm[1] = srs[160 + r32];
#pragma unroll
      for (int e = 0; e < 8; ++e) cs[e] = *(const LAS f32x4*)(scs + e * 8 + h * 4);
    });
    const float qs = cb < 5 ? 0.22360679774997896f * 1.4426950408889634f : 1.f;
    const unsigned i1 = L + (cb < 5 ? L_Q1 : L_K1), i2 = L + (cb < 5 ? L_Q2 : L_K2);
    rs[0] *= qs; rs[1] *= qs;
#pragma unroll
    for (int tp = 0; tp < 4; ++tp) {
      const int tn = tp >> 1, pr = tp & 1;
      const int o = (cb % 5) * 16 + wn * 8 + tp * 2 + h, hp = o / 5, pos = o - hp * 5;
      const unsigned ts = pos == 2 ? 512 : 1024;
      const unsigned a0 = (pos == 2 ? i2 + hp * 32768 : i1 + (2 * hp + (pos > 2)) * 65536 + (pos > 2 ? pos - 3 : pos) * 512) +
                          (panel * 4 + wm * 2) * ts + r32 * 16;
#pragma unroll
      for (int tm = 0; tm < 2; ++tm) {
        const f32x16 a = acc[tm][tn];
        const f32x4 u = (f32x4{a[pr * 8], a[pr * 8 + 1], a[pr * 8 + 2], a[pr * 8 + 3]} + cs[tp * 2] * nm[tm]) * rs[tm];
        const f32x4 v = (f32x4{a[pr * 8 + 4], a[pr * 8 + 5], a[pr * 8 + 6], a[pr * 8 + 7]} + cs[tp * 2 + 1] * nm[tm]) * rs[tm];
        st16(c.r, a0 + tm * ts, pair32(pk2(u[0], u[1]), pk2(u[2], u[3]), pk2(v[0], v[1]), pk2(v[2], v[3])));
      }
    }
  } else {
    float cs[2]; f32x4 rn[2][4];
    auto rd = [&](int tp) {
#pragma unroll
      for (int e = 0; e < 4; ++e) rn[tp & 1][e] = *(const LAS f32x4*)(srs + (e >> 1) * 128 + tp * 16 + (e & 1) * 8 + h * 4);
    };
    qkv_loop<false>(c, w, layer, acc, [&]() { cs[0] = scs[r32]; cs[1] = scs[32 + r32]; rd(0); rd(1); });
    unsigned a0[2];
#pragma unroll
    for (int tn = 0; tn < 2; ++tn) {
      const int cv = (cb - 10) * 128 + wn * 64 + tn * 32 + r32, head = cv / 20, dim = cv - head * 20;
      const int d = (head & 1) ? (dim < 4 ? dim + 16 : dim - 4) : dim;
      a0[tn] = L + L_V + head * 81920 + (panel * 8 + wm * 4) * 640 + h * 320 + d * 16;
    }
#pragma unroll
    for (int tp = 0; tp < 4; ++tp) {
      const int tm = tp >> 1, pr = tp & 1;
      u32x4 o[2];
#pragma unroll
      for (int tn = 0; tn < 2; ++tn) {
        const f32x16 a = acc[tm][tn];
        const f32x4 u = (f32x4{a[pr * 8], a[pr * 8 + 1], a[pr * 8 + 2], a[pr * 8 + 3]} + rn[pr][2] * cs[tn]) * rn[pr][0];
        const f32x4 v = (f32x4{a[pr * 8 + 4], a[pr * 8 + 5], a[pr * 8 + 6], a[pr * 8 + 7]} + rn[pr][3] * cs[tn]) * rn[pr][1];
        o[tn] = pair32(pk2(u[0], u[1]), pk2(u[2], u[3]), pk2(v[0], v[1]), pk2(v[2], v[3]));
      }
      if (tp < 2) rd(tp + 2);
      st16(c.r, a0[0] + tp * 640, o[0]); st16(c.r, a0[1] + tp * 640, o[1]);
      SBAR();
    }
  }
  SLOT(layer * 5);
  STAMP(4);
  publish(c, M_CNT4, 4, flg(FL_QKV + layer * 5, panel * 16 + cb), true);
  STAMP(5);
}
typedef Ring<8, QKV_NB, 10, 2> QkvB;
DI void qkv_load0(const Ctx& c, int lw, const uint16_t* x, const uint16_t* wqkv) {
  const int half = lw & 1;
  if (lw >= 2) {
    QkvB rg;
#pragma unroll
    for (int p = 0; p < 8; ++p) rg.voff[p] = qkv_w_off(c, half * 8 + p);
    rg.init((const char*)wqkv, 64 * 3840, 0, half);
    rg.template fill<2>(); rg.template run<2>();
  } else {
    Ring<8, QKV_NA, 10, 2> rg;
#pragma unroll
    for (int p = 0; p < 8; ++p) {
      const int mt = qkv_panel(c) * 8 + half * 4 + (p >> 1), r16 = (p & 1) * 8 + (c.lane >> 3);
      rg.voff[p] = (mt * 16 + r16) * 1280 + (((c.lane & 7) ^ ((r16 >> 1) & 7)) << 4);
    }
    rg.init((const char*)x, 128, QKV_A, half);
    rg.template fill<2>(); rg.template run<2>();
  }
}
DI void qkv_load1(const Ctx& c, int lw, const uint16_t* wqkv) {
  const int half = lw & 1;
  static_assert(QKV_NA == 4 && QKV_NB == 5, "schedule");
  if (lw >= 2) {
    QkvB rg;
#pragma unroll
    for (int p = 0; p < 8; ++p) rg.voff[p] = qkv_w_off(c, half * 8 + p);
    rg.init((const char*)wqkv, 64 * 3840, 0, half);
    sig_wait(c, 12, M_CNT4);
    rg.template fill_ahead<1>(c, 6);
    rg.rest();
  } else {
    Ring<8, QKV_NA, 10, 2> rg;
#pragma unroll
    for (int p = 0; p < 8; ++p) rg.voff[p] = WS_X2 + (qkv_panel(c) * 8 + half * 4 + (p >> 1)) * 20480 + (p & 1) * 1024 + c.lane * 16;
    rg.init(c.ws, 2048, QKV_A, half);
    Poll<1> pl;
    gate(c, pl, half, flg(FL_DN, qkv_panel(c) * 16 + (c.lane & 15)), 6, 28);
    rg.template fill<2>(); rg.template run<2>();
    pl.keep();
  }
}

DI f32x16 qk8(s16x4 k2, s16x4 q2) {
 return __builtin_amdgcn_mfma_f32_32x32x8bf16_1k(k2, q2, f32x16{}, 0, 0, 0); }
DI f32x16 qk16(bf16x8 k1, bf16x8 q1, f32x16 s) { return __builtin_amdgcn_mfma_f32_32x32x16_bf16(k1, q1, s, 0, 0, 0); }
DI f32x16 pv_lo(bf16x8 v, f32x16 p, f32x16 o) {
  return __builtin_amdgcn_smfmac_f32_32x32x32_bf16(v, __builtin_bit_cast(bf16x16, (f32x8)__builtin_shufflevector(p, p, 0, 1, 2, 3, 4, 5, 6, 7)), o, 0xDDDD, 0, 0);
}
DI f32x16 pv_hi(bf16x8 v, f32x16 p, f32x16 o) {
  return __builtin_amdgcn_smfmac_f32_32x32x32_bf16(v, __builtin_bit_cast(bf16x16, (f32x8)__builtin_shufflevector(p, p, 8, 9, 10, 11, 12, 13, 14, 15)), o, 0xDDDD, 0, 0);
}
DI float swap32(float v, int h) {
  auto r = __builtin_amdgcn_permlane32_swap(__float_as_uint(v), __float_as_uint(v), false, false);
  return __uint_as_float(h ? r[0] : r[1]);
}
#define EXP4(X, i) do { X[i] = __builtin_amdgcn_exp2f(X[i]); X[i + 1] = __builtin_amdgcn_exp2f(X[i + 1]); X[i + 2] = __builtin_amdgcn_exp2f(X[i + 2]); \
                        X[i + 3] = __builtin_amdgcn_exp2f(X[i + 3]); PIN(X); SBAR(); } while (0)
DI void att_compute(const Ctx& c, int w, int layer) {
  const int lane = c.lane, head = att_head(c), qb = att_qb(c);
  const int qp = w & 3, kp = w >> 2, r32 = lane & 31, h = lane >> 5;
  lds_ptr lds = c.lds;
  const unsigned L = WS_L + layer * L_SZ;
  SLOT(layer * 5 + 1);
  STAMP(0);
  for (int s = w * 64 + lane; s < 672; s += 512) {
    const unsigned v = s % 20 == 0 ? 0x3f803f80u : 0u;
    *(LAS u32x4*)(lds + AT_CONST + s * 16) = u32x4{v, v, v, v};
  }
  lds_ptr pk1 = lds + kp * 8192 + lane * 16;
  lds_ptr pk2_ = h ? lds + AT_CONST + 16 : lds + 16384 + kp * 4096 + r32 * 16 + (head & 1) * 8;
  lds_ptr pv = (r32 >= 20 ? lds + AT_CONST : lds + 24576 + kp * 10240) + h * 320 + r32 * 16;
  const int vmask = r32 >= 20 ? 0 : -1, kmask = h ? 0 : -1;
  f32x16 o0 = f32x16{}, o1 = f32x16{};
  f32x16 s0, s1, s2, s3 = f32x16{};
  STAMP(1);
  BARRIER();
  STAMP(2); STAMPC(0); WRESET();
  const bf16x8 q1a = ldsr16(lds + 2 * AT_SLOT + qp * 2048 + lane * 16), q1b = ldsr16(lds + 2 * AT_SLOT + qp * 2048 + 1024 + lane * 16);
  lds_ptr pq2 = h ? lds + AT_CONST + 16 : lds + 2 * AT_SLOT + 8192 + qp * 1024 + r32 * 16 + (head & 1) * 8;
  const s16x4 q2a = *(const LAS s16x4*)pq2, q2b = *(const LAS s16x4*)(pq2 + 512);
  bf16x8 k1 = ldsr16(pk1), vlo = ldsr16(pv), vhi = bf16x8{};
  s16x4 k2 = *(const LAS s16x4*)pk2_;
  s0 = qk16(k1, q1a, qk8(k2, q2a));
#define PH_EVEN(SC, SN, SP, KLOAD, VOFF) do { \
    SN = qk8(k2, q2b); SBAR(); EXP4(SC, 0); SN = qk16(k1, q1b, SN); SBAR(); KLOAD; SBAR(); EXP4(SC, 4); \
    o1 = pv_hi(vhi, SP, o1); SBAR(); vhi = ldsr16(pv + (VOFF) + 640); SBAR(); EXP4(SC, 8); o0 = pv_lo(vlo, SC, o0); SBAR(); EXP4(SC, 12); } while (0)
#define PH_ODD(SC, SN, SP, VLOAD) do { \
    SN = qk8(k2, q2a); SBAR(); EXP4(SC, 0); SN = qk16(k1, q1a, SN); SBAR(); EXP4(SC, 4); \
    o0 = pv_hi(vhi, SP, o0); SBAR(); EXP4(SC, 8); o1 = pv_lo(vlo, SC, o1); SBAR(); VLOAD; SBAR(); EXP4(SC, 12); } while (0)
#define KLD(t) do { k1 = ldsr16(pk1 + (t) * 1024); k2 = *(const LAS s16x4*)(pk2_ + (t) * 512); } while (0)
#define PRI(v) do { asm volatile("s_cmp_eq_u32 %0, 0\n\ts_cbranch_scc1 1f\n\ts_setprio " #v "\n1:" :: "s"(kp) : "scc"); SBAR(); } while (0)
#define HI(n) if (ATT_PRIO == (n)) PRI(1);
#define LO(n) if (ATT_PRIO == (n)) PRI(0);
#define TWO(t) HI(3) HI(4) HI(5) PH_EVEN(s0, s1, s3, KLD((t) + 1), (t) * 1280); PH_ODD(s1, s2, s0, vlo = ldsr16(pv + ((t) + 1) * 1280)); LO(4)
  for (int grp = 0; grp < 4; ++grp) {
    TWO(0) PH_EVEN(s2, s3, s1, if (grp == 0) BARRIER_NW(); KLD(2), 1280); LO(3) PH_ODD(s3, s0, s2, vlo = ldsr16(pv + 2 * 1280));
    TWO(2) PH_EVEN(s2, s3, s1, KLD(4), 3 * 1280); LO(3) LO(5) PH_ODD(s3, s0, s2, vlo = ldsr16(pv + 4 * 1280));
    TWO(4) PH_EVEN(s2, s3, s1, KLD(6), 5 * 1280); LO(3) PH_ODD(s3, s0, s2, vlo = ldsr16(pv + 6 * 1280));
    TWO(6)
    const int d = grp == 2 ? -2 * AT_SLOT : grp == 3 ? 0 : AT_SLOT;
    PH_EVEN(s2, s3, s1, pk1 += d; pk2_ += (d & kmask); if (grp != 3) BARRIER(); KLD(0), 7 * 1280);
    LO(3) LO(5) PH_ODD(s3, s0, s2, pv += (d & vmask); vlo = ldsr16(pv));
  }
  o1 = pv_hi(vhi, s3, o1);
  STAMP(3); STAMPC(1);
  __builtin_amdgcn_s_setprio(0);
  BARRIER();
  const f32x16 give = kp ? o0 : o1, keep = kp ? o1 : o0;
#pragma unroll
  for (int e = 0; e < 3; ++e)
    *(LAS f32x4*)(lds + AT_MERGE + (w * 3 + e) * 1024 + lane * 16) = f32x4{give[4 * e], give[4 * e + 1], give[4 * e + 2], give[4 * e + 3]};
  BARRIER();
  f32x4 a[3];
#pragma unroll
  for (int e = 0; e < 3; ++e)
    a[e] = f32x4{keep[4 * e], keep[4 * e + 1], keep[4 * e + 2], keep[4 * e + 3]} + *(const LAS f32x4*)(lds + AT_MERGE + ((w ^ 4) * 3 + e) * 1024 + lane * 16);
  const int qt = qb * 8 + qp * 2 + kp;
  float l = h ? a[2][0] : 0.f;
  l += swap32(l, h);
  if (__builtin_expect(__any(!(l >= 5.421e-20f && l <= 1.8447e19f)), 0)) {
    const unsigned o2 = (head >> 1) * 32768 + r32 * 16 + (head & 1) * 8;
    const bf16x8 fq1 = __builtin_bit_cast(bf16x8, ld16(c.r, L + L_Q1 + head * 65536 + qt * 1024 + lane * 16));
    u32x2 fq2 = ld8(c.r, L + L_Q2 + o2 + qt * 512);
    if (h) fq2 = u32x2{0u, 0u};
    const unsigned vi = L + L_V + head * 81920 + h * 320 + (r32 < 20 ? r32 : 0) * 16;
    f32x16 oo = f32x16{};
    float m = -INFINITY;
    const u32x4 one8 = {0x3f803f80u, 0x3f803f80u, 0x3f803f80u, 0x3f803f80u};
    for (int t = 0; t < 64; ++t) {
      const u32x4 fk1 = ld16(c.r, L + L_K1 + head * 65536 + t * 1024 + lane * 16);
      const u32x2 fk2 = ld8(c.r, L + L_K2 + o2 + t * 512);
      u32x4 v0 = ld16(c.r, vi + t * 1280), v1 = ld16(c.r, vi + t * 1280 + 640);
      if (r32 >= 20) { v0 = one8; v1 = one8; }
      f32x16 s = qk16(__builtin_bit_cast(bf16x8, fk1), fq1, qk8(__builtin_bit_cast(s16x4, fk2), __builtin_bit_cast(s16x4, fq2)));
      float tm = s[0];
#pragma unroll
      for (int r = 1; r < 16; ++r) tm = fmaxf(tm, s[r]);
      tm = fmaxf(tm, swap32(tm, h));
      const float mn = fmaxf(m, tm), al = __builtin_amdgcn_exp2f(m - mn);
      m = mn;
#pragma unroll
      for (int r = 0; r < 16; ++r) { s[r] = __builtin_amdgcn_exp2f(s[r] - mn); oo[r] *= al; }
      oo = pv_hi(__builtin_bit_cast(bf16x8, v1), s, pv_lo(__builtin_bit_cast(bf16x8, v0), s, oo));
    }
#pragma unroll
    for (int e = 0; e < 3; ++e) a[e] = f32x4{oo[4 * e], oo[4 * e + 1], oo[4 * e + 2], oo[4 * e + 3]};
    l = h ? a[2][0] : 0.f;
    l += swap32(l, h);
  }
  const float inv = 1.f / l;
  const unsigned A0 = pk2(a[0][0] * inv, a[0][1] * inv), A1 = pk2(a[0][2] * inv, a[0][3] * inv);
  const unsigned B0 = pk2(a[1][0] * inv, a[1][1] * inv), B1 = pk2(a[1][2] * inv, a[1][3] * inv);
  const u32x2 C = {pk2(a[2][0] * inv, a[2][1] * inv), pk2(a[2][2] * inv, a[2][3] * inv)};
  const int tok = qt * 32 + r32;
  const unsigned ob = L + L_O + (tok >> 4) * 20480;
  st16(c.r, ob + (head * 2 + h) * 256 + (tok & 15) * 16, pair32(A0, A1, B0, B1));
  if (!h) st8(c.r, ob + 16384 + head * 128 + (tok & 15) * 8, C);
  STAMP(4);
  publish(c, M_CNT, 8, flg(FL_ATT + layer * 5, qb * 32 + head), true);
  STAMP(5);
}
DI void retile_load(const Ctx& c, int J, u32x4 (&v)[5][4], const uint16_t* wo, const uint16_t* wg, const uint16_t* wu, const uint16_t* wd) {
  const int e = J < 216 ? J : J < 256 ? J - 216 : J - 256;
  const int strip = J < 216 ? (e >> 2) % 27 : e % 10, k0 = J < 216 ? (e & 3) * 5 : min((e / 10) * 5, 49), ld = J < 216 ? 3456 : 1280;
  const rsrc_t r = __builtin_amdgcn_make_buffer_rsrc((void*)(J < 108 ? wg : J < 216 ? wu : J < 256 ? wo : wd), 0,
                                                     J < 216 ? 640 * 3456 : J < 256 ? 640 * 1280 : 1728 * 1280, 0x00020000);
  const unsigned vo = (c.lane >> 3) * ld + (c.lane & 7) * 16;
#pragma unroll
  for (int u = 0; u < 5; ++u)
#pragma unroll
    for (int q = 0; q < 4; ++q) {
      const int ch = (k0 + u) * 4 + q;
      const int row = J < 216 || J >= 256 ? ch * 8 : ch < 64 ? (ch >> 1) * 20 + (ch & 2) * 2 + (ch & 1) * 8 : (ch - 64) * 40 + 16;
      v[u][q] = __builtin_amdgcn_raw_buffer_load_b128(r, vo, row * ld + strip * 128, 0);
    }
}
DI void retile_store(const Ctx& c, int J, lds_ptr scr, const u32x4 (&v)[5][4]) {
  const int e = J < 216 ? J : J < 256 ? J - 216 : J - 256, mat = J >= 108;
  const int strip = J < 216 ? (e >> 2) % 27 : e % 10, k0 = J < 216 ? (e & 3) * 5 : min((e / 10) * 5, 49);
  const unsigned ts = J < 216 ? 40960 : J < 256 ? 20480 : 55296;
  const unsigned dst = (J < 216 ? WS_WGU + mat * 20480 : J < 256 ? WS_WO : WS_WD) + strip * 4 * ts + k0 * 1024;
  const int i = c.lane & 15, g = c.lane >> 4;
  const bf16x8 ones = __builtin_bit_cast(bf16x8, u32x4{0x3f803f80u, 0x3f803f80u, 0x3f803f80u, 0x3f803f80u});
  f32x4 cs[4] = {f32x4{}, f32x4{}, f32x4{}, f32x4{}};
#pragma unroll
  for (int u = 0; u < 5; ++u) {
#pragma unroll
    for (int p = 0; p < 4; ++p) *(LAS u32x4*)(scr + p * 1024 + c.lane * 16) = v[u][p];
#pragma unroll
    for (int t = 0; t < 4; ++t) {
      const bf16x8 f = ldstr(scr + (g * 8 + (i >> 2)) * 128 + t * 32 + (i & 3) * 8, 512);
      __builtin_amdgcn_raw_buffer_store_b128(__builtin_bit_cast(u32x4, f), c.r, c.lane * 16, dst + u * 1024 + t * ts, 16);
      cs[t] = mfma16(f, ones, cs[t]);
    }
  }
  if (J < 216 && i == 0) {
#pragma unroll
    for (int t = 0; t < 4; ++t)
      __builtin_amdgcn_raw_buffer_store_b128(__builtin_bit_cast(u32x4, cs[t]), c.r, g * 16, WS_CSG + ((e & 3) * 3456 + (strip * 8 + t * 2 + mat) * 16) * 4, 16);
  }
}
DI int early_piece(int e) { return e < 4 ? (e & 1) + (e >> 1) * 8 : e < 6 ? 16 + (e - 4) * 4 : e < 9 ? 18 + e : 25 + e; }
DI int late_piece(int e) { return e < 6 ? 2 + e : e < 12 ? 4 + e : e < 15 ? 5 + e : e < 18 ? 6 + e : e < 25 ? 9 + e : 12 + e; }
DI void att_load(const Ctx& c, int lw, int layer, unsigned iss, const uint16_t* wo, const uint16_t* wg, const uint16_t* wu, const uint16_t* wd) {
  const int head = att_head(c), qb = att_qb(c), lane = c.lane;
  const char* Lp = c.ws + WS_L + layer * L_SZ;
  const unsigned vo = lane * 16;
  auto src_of = [&](int p) {
    return p < 16 ? L_K1 + head * 65536 + p * 1024 : p < 24 ? L_K2 + (head >> 1) * 32768 + (p - 16) * 1024 : L_V + head * 81920 + (p - 24) * 1024;
  };
  unsigned v0[14], m0[11], vg[11], sg[11];
#pragma unroll
  for (int q = 0; q < 11; ++q) {
    const int p = q < 3 ? early_piece(lw * 3 + q) : late_piece(lw * 8 + q - 3), pg = lw * 11 + q;
    v0[q] = src_of(p) + vo; m0[q] = p * 1024;
    vg[q] = src_of(pg) + vo; sg[q] = pg < 16 ? 16384 : pg < 24 ? 8192 : 20480;
    asm volatile("" : "+v"(v0[q]), "+s"(m0[q]));
  }
#pragma unroll
  for (int q = 0; q < 3; ++q) {
    const int p = lw * 3 + q;
    v0[11 + q] = (p < 8 ? L_Q1 + head * 65536 + qb * 8192 + p * 1024 : L_Q2 + (head >> 1) * 32768 + qb * 4096 + (p - 8) * 1024) + vo - q * 1024;
    PIN(v0[11 + q]);
  }
  auto issue = [&](int slot) {
#pragma unroll
    for (int q = 0; q < 11; ++q) { vg[q] += sg[q]; glds1(slot * AT_SLOT + (lw * 11 + q) * 1024, Lp, vg[q]); }
  };
  Poll<2> pl;
  if (lw == 0) {
    const int o0 = (head >> 1) * 5 + (head & 1) * 2, v0 = head * 20;
    if (POLL_GATE) sig_wait(c, iss, M_ISS);
    const int sel = lane >> 4, cb = sel == 0 ? 5 + (o0 >> 4) : sel == 1 ? 5 + ((o0 + 2) >> 4) : sel == 2 ? 10 + (v0 >> 7) : 10 + ((v0 + 19) >> 7);
    pl.run(c, flg(FL_QKV + layer * 5, (lane & 15) * 16 + cb), flg(FL_QKV + layer * 5, (qb * 2 + (lane & 1)) * 16 + ((o0 + (lane & 2)) >> 4)));
    sig_set(c, layer * 5 + 2);
    STAMPL(layer * 5 + 1, 6);
  } else sig_wait(c, layer * 5 + 2);
  glds3(2 * AT_SLOT + lw * 3072, Lp, v0[11], v0[12], v0[13]);
#pragma unroll
  for (int q = 0; q < 3; ++q) glds1(m0[q], Lp, v0[q]);
  wait_vm<0>(); BARRIER();
#pragma unroll
  for (int q = 3; q < 11; ++q) glds1(m0[q], Lp, v0[q]);
  pl.keep();
  wait_vm<0>(); BARRIER();
  issue(1);
  const int J = (lw - 1) * 256 + blockIdx.x;
  const bool rt = layer == 0 && lw > 0 && J < 366;
  u32x4 v[5][4];
  if (rt) { retile_load(c, J, v, wo, wg, wu, wd); wait_vm<20>(); } else wait_vm<0>();
  BARRIER();
  issue(2);
  if (rt) retile_store(c, J, c.lds + AT_SCR + (lw - 1) * 4096, v);
  if (layer == 0 && (lw == 1 || lw == 2)) publish(c, M_CNT3, 2, flg(FL_RT, blockIdx.x));
  wait_vm<0>(); BARRIER();
  issue(0);
  wait_vm<0>();
  if (layer == 0) {
    if (lw == 0) {
      if (c.j == 0) {
#pragma unroll 1
        for (int q = 0; q < 2; ++q) { pl.run(c, flg(FL_RT, q * 128 + lane), flg(FL_RT, q * 128 + 64 + lane)); wait_vm<0>(); pl.keep(); }
        if (lane < 32) __hip_atomic_store((GAS u64*)(c.ws + flg(FL_RT2, lane * 8 + c.xcd)), c.tgt, __ATOMIC_RELAXED, __HIP_MEMORY_SCOPE_AGENT);
      }
      poll(c, flg(FL_RT2, blockIdx.x));
      if (blockIdx.x == 0 && lane == 0) *(u64*)(c.ws + WS_EPOCH) = c.tgt;
    }
  }
  BARRIER();
  u32x4 pk[25];
#pragma unroll
  for (int e = 0; e < 25; ++e) pk[e] = ld16s(c.r, vo, WS_WO + (pr_cb(c) * 5 + e % 5) * 20480 + (lw * 5 + e / 5) * 1024);
#pragma unroll 1
  for (unsigned q = lw; q < 35; q += 4) glds1(AT_SCR + lw * 1024, c.ws + WS_WGU + ((gu_cb(c) * 27) >> 2) * 40960 + ((c.j & 7) * 35 + q) * 1024, vo);
  wait_vm<0>();
  BARRIER(); BARRIER();
  sig_wait(c, layer * 16 + 8, M_CNT);
#pragma unroll
  for (int e = 0; e < 25; ++e) { const int k = lw * 5 + e / 5; ldsw16(c.lds + (k >> 1) * 10240 + (k & 1) * 1024 + (e % 5) * 2048 + vo, pk[e]); }
}

template <int NS, int NB, int WIN>
DI void proj_compute(const Ctx& c, int w, int slot_id, unsigned a_off, const char* res_rm, unsigned res_off, unsigned out_off, unsigned st_off,
                     uint16_t* out, int arr) {
  constexpr bool RES = NS <= NB;
  const int panel = pr_panel(c), cb = pr_cb(c);
  SLOT(slot_id);
  STAMP(0);
  if (w >= 4) {
    if constexpr (RES) { BARRIER(); BARRIER(); }
    else { BARRIER(); BARRIER(); BARRIER(); BARRIER(); BARRIER(); BARRIER(); BARRIER(); }
    return;
  }
  const int lane = c.lane, i = lane & 15, g = lane >> 4, mt = panel * 4 + w;
  u32x2 res[5];
#pragma unroll
  for (int t = 0; t < 5; ++t)
    res[t] = res_rm ? *(const u32x2*)(res_rm + (mt * 16 + i) * 1280 + cb * 160 + t * 32 + g * 8)
                    : ld8(c.r, res_off + (mt * 80 + cb * 10 + t * 2 + (g >> 1)) * 256 + i * 16 + (g & 1) * 8);
  f32x4 acc[5];
#pragma unroll
  for (int t = 0; t < 5; ++t) { acc[t] = f32x4{bflo(res[t][0]), bfhi(res[t][0]), bflo(res[t][1]), bfhi(res[t][1])}; PIN(acc[t]); }
  lds_ptr pb = c.lds + lane * 16;
  unsigned av = a_off + mt * (NS * 2048) + lane * 16;
  u32x4 a[WIN];
  bf16x8 f[3][5];
  int sb = 0;
  STAMP(1);
  BARRIER();
  STAMP(2); STAMPC(0); WRESET();
  constexpr int AH = RES ? WO_AH : DN_AH;
  auto lda = [&](int k) {
    if (RES && k >= 16) {
      const unsigned o = a_off + mt * 20480 + 16384 + (k - 16) * 1024 + g * 256 + i * 8;
      const u32x2 lo = ld8(c.r, o), hi = ld8(c.r, o + 128);
      a[k % WIN] = u32x4{lo[0], lo[1], hi[0], hi[1]};
    } else a[k % WIN] = ld16(c.r, av + k * 1024);
  };
#pragma unroll
  for (int k = 0; k < AH; ++k) lda(k);
#pragma unroll
  for (int t = 0; t < 5; ++t) { f[0][t] = ldsr16(pb + t * 2048); f[1][t] = ldsr16(pb + t * 2048 + 1024); }
  SBAR();
  auto window = [&](bool first, bool last) {
#pragma unroll
    for (int k = 0; k < WIN; ++k) {
      const bool ld = !(last && k + 2 >= WIN);
      if ((k & 1) == 0) {
        if (RES) pb += 10240;
        else { pb += sb == NB - 1 ? -(NB - 1) * 10240 : 10240; sb = sb == NB - 1 ? 0 : sb + 1; }
        if (!ld) BARRIER(); else if (!RES && (k == 10 || (k == 0 && !first))) BARRIER_NW();
      }
#pragma unroll
      for (int t = 0; t < 5; ++t) {
        if (ld) f[(k + 2) % 3][t] = ldsr16(pb + t * 2048 + (k & 1) * 1024);
        acc[t] = mfma16(f[k % 3][t], __builtin_bit_cast(bf16x8, a[k]), acc[t]);
        SBAR();
      }
      if (!last || k + AH < WIN) { lda(k + AH); SBAR(); }
    }
    av += WIN * 1024;
  };
  static_assert(2 * NS % WIN == 0 && (2 * NS == WIN || WIN % 6 == 0), "window");
#pragma unroll 1
  for (int b = 1; b < 2 * NS / WIN; ++b) window(b == 1, false);
  window(false, true);
  STAMP(3); STAMPC(1);
  u32x2 o[5];
#pragma unroll
  for (int t = 0; t < 5; ++t) {
    o[t][0] = pk2(acc[t][0], acc[t][1]); o[t][1] = pk2(acc[t][2], acc[t][3]);
  }
  const u32x4 p0 = pair16(o[0][0], o[0][1], o[1][0], o[1][1]), p1 = pair16(o[2][0], o[2][1], o[3][0], o[3][1]),
              p2 = pair16(o[4][0], o[4][1], o[4][0], o[4][1]);
  const int ch = (g & 1) * 2 + (g >> 1);
  if (out) {
    char* po = (char*)out + (mt * 16 + i) * 1280 + cb * 160;
    *(u32x4*)(po + ch * 16) = p0; *(u32x4*)(po + 64 + ch * 16) = p1;
    if (!(g & 1)) *(u32x4*)(po + 128 + (g >> 1) * 16) = p2;
    STAMP(4);
    return;
  }
  const unsigned xo = out_off + (mt * 80 + cb * 10) * 256 + i * 16;
  st16(c.r, xo + ch * 256, p0); st16(c.r, xo + 1024 + ch * 256, p1);
  if (!(g & 1)) st16(c.r, xo + 2048 + (g >> 1) * 256, p2);
  const f32x4 s4 = (acc[0] + acc[1]) + (acc[2] + acc[3]) + acc[4];
  const float sum = rows4((s4[0] + s4[1]) + (s4[2] + s4[3])), mean = sum * (1.f / 80.f);
  f32x4 q4 = {0.f, 0.f, 0.f, 0.f};
#pragma unroll
  for (int t = 0; t < 5; ++t) { const f32x4 d = acc[t] - mean; q4 += d * d; }
  const float m2 = rows4((q4[0] + q4[1]) + (q4[2] + q4[3]));
  if (g == 0) st8(c.r, st_off + (mt >> 3) * 8192 + cb * 1024 + ((mt & 7) * 16 + i) * 8, u32x2{__float_as_uint(sum), __float_as_uint(m2)});
  STAMP(4);
  publish(c, M_CNT4, 4, flg(arr, panel * 8 + cb), true);
  STAMP(5);
}
template <int NF>
DI void gu_consumer(const Ctx& c, int layer, int f0, int mq) {
  const int panel = gu_panel(c), ft0 = (gu_cb(c) * 27) >> 2;
  const int lane = c.lane, r32 = lane & 31, h = lane >> 5;
  f32x16 acc[NF];
#pragma unroll
  for (int t = 0; t < NF; ++t) acc[t] = f32x16{};
  const int lo = (r32 >> 4) * 1024 + h * 256 + (r32 & 15) * 16;
  lds_ptr pa = c.lds + GU_A + mq * 2048 + lo, pb = c.lds + f0 * 2048 + lo;
  bf16x8 x0[2], x1[2], wa[NF], wb0[NF], wb1[NF];
  auto step = [&](const bf16x8* xa, const bf16x8* wb, bf16x8* xn, bf16x8* wn_, bool ld) {
#pragma unroll
    for (int q = 0; q < 2 * NF; ++q) {
      if (ld) {
        if (q == 0) { xn[0] = ldsr16(pa); xn[1] = ldsr16(pa + 512); }
        if (q >= 1 && q <= NF) wn_[q - 1] = ldsr16(pb + (q - 1) * 2048 + 512);
        if (q >= NF) wa[q - NF] = ldsr16(pb + (q - NF) * 2048);
      }
      if (q < NF) acc[q] = mfma32(wa[q], xa[0], acc[q]); else acc[q - NF] = mfma32(wb[q - NF], xa[1], acc[q - NF]);
      SBAR();
    }
  };
  static_assert(GU_NA == GU_NB, "one slot counter");
  int sl = 0;
  auto adv = [&]() {
    pa += sl == GU_NA - 1 ? -(GU_NA - 1) * 8192 : 8192; pb += sl == GU_NB - 1 ? -(GU_NB - 1) * 14336 : 14336;
    sl = sl == GU_NA - 1 ? 0 : sl + 1;
  };
  SLOT(layer * 5 + 3);
  STAMP(0);
  STAMP(1);
  BARRIER();
  STAMP(2); STAMPC(0); WRESET();
  x0[0] = ldsr16(pa); x0[1] = ldsr16(pa + 512);
#pragma unroll
  for (int t = 0; t < NF; ++t) { wa[t] = ldsr16(pb + t * 2048); wb0[t] = ldsr16(pb + t * 2048 + 512); }
#pragma unroll 1
  for (int st = 0; st < 9; ++st) {
    adv(); BARRIER_NW(); step(x0, wb0, x1, wb1, true);
    adv(); BARRIER_NW(); step(x1, wb1, x0, wb0, true);
  }
  adv(); BARRIER_NW(); step(x0, wb0, x1, wb1, true);
  BARRIER();
  const LAS float* srs = (const LAS float*)(c.lds + M_SST) + mq * 32 + r32;
  const LAS float* scs = (const LAS float*)(c.lds + M_SCS) + f0 * 32 + h * 4;
  float rs = srs[0], nm = srs[128];
  f32x4 cs[2][4];
#pragma unroll
  for (int e = 0; e < 8; ++e) cs[e >> 2][e & 3] = *(const LAS f32x4*)(scs + e * 8);
  SBAR();
  step(x1, wb1, x0, wb0, false);
  STAMP(3); STAMPC(1);
  rs *= rs;
  const unsigned ho = WS_L + layer * L_SZ + L_H + ((panel * 8 + mq * 2 + (r32 >> 4)) * 216 + (ft0 + f0) * 2 + h) * 256 + (r32 & 15) * 16;
#pragma unroll
  for (int f = 0; f < NF; ++f) {
    unsigned p[2][2];
#pragma unroll
    for (int e = 0; e < 2; ++e) {
      const f32x16 a = acc[f];
      f32x4 gt = f32x4{a[e * 4], a[e * 4 + 1], a[e * 4 + 2], a[e * 4 + 3]} + cs[f & 1][e] * nm;
      const f32x4 up = (f32x4{a[8 + e * 4], a[9 + e * 4], a[10 + e * 4], a[11 + e * 4]} + cs[f & 1][2 + e] * nm) * rs;
      gt = __builtin_elementwise_max(gt, f32x4{0.f, 0.f, 0.f, 0.f}) * up;
      p[e][0] = pk2(gt[0], gt[1]); p[e][1] = pk2(gt[2], gt[3]);
    }
    if (f + 2 < NF) {
#pragma unroll
      for (int e = 0; e < 4; ++e) cs[f & 1][e] = *(const LAS f32x4*)(scs + (f + 2) * 32 + e * 8);
    }
    st16(c.r, ho + f * 512, pair32(p[0][0], p[0][1], p[1][0], p[1][1]));
    SBAR();
  }
  STAMP(4);
  publish(c, M_CNT, 8, flg(FL_GU + layer * 5, panel * 16 + gu_cb(c)), true);
  STAMP(5);
}
DI void mlp_load(const Ctx& c0, int lw, int layer, unsigned iss, bool qkv_next, const uint16_t* wqkv) {
  const int half = lw & 1;
  {
    const Ctx c = fresh(c0);
    if (lw == 0) { poll(c, flg(FL_ATT + layer * 5, (pr_panel(c) >> 2) * 32 + (c.lane & 31))); STAMPL(layer * 5 + 2, 6); }
    BARRIER_L(); BARRIER_L();
  }
  { const int w = lw + 8; (void)w; WDUMP(layer * 5 + 2); }
  {
    const Ctx c = fresh(c0);
    const int ft0 = (gu_cb(c) * 27) >> 2;
    static_assert(GU_NA == 7 && GU_NB == 7, "schedule");
    if (lw >= 2) {
      Ring<7, GU_NB, 20, 2> rg;
#pragma unroll
      for (int p = 0; p < 7; ++p) rg.voff[p] = WS_WGU + (ft0 * 2 + half * 7 + p) * 20480 + c.lane * 16;
      rg.init(c.ws, 1024, 0, half);
      if (half == 0) ln_cols<4>(c, 56, WS_CSG + ft0 * 128, 13824);
      sig_wait(c, iss - layer * 16 + 4, M_CNT4);
      rg.template fill_ahead<1>(c, layer * 5 + 4);
      rg.rest();
    } else {
      Ring<4, GU_NA, 20, 2> rg;
#pragma unroll
      for (int p = 0; p < 4; ++p) rg.voff[p] = WS_L + layer * L_SZ + L_X1 + (gu_panel(c) * 8 + half * 4 + p) * 20480 + c.lane * 16;
      rg.init(c.ws, 1024, GU_A, half);
      Poll<1> pl;
      gate(c, pl, half, flg(FL_WO + layer * 5, gu_panel(c) * 16 + (c.lane & 15)), layer * 5 + 4, iss + 12);
      rg.template fill<3>();
      rg.template head<3>();
      pl.keep();
      wait_vm<16>(); BARRIER_L();
      f32x2 st[8];
#pragma unroll
      for (int i = 0; i < 8; ++i)
        st[i] = __builtin_bit_cast(f32x2, ld8(c.r, WS_ST + (layer * 2 + 1) * STB + gu_panel(c) * 8192 + i * 1024 + (half * 64 + c.lane) * 8));
      wait_vm<12 + 8>(); BARRIER_L();
      wait_vm<8 + 8>(); BARRIER_L();
      ln_rows(c, half * 64 + c.lane, st);
      wait_vm<0>(); BARRIER_L();
      BARRIER_L(); wait_vm<0>(); BARRIER_L();
    }
  }
  { const int w = lw + 8; (void)w; WDUMP(layer * 5 + 3); }
  {
    const Ctx c = fresh(c0);
    if (lw >= 2) {
      Ring<5, DN_NB, 27, 2> rg;
#pragma unroll
      for (int p = 0; p < 5; ++p) { const int q = half * 5 + p; rg.voff[p] = WS_WD + (pr_cb(c) * 5 + (q >> 1)) * 55296 + (q & 1) * 1024 + c.lane * 16; }
      rg.init(c.ws, 2048, 0, half);
      sig_wait(c, layer * 16 + 16, M_CNT);
      rg.template fill_ahead<6>(c, layer * 5 + 5);
      wait_vm<(DN_NB - 10) * 5>(); BARRIER_L();
      rg.template fill<5>();
      wait_vm<5 * 5>(); BARRIER_L();
      rg.template fill<4>();
      wait_vm<5 * 5>(); BARRIER_L();
      rg.template fill<3>();
      wait_vm<3 * 5>(); BARRIER_L();
      wait_vm<0>(); BARRIER_L();
      BARRIER_L();
    } else {
      if (lw == 0) {
        sig_wait(c, iss + 20, M_ISS);
        poll(c, flg(FL_GU + layer * 5, (pr_panel(c) >> 1) * 16 + (c.lane & 15))); sig_set(c, layer * 5 + 5); STAMPL(layer * 5 + 4, 6);
      }
#pragma unroll 1
      for (int k = 0; k < 7; ++k) BARRIER_L();
    }
  }
  { const int w = lw + 8; (void)w; WDUMP(layer * 5 + 4); }
  if (qkv_next) qkv_load1(fresh(c0), lw, wqkv);
}

__global__ __launch_bounds__(768) void fused_kernel(const uint16_t* __restrict__ x, const uint16_t* __restrict__ wqkv, const uint16_t* __restrict__ wo,
                                                    const uint16_t* __restrict__ wg, const uint16_t* __restrict__ wu,
                                                    const uint16_t* __restrict__ wd, uint16_t* __restrict__ out, char* __restrict__ ws) {
  extern __shared__ __attribute__((aligned(16))) char smem[];
  Ctx c;
  c.ws = ws; c.lds = (lds_ptr)smem; c.lane = threadIdx.x & 63; c.xcd = blockIdx.x & 7; c.j = blockIdx.x >> 3;
  c.r = __builtin_amdgcn_make_buffer_rsrc(ws, 0, WS_END, 0x00020000);
  const int w = __builtin_amdgcn_readfirstlane(threadIdx.x >> 6);
  SPANST(0);
  c.tgt = 0;
  if (threadIdx.x < 16) *(LAS unsigned*)(c.lds + M_CNT + threadIdx.x * 4) = 0u;
  const bool qkv_on = qkv_cb(c) != 15;
  Ctx c0 = c;
  WDUMP(15);
  if (w >= 8) __builtin_amdgcn_s_setprio(3);
  if (w >= 8) {
    if (qkv_on) qkv_load0(c, w - 8, x, wqkv); else BARRIER();
    c0.tgt = epoch(ws);
#pragma unroll 1
    for (int layer = 0; layer < 2; ++layer) {
      const unsigned iss = layer * 24 + (qkv_on ? (layer + 1) * 4 : 0);
      WDUMP(layer * 5);
      att_load(fresh(c0), opq(w - 8), opq(layer), iss, wo, wg, wu, wd);
      WDUMP(layer * 5 + 1);
      mlp_load(c0, opq(w - 8), opq(layer), iss, layer == 0 && qkv_on, wqkv);
    }
  } else {
    if (!qkv_on) BARRIER();
#define TLB_PF 524288
    if (w == 2 || w == 3) {
      const unsigned o = (w - 2) * 64 + c.lane;
      const unsigned t = o * TLB_PF < WS_END ? __builtin_amdgcn_raw_buffer_load_b32(c.r, o * TLB_PF + 2048, 0, 16) : out[(o & 1) * 1048576];
      asm volatile("" ::"v"(t));
    }
    if (w < 2) {
      const char* pc; constexpr unsigned len = 48 * 1024;
      asm volatile("s_getpc_b64 %0" : "=s"(pc));
      const unsigned o = (c.j * 2 + w) * 1024 + c.lane * 16;
      if (o + 16 <= len) { const u32x4 t = *(const GAS u32x4*)(pc + o); asm volatile("" ::"v"(t)); }
    }
#pragma unroll 1
    for (int layer = 0; layer < 2; ++layer) {
      const unsigned L = WS_L + layer * L_SZ;
      qkv_compute(fresh(c0), c0.tgt, opq(w), opq(layer), x, wqkv);
      WDUMP(layer * 5);
      att_compute(fresh(c0), opq(w), opq(layer));
      WDUMP(layer * 5 + 1);
      proj_compute<10, WO_NB, 20>(fresh(c0), opq(w), layer * 5 + 2, L + L_O, layer == 0 ? (const char*)x : nullptr, WS_X2, L + L_X1,
                                  WS_ST + (layer * 2 + 1) * STB, nullptr, FL_WO + layer * 5);
      WDUMP(layer * 5 + 2);
      if (w < 4) gu_consumer<4>(fresh(c0), opq(layer), 0, opq(w)); else gu_consumer<3>(fresh(c0), opq(layer), 4, opq(w - 4));
      WDUMP(layer * 5 + 3);
      proj_compute<27, DN_NB, 18>(fresh(c0), opq(w), layer * 5 + 4, L + L_H, nullptr, L + L_X1, WS_X2, WS_ST + 2 * STB, layer ? out : nullptr,
                                  FL_DN + layer * 5);
      WDUMP(layer * 5 + 4);
    }
  }
}

extern "C" void kernel_launch(void* const* d_in, const int* in_sizes, int n_in, void* d_out, int out_size, void* d_ws, size_t ws_size,
                              hipStream_t stream) {
  (void)hipFuncSetAttribute((const void*)fused_kernel, hipFuncAttributeMaxDynamicSharedMemorySize, LDS_BYTES);
  fused_kernel<<<256, 768, LDS_BYTES, stream>>>((const uint16_t*)d_in[0], (const uint16_t*)d_in[1], (const uint16_t*)d_in[2],
                                                (const uint16_t*)d_in[4], (const uint16_t*)d_in[5], (const uint16_t*)d_in[6],
                                                (uint16_t*)d_out, (char*)d_ws);
}
```

```cpp
#include <hip/hip_runtime.h>
#include <hip/hip_bf16.h>
#include <stdint.h>

typedef __attribute__((ext_vector_type(8))) __bf16 bf16x8;
typedef __attribute__((ext_vector_type(16))) __bf16 bf16x16;
typedef __attribute__((ext_vector_type(4))) short s16x4;
typedef __attribute__((ext_vector_type(8))) short s16x8;
typedef __attribute__((ext_vector_type(2))) __bf16 bf16x2;
typedef __attribute__((ext_vector_type(2))) float f32x2;
typedef __attribute__((ext_vector_type(4))) float f32x4;
typedef __attribute__((ext_vector_type(8))) float f32x8;
typedef __attribute__((ext_vector_type(16))) float f32x16;
typedef __attribute__((ext_vector_type(2))) unsigned u32x2;
typedef __attribute__((ext_vector_type(4))) unsigned u32x4;
typedef unsigned long long u64;
#define LAS __attribute__((address_space(3)))
#define GAS __attribute__((address_space(1)))
typedef LAS char* lds_ptr;
typedef __amdgpu_buffer_rsrc_t rsrc_t;
#define DI __device__ __forceinline__

constexpr unsigned XB = 2048u * 640 * 2;
constexpr unsigned WS_EPOCH = 0;
constexpr unsigned WS_FLAG = 4096;
constexpr int FL_P0 = 0, FL_RT = 1, FL_QKV = 2, FL_ATT = 3, FL_WO = 4, FL_GU = 5, FL_DN = 6, FL_RT2 = 12;
constexpr unsigned WS_WO  = WS_FLAG + 13u * 32768;
constexpr unsigned WS_WGU = WS_WO + 640u * 640 * 2;
constexpr unsigned WS_WD  = WS_WGU + 3456u * 640 * 2;
constexpr unsigned WS_CSQ = WS_WD + 640u * 1728 * 2;
constexpr unsigned WS_CSG = WS_CSQ + 8u * 1920 * 4;
constexpr unsigned WS_ST  = WS_CSG + 4u * 3456 * 4;
constexpr unsigned STB = 2048u * 64;
constexpr unsigned WS_X2  = WS_ST + 4 * STB;
constexpr unsigned WS_L   = WS_X2 + XB;
constexpr unsigned L_Q1 = 0, L_Q2 = L_Q1 + 32u * 65536, L_K1 = L_Q2 + 16u * 32768, L_K2 = L_K1 + 32u * 65536, L_V = L_K2 + 16u * 32768,
                   L_O = L_V + XB, L_X1 = L_O + XB, L_H = L_X1 + XB, L_SZ = L_H + 2048u * 1728 * 2;
constexpr unsigned WS_END = WS_L + 2 * L_SZ;

constexpr int LDS_BYTES = 163840, MISC = 159744, M_SST = MISC, M_SCS = MISC + 1024, M_CNT = MISC + 2048, M_CNT2 = M_CNT + 8, M_CNT3 = M_CNT + 16, M_CNT4 = M_CNT + 24, M_SIG = M_CNT + 32, M_ISS = M_CNT + 40;
constexpr int QKV_NB = 5, QKV_NA = 4, QKV_A = QKV_NB * 16384;
constexpr int WO_NB = 10, DN_NB = 15;
constexpr int GU_NB = 7, GU_NA = 7, GU_A = GU_NB * 14336;
constexpr int AT_SLOT = 45056, AT_CONST = 3 * AT_SLOT, AT_SCR = AT_CONST + 10752, AT_MERGE = 3 * AT_SLOT;
static_assert(QKV_A + QKV_NA * 16384 <= MISC && DN_NB * 10240 <= MISC && GU_A + GU_NA * 8192 <= MISC, "lds");
static_assert(WO_NB * 10240 <= AT_MERGE && AT_MERGE + 24576 <= MISC && AT_SCR + 3 * 4096 <= MISC, "lds");

DI unsigned pk2(float a, float b) { f32x2 v = {a, b}; bf16x2 r = __builtin_convertvector(v, bf16x2); return __builtin_bit_cast(unsigned, r); }
DI float bflo(unsigned u) { return __uint_as_float(u << 16); }
DI float bfhi(unsigned u) { return __uint_as_float(u & 0xffff0000u); }
DI f32x4 mfma16(bf16x8 a, bf16x8 b, f32x4 c) { return __builtin_amdgcn_mfma_f32_16x16x32_bf16(a, b, c, 0, 0, 0); }
DI f32x16 mfma32(bf16x8 a, bf16x8 b, f32x16 c) { return __builtin_amdgcn_mfma_f32_32x32x16_bf16(a, b, c, 0, 0, 0); }
DI bf16x8 ldsr16(lds_ptr p) { return *(const LAS bf16x8*)p; }
DI bf16x8 ldstr(lds_ptr p, int second) {
  const s16x4 a = __builtin_amdgcn_ds_read_tr16_b64_v4i16((LAS s16x4*)p), b = __builtin_amdgcn_ds_read_tr16_b64_v4i16((LAS s16x4*)(p + second));
  return __builtin_bit_cast(bf16x8, (s16x8)__builtin_shufflevector(a, b, 0, 1, 2, 3, 4, 5, 6, 7));
}
#define BARRIER() asm volatile("s_waitcnt lgkmcnt(0)\n\ts_barrier" ::: "memory")
template <int N> DI void wait_vm() { asm volatile("s_waitcnt vmcnt(%0)" ::"n"(N > 63 ? 63 : N) : "memory"); }
#define WDUMP(id)
#define WRESET()
#define BARRIER_L() BARRIER()
#define BARRIER_NW() asm volatile("s_barrier" ::: "memory")
#define SBAR() __builtin_amdgcn_sched_barrier(0)
#define PIN(x) asm volatile("" : "+v"(x))
#define GL_HEAD "s_mov_b32 m0, %0\n\ts_nop 3\n\t"
#define GL(v, off) "global_load_lds_dwordx4 %" #v ", %1 offset:" #off " sc1\n\t"
DI void glds1(unsigned m, const char* sb, unsigned v0) { asm volatile(GL_HEAD GL(2, 0)::"s"(m), "s"(sb), "v"(v0) : "memory"); }
DI void glds2(unsigned m, const char* sb, unsigned v0, unsigned v1) {
  asm volatile(GL_HEAD GL(2, 0) GL(3, 1024)::"s"(m), "s"(sb), "v"(v0), "v"(v1) : "memory");
}
DI void glds3(unsigned m, const char* sb, unsigned v0, unsigned v1, unsigned v2) {
  asm volatile(GL_HEAD GL(2, 0) GL(3, 1024) GL(4, 2048)::"s"(m), "s"(sb), "v"(v0), "v"(v1), "v"(v2) : "memory");
}
DI void glds4(unsigned m, const char* sb, unsigned v0, unsigned v1, unsigned v2, unsigned v3) {
  asm volatile(GL_HEAD GL(2, 0) GL(3, 1024) GL(4, 2048) GL(5, 3072)::"s"(m), "s"(sb), "v"(v0), "v"(v1), "v"(v2), "v"(v3) : "memory");
}
DI void st16(rsrc_t r, unsigned off, u32x4 v) { __builtin_amdgcn_raw_buffer_store_b128(v, r, off, 0, 16); }
DI void st8(rsrc_t r, unsigned off, u32x2 v) { __builtin_amdgcn_raw_buffer_store_b64(v, r, off, 0, 16); }
DI void st4(rsrc_t r, unsigned off, unsigned v) { __builtin_amdgcn_raw_buffer_store_b32(v, r, off, 0, 16); }
DI u32x4 ld16(rsrc_t r, unsigned off) { return __builtin_amdgcn_raw_buffer_load_b128(r, off, 0, 16); }
DI u32x2 ld8(rsrc_t r, unsigned off) { return __builtin_amdgcn_raw_buffer_load_b64(r, off, 0, 16); }
DI u32x4 ld16s(rsrc_t r, unsigned voff, unsigned soff) { return __builtin_amdgcn_raw_buffer_load_b128(r, voff, soff, 16); }
DI void ldsw16(lds_ptr p, u32x4 v) { *(LAS u32x4*)p = v; }
DI f32x2 ld8_async(unsigned voff, const char* sbase) {
  f32x2 v; asm volatile("s_nop 4\n\tglobal_load_dwordx2 %0, %1, %2 sc1" : "=v"(v) : "v"(voff), "s"(sbase) : "memory"); return v;
}
DI u32x4 pair32(unsigned a0, unsigned a1, unsigned b0, unsigned b1) {
  auto x = __builtin_amdgcn_permlane32_swap(a0, b0, false, false);
  auto y = __builtin_amdgcn_permlane32_swap(a1, b1, false, false);
  return u32x4{x[0], y[0], x[1], y[1]};
}
template <int N> DI float ror16(float v) { return __int_as_float(__builtin_amdgcn_update_dpp(0, __float_as_int(v), 0x120 + N, 0xf, 0xf, false)); }
DI float sum32(float v) {
  v += ror16<8>(v); v += ror16<4>(v); v += ror16<2>(v); v += ror16<1>(v);
  auto a = __builtin_amdgcn_permlane16_swap(__float_as_uint(v), __float_as_uint(v), false, false);
  return __uint_as_float(a[0]) + __uint_as_float(a[1]);
}
DI u32x4 pair16(unsigned a0, unsigned a1, unsigned b0, unsigned b1) {
  auto x = __builtin_amdgcn_permlane16_swap(a0, b0, false, false);
  auto y = __builtin_amdgcn_permlane16_swap(a1, b1, false, false);
  return u32x4{x[0], y[0], x[1], y[1]};
}

DI float rows4(float v) {
  auto a = __builtin_amdgcn_permlane16_swap(__float_as_uint(v), __float_as_uint(v), false, false);
  v = __uint_as_float(a[0]) + __uint_as_float(a[1]);
  auto b = __builtin_amdgcn_permlane32_swap(__float_as_uint(v), __float_as_uint(v), false, false);
  return __uint_as_float(b[0]) + __uint_as_float(b[1]);
}

#define STAMP(id)
#define STAMPC(id)
#define STAMPL(sl, id)
#define SLOT(n)

#define WARM_GU 1
#define POLL_DEPTH 1
#define POLL_GATE 1
#define POLL_SLEEP 1
#define ATT_PRIO 3
#define ATT_ORD 0
#define WO_AH 6
#define DN_AH 8
#define ATT_ROT 1
#define GU_SKIP 1
#define ATT_LATE 4
#define PACE 16
#define SPANST(id)
struct Ctx {
  char* ws; rsrc_t r; lds_ptr lds; u64 tgt; int lane, xcd, j;
};
DI Ctx fresh(const Ctx& c0) {
  Ctx c = c0;
  asm volatile("v_mbcnt_lo_u32_b32 %0, -1, 0\n\tv_mbcnt_hi_u32_b32 %0, -1, %0" : "=v"(c.lane), "+s"(c.xcd), "+s"(c.j), "+s"(c.ws));
  return c;
}
DI int opq(int v) { asm volatile("" : "+s"(v)); return v; }
constexpr int SPIN_CAP = 1 << 18;

DI u64 epoch(const char* ws) {
  u64 e; asm volatile("s_nop 4\n\ts_load_dwordx2 %0, %1, 0x0\n\ts_waitcnt lgkmcnt(0)" : "=s"(e) : "s"(ws + WS_EPOCH) : "memory"); return e + 1;
}
DI unsigned flg(int arr, int unit) { return WS_FLAG + (arr * 256 + unit) * 128; }
DI void publish(const Ctx& c, int cnt_off, unsigned n, unsigned to, bool mark = false) {
  if (mark && c.lane == 0) __hip_atomic_fetch_add((LAS unsigned*)(c.lds + M_ISS), 1u, __ATOMIC_RELAXED, __HIP_MEMORY_SCOPE_WORKGROUP);
  wait_vm<0>();
  if (c.lane == 0) {
    const unsigned old = __hip_atomic_fetch_add((LAS unsigned*)(c.lds + cnt_off), 1u, __ATOMIC_RELAXED, __HIP_MEMORY_SCOPE_WORKGROUP);
    if (old % n == n - 1) __hip_atomic_store((GAS u64*)(c.ws + to), c.tgt, __ATOMIC_RELAXED, __HIP_MEMORY_SCOPE_AGENT);
  }
}
template <int N> struct Poll {
  u64 v[POLL_DEPTH][N];
  DI void run(const Ctx& c, unsigned o0, unsigned o1 = 0) {

    auto ld = [&](int r) {
      asm volatile("global_load_dwordx2 %0, %1, %2 sc1" : "=v"(v[r][0]) : "v"(o0), "s"(c.ws) : "memory");
      if constexpr (N == 2) asm volatile("global_load_dwordx2 %0, %1, %2 sc1" : "=v"(v[r][N - 1]) : "v"(o1), "s"(c.ws) : "memory");
      __builtin_amdgcn_s_sleep(POLL_SLEEP);
    };
    auto ok = [&](int r) {
      if constexpr (N == 2) asm volatile("s_waitcnt vmcnt(%2)" : "+v"(v[r][0]), "+v"(v[r][N - 1]) : "n"((POLL_DEPTH - 1) * N) : "memory");
      else asm volatile("s_waitcnt vmcnt(%1)" : "+v"(v[r][0]) : "n"(POLL_DEPTH - 1) : "memory");
      return __all(v[r][0] == c.tgt && v[r][N - 1] == c.tgt);
    };
    asm volatile("s_nop 4" ::: "memory");
    ld(0); if (POLL_DEPTH > 1) ld(1);
#pragma unroll 1
    for (int spin = 0; spin < SPIN_CAP; ++spin) {
      if (ok(0)) break; ld(0);
      if (POLL_DEPTH > 1) { if (ok(1)) break; ld(1); }
    }
  }
  DI void keep() {
#pragma unroll
    for (int r = 0; r < POLL_DEPTH; ++r) { asm volatile("" : "+v"(v[r][0])); if constexpr (N == 2) asm volatile("" : "+v"(v[r][N - 1])); }
  }
};
DI void poll(const Ctx& c, unsigned slot) { Poll<1> p; p.run(c, slot); wait_vm<0>(); p.keep(); }
DI void sig_set(const Ctx& c, unsigned v) { if (c.lane == 0) __hip_atomic_store((LAS unsigned*)(c.lds + M_SIG), v, __ATOMIC_RELAXED, __HIP_MEMORY_SCOPE_WORKGROUP); }
DI void sig_wait(const Ctx& c, unsigned v, int off = M_SIG, bool calm = false) {
#pragma unroll 1
  for (int spin = 0; spin < SPIN_CAP * 16; ++spin) {
    if (__builtin_amdgcn_readfirstlane(__hip_atomic_load((LAS unsigned*)(c.lds + off), __ATOMIC_RELAXED, __HIP_MEMORY_SCOPE_WORKGROUP)) >= v) break;
    if (calm) __builtin_amdgcn_s_sleep(8); else if (off != M_SIG) __builtin_amdgcn_s_sleep(1);
  }
  asm volatile("" ::: "memory");
}

DI void gate(const Ctx& c, Poll<1>& p, int half, unsigned slot, unsigned sig, unsigned iss) {
  if (half == 0) { if (POLL_GATE) sig_wait(c, iss, M_ISS); p.run(c, slot); sig_set(c, sig); STAMPL(sig - 1, 6); } else sig_wait(c, sig);
}

template <int PP, int NSLOT, int NS, int LAG = 1>
struct Ring {
  unsigned voff[PP];
  const char* base;
  unsigned adv, l0;
  int slot;
  DI void init(const char* b, unsigned a, unsigned lds0, int half) {
    base = b - 4096; adv = a; l0 = lds0 + half * PP * 1024; slot = 0;
#pragma unroll
    for (int p = 0; p < PP; ++p) voff[p] += 4096 - (p & 3) * 1024;
  }
  DI void issue() {
    const unsigned m = l0 + slot * (2 * PP * 1024);
    glds4(m, base, voff[0], voff[1], voff[2], voff[3]);
    if constexpr (PP == 5) glds1(m + 4096, base, voff[4]);
    if constexpr (PP == 7) glds3(m + 4096, base, voff[4], voff[5], voff[6]);
    if constexpr (PP == 8) glds4(m + 4096, base, voff[4], voff[5], voff[6], voff[7]);
    base += adv; slot = slot == NSLOT - 1 ? 0 : slot + 1;
  }
  template <int N = NSLOT> DI void fill() {
#pragma unroll 1
    for (int s = 0; s < N; ++s) issue();
  }
  template <int NEED> DI void fill_ahead(const Ctx& c, unsigned sig) {
    int n = 0;
#pragma unroll 1
    while (n < NSLOT) {
      issue(); wait_vm<PACE>(); ++n;
      if (n * PP >= NEED * PP + PACE &&
          __builtin_amdgcn_readfirstlane(__hip_atomic_load((LAS unsigned*)(c.lds + M_SIG), __ATOMIC_RELAXED, __HIP_MEMORY_SCOPE_WORKGROUP)) >= sig) break;
    }
    if (n == NSLOT) wait_vm<(NSLOT - NEED) * PP>();
    BARRIER_L(); WRESET();
#pragma unroll 1
    for (; n < NSLOT; ++n) issue();
  }
  DI void rest() { range<NSLOT - 2, NSLOT - LAG>(); steady(); range<NSLOT - 1 - LAG, 0>(); BARRIER_L(); }
  template <int R, int E> DI void range() { if constexpr (R >= E) { wait_vm<R * PP>(); BARRIER_L(); range<R - 1, E>(); } }
  template <int D0 = NSLOT> DI void head() {
    wait_vm<(D0 - 1) * PP>(); BARRIER_L(); WRESET();
    if constexpr (D0 < NSLOT) fill<NSLOT - D0>();
    range<NSLOT - 2, NSLOT - LAG>();
    steady();
  }
  DI void steady() {
#pragma unroll 1
    for (int k = LAG; k < NS - NSLOT + LAG; ++k) { wait_vm<(NSLOT - 1 - LAG) * PP>(); BARRIER_L(); issue(); }
  }
  template <int D0 = NSLOT> DI void run() { head<D0>(); range<NSLOT - 1 - LAG, 0>(); BARRIER_L(); }
};

DI void ln_rows(const Ctx& c, int row, const f32x2 (&p)[8]) {
  f32x2 t = p[0];
#pragma unroll
  for (int i = 1; i < 8; ++i) t += p[i];
  const float m8 = t[0] * 0.125f; float q = 0.f;
#pragma unroll
  for (int i = 0; i < 8; ++i) { const float a = p[i][0] - m8; q += a * a; }
  const float rstd = __builtin_amdgcn_rsqf((t[1] + q * (1.f / 80.f)) * (1.f / 640.f) + 1e-5f);
  *(LAS float*)(c.lds + M_SST + row * 4) = rstd;
  *(LAS float*)(c.lds + M_SST + 512 + row * 4) = -t[0] * (1.f / 640.f);
}
template <int NP> DI void ln_cols_sum(const Ctx& c, int n4, const u32x4 (&q)[NP]) {
  f32x4 cs = __builtin_bit_cast(f32x4, q[0]);
#pragma unroll
  for (int i = 1; i < NP; ++i) cs += __builtin_bit_cast(f32x4, q[i]);
  if (c.lane < n4) *(LAS f32x4*)(c.lds + M_SCS + c.lane * 16) = cs;
}
template <int NP> DI void ln_cols(const Ctx& c, int n4, unsigned off, unsigned pitch) {
  u32x4 q[NP];
#pragma unroll
  for (int i = 0; i < NP; ++i) q[i] = ld16(c.r, off + i * pitch + (c.lane < n4 ? c.lane : 0) * 16);
  ln_cols_sum<NP>(c, n4, q);
}

#define MAP 0
DI int qkv_panel(const Ctx& c) { return (c.xcd & 3) * 4 + (c.j & 3); }
DI int qkv_cb(const Ctx& c) { return (c.xcd >> 2) * 8 + (c.j >> 2); }
DI int pr_panel(const Ctx& c) { return (c.xcd & 3) * 8 + (c.j & 7); }
DI int pr_cb(const Ctx& c) { return (c.xcd >> 2) * 4 + (c.j >> 3); }
DI int gu_panel(const Ctx& c) { return (c.xcd & 1) * 8 + (c.j & 7); }
DI int gu_cb(const Ctx& c) { return (c.xcd >> 1) * 4 + (c.j >> 3); }
DI int att_head(const Ctx& c) { return c.xcd * 4 + (c.j & 3); }
DI int att_qb(const Ctx& c) { return c.j >> 2; }

struct Phase0 {
  u32x4 v[3], t[10];
  DI void load(const Ctx& c, int hw, const uint16_t* x, const uint16_t* wqkv) {
    const int lane = c.lane, r32 = lane & 31, b = blockIdx.x, row = b * 8 + hw * 2 + (lane >> 5);
    v[0] = *(const u32x4*)(x + row * 640 + r32 * 8); v[1] = *(const u32x4*)(x + row * 640 + 256 + r32 * 8);
    v[2] = *(const u32x4*)(x + row * 640 + 512 + (r32 & 15) * 8);
    if (hw == 3 && b < 240) {
      const rsrc_t r = __builtin_amdgcn_make_buffer_rsrc((void*)wqkv, 0, 640 * 3840, 0x00020000);
#pragma unroll
      for (int i = 0; i < 10; ++i)
        t[i] = __builtin_amdgcn_raw_buffer_load_b128(r, (lane >> 3) * 3840 + (lane & 7) * 16, ((b / 30) * 80 + i * 8) * 3840 + (b % 30) * 128, 0);
    }
  }
  DI void rows(const Ctx& c, int hw) {
    const int lane = c.lane, r32 = lane & 31, row = blockIdx.x * 8 + hw * 2 + (lane >> 5);
    const float w2 = r32 < 16 ? 1.f : 0.f;
    float sum = 0.f, s2 = 0.f;
#pragma unroll
    for (int e = 0; e < 4; ++e) { sum += (bflo(v[0][e]) + bfhi(v[0][e])) + (bflo(v[1][e]) + bfhi(v[1][e])); s2 += bflo(v[2][e]) + bfhi(v[2][e]); }
    sum = sum32(sum + w2 * s2);
    const float mean = sum * (1.f / 640.f); float m2 = 0.f; s2 = 0.f;
#pragma unroll
    for (int e = 0; e < 4; ++e) {
      const float a0 = bflo(v[0][e]) - mean, a1 = bfhi(v[0][e]) - mean, a2 = bflo(v[1][e]) - mean, a3 = bfhi(v[1][e]) - mean;
      const float a4 = bflo(v[2][e]) - mean, a5 = bfhi(v[2][e]) - mean;
      m2 += a0 * a0 + a1 * a1 + a2 * a2 + a3 * a3; s2 += a4 * a4 + a5 * a5;
    }
    m2 = sum32(m2 + w2 * s2);
    if (r32 < 8) st8(c.r, WS_ST + (row >> 7) * 8192 + r32 * 1024 + (row & 127) * 8, u32x2{__float_as_uint(sum * 0.125f), __float_as_uint(m2 * 0.125f)});
  }
  float s[8];
  DI void cols1(int hw) {
    if (hw == 3 && blockIdx.x < 240) {
      unsigned ul = 0x00003f80u, uh = 0x3f800000u;
      asm volatile("" : "+v"(ul), "+v"(uh));
      const bf16x2 lo = __builtin_bit_cast(bf16x2, ul), hi = __builtin_bit_cast(bf16x2, uh);
#pragma unroll
      for (int e = 0; e < 4; ++e) {
        s[2 * e] = 0.f; s[2 * e + 1] = 0.f;
#pragma unroll
        for (int i = 0; i < 10; ++i) {
          const unsigned u = t[i][e];
          s[2 * e] = __builtin_amdgcn_fdot2_f32_bf16(__builtin_bit_cast(bf16x2, u), lo, s[2 * e], false);
          s[2 * e + 1] = __builtin_amdgcn_fdot2_f32_bf16(__builtin_bit_cast(bf16x2, u), hi, s[2 * e + 1], false);
        }
      }
#pragma unroll
      for (int e = 0; e < 8; ++e) PIN(s[e]);
    }
  }
  DI void cols2(const Ctx& c, int hw) {
    const int lane = c.lane, b = blockIdx.x;
    if (hw == 3 && b < 240) {
#pragma unroll
      for (int e = 0; e < 8; ++e) s[e] = rows4(s[e] + ror16<8>(s[e]));
      const int hi = (lane >> 3) & 1;
      const f32x4 o = hi ? f32x4{s[4], s[5], s[6], s[7]} : f32x4{s[0], s[1], s[2], s[3]};
      if (lane < 16) st16(c.r, WS_CSQ + ((b / 30) * 1920 + (b % 30) * 64 + (lane & 7) * 8 + hi * 4) * 4, __builtin_bit_cast(u32x4, o));
    }
  }
};

template <bool SWAP, class PRE>
DI void qkv_loop(const Ctx& c, int w, int layer, f32x16 (&acc)[2][2], PRE pre) {
  const int lane = c.lane, i = lane & 15, g = lane >> 4, wm = w & 1, wn = w >> 1;
  lds_ptr pa[4];
#pragma unroll
  for (int s = 0; s < 4; ++s)
    pa[s] = c.lds + QKV_A + wm * 8192 + (g & 1) * 2048 +
            (layer == 0 ? (i >> 3) * 1024 + (i & 7) * 128 + (((2 * s + (g >> 1)) ^ ((i >> 1) & 7)) << 4) : (2 * s + (g >> 1)) * 256 + i * 16);
  const int rb = ((g >> 1) * 8 + (i >> 2)) * 256 + (i & 3) * 8, u = (wn * 4 + (g & 1)) ^ ((i >> 2) << 1);
  lds_ptr pb0 = c.lds + rb + (u << 5), pb1 = c.lds + rb + ((u ^ 2) << 5);
  bf16x8 x0[4], x1[4], w0[4], w1[4];
  auto rdx = [&](bf16x8* xn, int hs, int e) { xn[e] = ldsr16(pa[hs * 2 + (e >> 1)] + (e & 1) * 4096); };
  auto rdw = [&](bf16x8* wn_, int hs, int e) { wn_[e] = ldstr(((e & 1) ? pb1 : pb0) + (hs * 2 + (e >> 1)) * 4096, 1024); };
  auto step = [&](const bf16x8* xa, const bf16x8* wb, bf16x8* xn, bf16x8* wn_, int hs, bool ld) {
#pragma unroll
    for (int q = 0; q < 8; ++q) {
      if (ld) { if (q & 1) rdx(xn, hs, q >> 1); else rdw(wn_, hs, q >> 1); }
      const int s = q >> 2, tm = q & 1, tn = (q >> 1) & 1;
      acc[tm][tn] = SWAP ? mfma32(wb[s * 2 + tn], xa[s * 2 + tm], acc[tm][tn]) : mfma32(xa[s * 2 + tm], wb[s * 2 + tn], acc[tm][tn]);
      SBAR();
    }
  };
  SLOT(layer * 5);
  if (layer) STAMP(0);
  STAMP(1);
  BARRIER();
  STAMP(2); STAMPC(0); WRESET();
#pragma unroll
  for (int e = 0; e < 4; ++e) { rdw(w0, 0, e); rdx(x0, 0, e); }
  int sa = 0, sb = 0;
#pragma unroll 1
  for (int st = 0; st < 9; ++st) {
    step(x0, w0, x1, w1, 1, true);
    const int da = sa == QKV_NA - 1 ? -(QKV_NA - 1) * 16384 : 16384, db = sb == QKV_NB - 1 ? -(QKV_NB - 1) * 16384 : 16384;
    sa = sa == QKV_NA - 1 ? 0 : sa + 1; sb = sb == QKV_NB - 1 ? 0 : sb + 1;
    pa[0] += da; pa[1] += da; pa[2] += da; pa[3] += da; pb0 += db; pb1 += db;
    BARRIER_NW();
    step(x1, w1, x0, w0, 0, true);
  }
  step(x0, w0, x1, w1, 1, true);
  BARRIER();
  pre(); SBAR();
  step(x1, w1, x0, w0, 0, false);
  STAMP(3); STAMPC(1);
}
DI unsigned qkv_w_off(const Ctx& c, int q) {
  const int row = q * 4 + (c.lane >> 4);
  return row * 3840 + qkv_cb(c) * 256 + (((c.lane & 15) ^ ((row & 3) << 2)) << 4);
}
DI void qkv_helper(Ctx c, u64& tgt, int hw, int layer, const uint16_t* x, const uint16_t* wqkv) {
  const int panel = qkv_panel(c), cb = qkv_cb(c), l = c.lane;
  const bool on = cb != 15;
  if (layer == 0) {
    Phase0 p0;
    p0.load(c, hw, x, wqkv);
    c.tgt = tgt = epoch(c.ws);
    if (on) BARRIER();
    p0.cols1(hw);
    if (on && hw == 3) BARRIER();
    p0.rows(c, hw);
    if (on && hw != 3) BARRIER();
    p0.cols2(c, hw);
    if (on) { BARRIER(); BARRIER(); }
    publish(c, M_CNT2, 4, flg(FL_P0, blockIdx.x));
    if (!on) return;
    BARRIER(); BARRIER();
    const unsigned at = flg(FL_P0, hw < 2 ? panel * 16 + hw * 8 + (l & 7) : (l & 7) * 30 + cb * 2 + ((l >> 3) & 1));
    const u64 v = __hip_atomic_load((const GAS u64*)(c.ws + at), __ATOMIC_RELAXED, __HIP_MEMORY_SCOPE_AGENT);
    BARRIER();
    if (!__all(v == c.tgt)) poll(c, at);
    if (hw < 2) {
      f32x2 p[8];
#pragma unroll
      for (int i = 0; i < 8; ++i) p[i] = __builtin_bit_cast(f32x2, ld8(c.r, WS_ST + panel * 8192 + i * 1024 + (hw * 64 + l) * 8));
      BARRIER(); BARRIER();
      ln_rows(c, hw * 64 + l, p);
    } else if (hw == 2) {
      u32x4 q[8];
#pragma unroll
      for (int i = 0; i < 8; ++i) q[i] = ld16(c.r, WS_CSQ + cb * 512 + i * 7680 + (l & 31) * 16);
      BARRIER(); BARRIER();
      ln_cols_sum<8>(c, 32, q);
    } else { BARRIER(); BARRIER(); }
    BARRIER(); BARRIER();
  } else if (on) {
    if (hw == 2) ln_cols<8>(c, 32, WS_CSQ + cb * 512, 7680);
    BARRIER(); BARRIER(); BARRIER(); BARRIER(); BARRIER(); BARRIER();
    if (hw < 2) {
      f32x2 p[8];
#pragma unroll
      for (int i = 0; i < 8; ++i) p[i] = __builtin_bit_cast(f32x2, ld8(c.r, WS_ST + 2 * STB + panel * 8192 + i * 1024 + (hw * 64 + l) * 8));
      BARRIER(); BARRIER();
      ln_rows(c, hw * 64 + l, p);
    } else { BARRIER(); BARRIER(); }
    BARRIER(); BARRIER(); BARRIER();
  }
}
DI void qkv_compute(Ctx c, u64& tgt, int w, int layer, const uint16_t* x, const uint16_t* wqkv) {
  const int panel = qkv_panel(c), cb = qkv_cb(c);
  if (w >= 4) { qkv_helper(c, tgt, w - 4, layer, x, wqkv); return; }
  if (layer == 0) c.tgt = tgt = epoch(c.ws);
  if (cb == 15) return;
  const int lane = c.lane, r32 = lane & 31, h = lane >> 5, wm = w & 1, wn = w >> 1;
  const unsigned L = WS_L + layer * L_SZ;
  f32x16 acc[2][2] = {{f32x16{}, f32x16{}}, {f32x16{}, f32x16{}}};
  const LAS float* srs = (const LAS float*)(c.lds + M_SST) + wm * 64;
  const LAS float* scs = (const LAS float*)(c.lds + M_SCS) + wn * 64;
  if (cb < 10) {
    float rs[2], nm[2]; f32x4 cs[8];
    qkv_loop<true>(c, w, layer, acc, [&]() {
      rs[0] = srs[r32]; rs[1] = srs[32 + r32]; nm[0] = srs[128 + r32]; nm[1] = srs[160 + r32];
#pragma unroll
      for (int e = 0; e < 8; ++e) cs[e] = *(const LAS f32x4*)(scs + e * 8 + h * 4);
    });
    const float qs = cb < 5 ? 0.22360679774997896f * 1.4426950408889634f : 1.f;
    const unsigned i1 = L + (cb < 5 ? L_Q1 : L_K1), i2 = L + (cb < 5 ? L_Q2 : L_K2);
    rs[0] *= qs; rs[1] *= qs;
#pragma unroll
    for (int tp = 0; tp < 4; ++tp) {
      const int tn = tp >> 1, pr = tp & 1;
      const int o = (cb % 5) * 16 + wn * 8 + tp * 2 + h, hp = o / 5, pos = o - hp * 5;
      const unsigned ts = pos == 2 ? 512 : 1024;
      const unsigned a0 = (pos == 2 ? i2 + hp * 32768 : i1 + (2 * hp + (pos > 2)) * 65536 + (pos > 2 ? pos - 3 : pos) * 512) +
                          (panel * 4 + wm * 2) * ts + r32 * 16;
#pragma unroll
      for (int tm = 0; tm < 2; ++tm) {
        const f32x16 a = acc[tm][tn];
        const f32x4 u = (f32x4{a[pr * 8], a[pr * 8 + 1], a[pr * 8 + 2], a[pr * 8 + 3]} + cs[tp * 2] * nm[tm]) * rs[tm];
        const f32x4 v = (f32x4{a[pr * 8 + 4], a[pr * 8 + 5], a[pr * 8 + 6], a[pr * 8 + 7]} + cs[tp * 2 + 1] * nm[tm]) * rs[tm];
        st16(c.r, a0 + tm * ts, pair32(pk2(u[0], u[1]), pk2(u[2], u[3]), pk2(v[0], v[1]), pk2(v[2], v[3])));
      }
    }
  } else {
    float cs[2]; f32x4 rn[2][4];
    auto rd = [&](int tp) {
#pragma unroll
      for (int e = 0; e < 4; ++e) rn[tp & 1][e] = *(const LAS f32x4*)(srs + (e >> 1) * 128 + tp * 16 + (e & 1) * 8 + h * 4);
    };
    qkv_loop<false>(c, w, layer, acc, [&]() { cs[0] = scs[r32]; cs[1] = scs[32 + r32]; rd(0); rd(1); });
    unsigned a0[2];
#pragma unroll
    for (int tn = 0; tn < 2; ++tn) {
      const int cv = (cb - 10) * 128 + wn * 64 + tn * 32 + r32, head = cv / 20, dim = cv - head * 20;
      const int d = (head & 1) ? (dim < 4 ? dim + 16 : dim - 4) : dim;
      a0[tn] = L + L_V + head * 81920 + (panel * 8 + wm * 4) * 640 + h * 320 + d * 16;
    }
#pragma unroll
    for (int tp = 0; tp < 4; ++tp) {
      const int tm = tp >> 1, pr = tp & 1;
      u32x4 o[2];
#pragma unroll
      for (int tn = 0; tn < 2; ++tn) {
        const f32x16 a = acc[tm][tn];
        const f32x4 u = (f32x4{a[pr * 8], a[pr * 8 + 1], a[pr * 8 + 2], a[pr * 8 + 3]} + rn[pr][2] * cs[tn]) * rn[pr][0];
        const f32x4 v = (f32x4{a[pr * 8 + 4], a[pr * 8 + 5], a[pr * 8 + 6], a[pr * 8 + 7]} + rn[pr][3] * cs[tn]) * rn[pr][1];
        o[tn] = pair32(pk2(u[0], u[1]), pk2(u[2], u[3]), pk2(v[0], v[1]), pk2(v[2], v[3]));
      }
      if (tp < 2) rd(tp + 2);
      st16(c.r, a0[0] + tp * 640, o[0]); st16(c.r, a0[1] + tp * 640, o[1]);
      SBAR();
    }
  }
  SLOT(layer * 5);
  STAMP(4);
  publish(c, M_CNT4, 4, flg(FL_QKV + layer * 5, panel * 16 + cb), true);
  STAMP(5);
}
typedef Ring<8, QKV_NB, 10, 2> QkvB;
DI void qkv_load0(const Ctx& c, int lw, const uint16_t* x, const uint16_t* wqkv) {
  const int half = lw & 1;
  if (lw >= 2) {
    QkvB rg;
#pragma unroll
    for (int p = 0; p < 8; ++p) rg.voff[p] = qkv_w_off(c, half * 8 + p);
    rg.init((const char*)wqkv, 64 * 3840, 0, half);
    rg.template fill<3>(); rg.template run<3>();
  } else {
    Ring<8, QKV_NA, 10, 2> rg;
#pragma unroll
    for (int p = 0; p < 8; ++p) {
      const int mt = qkv_panel(c) * 8 + half * 4 + (p >> 1), r16 = (p & 1) * 8 + (c.lane >> 3);
      rg.voff[p] = (mt * 16 + r16) * 1280 + (((c.lane & 7) ^ ((r16 >> 1) & 7)) << 4);
    }
    rg.init((const char*)x, 128, QKV_A, half);
    rg.template fill<3>(); rg.template run<3>();
  }
}
DI void qkv_load1(const Ctx& c, int lw, const uint16_t* wqkv) {
  const int half = lw & 1;
  static_assert(QKV_NA == 4 && QKV_NB == 5, "schedule");
  if (lw >= 2) {
    QkvB rg;
#pragma unroll
    for (int p = 0; p < 8; ++p) rg.voff[p] = qkv_w_off(c, half * 8 + p);
    rg.init((const char*)wqkv, 64 * 3840, 0, half);
    sig_wait(c, 12, M_CNT4);
    rg.template fill_ahead<1>(c, 6);
    rg.rest();
  } else {
    Ring<8, QKV_NA, 10, 2> rg;
#pragma unroll
    for (int p = 0; p < 8; ++p) rg.voff[p] = WS_X2 + (qkv_panel(c) * 8 + half * 4 + (p >> 1)) * 20480 + (p & 1) * 1024 + c.lane * 16;
    rg.init(c.ws, 2048, QKV_A, half);
    Poll<1> pl;
    gate(c, pl, half, flg(FL_DN, qkv_panel(c) * 16 + (c.lane & 15)), 6, 28);
    rg.template fill<2>(); rg.template run<2>();
    pl.keep();
  }
}

DI f32x16 qk8(s16x4 k2, s16x4 q2) {
 return __builtin_amdgcn_mfma_f32_32x32x8bf16_1k(k2, q2, f32x16{}, 0, 0, 0); }
DI f32x16 qk16(bf16x8 k1, bf16x8 q1, f32x16 s) { return __builtin_amdgcn_mfma_f32_32x32x16_bf16(k1, q1, s, 0, 0, 0); }
DI f32x16 pv_lo(bf16x8 v, f32x16 p, f32x16 o) {
  return __builtin_amdgcn_smfmac_f32_32x32x32_bf16(v, __builtin_bit_cast(bf16x16, (f32x8)__builtin_shufflevector(p, p, 0, 1, 2, 3, 4, 5, 6, 7)), o, 0xDDDD, 0, 0);
}
DI f32x16 pv_hi(bf16x8 v, f32x16 p, f32x16 o) {
  return __builtin_amdgcn_smfmac_f32_32x32x32_bf16(v, __builtin_bit_cast(bf16x16, (f32x8)__builtin_shufflevector(p, p, 8, 9, 10, 11, 12, 13, 14, 15)), o, 0xDDDD, 0, 0);
}
DI float swap32(float v, int h) {
  auto r = __builtin_amdgcn_permlane32_swap(__float_as_uint(v), __float_as_uint(v), false, false);
  return __uint_as_float(h ? r[0] : r[1]);
}
#define EXP4(X, i) do { X[i] = __builtin_amdgcn_exp2f(X[i]); X[i + 1] = __builtin_amdgcn_exp2f(X[i + 1]); X[i + 2] = __builtin_amdgcn_exp2f(X[i + 2]); \
                        X[i + 3] = __builtin_amdgcn_exp2f(X[i + 3]); PIN(X); SBAR(); } while (0)
DI void att_compute(const Ctx& c, int w, int layer) {
  const int lane = c.lane, head = att_head(c), qb = att_qb(c);
  const int qp = w & 3, kp = w >> 2, r32 = lane & 31, h = lane >> 5;
  lds_ptr lds = c.lds;
  const unsigned L = WS_L + layer * L_SZ;
  SLOT(layer * 5 + 1);
  STAMP(0);
  for (int s = w * 64 + lane; s < 672; s += 512) {
    const unsigned v = s % 20 == 0 ? 0x3f803f80u : 0u;
    *(LAS u32x4*)(lds + AT_CONST + s * 16) = u32x4{v, v, v, v};
  }
  lds_ptr pk1 = lds + kp * 8192 + lane * 16;
  lds_ptr pk2_ = h ? lds + AT_CONST + 16 : lds + 16384 + kp * 4096 + r32 * 16 + (head & 1) * 8;
  lds_ptr pv = (r32 >= 20 ? lds + AT_CONST : lds + 24576 + kp * 10240) + h * 320 + r32 * 16;
  const int vmask = r32 >= 20 ? 0 : -1, kmask = h ? 0 : -1;
  f32x16 o0 = f32x16{}, o1 = f32x16{};
  f32x16 s0, s1, s2, s3 = f32x16{};
  STAMP(1);
  BARRIER();
  STAMP(2); STAMPC(0); WRESET();
  const bf16x8 q1a = ldsr16(lds + 2 * AT_SLOT + qp * 2048 + lane * 16), q1b = ldsr16(lds + 2 * AT_SLOT + qp * 2048 + 1024 + lane * 16);
  lds_ptr pq2 = h ? lds + AT_CONST + 16 : lds + 2 * AT_SLOT + 8192 + qp * 1024 + r32 * 16 + (head & 1) * 8;
  const s16x4 q2a = *(const LAS s16x4*)pq2, q2b = *(const LAS s16x4*)(pq2 + 512);
  bf16x8 k1 = ldsr16(pk1), vlo = ldsr16(pv), vhi = bf16x8{};
  s16x4 k2 = *(const LAS s16x4*)pk2_;
  s0 = qk16(k1, q1a, qk8(k2, q2a));
#define PH_EVEN(SC, SN, SP, KLOAD, VOFF) do { \
    SN = qk8(k2, q2b); SBAR(); EXP4(SC, 0); SN = qk16(k1, q1b, SN); SBAR(); KLOAD; SBAR(); EXP4(SC, 4); \
    o1 = pv_hi(vhi, SP, o1); SBAR(); vhi = ldsr16(pv + (VOFF) + 640); SBAR(); EXP4(SC, 8); o0 = pv_lo(vlo, SC, o0); SBAR(); EXP4(SC, 12); } while (0)
#define PH_ODD(SC, SN, SP, VLOAD) do { \
    SN = qk8(k2, q2a); SBAR(); EXP4(SC, 0); SN = qk16(k1, q1a, SN); SBAR(); EXP4(SC, 4); \
    o0 = pv_hi(vhi, SP, o0); SBAR(); EXP4(SC, 8); o1 = pv_lo(vlo, SC, o1); SBAR(); VLOAD; SBAR(); EXP4(SC, 12); } while (0)
#define KLD(t) do { k1 = ldsr16(pk1 + (t) * 1024); k2 = *(const LAS s16x4*)(pk2_ + (t) * 512); } while (0)
#define PRI(v) do { asm volatile("s_cmp_eq_u32 %0, 0\n\ts_cbranch_scc1 1f\n\ts_setprio " #v "\n1:" :: "s"(kp) : "scc"); SBAR(); } while (0)
#define HI(n) if (ATT_PRIO == (n)) PRI(1);
#define LO(n) if (ATT_PRIO == (n)) PRI(0);
#define TWO(t) HI(3) HI(4) HI(5) PH_EVEN(s0, s1, s3, KLD((t) + 1), (t) * 1280); PH_ODD(s1, s2, s0, vlo = ldsr16(pv + ((t) + 1) * 1280)); LO(4)
  for (int grp = 0; grp < 4; ++grp) {
    TWO(0) PH_EVEN(s2, s3, s1, if (grp == 0) BARRIER_NW(); KLD(2), 1280); LO(3) PH_ODD(s3, s0, s2, vlo = ldsr16(pv + 2 * 1280));
    TWO(2) PH_EVEN(s2, s3, s1, KLD(4), 3 * 1280); LO(3) LO(5) PH_ODD(s3, s0, s2, vlo = ldsr16(pv + 4 * 1280));
    TWO(4) PH_EVEN(s2, s3, s1, KLD(6), 5 * 1280); LO(3) PH_ODD(s3, s0, s2, vlo = ldsr16(pv + 6 * 1280));
    TWO(6)
    const int d = grp == 2 ? -2 * AT_SLOT : grp == 3 ? 0 : AT_SLOT;
    PH_EVEN(s2, s3, s1, pk1 += d; pk2_ += (d & kmask); if (grp != 3) BARRIER(); KLD(0), 7 * 1280);
    LO(3) LO(5) PH_ODD(s3, s0, s2, pv += (d & vmask); vlo = ldsr16(pv));
  }
  o1 = pv_hi(vhi, s3, o1);
  STAMP(3); STAMPC(1);
  __builtin_amdgcn_s_setprio(0);
  BARRIER();
  const f32x16 give = kp ? o0 : o1, keep = kp ? o1 : o0;
#pragma unroll
  for (int e = 0; e < 3; ++e)
    *(LAS f32x4*)(lds + AT_MERGE + (w * 3 + e) * 1024 + lane * 16) = f32x4{give[4 * e], give[4 * e + 1], give[4 * e + 2], give[4 * e + 3]};
  BARRIER();
  f32x4 a[3];
#pragma unroll
  for (int e = 0; e < 3; ++e)
    a[e] = f32x4{keep[4 * e], keep[4 * e + 1], keep[4 * e + 2], keep[4 * e + 3]} + *(const LAS f32x4*)(lds + AT_MERGE + ((w ^ 4) * 3 + e) * 1024 + lane * 16);
  const int qt = qb * 8 + qp * 2 + kp;
  float l = h ? a[2][0] : 0.f;
  l += swap32(l, h);
  if (__builtin_expect(__any(!(l >= 5.421e-20f && l <= 1.8447e19f)), 0)) {
    const unsigned o2 = (head >> 1) * 32768 + r32 * 16 + (head & 1) * 8;
    const bf16x8 fq1 = __builtin_bit_cast(bf16x8, ld16(c.r, L + L_Q1 + head * 65536 + qt * 1024 + lane * 16));
    u32x2 fq2 = ld8(c.r, L + L_Q2 + o2 + qt * 512);
    if (h) fq2 = u32x2{0u, 0u};
    const unsigned vi = L + L_V + head * 81920 + h * 320 + (r32 < 20 ? r32 : 0) * 16;
    f32x16 oo = f32x16{};
    float m = -INFINITY;
    const u32x4 one8 = {0x3f803f80u, 0x3f803f80u, 0x3f803f80u, 0x3f803f80u};
    for (int t = 0; t < 64; ++t) {
      const u32x4 fk1 = ld16(c.r, L + L_K1 + head * 65536 + t * 1024 + lane * 16);
      const u32x2 fk2 = ld8(c.r, L + L_K2 + o2 + t * 512);
      u32x4 v0 = ld16(c.r, vi + t * 1280), v1 = ld16(c.r, vi + t * 1280 + 640);
      if (r32 >= 20) { v0 = one8; v1 = one8; }
      f32x16 s = qk16(__builtin_bit_cast(bf16x8, fk1), fq1, qk8(__builtin_bit_cast(s16x4, fk2), __builtin_bit_cast(s16x4, fq2)));
      float tm = s[0];
#pragma unroll
      for (int r = 1; r < 16; ++r) tm = fmaxf(tm, s[r]);
      tm = fmaxf(tm, swap32(tm, h));
      const float mn = fmaxf(m, tm), al = __builtin_amdgcn_exp2f(m - mn);
      m = mn;
#pragma unroll
      for (int r = 0; r < 16; ++r) { s[r] = __builtin_amdgcn_exp2f(s[r] - mn); oo[r] *= al; }
      oo = pv_hi(__builtin_bit_cast(bf16x8, v1), s, pv_lo(__builtin_bit_cast(bf16x8, v0), s, oo));
    }
#pragma unroll
    for (int e = 0; e < 3; ++e) a[e] = f32x4{oo[4 * e], oo[4 * e + 1], oo[4 * e + 2], oo[4 * e + 3]};
    l = h ? a[2][0] : 0.f;
    l += swap32(l, h);
  }
  const float inv = 1.f / l;
  const unsigned A0 = pk2(a[0][0] * inv, a[0][1] * inv), A1 = pk2(a[0][2] * inv, a[0][3] * inv);
  const unsigned B0 = pk2(a[1][0] * inv, a[1][1] * inv), B1 = pk2(a[1][2] * inv, a[1][3] * inv);
  const u32x2 C = {pk2(a[2][0] * inv, a[2][1] * inv), pk2(a[2][2] * inv, a[2][3] * inv)};
  const int tok = qt * 32 + r32;
  const unsigned ob = L + L_O + (tok >> 4) * 20480;
  st16(c.r, ob + (head * 2 + h) * 256 + (tok & 15) * 16, pair32(A0, A1, B0, B1));
  if (!h) st8(c.r, ob + 16384 + head * 128 + (tok & 15) * 8, C);
  STAMP(4);
  publish(c, M_CNT, 8, flg(FL_ATT + layer * 5, qb * 32 + head), true);
  STAMP(5);
}
DI void retile_load(const Ctx& c, int J, u32x4 (&v)[5][4], const uint16_t* wo, const uint16_t* wg, const uint16_t* wu, const uint16_t* wd) {
  const int e = J < 216 ? J : J < 256 ? J - 216 : J - 256;
  const int strip = J < 216 ? (e >> 2) % 27 : e % 10, k0 = J < 216 ? (e & 3) * 5 : min((e / 10) * 5, 49), ld = J < 216 ? 3456 : 1280;
  const rsrc_t r = __builtin_amdgcn_make_buffer_rsrc((void*)(J < 108 ? wg : J < 216 ? wu : J < 256 ? wo : wd), 0,
                                                     J < 216 ? 640 * 3456 : J < 256 ? 640 * 1280 : 1728 * 1280, 0x00020000);
  const unsigned vo = (c.lane >> 3) * ld + (c.lane & 7) * 16;
#pragma unroll
  for (int u = 0; u < 5; ++u)
#pragma unroll
    for (int q = 0; q < 4; ++q) {
      const int ch = (k0 + u) * 4 + q;
      const int row = J < 216 || J >= 256 ? ch * 8 : ch < 64 ? (ch >> 1) * 20 + (ch & 2) * 2 + (ch & 1) * 8 : (ch - 64) * 40 + 16;
      v[u][q] = __builtin_amdgcn_raw_buffer_load_b128(r, vo, row * ld + strip * 128, 0);
    }
}
DI void retile_store(const Ctx& c, int J, lds_ptr scr, const u32x4 (&v)[5][4]) {
  const int e = J < 216 ? J : J < 256 ? J - 216 : J - 256, mat = J >= 108;
  const int strip = J < 216 ? (e >> 2) % 27 : e % 10, k0 = J < 216 ? (e & 3) * 5 : min((e / 10) * 5, 49);
  const unsigned ts = J < 216 ? 40960 : J < 256 ? 20480 : 55296;
  const unsigned dst = (J < 216 ? WS_WGU + mat * 20480 : J < 256 ? WS_WO : WS_WD) + strip * 4 * ts + k0 * 1024;
  const int i = c.lane & 15, g = c.lane >> 4;
  const bf16x8 ones = __builtin_bit_cast(bf16x8, u32x4{0x3f803f80u, 0x3f803f80u, 0x3f803f80u, 0x3f803f80u});
  f32x4 cs[4] = {f32x4{}, f32x4{}, f32x4{}, f32x4{}};
#pragma unroll
  for (int u = 0; u < 5; ++u) {
#pragma unroll
    for (int p = 0; p < 4; ++p) *(LAS u32x4*)(scr + p * 1024 + c.lane * 16) = v[u][p];
#pragma unroll
    for (int t = 0; t < 4; ++t) {
      const bf16x8 f = ldstr(scr + (g * 8 + (i >> 2)) * 128 + t * 32 + (i & 3) * 8, 512);
      __builtin_amdgcn_raw_buffer_store_b128(__builtin_bit_cast(u32x4, f), c.r, c.lane * 16, dst + u * 1024 + t * ts, 16);
      cs[t] = mfma16(f, ones, cs[t]);
    }
  }
  if (J < 216 && i == 0) {
#pragma unroll
    for (int t = 0; t < 4; ++t)
      __builtin_amdgcn_raw_buffer_store_b128(__builtin_bit_cast(u32x4, cs[t]), c.r, g * 16, WS_CSG + ((e & 3) * 3456 + (strip * 8 + t * 2 + mat) * 16) * 4, 16);
  }
}
DI int early_piece(int e) { return e < 4 ? (e & 1) + (e >> 1) * 8 : e < 6 ? 16 + (e - 4) * 4 : e < 9 ? 18 + e : 25 + e; }
DI int late_piece(int e) { return e < 6 ? 2 + e : e < 12 ? 4 + e : e < 15 ? 5 + e : e < 18 ? 6 + e : e < 25 ? 9 + e : 12 + e; }
DI void att_load(const Ctx& c, int lw, int layer, unsigned iss, const uint16_t* wo, const uint16_t* wg, const uint16_t* wu, const uint16_t* wd) {
  const int head = att_head(c), qb = att_qb(c), lane = c.lane;
  const char* Lp = c.ws + WS_L + layer * L_SZ;
  const unsigned vo = lane * 16;
  int grp = ATT_ROT ? qb >> 1 : 0;
  auto src_of = [&](int p) {
    return p < 16 ? L_K1 + head * 65536 + grp * 16384 + p * 1024
                  : p < 24 ? L_K2 + (head >> 1) * 32768 + grp * 8192 + (p - 16) * 1024 : L_V + head * 81920 + grp * 20480 + (p - 24) * 1024;
  };
  unsigned v0[14], m0[11], vg[11], sg[11];
#pragma unroll
  for (int q = 0; q < 11; ++q) {
    const int p = q < 3 ? early_piece(lw * 3 + q) : late_piece(lw * 8 + q - 3), pg = lw * 11 + q;
    v0[q] = src_of(p) + vo; m0[q] = p * 1024;
    vg[q] = src_of(pg) + vo; sg[q] = pg < 16 ? 16384 : pg < 24 ? 8192 : 20480;
    asm volatile("" : "+v"(v0[q]), "+s"(m0[q]));
  }
#pragma unroll
  for (int q = 0; q < 3; ++q) {
    const int p = lw * 3 + q;
    v0[11 + q] = (p < 8 ? L_Q1 + head * 65536 + qb * 8192 + p * 1024 : L_Q2 + (head >> 1) * 32768 + qb * 4096 + (p - 8) * 1024) + vo - q * 1024;
    PIN(v0[11 + q]);
  }
  auto issue = [&](int slot) {
#pragma unroll
    for (int q = 0; q < 11; ++q) { vg[q] += grp == 3 ? -3 * sg[q] : sg[q]; glds1(slot * AT_SLOT + (lw * 11 + q) * 1024, Lp, vg[q]); }
    grp = (grp + 1) & 3;
  };
  const int o0 = (head >> 1) * 5 + (head & 1) * 2, c0 = head * 20;
  auto kv_cb = [&](int sel) { return sel == 0 ? 5 + (o0 >> 4) : sel == 1 ? 5 + ((o0 + 2) >> 4) : sel == 2 ? 10 + (c0 >> 7) : 10 + ((c0 + 19) >> 7); };
  if (lw == 0) {
    sig_wait(c, iss, M_ISS);
    poll(c, flg(FL_QKV + layer * 5, lane & 16 ? (qb * 2 + (lane & 1)) * 16 + ((o0 + (lane & 2)) >> 4) : (grp * 4 + (lane & 3)) * 16 + kv_cb((lane >> 2) & 3)));
    sig_set(c, layer * 5 + 2);
    STAMPL(layer * 5 + 1, 6);
  } else sig_wait(c, layer * 5 + 2);
  glds3(2 * AT_SLOT + lw * 3072, Lp, v0[11], v0[12], v0[13]);
#pragma unroll
  for (int q = 0; q < 3; ++q) glds1(m0[q], Lp, v0[q]);
#pragma unroll
  for (int q = 3; q < 3 + ATT_LATE; ++q) glds1(m0[q], Lp, v0[q]);
  wait_vm<ATT_LATE>(); BARRIER();
#pragma unroll
  for (int q = 3 + ATT_LATE; q < 11; ++q) glds1(m0[q], Lp, v0[q]);
  wait_vm<0>(); BARRIER();
  if (lw == 0) { poll(c, flg(FL_QKV + layer * 5, (lane & 15) * 16 + kv_cb(lane >> 4))); sig_set(c, layer * 5 + 3); } else sig_wait(c, layer * 5 + 3, M_SIG, true);
  issue(1);
  const int J = (lw - 1) * 256 + blockIdx.x;
  const bool rt = layer == 0 && lw > 0 && J < 366;
  u32x4 v[5][4];
  if (rt) { retile_load(c, J, v, wo, wg, wu, wd); wait_vm<20>(); } else wait_vm<0>();
  BARRIER();
  issue(2);
  if (rt) retile_store(c, J, c.lds + AT_SCR + (lw - 1) * 4096, v);
  if (layer == 0 && (lw == 1 || lw == 2)) publish(c, M_CNT3, 2, flg(FL_RT, blockIdx.x));
  wait_vm<0>(); BARRIER();
  issue(0);
  wait_vm<0>();
  if (layer == 0) {
    if (lw == 0) {
      if (c.j == 0) {
        Poll<2> pl;
#pragma unroll 1
        for (int q = 0; q < 2; ++q) { pl.run(c, flg(FL_RT, q * 128 + lane), flg(FL_RT, q * 128 + 64 + lane)); wait_vm<0>(); pl.keep(); }
        if (lane < 32) __hip_atomic_store((GAS u64*)(c.ws + flg(FL_RT2, lane * 8 + c.xcd)), c.tgt, __ATOMIC_RELAXED, __HIP_MEMORY_SCOPE_AGENT);
      }
      poll(c, flg(FL_RT2, blockIdx.x));
      if (blockIdx.x == 0 && lane == 0) *(u64*)(c.ws + WS_EPOCH) = c.tgt;
    }
  }
  BARRIER();
  u32x4 pk[25];
#pragma unroll
  for (int e = 0; e < 25; ++e) pk[e] = ld16s(c.r, vo, WS_WO + (pr_cb(c) * 5 + e % 5) * 20480 + (lw * 5 + e / 5) * 1024);
#pragma unroll 1
  for (unsigned q = lw; q < 35; q += 4) glds1(AT_SCR + lw * 1024, c.ws + WS_WGU + ((gu_cb(c) * 27) >> 2) * 40960 + ((c.j & 7) * 35 + q) * 1024, vo);
  wait_vm<0>();
  BARRIER(); BARRIER();
  sig_wait(c, layer * 16 + 8, M_CNT);
#pragma unroll
  for (int e = 0; e < 25; ++e) { const int k = lw * 5 + e / 5; ldsw16(c.lds + (k >> 1) * 10240 + (k & 1) * 1024 + (e % 5) * 2048 + vo, pk[e]); }
}

template <int NS, int NB, int WIN>
DI void proj_compute(const Ctx& c, int w, int slot_id, unsigned a_off, const char* res_rm, unsigned res_off, unsigned out_off, unsigned st_off,
                     uint16_t* out, int arr) {
  constexpr bool RES = NS <= NB;
  const int panel = pr_panel(c), cb = pr_cb(c);
  SLOT(slot_id);
  STAMP(0);
  if (w >= 4) {
    if constexpr (RES) { BARRIER(); BARRIER(); }
    else {
      static_assert(RES || (NS == 27 && NB == 15 && WIN == 18), "schedule");
      const int par = (w >> 1) & 1, half = w & 1;
      u32x4 pk[15];
#pragma unroll
      for (int e = 0; e < 15; ++e) {
        const int q = half * 5 + e % 5;
        pk[e] = ld16s(c.r, c.lane * 16, WS_WD + (cb * 5 + (q >> 1)) * 55296 + ((15 + par + (e / 5) * 2) * 2 + (q & 1)) * 1024);
      }
      lds_ptr pw = c.lds + par * 10240 + half * 5120 + c.lane * 16;
      BARRIER(); BARRIER();
#pragma unroll
      for (int e = 0; e < 10; ++e) ldsw16(pw + (e / 5) * 20480 + (e % 5) * 1024, pk[e]);
      BARRIER();
#pragma unroll
      for (int e = 10; e < 15; ++e) ldsw16(pw + 40960 + (e % 5) * 1024, pk[e]);
      BARRIER(); BARRIER(); BARRIER(); BARRIER();
    }
    return;
  }
  const int lane = c.lane, i = lane & 15, g = lane >> 4, mt = panel * 4 + w;
  u32x2 res[5];
#pragma unroll
  for (int t = 0; t < 5; ++t)
    res[t] = res_rm ? *(const u32x2*)(res_rm + (mt * 16 + i) * 1280 + cb * 160 + t * 32 + g * 8)
                    : ld8(c.r, res_off + (mt * 80 + cb * 10 + t * 2 + (g >> 1)) * 256 + i * 16 + (g & 1) * 8);
  f32x4 acc[5];
#pragma unroll
  for (int t = 0; t < 5; ++t) { acc[t] = f32x4{bflo(res[t][0]), bfhi(res[t][0]), bflo(res[t][1]), bfhi(res[t][1])}; PIN(acc[t]); }
  lds_ptr pb = c.lds + lane * 16;
  unsigned av = a_off + mt * (NS * 2048) + lane * 16;
  u32x4 a[WIN];
  bf16x8 f[3][5];
  int sb = 0;
  STAMP(1);
  BARRIER();
  STAMP(2); STAMPC(0); WRESET();
  constexpr int AH = RES ? WO_AH : DN_AH;
  auto lda = [&](int k) {
    if (RES && k >= 16) {
      const unsigned o = a_off + mt * 20480 + 16384 + (k - 16) * 1024 + g * 256 + i * 8;
      const u32x2 lo = ld8(c.r, o), hi = ld8(c.r, o + 128);
      a[k % WIN] = u32x4{lo[0], lo[1], hi[0], hi[1]};
    } else a[k % WIN] = ld16(c.r, av + k * 1024);
  };
#pragma unroll
  for (int k = 0; k < AH; ++k) lda(k);
#pragma unroll
  for (int t = 0; t < 5; ++t) { f[0][t] = ldsr16(pb + t * 2048); f[1][t] = ldsr16(pb + t * 2048 + 1024); }
  SBAR();
  auto window = [&](bool first, bool last) {
#pragma unroll
    for (int k = 0; k < WIN; ++k) {
      const bool ld = !(last && k + 2 >= WIN);
      if ((k & 1) == 0) {
        if (RES) pb += 10240;
        else { pb += sb == NB - 1 ? -(NB - 1) * 10240 : 10240; sb = sb == NB - 1 ? 0 : sb + 1; }
        if (!ld) BARRIER(); else if (!RES && (k == 10 || (k == 0 && !first))) BARRIER_NW();
      }
#pragma unroll
      for (int t = 0; t < 5; ++t) {
        if (ld) f[(k + 2) % 3][t] = ldsr16(pb + t * 2048 + (k & 1) * 1024);
        acc[t] = mfma16(f[k % 3][t], __builtin_bit_cast(bf16x8, a[k]), acc[t]);
        SBAR();
      }
      if (!last || k + AH < WIN) { lda(k + AH); SBAR(); }
    }
    av += WIN * 1024;
  };
  static_assert(2 * NS % WIN == 0 && (2 * NS == WIN || WIN % 6 == 0), "window");
#pragma unroll 1
  for (int b = 1; b < 2 * NS / WIN; ++b) window(b == 1, false);
  window(false, true);
  STAMP(3); STAMPC(1);
  u32x2 o[5];
#pragma unroll
  for (int t = 0; t < 5; ++t) {
    o[t][0] = pk2(acc[t][0], acc[t][1]); o[t][1] = pk2(acc[t][2], acc[t][3]);
  }
  const u32x4 p0 = pair16(o[0][0], o[0][1], o[1][0], o[1][1]), p1 = pair16(o[2][0], o[2][1], o[3][0], o[3][1]),
              p2 = pair16(o[4][0], o[4][1], o[4][0], o[4][1]);
  const int ch = (g & 1) * 2 + (g >> 1);
  if (out) {
    char* po = (char*)out + (mt * 16 + i) * 1280 + cb * 160;
    *(u32x4*)(po + ch * 16) = p0; *(u32x4*)(po + 64 + ch * 16) = p1;
    if (!(g & 1)) *(u32x4*)(po + 128 + (g >> 1) * 16) = p2;
    STAMP(4);
    return;
  }
  const unsigned xo = out_off + (mt * 80 + cb * 10) * 256 + i * 16;
  st16(c.r, xo + ch * 256, p0); st16(c.r, xo + 1024 + ch * 256, p1);
  if (!(g & 1)) st16(c.r, xo + 2048 + (g >> 1) * 256, p2);
  const f32x4 s4 = (acc[0] + acc[1]) + (acc[2] + acc[3]) + acc[4];
  const float sum = rows4((s4[0] + s4[1]) + (s4[2] + s4[3])), mean = sum * (1.f / 80.f);
  f32x4 q4 = {0.f, 0.f, 0.f, 0.f};
#pragma unroll
  for (int t = 0; t < 5; ++t) { const f32x4 d = acc[t] - mean; q4 += d * d; }
  const float m2 = rows4((q4[0] + q4[1]) + (q4[2] + q4[3]));
  if (g == 0) st8(c.r, st_off + (mt >> 3) * 8192 + cb * 1024 + ((mt & 7) * 16 + i) * 8, u32x2{__float_as_uint(sum), __float_as_uint(m2)});
  STAMP(4);
  publish(c, M_CNT4, 4, flg(arr, panel * 8 + cb), true);
  STAMP(5);
}
template <int NF>
DI void gu_consumer(const Ctx& c, int layer, int f0, int mq) {
  const int panel = gu_panel(c), ft0 = (gu_cb(c) * 27) >> 2;
  const int lane = c.lane, r32 = lane & 31, h = lane >> 5;
  f32x16 acc[NF];
#pragma unroll
  for (int t = 0; t < NF; ++t) acc[t] = f32x16{};
  const int lo = (r32 >> 4) * 1024 + h * 256 + (r32 & 15) * 16;
  lds_ptr pa = c.lds + GU_A + mq * 2048 + lo, pb = c.lds + f0 * 2048 + lo;
  bf16x8 x0[2], x1[2], wa[NF], wb0[NF], wb1[NF];
  auto step = [&](const bf16x8* xa, const bf16x8* wb, bf16x8* xn, bf16x8* wn_, bool ld) {
#pragma unroll
    for (int q = 0; q < 2 * NF; ++q) {
      if (ld) {
        if (q == 0) { xn[0] = ldsr16(pa); xn[1] = ldsr16(pa + 512); }
        if (q >= 1 && q <= NF) wn_[q - 1] = ldsr16(pb + (q - 1) * 2048 + 512);
        if (q >= NF) wa[q - NF] = ldsr16(pb + (q - NF) * 2048);
      }
      if (q < NF) acc[q] = mfma32(wa[q], xa[0], acc[q]); else acc[q - NF] = mfma32(wb[q - NF], xa[1], acc[q - NF]);
      SBAR();
    }
  };
  static_assert(GU_NA == GU_NB, "one slot counter");
  int sl = 0;
  auto adv = [&]() {
    pa += sl == GU_NA - 1 ? -(GU_NA - 1) * 8192 : 8192; pb += sl == GU_NB - 1 ? -(GU_NB - 1) * 14336 : 14336;
    sl = sl == GU_NA - 1 ? 0 : sl + 1;
  };
  SLOT(layer * 5 + 3);
  STAMP(0);
  STAMP(1);
  BARRIER();
  STAMP(2); STAMPC(0); WRESET();
  x0[0] = ldsr16(pa); x0[1] = ldsr16(pa + 512);
#pragma unroll
  for (int t = 0; t < NF; ++t) { wa[t] = ldsr16(pb + t * 2048); wb0[t] = ldsr16(pb + t * 2048 + 512); }
#pragma unroll 1
  for (int st = 0; st < 9; ++st) {
    adv(); BARRIER_NW(); step(x0, wb0, x1, wb1, true);
    adv(); BARRIER_NW(); step(x1, wb1, x0, wb0, true);
  }
  adv(); BARRIER_NW(); step(x0, wb0, x1, wb1, true);
  BARRIER();
  const LAS float* srs = (const LAS float*)(c.lds + M_SST) + mq * 32 + r32;
  const LAS float* scs = (const LAS float*)(c.lds + M_SCS) + f0 * 32 + h * 4;
  float rs = srs[0], nm = srs[128];
  f32x4 cs[2][4];
#pragma unroll
  for (int e = 0; e < 8; ++e) cs[e >> 2][e & 3] = *(const LAS f32x4*)(scs + e * 8);
  SBAR();
  step(x1, wb1, x0, wb0, false);
  STAMP(3); STAMPC(1);
  rs *= rs;
  const unsigned ho = WS_L + layer * L_SZ + L_H + ((panel * 8 + mq * 2 + (r32 >> 4)) * 216 + (ft0 + f0) * 2 + h) * 256 + (r32 & 15) * 16;
#pragma unroll
  for (int f = 0; f < NF; ++f) {
    unsigned p[2][2];
#pragma unroll
    for (int e = 0; e < 2; ++e) {
      const f32x16 a = acc[f];
      f32x4 gt = f32x4{a[e * 4], a[e * 4 + 1], a[e * 4 + 2], a[e * 4 + 3]} + cs[f & 1][e] * nm;
      const f32x4 up = (f32x4{a[8 + e * 4], a[9 + e * 4], a[10 + e * 4], a[11 + e * 4]} + cs[f & 1][2 + e] * nm) * rs;
      gt = __builtin_elementwise_max(gt, f32x4{0.f, 0.f, 0.f, 0.f}) * up;
      p[e][0] = pk2(gt[0], gt[1]); p[e][1] = pk2(gt[2], gt[3]);
    }
    if (f + 2 < NF) {
#pragma unroll
      for (int e = 0; e < 4; ++e) cs[f & 1][e] = *(const LAS f32x4*)(scs + (f + 2) * 32 + e * 8);
    }
    st16(c.r, ho + f * 512, pair32(p[0][0], p[0][1], p[1][0], p[1][1]));
    SBAR();
  }
  STAMP(4);
  publish(c, M_CNT, 8, flg(FL_GU + layer * 5, panel * 16 + gu_cb(c)), true);
  STAMP(5);
}
DI void mlp_load(const Ctx& c0, int lw, int layer, unsigned iss, bool qkv_next, const uint16_t* wqkv) {
  const int half = lw & 1;
  {
    const Ctx c = fresh(c0);
    if (lw == 0) { poll(c, flg(FL_ATT + layer * 5, (pr_panel(c) >> 2) * 32 + (c.lane & 31))); STAMPL(layer * 5 + 2, 6); }
    BARRIER_L(); BARRIER_L();
  }
  { const int w = lw + 8; (void)w; WDUMP(layer * 5 + 2); }
  {
    const Ctx c = fresh(c0);
    const int ft0 = (gu_cb(c) * 27) >> 2;
    u32x4 pk[20];
    auto park = [&](int e0) {
#pragma unroll
      for (int e = e0; e < e0 + 4; ++e) {
        const int q = half * 5 + e % 5;
        pk[e] = ld16s(c.r, c.lane * 16, WS_WD + (pr_cb(c) * 5 + (q >> 1)) * 55296 + (((e / 5) * 2 + (lw >> 1)) * 2 + (q & 1)) * 1024);
      }
    };
    static_assert(GU_NA == 7 && GU_NB == 7, "schedule");
    if (lw >= 2) {
      Ring<7, GU_NB, 20, 2> rg;
#pragma unroll
      for (int p = 0; p < 7; ++p) rg.voff[p] = WS_WGU + (ft0 * 2 + half * 7 + p) * 20480 + c.lane * 16;
      rg.init(c.ws, 1024, 0, half);
      if (half == 0) ln_cols<4>(c, 56, WS_CSG + ft0 * 128, 13824);
      sig_wait(c, iss - layer * 16 + 4, M_CNT4);
      rg.template fill_ahead<1>(c, layer * 5 + 4);
      rg.template range<5, 5>(); rg.steady();
      wait_vm<28>(); BARRIER_L(); park(0);
      wait_vm<21 + 4>(); BARRIER_L(); park(4);
      wait_vm<14 + 8>(); BARRIER_L(); park(8);
      wait_vm<7 + 12>(); BARRIER_L(); park(12);
      wait_vm<16>(); BARRIER_L(); park(16);
    } else {
      Ring<4, GU_NA, 20, 2> rg;
#pragma unroll
      for (int p = 0; p < 4; ++p) rg.voff[p] = WS_L + layer * L_SZ + L_X1 + (gu_panel(c) * 8 + half * 4 + p) * 20480 + c.lane * 16;
      rg.init(c.ws, 1024, GU_A, half);
      Poll<1> pl;
      gate(c, pl, half, flg(FL_WO + layer * 5, gu_panel(c) * 16 + (c.lane & 15)), layer * 5 + 4, iss + 12);
      rg.template fill<3>();
      rg.template head<3>();
      pl.keep();
      f32x2 st[8];
#pragma unroll
      for (int i = 0; i < 8; ++i)
        st[i] = __builtin_bit_cast(f32x2, ld8(c.r, WS_ST + (layer * 2 + 1) * STB + gu_panel(c) * 8192 + i * 1024 + (half * 64 + c.lane) * 8));
      wait_vm<16 + 8>(); BARRIER_L(); park(0);
      wait_vm<12 + 8 + 4>(); BARRIER_L(); park(4);
      wait_vm<8 + 8 + 8>(); BARRIER_L(); ln_rows(c, half * 64 + c.lane, st);
      wait_vm<8>(); BARRIER_L(); park(8); park(12);
      BARRIER_L(); park(16);
    }
    BARRIER_L();
#pragma unroll
    for (int e = 0; e < 20; ++e) PIN(pk[e]);
    { const int w = lw + 8; (void)w; WDUMP(layer * 5 + 3); }
    auto dump = [&]() {
#pragma unroll
      for (int e = 0; e < 20; ++e) ldsw16(c.lds + ((e / 5) * 2 + (lw >> 1)) * 10240 + (half * 5 + e % 5) * 1024 + c.lane * 16, pk[e]);
    };
    if (lw >= 2) {
      Ring<5, DN_NB, 27, 2> rg;
#pragma unroll
      for (int p = 0; p < 5; ++p) { const int q = half * 5 + p; rg.voff[p] = WS_WD + (pr_cb(c) * 5 + (q >> 1)) * 55296 + (q & 1) * 1024 + c.lane * 16; }
      rg.init(c.ws + 8 * 2048, 2048, 0, half);
      rg.slot = 8;
      sig_wait(c, layer * 16 + 16, M_CNT);
      rg.template fill<3>(); dump(); rg.template fill<4>();
      BARRIER_L();
      wait_vm<5 * 5>(); BARRIER_L();
      wait_vm<0>(); BARRIER_L();
      rg.base += 6 * 2048; rg.slot = 6;
      rg.template fill<3>();
      BARRIER_L();
      rg.template fill<3>();
      wait_vm<3 * 5>(); BARRIER_L();
      wait_vm<0>(); BARRIER_L();
      BARRIER_L();
    } else {
      sig_wait(c, layer * 16 + 16, M_CNT);
      dump();
      if (lw == 0) { poll(c, flg(FL_GU + layer * 5, (pr_panel(c) >> 1) * 16 + (c.lane & 15))); STAMPL(layer * 5 + 4, 6); }
#pragma unroll 1
      for (int k = 0; k < 7; ++k) BARRIER_L();
    }
  }
  { const int w = lw + 8; (void)w; WDUMP(layer * 5 + 4); }
  if (qkv_next) qkv_load1(fresh(c0), lw, wqkv);
}

__global__ __launch_bounds__(768) void fused_kernel(const uint16_t* __restrict__ x, const uint16_t* __restrict__ wqkv, const uint16_t* __restrict__ wo,
                                                    const uint16_t* __restrict__ wg, const uint16_t* __restrict__ wu,
                                                    const uint16_t* __restrict__ wd, uint16_t* __restrict__ out, char* __restrict__ ws) {
  extern __shared__ __attribute__((aligned(16))) char smem[];
  Ctx c;
  c.ws = ws; c.lds = (lds_ptr)smem; c.lane = threadIdx.x & 63; c.xcd = blockIdx.x & 7; c.j = blockIdx.x >> 3;
  c.r = __builtin_amdgcn_make_buffer_rsrc(ws, 0, WS_END, 0x00020000);
  const int w = __builtin_amdgcn_readfirstlane(threadIdx.x >> 6);
  SPANST(0);
  c.tgt = 0;
  if (threadIdx.x < 16) *(LAS unsigned*)(c.lds + M_CNT + threadIdx.x * 4) = 0u;
  const bool qkv_on = qkv_cb(c) != 15;
  Ctx c0 = c;
  WDUMP(15);
  if (w >= 8) __builtin_amdgcn_s_setprio(3);
  if (w >= 8) {
    if (qkv_on) qkv_load0(c, w - 8, x, wqkv); else BARRIER();
    c0.tgt = epoch(ws);
#pragma unroll 1
    for (int layer = 0; layer < 2; ++layer) {
      const unsigned iss = layer * 24 + (qkv_on ? (layer + 1) * 4 : 0);
      WDUMP(layer * 5);
      att_load(fresh(c0), opq(w - 8), opq(layer), iss, wo, wg, wu, wd);
      WDUMP(layer * 5 + 1);
      mlp_load(c0, opq(w - 8), opq(layer), iss, layer == 0 && qkv_on, wqkv);
    }
  } else {
    if (!qkv_on) BARRIER();
#define TLB_PF 524288
    if (w == 2 || w == 3) {
      const unsigned o = (w - 2) * 64 + c.lane;
      const unsigned t = o * TLB_PF < WS_END ? __builtin_amdgcn_raw_buffer_load_b32(c.r, o * TLB_PF + 2048, 0, 16) : out[(o & 1) * 1048576];
      asm volatile("" ::"v"(t));
    }
    if (w < 2) {
      const char* pc; constexpr unsigned len = 58 * 1024;
      asm volatile("s_getpc_b64 %0" : "=s"(pc));
      const unsigned o = (c.j * 2 + w) * 1024 + c.lane * 16;
      if (o + 16 <= len) { const u32x4 t = *(const GAS u32x4*)(pc + o); asm volatile("" ::"v"(t)); }
    }
#pragma unroll 1
    for (int layer = 0; layer < 2; ++layer) {
      const unsigned L = WS_L + layer * L_SZ;
      qkv_compute(fresh(c0), c0.tgt, opq(w), opq(layer), x, wqkv);
      WDUMP(layer * 5);
      att_compute(fresh(c0), opq(w), opq(layer));
      WDUMP(layer * 5 + 1);
      proj_compute<10, WO_NB, 20>(fresh(c0), opq(w), layer * 5 + 2, L + L_O, layer == 0 ? (const char*)x : nullptr, WS_X2, L + L_X1,
                                  WS_ST + (layer * 2 + 1) * STB, nullptr, FL_WO + layer * 5);
      WDUMP(layer * 5 + 2);
      if (w < 4) gu_consumer<4>(fresh(c0), opq(layer), 0, opq(w));
      else if (GU_SKIP && (gu_cb(c0) & 3) == 0) gu_consumer<2>(fresh(c0), opq(layer), 4, opq(w - 4));
      else gu_consumer<3>(fresh(c0), opq(layer), 4, opq(w - 4));
      WDUMP(layer * 5 + 3);
      proj_compute<27, DN_NB, 18>(fresh(c0), opq(w), layer * 5 + 4, L + L_H, nullptr, L + L_X1, WS_X2, WS_ST + 2 * STB, layer ? out : nullptr,
                                  FL_DN + layer * 5);
      WDUMP(layer * 5 + 4);
    }
  }
}

extern "C" void kernel_launch(void* const* d_in, const int* in_sizes, int n_in, void* d_out, int out_size, void* d_ws, size_t ws_size,
                              hipStream_t stream) {
  (void)hipFuncSetAttribute((const void*)fused_kernel, hipFuncAttributeMaxDynamicSharedMemorySize, LDS_BYTES);
  fused_kernel<<<256, 768, LDS_BYTES, stream>>>((const uint16_t*)d_in[0], (const uint16_t*)d_in[1], (const uint16_t*)d_in[2],
                                                (const uint16_t*)d_in[4], (const uint16_t*)d_in[5], (const uint16_t*)d_in[6],
                                                (uint16_t*)d_out, (char*)d_ws);
}
```

```cpp
#include <hip/hip_runtime.h>
#include <hip/hip_bf16.h>
#include <stdint.h>

typedef __attribute__((ext_vector_type(8))) __bf16 bf16x8;
typedef __attribute__((ext_vector_type(16))) __bf16 bf16x16;
typedef __attribute__((ext_vector_type(4))) short s16x4;
typedef __attribute__((ext_vector_type(8))) short s16x8;
typedef __attribute__((ext_vector_type(2))) __bf16 bf16x2;
typedef __attribute__((ext_vector_type(2))) float f32x2;
typedef __attribute__((ext_vector_type(4))) float f32x4;
typedef __attribute__((ext_vector_type(8))) float f32x8;
typedef __attribute__((ext_vector_type(16))) float f32x16;
typedef __attribute__((ext_vector_type(2))) unsigned u32x2;
typedef __attribute__((ext_vector_type(4))) unsigned u32x4;
typedef unsigned long long u64;
#define LAS __attribute__((address_space(3)))
#define GAS __attribute__((address_space(1)))
typedef LAS char* lds_ptr;
typedef __amdgpu_buffer_rsrc_t rsrc_t;
#define DI __device__ __forceinline__

constexpr unsigned XB = 2048u * 640 * 2;
constexpr unsigned WS_EPOCH = 0;
constexpr unsigned WS_FLAG = 4096;
constexpr int FL_P0 = 0, FL_RT = 1, FL_QKV = 2, FL_ATT = 3, FL_WO = 4, FL_GU = 5, FL_DN = 6, FL_RT2 = 12;
constexpr unsigned WS_WO  = WS_FLAG + 13u * 32768;
constexpr unsigned WS_WGU = WS_WO + 640u * 640 * 2;
constexpr unsigned WS_WD  = WS_WGU + 3456u * 640 * 2;
constexpr unsigned WS_CSQ = WS_WD + 640u * 1728 * 2;
constexpr unsigned WS_CSG = WS_CSQ + 8u * 1920 * 4;
constexpr unsigned WS_ST  = WS_CSG + 4u * 3456 * 4;
constexpr unsigned STB = 2048u * 64;
constexpr unsigned WS_X2  = WS_ST + 4 * STB;
constexpr unsigned WS_L   = WS_X2 + XB;
constexpr unsigned L_Q1 = 0, L_Q2 = L_Q1 + 32u * 65536, L_K1 = L_Q2 + 16u * 32768, L_K2 = L_K1 + 32u * 65536, L_V = L_K2 + 16u * 32768,
                   L_O = L_V + XB, L_X1 = L_O + XB, L_H = L_X1 + XB, L_SZ = L_H + 2048u * 1728 * 2;
constexpr unsigned WS_END = WS_L + 2 * L_SZ;

constexpr int LDS_BYTES = 163840, MISC = 159744, M_SST = MISC, M_SCS = MISC + 1024, M_CNT = MISC + 2048, M_CNT2 = M_CNT + 8, M_CNT3 = M_CNT + 16, M_CNT4 = M_CNT + 24, M_SIG = M_CNT + 32, M_ISS = M_CNT + 40;
constexpr int QKV_NB = 5, QKV_NA = 4, QKV_A = QKV_NB * 16384;
constexpr int WO_NB = 10, DN_NB = 15;
constexpr int GU_NB = 7, GU_NA = 7, GU_A = GU_NB * 14336;
constexpr int AT_SLOT = 45056, AT_CONST = 3 * AT_SLOT, AT_SCR = AT_CONST + 10752, AT_MERGE = 3 * AT_SLOT;
static_assert(QKV_A + QKV_NA * 16384 <= MISC && DN_NB * 10240 <= MISC && GU_A + GU_NA * 8192 <= MISC, "lds");
static_assert(WO_NB * 10240 <= AT_MERGE && AT_MERGE + 24576 <= MISC && AT_SCR + 3 * 4096 <= MISC, "lds");

DI unsigned pk2(float a, float b) { f32x2 v = {a, b}; bf16x2 r = __builtin_convertvector(v, bf16x2); return __builtin_bit_cast(unsigned, r); }
DI float bflo(unsigned u) { return __uint_as_float(u << 16); }
DI float bfhi(unsigned u) { return __uint_as_float(u & 0xffff0000u); }
DI f32x4 mfma16(bf16x8 a, bf16x8 b, f32x4 c) { return __builtin_amdgcn_mfma_f32_16x16x32_bf16(a, b, c, 0, 0, 0); }
DI f32x16 mfma32(bf16x8 a, bf16x8 b, f32x16 c) { return __builtin_amdgcn_mfma_f32_32x32x16_bf16(a, b, c, 0, 0, 0); }
DI bf16x8 ldsr16(lds_ptr p) { return *(const LAS bf16x8*)p; }
DI bf16x8 ldstr(lds_ptr p, int second) {
  const s16x4 a = __builtin_amdgcn_ds_read_tr16_b64_v4i16((LAS s16x4*)p), b = __builtin_amdgcn_ds_read_tr16_b64_v4i16((LAS s16x4*)(p + second));
  return __builtin_bit_cast(bf16x8, (s16x8)__builtin_shufflevector(a, b, 0, 1, 2, 3, 4, 5, 6, 7));
}
#define BARRIER() asm volatile("s_waitcnt lgkmcnt(0)\n\ts_barrier" ::: "memory")
template <int N> DI void wait_vm() { asm volatile("s_waitcnt vmcnt(%0)" ::"n"(N > 63 ? 63 : N) : "memory"); }
#define WDUMP(id)
#define WRESET()
#define BARRIER_L() BARRIER()
#define BARRIER_NW() asm volatile("s_barrier" ::: "memory")
#define SBAR() __builtin_amdgcn_sched_barrier(0)
#define PIN(x) asm volatile("" : "+v"(x))
#define GL_HEAD "s_mov_b32 m0, %0\n\ts_nop 3\n\t"
#define GL(v, off) "global_load_lds_dwordx4 %" #v ", %1 offset:" #off " sc1\n\t"
DI void glds1(unsigned m, const char* sb, unsigned v0) { asm volatile(GL_HEAD GL(2, 0)::"s"(m), "s"(sb), "v"(v0) : "memory"); }
DI void glds2(unsigned m, const char* sb, unsigned v0, unsigned v1) {
  asm volatile(GL_HEAD GL(2, 0) GL(3, 1024)::"s"(m), "s"(sb), "v"(v0), "v"(v1) : "memory");
}
DI void glds3(unsigned m, const char* sb, unsigned v0, unsigned v1, unsigned v2) {
  asm volatile(GL_HEAD GL(2, 0) GL(3, 1024) GL(4, 2048)::"s"(m), "s"(sb), "v"(v0), "v"(v1), "v"(v2) : "memory");
}
DI void glds4(unsigned m, const char* sb, unsigned v0, unsigned v1, unsigned v2, unsigned v3) {
  asm volatile(GL_HEAD GL(2, 0) GL(3, 1024) GL(4, 2048) GL(5, 3072)::"s"(m), "s"(sb), "v"(v0), "v"(v1), "v"(v2), "v"(v3) : "memory");
}
DI void st16(rsrc_t r, unsigned off, u32x4 v) { __builtin_amdgcn_raw_buffer_store_b128(v, r, off, 0, 16); }
DI void st8(rsrc_t r, unsigned off, u32x2 v) { __builtin_amdgcn_raw_buffer_store_b64(v, r, off, 0, 16); }
DI void st4(rsrc_t r, unsigned off, unsigned v) { __builtin_amdgcn_raw_buffer_store_b32(v, r, off, 0, 16); }
DI u32x4 ld16(rsrc_t r, unsigned off) { return __builtin_amdgcn_raw_buffer_load_b128(r, off, 0, 16); }
DI u32x2 ld8(rsrc_t r, unsigned off) { return __builtin_amdgcn_raw_buffer_load_b64(r, off, 0, 16); }
DI u32x4 ld16s(rsrc_t r, unsigned voff, unsigned soff) { return __builtin_amdgcn_raw_buffer_load_b128(r, voff, soff, 16); }
DI void ldsw16(lds_ptr p, u32x4 v) { *(LAS u32x4*)p = v; }
DI f32x2 ld8_async(unsigned voff, const char* sbase) {
  f32x2 v; asm volatile("s_nop 4\n\tglobal_load_dwordx2 %0, %1, %2 sc1" : "=v"(v) : "v"(voff), "s"(sbase) : "memory"); return v;
}
DI u32x4 pair32(unsigned a0, unsigned a1, unsigned b0, unsigned b1) {
  auto x = __builtin_amdgcn_permlane32_swap(a0, b0, false, false);
  auto y = __builtin_amdgcn_permlane32_swap(a1, b1, false, false);
  return u32x4{x[0], y[0], x[1], y[1]};
}
template <int N> DI float ror16(float v) { return __int_as_float(__builtin_amdgcn_update_dpp(0, __float_as_int(v), 0x120 + N, 0xf, 0xf, false)); }
DI float sum32(float v) {
  v += ror16<8>(v); v += ror16<4>(v); v += ror16<2>(v); v += ror16<1>(v);
  auto a = __builtin_amdgcn_permlane16_swap(__float_as_uint(v), __float_as_uint(v), false, false);
  return __uint_as_float(a[0]) + __uint_as_float(a[1]);
}
DI u32x4 pair16(unsigned a0, unsigned a1, unsigned b0, unsigned b1) {
  auto x = __builtin_amdgcn_permlane16_swap(a0, b0, false, false);
  auto y = __builtin_amdgcn_permlane16_swap(a1, b1, false, false);
  return u32x4{x[0], y[0], x[1], y[1]};
}

DI float rows4(float v) {
  auto a = __builtin_amdgcn_permlane16_swap(__float_as_uint(v), __float_as_uint(v), false, false);
  v = __uint_as_float(a[0]) + __uint_as_float(a[1]);
  auto b = __builtin_amdgcn_permlane32_swap(__float_as_uint(v), __float_as_uint(v), false, false);
  return __uint_as_float(b[0]) + __uint_as_float(b[1]);
}

#define STAMP(id)
#define STAMPC(id)
#define STAMPL(sl, id)
#define SLOT(n)

#define WARM_GU 1
#define POLL_DEPTH 1
#define POLL_GATE 1
#define POLL_SLEEP 1
#define ATT_PRIO 3
#define ATT_ORD 0
#define WO_AH 6
#define DN_AH 8
#define ATT_ROT 1
#define GU_SKIP 1
#define ATT_LATE 4
#define GU16 1
#define QKV16 0
#define PACE 16
#define SPANST(id)
struct Ctx {
  char* ws; rsrc_t r; lds_ptr lds; u64 tgt; int lane, xcd, j;
};
DI Ctx fresh(const Ctx& c0) {
  Ctx c = c0;
  asm volatile("v_mbcnt_lo_u32_b32 %0, -1, 0\n\tv_mbcnt_hi_u32_b32 %0, -1, %0" : "=v"(c.lane), "+s"(c.xcd), "+s"(c.j), "+s"(c.ws));
  return c;
}
DI int opq(int v) { asm volatile("" : "+s"(v)); return v; }
constexpr int SPIN_CAP = 1 << 18;

DI u64 epoch(const char* ws) {
  u64 e; asm volatile("s_nop 4\n\ts_load_dwordx2 %0, %1, 0x0\n\ts_waitcnt lgkmcnt(0)" : "=s"(e) : "s"(ws + WS_EPOCH) : "memory"); return e + 1;
}
DI unsigned flg(int arr, int unit) { return WS_FLAG + (arr * 256 + unit) * 128; }
DI void publish(const Ctx& c, int cnt_off, unsigned n, unsigned to, bool mark = false) {
  if (mark && c.lane == 0) __hip_atomic_fetch_add((LAS unsigned*)(c.lds + M_ISS), 1u, __ATOMIC_RELAXED, __HIP_MEMORY_SCOPE_WORKGROUP);
  wait_vm<0>();
  if (c.lane == 0) {
    const unsigned old = __hip_atomic_fetch_add((LAS unsigned*)(c.lds + cnt_off), 1u, __ATOMIC_RELAXED, __HIP_MEMORY_SCOPE_WORKGROUP);
    if (old % n == n - 1) __hip_atomic_store((GAS u64*)(c.ws + to), c.tgt, __ATOMIC_RELAXED, __HIP_MEMORY_SCOPE_AGENT);
  }
}
template <int N> struct Poll {
  u64 v[POLL_DEPTH][N];
  DI void run(const Ctx& c, unsigned o0, unsigned o1 = 0) {

    auto ld = [&](int r) {
      asm volatile("global_load_dwordx2 %0, %1, %2 sc1" : "=v"(v[r][0]) : "v"(o0), "s"(c.ws) : "memory");
      if constexpr (N == 2) asm volatile("global_load_dwordx2 %0, %1, %2 sc1" : "=v"(v[r][N - 1]) : "v"(o1), "s"(c.ws) : "memory");
      __builtin_amdgcn_s_sleep(POLL_SLEEP);
    };
    auto ok = [&](int r) {
      if constexpr (N == 2) asm volatile("s_waitcnt vmcnt(%2)" : "+v"(v[r][0]), "+v"(v[r][N - 1]) : "n"((POLL_DEPTH - 1) * N) : "memory");
      else asm volatile("s_waitcnt vmcnt(%1)" : "+v"(v[r][0]) : "n"(POLL_DEPTH - 1) : "memory");
      return __all(v[r][0] == c.tgt && v[r][N - 1] == c.tgt);
    };
    asm volatile("s_nop 4" ::: "memory");
    ld(0); if (POLL_DEPTH > 1) ld(1);
#pragma unroll 1
    for (int spin = 0; spin < SPIN_CAP; ++spin) {
      if (ok(0)) break; ld(0);
      if (POLL_DEPTH > 1) { if (ok(1)) break; ld(1); }
    }
  }
  DI void keep() {
#pragma unroll
    for (int r = 0; r < POLL_DEPTH; ++r) { asm volatile("" : "+v"(v[r][0])); if constexpr (N == 2) asm volatile("" : "+v"(v[r][N - 1])); }
  }
};
DI void poll(const Ctx& c, unsigned slot) { Poll<1> p; p.run(c, slot); wait_vm<0>(); p.keep(); }
DI void sig_set(const Ctx& c, unsigned v) { if (c.lane == 0) __hip_atomic_store((LAS unsigned*)(c.lds + M_SIG), v, __ATOMIC_RELAXED, __HIP_MEMORY_SCOPE_WORKGROUP); }
DI void sig_wait(const Ctx& c, unsigned v, int off = M_SIG, bool calm = false) {
#pragma unroll 1
  for (int spin = 0; spin < SPIN_CAP * 16; ++spin) {
    if (__builtin_amdgcn_readfirstlane(__hip_atomic_load((LAS unsigned*)(c.lds + off), __ATOMIC_RELAXED, __HIP_MEMORY_SCOPE_WORKGROUP)) >= v) break;
    if (calm) __builtin_amdgcn_s_sleep(8); else if (off != M_SIG) __builtin_amdgcn_s_sleep(1);
  }
  asm volatile("" ::: "memory");
}

DI void gate(const Ctx& c, Poll<1>& p, int half, unsigned slot, unsigned sig, unsigned iss) {
  if (half == 0) { if (POLL_GATE) sig_wait(c, iss, M_ISS); p.run(c, slot); sig_set(c, sig); STAMPL(sig - 1, 6); } else sig_wait(c, sig);
}

template <int PP, int NSLOT, int NS, int LAG = 1>
struct Ring {
  unsigned voff[PP];
  const char* base;
  unsigned adv, l0;
  int slot;
  DI void init(const char* b, unsigned a, unsigned lds0, int half) {
    base = b - 4096; adv = a; l0 = lds0 + half * PP * 1024; slot = 0;
#pragma unroll
    for (int p = 0; p < PP; ++p) voff[p] += 4096 - (p & 3) * 1024;
  }
  DI void issue() {
    const unsigned m = l0 + slot * (2 * PP * 1024);
    glds4(m, base, voff[0], voff[1], voff[2], voff[3]);
    if constexpr (PP == 5) glds1(m + 4096, base, voff[4]);
    if constexpr (PP == 7) glds3(m + 4096, base, voff[4], voff[5], voff[6]);
    if constexpr (PP == 8) glds4(m + 4096, base, voff[4], voff[5], voff[6], voff[7]);
    base += adv; slot = slot == NSLOT - 1 ? 0 : slot + 1;
  }
  template <int N = NSLOT> DI void fill() {
#pragma unroll 1
    for (int s = 0; s < N; ++s) issue();
  }
  template <int NEED> DI void fill_ahead(const Ctx& c, unsigned sig) {
    int n = 0;
#pragma unroll 1
    while (n < NSLOT) {
      issue(); wait_vm<PACE>(); ++n;
      if (n * PP >= NEED * PP + PACE &&
          __builtin_amdgcn_readfirstlane(__hip_atomic_load((LAS unsigned*)(c.lds + M_SIG), __ATOMIC_RELAXED, __HIP_MEMORY_SCOPE_WORKGROUP)) >= sig) break;
    }
    if (n == NSLOT) wait_vm<(NSLOT - NEED) * PP>();
    BARRIER_L(); WRESET();
#pragma unroll 1
    for (; n < NSLOT; ++n) issue();
  }
  DI void rest() { range<NSLOT - 2, NSLOT - LAG>(); steady(); range<NSLOT - 1 - LAG, 0>(); BARRIER_L(); }
  template <int R, int E> DI void range() { if constexpr (R >= E) { wait_vm<R * PP>(); BARRIER_L(); range<R - 1, E>(); } }
  template <int D0 = NSLOT> DI void head() {
    wait_vm<(D0 - 1) * PP>(); BARRIER_L(); WRESET();
    if constexpr (D0 < NSLOT) fill<NSLOT - D0>();
    range<NSLOT - 2, NSLOT - LAG>();
    steady();
  }
  DI void steady() {
#pragma unroll 1
    for (int k = LAG; k < NS - NSLOT + LAG; ++k) { wait_vm<(NSLOT - 1 - LAG) * PP>(); BARRIER_L(); issue(); }
  }
  template <int D0 = NSLOT> DI void run() { head<D0>(); range<NSLOT - 1 - LAG, 0>(); BARRIER_L(); }
};

DI void ln_rows(const Ctx& c, int row, const f32x2 (&p)[8]) {
  f32x2 t = p[0];
#pragma unroll
  for (int i = 1; i < 8; ++i) t += p[i];
  const float m8 = t[0] * 0.125f; float q = 0.f;
#pragma unroll
  for (int i = 0; i < 8; ++i) { const float a = p[i][0] - m8; q += a * a; }
  const float rstd = __builtin_amdgcn_rsqf((t[1] + q * (1.f / 80.f)) * (1.f / 640.f) + 1e-5f);
  *(LAS float*)(c.lds + M_SST + row * 4) = rstd;
  *(LAS float*)(c.lds + M_SST + 512 + row * 4) = -t[0] * (1.f / 640.f);
}
template <int NP> DI void ln_cols_sum(const Ctx& c, int n4, const u32x4 (&q)[NP]) {
  f32x4 cs = __builtin_bit_cast(f32x4, q[0]);
#pragma unroll
  for (int i = 1; i < NP; ++i) cs += __builtin_bit_cast(f32x4, q[i]);
  if (c.lane < n4) *(LAS f32x4*)(c.lds + M_SCS + c.lane * 16) = cs;
}
template <int NP> DI void ln_cols(const Ctx& c, int n4, unsigned off, unsigned pitch) {
  u32x4 q[NP];
#pragma unroll
  for (int i = 0; i < NP; ++i) q[i] = ld16(c.r, off + i * pitch + (c.lane < n4 ? c.lane : 0) * 16);
  ln_cols_sum<NP>(c, n4, q);
}

#define MAP 0
DI int qkv_panel(const Ctx& c) { return (c.xcd & 3) * 4 + (c.j & 3); }
DI int qkv_cb(const Ctx& c) { return (c.xcd >> 2) * 8 + (c.j >> 2); }
DI int pr_panel(const Ctx& c) { return (c.xcd & 3) * 8 + (c.j & 7); }
DI int pr_cb(const Ctx& c) { return (c.xcd >> 2) * 4 + (c.j >> 3); }
DI int gu_panel(const Ctx& c) { return (c.xcd & 1) * 8 + (c.j & 7); }
DI int gu_cb(const Ctx& c) { return (c.xcd >> 1) * 4 + (c.j >> 3); }
DI int att_head(const Ctx& c) { return c.xcd * 4 + (c.j & 3); }
DI int att_qb(const Ctx& c) { return c.j >> 2; }

struct Phase0 {
  u32x4 v[3], t[10];
  DI void load(const Ctx& c, int hw, const uint16_t* x, const uint16_t* wqkv) {
    const int lane = c.lane, r32 = lane & 31, b = blockIdx.x, row = b * 8 + hw * 2 + (lane >> 5);
    v[0] = *(const u32x4*)(x + row * 640 + r32 * 8); v[1] = *(const u32x4*)(x + row * 640 + 256 + r32 * 8);
    v[2] = *(const u32x4*)(x + row * 640 + 512 + (r32 & 15) * 8);
    if (hw == 3 && b < 240) {
      const rsrc_t r = __builtin_amdgcn_make_buffer_rsrc((void*)wqkv, 0, 640 * 3840, 0x00020000);
#pragma unroll
      for (int i = 0; i < 10; ++i)
        t[i] = __builtin_amdgcn_raw_buffer_load_b128(r, (lane >> 3) * 3840 + (lane & 7) * 16, ((b / 30) * 80 + i * 8) * 3840 + (b % 30) * 128, 0);
    }
  }
  DI void rows(const Ctx& c, int hw) {
    const int lane = c.lane, r32 = lane & 31, row = blockIdx.x * 8 + hw * 2 + (lane >> 5);
    const float w2 = r32 < 16 ? 1.f : 0.f;
    float sum = 0.f, s2 = 0.f;
#pragma unroll
    for (int e = 0; e < 4; ++e) { sum += (bflo(v[0][e]) + bfhi(v[0][e])) + (bflo(v[1][e]) + bfhi(v[1][e])); s2 += bflo(v[2][e]) + bfhi(v[2][e]); }
    sum = sum32(sum + w2 * s2);
    const float mean = sum * (1.f / 640.f); float m2 = 0.f; s2 = 0.f;
#pragma unroll
    for (int e = 0; e < 4; ++e) {
      const float a0 = bflo(v[0][e]) - mean, a1 = bfhi(v[0][e]) - mean, a2 = bflo(v[1][e]) - mean, a3 = bfhi(v[1][e]) - mean;
      const float a4 = bflo(v[2][e]) - mean, a5 = bfhi(v[2][e]) - mean;
      m2 += a0 * a0 + a1 * a1 + a2 * a2 + a3 * a3; s2 += a4 * a4 + a5 * a5;
    }
    m2 = sum32(m2 + w2 * s2);
    if (r32 < 8) st8(c.r, WS_ST + (row >> 7) * 8192 + r32 * 1024 + (row & 127) * 8, u32x2{__float_as_uint(sum * 0.125f), __float_as_uint(m2 * 0.125f)});
  }
  float s[8];
  DI void cols1(int hw) {
    if (hw == 3 && blockIdx.x < 240) {
      unsigned ul = 0x00003f80u, uh = 0x3f800000u;
      asm volatile("" : "+v"(ul), "+v"(uh));
      const bf16x2 lo = __builtin_bit_cast(bf16x2, ul), hi = __builtin_bit_cast(bf16x2, uh);
#pragma unroll
      for (int e = 0; e < 4; ++e) {
        s[2 * e] = 0.f; s[2 * e + 1] = 0.f;
#pragma unroll
        for (int i = 0; i < 10; ++i) {
          const unsigned u = t[i][e];
          s[2 * e] = __builtin_amdgcn_fdot2_f32_bf16(__builtin_bit_cast(bf16x2, u), lo, s[2 * e], false);
          s[2 * e + 1] = __builtin_amdgcn_fdot2_f32_bf16(__builtin_bit_cast(bf16x2, u), hi, s[2 * e + 1], false);
        }
      }
#pragma unroll
      for (int e = 0; e < 8; ++e) PIN(s[e]);
    }
  }
  DI void cols2(const Ctx& c, int hw) {
    const int lane = c.lane, b = blockIdx.x;
    if (hw == 3 && b < 240) {
#pragma unroll
      for (int e = 0; e < 8; ++e) s[e] = rows4(s[e] + ror16<8>(s[e]));
      const int hi = (lane >> 3) & 1;
      const f32x4 o = hi ? f32x4{s[4], s[5], s[6], s[7]} : f32x4{s[0], s[1], s[2], s[3]};
      if (lane < 16) st16(c.r, WS_CSQ + ((b / 30) * 1920 + (b % 30) * 64 + (lane & 7) * 8 + hi * 4) * 4, __builtin_bit_cast(u32x4, o));
    }
  }
};

template <bool SWAP, class PRE>
DI void qkv_loop(const Ctx& c, int w, int layer, f32x16 (&acc)[2][2], PRE pre) {
  const int lane = c.lane, i = lane & 15, g = lane >> 4, wm = w & 1, wn = w >> 1;
  lds_ptr pa[4];
#pragma unroll
  for (int s = 0; s < 4; ++s)
    pa[s] = c.lds + QKV_A + wm * 8192 + (g & 1) * 2048 +
            (layer == 0 ? (i >> 3) * 1024 + (i & 7) * 128 + (((2 * s + (g >> 1)) ^ ((i >> 1) & 7)) << 4) : (2 * s + (g >> 1)) * 256 + i * 16);
  const int rb = ((g >> 1) * 8 + (i >> 2)) * 256 + (i & 3) * 8, u = (wn * 4 + (g & 1)) ^ ((i >> 2) << 1);
  lds_ptr pb0 = c.lds + rb + (u << 5), pb1 = c.lds + rb + ((u ^ 2) << 5);
  bf16x8 x0[4], x1[4], w0[4], w1[4];
  auto rdx = [&](bf16x8* xn, int hs, int e) { xn[e] = ldsr16(pa[hs * 2 + (e >> 1)] + (e & 1) * 4096); };
  auto rdw = [&](bf16x8* wn_, int hs, int e) { wn_[e] = ldstr(((e & 1) ? pb1 : pb0) + (hs * 2 + (e >> 1)) * 4096, 1024); };
  auto step = [&](const bf16x8* xa, const bf16x8* wb, bf16x8* xn, bf16x8* wn_, int hs, bool ld) {
#pragma unroll
    for (int q = 0; q < 8; ++q) {
      if (ld) { if (q & 1) rdx(xn, hs, q >> 1); else rdw(wn_, hs, q >> 1); }
      const int s = q >> 2, tm = q & 1, tn = (q >> 1) & 1;
      acc[tm][tn] = SWAP ? mfma32(wb[s * 2 + tn], xa[s * 2 + tm], acc[tm][tn]) : mfma32(xa[s * 2 + tm], wb[s * 2 + tn], acc[tm][tn]);
      SBAR();
    }
  };
  SLOT(layer * 5);
  if (layer) STAMP(0);
  STAMP(1);
  BARRIER();
  STAMP(2); STAMPC(0); WRESET();
#pragma unroll
  for (int e = 0; e < 4; ++e) { rdw(w0, 0, e); rdx(x0, 0, e); }
  int sa = 0, sb = 0;
#pragma unroll 1
  for (int st = 0; st < 9; ++st) {
    step(x0, w0, x1, w1, 1, true);
    const int da = sa == QKV_NA - 1 ? -(QKV_NA - 1) * 16384 : 16384, db = sb == QKV_NB - 1 ? -(QKV_NB - 1) * 16384 : 16384;
    sa = sa == QKV_NA - 1 ? 0 : sa + 1; sb = sb == QKV_NB - 1 ? 0 : sb + 1;
    pa[0] += da; pa[1] += da; pa[2] += da; pa[3] += da; pb0 += db; pb1 += db;
    BARRIER_NW();
    step(x1, w1, x0, w0, 0, true);
  }
  step(x0, w0, x1, w1, 1, true);
  BARRIER();
  pre(); SBAR();
  step(x1, w1, x0, w0, 0, false);
  STAMP(3); STAMPC(1);
}
DI unsigned qkv_w_off(const Ctx& c, int q) {
  const int row = q * 4 + (c.lane >> 4);
  return row * 3840 + qkv_cb(c) * 256 + (((c.lane & 15) ^ (QKV16 ? ((row & 3) << 1) | (row & 8) : (row & 3) << 2)) << 4);
}
DI void qkv_helper(Ctx c, u64& tgt, int hw, int layer, const uint16_t* x, const uint16_t* wqkv) {
  const int panel = qkv_panel(c), cb = qkv_cb(c), l = c.lane;
  const bool on = cb != 15;
  if (layer == 0) {
    Phase0 p0;
    p0.load(c, hw, x, wqkv);
    c.tgt = tgt = epoch(c.ws);
    if (on) BARRIER();
    p0.cols1(hw);
    if (on && hw == 3) BARRIER();
    p0.rows(c, hw);
    if (on && hw != 3) BARRIER();
    p0.cols2(c, hw);
    if (on) { BARRIER(); BARRIER(); }
    publish(c, M_CNT2, 4, flg(FL_P0, blockIdx.x));
    if (!on) return;
    BARRIER(); BARRIER();
    const unsigned at = flg(FL_P0, hw < 2 ? panel * 16 + hw * 8 + (l & 7) : (l & 7) * 30 + cb * 2 + ((l >> 3) & 1));
    const u64 v = __hip_atomic_load((const GAS u64*)(c.ws + at), __ATOMIC_RELAXED, __HIP_MEMORY_SCOPE_AGENT);
    BARRIER();
    if (!__all(v == c.tgt)) poll(c, at);
    if (hw < 2) {
      f32x2 p[8];
#pragma unroll
      for (int i = 0; i < 8; ++i) p[i] = __builtin_bit_cast(f32x2, ld8(c.r, WS_ST + panel * 8192 + i * 1024 + (hw * 64 + l) * 8));
      BARRIER(); BARRIER();
      ln_rows(c, hw * 64 + l, p);
    } else if (hw == 2) {
      u32x4 q[8];
#pragma unroll
      for (int i = 0; i < 8; ++i) q[i] = ld16(c.r, WS_CSQ + cb * 512 + i * 7680 + (l & 31) * 16);
      BARRIER(); BARRIER();
      ln_cols_sum<8>(c, 32, q);
    } else { BARRIER(); BARRIER(); }
    BARRIER(); BARRIER();
  } else if (on) {
    if (hw == 2) ln_cols<8>(c, 32, WS_CSQ + cb * 512, 7680);
    BARRIER(); BARRIER(); BARRIER(); BARRIER(); BARRIER(); BARRIER();
    if (hw < 2) {
      f32x2 p[8];
#pragma unroll
      for (int i = 0; i < 8; ++i) p[i] = __builtin_bit_cast(f32x2, ld8(c.r, WS_ST + 2 * STB + panel * 8192 + i * 1024 + (hw * 64 + l) * 8));
      BARRIER(); BARRIER();
      ln_rows(c, hw * 64 + l, p);
    } else { BARRIER(); BARRIER(); }
    BARRIER(); BARRIER(); BARRIER();
  }
}
DI void qkv_compute(Ctx c, u64& tgt, int w, int layer, const uint16_t* x, const uint16_t* wqkv) {
  const int panel = qkv_panel(c), cb = qkv_cb(c);
  if (w >= 4) { qkv_helper(c, tgt, w - 4, layer, x, wqkv); return; }
  if (layer == 0) c.tgt = tgt = epoch(c.ws);
  if (cb == 15) return;
  const int lane = c.lane, r32 = lane & 31, h = lane >> 5, wm = w & 1, wn = w >> 1;
  const unsigned L = WS_L + layer * L_SZ;
  f32x16 acc[2][2] = {{f32x16{}, f32x16{}}, {f32x16{}, f32x16{}}};
  const LAS float* srs = (const LAS float*)(c.lds + M_SST) + wm * 64;
  const LAS float* scs = (const LAS float*)(c.lds + M_SCS) + wn * 64;
  if (cb < 10) {
    float rs[2], nm[2]; f32x4 cs[8];
    qkv_loop<true>(c, w, layer, acc, [&]() {
      rs[0] = srs[r32]; rs[1] = srs[32 + r32]; nm[0] = srs[128 + r32]; nm[1] = srs[160 + r32];
#pragma unroll
      for (int e = 0; e < 8; ++e) cs[e] = *(const LAS f32x4*)(scs + e * 8 + h * 4);
    });
    const float qs = cb < 5 ? 0.22360679774997896f * 1.4426950408889634f : 1.f;
    const unsigned i1 = L + (cb < 5 ? L_Q1 : L_K1), i2 = L + (cb < 5 ? L_Q2 : L_K2);
    rs[0] *= qs; rs[1] *= qs;
#pragma unroll
    for (int tp = 0; tp < 4; ++tp) {
      const int tn = tp >> 1, pr = tp & 1;
      const int o = (cb % 5) * 16 + wn * 8 + tp * 2 + h, hp = o / 5, pos = o - hp * 5;
      const unsigned ts = pos == 2 ? 512 : 1024;
      const unsigned a0 = (pos == 2 ? i2 + hp * 32768 : i1 + (2 * hp + (pos > 2)) * 65536 + (pos > 2 ? pos - 3 : pos) * 512) +
                          (panel * 4 + wm * 2) * ts + r32 * 16;
#pragma unroll
      for (int tm = 0; tm < 2; ++tm) {
        const f32x16 a = acc[tm][tn];
        const f32x4 u = (f32x4{a[pr * 8], a[pr * 8 + 1], a[pr * 8 + 2], a[pr * 8 + 3]} + cs[tp * 2] * nm[tm]) * rs[tm];
        const f32x4 v = (f32x4{a[pr * 8 + 4], a[pr * 8 + 5], a[pr * 8 + 6], a[pr * 8 + 7]} + cs[tp * 2 + 1] * nm[tm]) * rs[tm];
        st16(c.r, a0 + tm * ts, pair32(pk2(u[0], u[1]), pk2(u[2], u[3]), pk2(v[0], v[1]), pk2(v[2], v[3])));
      }
    }
  } else {
    float cs[2]; f32x4 rn[2][4];
    auto rd = [&](int tp) {
#pragma unroll
      for (int e = 0; e < 4; ++e) rn[tp & 1][e] = *(const LAS f32x4*)(srs + (e >> 1) * 128 + tp * 16 + (e & 1) * 8 + h * 4);
    };
    qkv_loop<false>(c, w, layer, acc, [&]() { cs[0] = scs[r32]; cs[1] = scs[32 + r32]; rd(0); rd(1); });
    unsigned a0[2];
#pragma unroll
    for (int tn = 0; tn < 2; ++tn) {
      const int cv = (cb - 10) * 128 + wn * 64 + tn * 32 + r32, head = cv / 20, dim = cv - head * 20;
      const int d = (head & 1) ? (dim < 4 ? dim + 16 : dim - 4) : dim;
      a0[tn] = L + L_V + head * 81920 + (panel * 8 + wm * 4) * 640 + h * 320 + d * 16;
    }
#pragma unroll
    for (int tp = 0; tp < 4; ++tp) {
      const int tm = tp >> 1, pr = tp & 1;
      u32x4 o[2];
#pragma unroll
      for (int tn = 0; tn < 2; ++tn) {
        const f32x16 a = acc[tm][tn];
        const f32x4 u = (f32x4{a[pr * 8], a[pr * 8 + 1], a[pr * 8 + 2], a[pr * 8 + 3]} + rn[pr][2] * cs[tn]) * rn[pr][0];
        const f32x4 v = (f32x4{a[pr * 8 + 4], a[pr * 8 + 5], a[pr * 8 + 6], a[pr * 8 + 7]} + rn[pr][3] * cs[tn]) * rn[pr][1];
        o[tn] = pair32(pk2(u[0], u[1]), pk2(u[2], u[3]), pk2(v[0], v[1]), pk2(v[2], v[3]));
      }
      if (tp < 2) rd(tp + 2);
      st16(c.r, a0[0] + tp * 640, o[0]); st16(c.r, a0[1] + tp * 640, o[1]);
      SBAR();
    }
  }
  SLOT(layer * 5);
  STAMP(4);
  publish(c, M_CNT4, 4, flg(FL_QKV + layer * 5, panel * 16 + cb), true);
  STAMP(5);
}
typedef Ring<8, QKV_NB, 10, 2> QkvB;
DI void qkv_load0(const Ctx& c, int lw, const uint16_t* x, const uint16_t* wqkv) {
  const int half = lw & 1;
  if (lw >= 2) {
    QkvB rg;
#pragma unroll
    for (int p = 0; p < 8; ++p) rg.voff[p] = qkv_w_off(c, half * 8 + p);
    rg.init((const char*)wqkv, 64 * 3840, 0, half);
    rg.template fill<3>(); rg.template run<3>();
  } else {
    Ring<8, QKV_NA, 10, 2> rg;
#pragma unroll
    for (int p = 0; p < 8; ++p) {
      const int mt = qkv_panel(c) * 8 + half * 4 + (p >> 1), r16 = (p & 1) * 8 + (c.lane >> 3);
      rg.voff[p] = (mt * 16 + r16) * 1280 + (((c.lane & 7) ^ ((r16 >> 1) & 7)) << 4);
    }
    rg.init((const char*)x, 128, QKV_A, half);
    rg.template fill<3>(); rg.template run<3>();
  }
}
DI void qkv_load1(const Ctx& c, int lw, const uint16_t* wqkv) {
  const int half = lw & 1;
  static_assert(QKV_NA == 4 && QKV_NB == 5, "schedule");
  if (lw >= 2) {
    QkvB rg;
#pragma unroll
    for (int p = 0; p < 8; ++p) rg.voff[p] = qkv_w_off(c, half * 8 + p);
    rg.init((const char*)wqkv, 64 * 3840, 0, half);
    sig_wait(c, 12, M_CNT4);
    rg.template fill_ahead<1>(c, 6);
    rg.rest();
  } else {
    Ring<8, QKV_NA, 10, 2> rg;
#pragma unroll
    for (int p = 0; p < 8; ++p) rg.voff[p] = WS_X2 + (qkv_panel(c) * 8 + half * 4 + (p >> 1)) * 20480 + (p & 1) * 1024 + c.lane * 16;
    rg.init(c.ws, 2048, QKV_A, half);
    Poll<1> pl;
    gate(c, pl, half, flg(FL_DN, qkv_panel(c) * 16 + (c.lane & 15)), 6, 28);
    rg.template fill<2>(); rg.template run<2>();
    pl.keep();
  }
}

DI f32x16 qk8(s16x4 k2, s16x4 q2) {
 return __builtin_amdgcn_mfma_f32_32x32x8bf16_1k(k2, q2, f32x16{}, 0, 0, 0); }
DI f32x16 qk16(bf16x8 k1, bf16x8 q1, f32x16 s) { return __builtin_amdgcn_mfma_f32_32x32x16_bf16(k1, q1, s, 0, 0, 0); }
DI f32x16 pv_lo(bf16x8 v, f32x16 p, f32x16 o) {
  return __builtin_amdgcn_smfmac_f32_32x32x32_bf16(v, __builtin_bit_cast(bf16x16, (f32x8)__builtin_shufflevector(p, p, 0, 1, 2, 3, 4, 5, 6, 7)), o, 0xDDDD, 0, 0);
}
DI f32x16 pv_hi(bf16x8 v, f32x16 p, f32x16 o) {
  return __builtin_amdgcn_smfmac_f32_32x32x32_bf16(v, __builtin_bit_cast(bf16x16, (f32x8)__builtin_shufflevector(p, p, 8, 9, 10, 11, 12, 13, 14, 15)), o, 0xDDDD, 0, 0);
}
DI float swap32(float v, int h) {
  auto r = __builtin_amdgcn_permlane32_swap(__float_as_uint(v), __float_as_uint(v), false, false);
  return __uint_as_float(h ? r[0] : r[1]);
}
#define EXP4(X, i) do { X[i] = __builtin_amdgcn_exp2f(X[i]); X[i + 1] = __builtin_amdgcn_exp2f(X[i + 1]); X[i + 2] = __builtin_amdgcn_exp2f(X[i + 2]); \
                        X[i + 3] = __builtin_amdgcn_exp2f(X[i + 3]); PIN(X); SBAR(); } while (0)
DI void att_compute(const Ctx& c, int w, int layer) {
  const int lane = c.lane, head = att_head(c), qb = att_qb(c);
  const int qp = w & 3, kp = w >> 2, r32 = lane & 31, h = lane >> 5;
  lds_ptr lds = c.lds;
  const unsigned L = WS_L + layer * L_SZ;
  SLOT(layer * 5 + 1);
  STAMP(0);
  for (int s = w * 64 + lane; s < 672; s += 512) {
    const unsigned v = s % 20 == 0 ? 0x3f803f80u : 0u;
    *(LAS u32x4*)(lds + AT_CONST + s * 16) = u32x4{v, v, v, v};
  }
  lds_ptr pk1 = lds + kp * 8192 + lane * 16;
  lds_ptr pk2_ = h ? lds + AT_CONST + 16 : lds + 16384 + kp * 4096 + r32 * 16 + (head & 1) * 8;
  lds_ptr pv = (r32 >= 20 ? lds + AT_CONST : lds + 24576 + kp * 10240) + h * 320 + r32 * 16;
  const int vmask = r32 >= 20 ? 0 : -1, kmask = h ? 0 : -1;
  f32x16 o0 = f32x16{}, o1 = f32x16{};
  f32x16 s0, s1, s2, s3 = f32x16{};
  STAMP(1);
  BARRIER();
  STAMP(2); STAMPC(0); WRESET();
  const bf16x8 q1a = ldsr16(lds + 2 * AT_SLOT + qp * 2048 + lane * 16), q1b = ldsr16(lds + 2 * AT_SLOT + qp * 2048 + 1024 + lane * 16);
  lds_ptr pq2 = h ? lds + AT_CONST + 16 : lds + 2 * AT_SLOT + 8192 + qp * 1024 + r32 * 16 + (head & 1) * 8;
  const s16x4 q2a = *(const LAS s16x4*)pq2, q2b = *(const LAS s16x4*)(pq2 + 512);
  bf16x8 k1 = ldsr16(pk1), vlo = ldsr16(pv), vhi = bf16x8{};
  s16x4 k2 = *(const LAS s16x4*)pk2_;
  s0 = qk16(k1, q1a, qk8(k2, q2a));
#define PH_EVEN(SC, SN, SP, KLOAD, VOFF) do { \
    SN = qk8(k2, q2b); SBAR(); EXP4(SC, 0); SN = qk16(k1, q1b, SN); SBAR(); KLOAD; SBAR(); EXP4(SC, 4); \
    o1 = pv_hi(vhi, SP, o1); SBAR(); vhi = ldsr16(pv + (VOFF) + 640); SBAR(); EXP4(SC, 8); o0 = pv_lo(vlo, SC, o0); SBAR(); EXP4(SC, 12); } while (0)
#define PH_ODD(SC, SN, SP, VLOAD) do { \
    SN = qk8(k2, q2a); SBAR(); EXP4(SC, 0); SN = qk16(k1, q1a, SN); SBAR(); EXP4(SC, 4); \
    o0 = pv_hi(vhi, SP, o0); SBAR(); EXP4(SC, 8); o1 = pv_lo(vlo, SC, o1); SBAR(); VLOAD; SBAR(); EXP4(SC, 12); } while (0)
#define KLD(t) do { k1 = ldsr16(pk1 + (t) * 1024); k2 = *(const LAS s16x4*)(pk2_ + (t) * 512); } while (0)
#define PRI(v) do { asm volatile("s_cmp_eq_u32 %0, 0\n\ts_cbranch_scc1 1f\n\ts_setprio " #v "\n1:" :: "s"(kp) : "scc"); SBAR(); } while (0)
#define HI(n) if (ATT_PRIO == (n)) PRI(1);
#define LO(n) if (ATT_PRIO == (n)) PRI(0);
#define TWO(t) HI(3) HI(4) HI(5) PH_EVEN(s0, s1, s3, KLD((t) + 1), (t) * 1280); PH_ODD(s1, s2, s0, vlo = ldsr16(pv + ((t) + 1) * 1280)); LO(4)
  for (int grp = 0; grp < 4; ++grp) {
    TWO(0) PH_EVEN(s2, s3, s1, if (grp == 0) BARRIER_NW(); KLD(2), 1280); LO(3) PH_ODD(s3, s0, s2, vlo = ldsr16(pv + 2 * 1280));
    TWO(2) PH_EVEN(s2, s3, s1, KLD(4), 3 * 1280); LO(3) LO(5) PH_ODD(s3, s0, s2, vlo = ldsr16(pv + 4 * 1280));
    TWO(4) PH_EVEN(s2, s3, s1, KLD(6), 5 * 1280); LO(3) PH_ODD(s3, s0, s2, vlo = ldsr16(pv + 6 * 1280));
    TWO(6)
    const int d = grp == 2 ? -2 * AT_SLOT : grp == 3 ? 0 : AT_SLOT;
    PH_EVEN(s2, s3, s1, pk1 += d; pk2_ += (d & kmask); if (grp != 3) BARRIER(); KLD(0), 7 * 1280);
    LO(3) LO(5) PH_ODD(s3, s0, s2, pv += (d & vmask); vlo = ldsr16(pv));
  }
  o1 = pv_hi(vhi, s3, o1);
  STAMP(3); STAMPC(1);
  __builtin_amdgcn_s_setprio(0);
  BARRIER();
  const f32x16 give = kp ? o0 : o1, keep = kp ? o1 : o0;
#pragma unroll
  for (int e = 0; e < 3; ++e)
    *(LAS f32x4*)(lds + AT_MERGE + (w * 3 + e) * 1024 + lane * 16) = f32x4{give[4 * e], give[4 * e + 1], give[4 * e + 2], give[4 * e + 3]};
  BARRIER();
  f32x4 a[3];
#pragma unroll
  for (int e = 0; e < 3; ++e)
    a[e] = f32x4{keep[4 * e], keep[4 * e + 1], keep[4 * e + 2], keep[4 * e + 3]} + *(const LAS f32x4*)(lds + AT_MERGE + ((w ^ 4) * 3 + e) * 1024 + lane * 16);
  const int qt = qb * 8 + qp * 2 + kp;
  float l = h ? a[2][0] : 0.f;
  l += swap32(l, h);
  if (__builtin_expect(__any(!(l >= 5.421e-20f && l <= 1.8447e19f)), 0)) {
    const unsigned o2 = (head >> 1) * 32768 + r32 * 16 + (head & 1) * 8;
    const bf16x8 fq1 = __builtin_bit_cast(bf16x8, ld16(c.r, L + L_Q1 + head * 65536 + qt * 1024 + lane * 16));
    u32x2 fq2 = ld8(c.r, L + L_Q2 + o2 + qt * 512);
    if (h) fq2 = u32x2{0u, 0u};
    const unsigned vi = L + L_V + head * 81920 + h * 320 + (r32 < 20 ? r32 : 0) * 16;
    f32x16 oo = f32x16{};
    float m = -INFINITY;
    const u32x4 one8 = {0x3f803f80u, 0x3f803f80u, 0x3f803f80u, 0x3f803f80u};
    for (int t = 0; t < 64; ++t) {
      const u32x4 fk1 = ld16(c.r, L + L_K1 + head * 65536 + t * 1024 + lane * 16);
      const u32x2 fk2 = ld8(c.r, L + L_K2 + o2 + t * 512);
      u32x4 v0 = ld16(c.r, vi + t * 1280), v1 = ld16(c.r, vi + t * 1280 + 640);
      if (r32 >= 20) { v0 = one8; v1 = one8; }
      f32x16 s = qk16(__builtin_bit_cast(bf16x8, fk1), fq1, qk8(__builtin_bit_cast(s16x4, fk2), __builtin_bit_cast(s16x4, fq2)));
      float tm = s[0];
#pragma unroll
      for (int r = 1; r < 16; ++r) tm = fmaxf(tm, s[r]);
      tm = fmaxf(tm, swap32(tm, h));
      const float mn = fmaxf(m, tm), al = __builtin_amdgcn_exp2f(m - mn);
      m = mn;
#pragma unroll
      for (int r = 0; r < 16; ++r) { s[r] = __builtin_amdgcn_exp2f(s[r] - mn); oo[r] *= al; }
      oo = pv_hi(__builtin_bit_cast(bf16x8, v1), s, pv_lo(__builtin_bit_cast(bf16x8, v0), s, oo));
    }
#pragma unroll
    for (int e = 0; e < 3; ++e) a[e] = f32x4{oo[4 * e], oo[4 * e + 1], oo[4 * e + 2], oo[4 * e + 3]};
    l = h ? a[2][0] : 0.f;
    l += swap32(l, h);
  }
  const float inv = 1.f / l;
  const unsigned A0 = pk2(a[0][0] * inv, a[0][1] * inv), A1 = pk2(a[0][2] * inv, a[0][3] * inv);
  const unsigned B0 = pk2(a[1][0] * inv, a[1][1] * inv), B1 = pk2(a[1][2] * inv, a[1][3] * inv);
  const u32x2 C = {pk2(a[2][0] * inv, a[2][1] * inv), pk2(a[2][2] * inv, a[2][3] * inv)};
  const int tok = qt * 32 + r32;
  const unsigned ob = L + L_O + (tok >> 4) * 20480;
  st16(c.r, ob + (head * 2 + h) * 256 + (tok & 15) * 16, pair32(A0, A1, B0, B1));
  if (!h) st8(c.r, ob + 16384 + head * 128 + (tok & 15) * 8, C);
  STAMP(4);
  publish(c, M_CNT, 8, flg(FL_ATT + layer * 5, qb * 32 + head), true);
  STAMP(5);
}
DI void retile_load(const Ctx& c, int J, u32x4 (&v)[5][4], const uint16_t* wo, const uint16_t* wg, const uint16_t* wu, const uint16_t* wd) {
  const int e = J < 216 ? J : J < 256 ? J - 216 : J - 256;
  const int strip = J < 216 ? (e >> 2) % 27 : e % 10, k0 = J < 216 ? (e & 3) * 5 : min((e / 10) * 5, 49), ld = J < 216 ? 3456 : 1280;
  const rsrc_t r = __builtin_amdgcn_make_buffer_rsrc((void*)(J < 108 ? wg : J < 216 ? wu : J < 256 ? wo : wd), 0,
                                                     J < 216 ? 640 * 3456 : J < 256 ? 640 * 1280 : 1728 * 1280, 0x00020000);
  const unsigned vo = (c.lane >> 3) * ld + (c.lane & 7) * 16;
#pragma unroll
  for (int u = 0; u < 5; ++u)
#pragma unroll
    for (int q = 0; q < 4; ++q) {
      const int ch = (k0 + u) * 4 + q;
      const int row = J < 216 || J >= 256 ? ch * 8 : ch < 64 ? (ch >> 1) * 20 + (ch & 2) * 2 + (ch & 1) * 8 : (ch - 64) * 40 + 16;
      v[u][q] = __builtin_amdgcn_raw_buffer_load_b128(r, vo, row * ld + strip * 128, 0);
    }
}
DI void retile_store(const Ctx& c, int J, lds_ptr scr, const u32x4 (&v)[5][4]) {
  const int e = J < 216 ? J : J < 256 ? J - 216 : J - 256, mat = J >= 108;
  const int strip = J < 216 ? (e >> 2) % 27 : e % 10, k0 = J < 216 ? (e & 3) * 5 : min((e / 10) * 5, 49);
  const unsigned ts = J < 216 ? 40960 : J < 256 ? 20480 : 55296;
  const unsigned dst = (J < 216 ? WS_WGU + mat * 20480 : J < 256 ? WS_WO : WS_WD) + strip * 4 * ts + k0 * 1024;
  const int i = c.lane & 15, g = c.lane >> 4;
  const bf16x8 ones = __builtin_bit_cast(bf16x8, u32x4{0x3f803f80u, 0x3f803f80u, 0x3f803f80u, 0x3f803f80u});
  f32x4 cs[4] = {f32x4{}, f32x4{}, f32x4{}, f32x4{}};
#pragma unroll
  for (int u = 0; u < 5; ++u) {
#pragma unroll
    for (int p = 0; p < 4; ++p) *(LAS u32x4*)(scr + p * 1024 + c.lane * 16) = v[u][p];
#pragma unroll
    for (int t = 0; t < 4; ++t) {
      const bf16x8 f = ldstr(scr + (g * 8 + (i >> 2)) * 128 + t * 32 + (i & 3) * 8, 512);
      __builtin_amdgcn_raw_buffer_store_b128(__builtin_bit_cast(u32x4, f), c.r, c.lane * 16, dst + u * 1024 + t * ts, 16);
      cs[t] = mfma16(f, ones, cs[t]);
    }
  }
  if (J < 216 && i == 0) {
#pragma unroll
    for (int t = 0; t < 4; ++t)
      __builtin_amdgcn_raw_buffer_store_b128(__builtin_bit_cast(u32x4, cs[t]), c.r, g * 16, WS_CSG + ((e & 3) * 3456 + (strip * 8 + t * 2 + mat) * 16) * 4, 16);
  }
}
DI int early_piece(int e) { return e < 4 ? (e & 1) + (e >> 1) * 8 : e < 6 ? 16 + (e - 4) * 4 : e < 9 ? 18 + e : 25 + e; }
DI int late_piece(int e) { return e < 6 ? 2 + e : e < 12 ? 4 + e : e < 15 ? 5 + e : e < 18 ? 6 + e : e < 25 ? 9 + e : 12 + e; }
DI void att_load(const Ctx& c, int lw, int layer, unsigned iss, const uint16_t* wo, const uint16_t* wg, const uint16_t* wu, const uint16_t* wd) {
  const int head = att_head(c), qb = att_qb(c), lane = c.lane;
  const char* Lp = c.ws + WS_L + layer * L_SZ;
  const unsigned vo = lane * 16;
  int grp = ATT_ROT ? qb >> 1 : 0;
  auto src_of = [&](int p) {
    return p < 16 ? L_K1 + head * 65536 + grp * 16384 + p * 1024
                  : p < 24 ? L_K2 + (head >> 1) * 32768 + grp * 8192 + (p - 16) * 1024 : L_V + head * 81920 + grp * 20480 + (p - 24) * 1024;
  };
  unsigned v0[14], m0[11], vg[11], sg[11];
#pragma unroll
  for (int q = 0; q < 11; ++q) {
    const int p = q < 3 ? early_piece(lw * 3 + q) : late_piece(lw * 8 + q - 3), pg = lw * 11 + q;
    v0[q] = src_of(p) + vo; m0[q] = p * 1024;
    vg[q] = src_of(pg) + vo; sg[q] = pg < 16 ? 16384 : pg < 24 ? 8192 : 20480;
    asm volatile("" : "+v"(v0[q]), "+s"(m0[q]));
  }
#pragma unroll
  for (int q = 0; q < 3; ++q) {
    const int p = lw * 3 + q;
    v0[11 + q] = (p < 8 ? L_Q1 + head * 65536 + qb * 8192 + p * 1024 : L_Q2 + (head >> 1) * 32768 + qb * 4096 + (p - 8) * 1024) + vo - q * 1024;
    PIN(v0[11 + q]);
  }
  auto issue = [&](int slot) {
#pragma unroll
    for (int q = 0; q < 11; ++q) { vg[q] += grp == 3 ? -3 * sg[q] : sg[q]; glds1(slot * AT_SLOT + (lw * 11 + q) * 1024, Lp, vg[q]); }
    grp = (grp + 1) & 3;
  };
  const int o0 = (head >> 1) * 5 + (head & 1) * 2, c0 = head * 20;
  auto kv_cb = [&](int sel) { return sel == 0 ? 5 + (o0 >> 4) : sel == 1 ? 5 + ((o0 + 2) >> 4) : sel == 2 ? 10 + (c0 >> 7) : 10 + ((c0 + 19) >> 7); };
  if (lw == 0) {
    sig_wait(c, iss, M_ISS);
    poll(c, flg(FL_QKV + layer * 5, lane & 16 ? (qb * 2 + (lane & 1)) * 16 + ((o0 + (lane & 2)) >> 4) : (grp * 4 + (lane & 3)) * 16 + kv_cb((lane >> 2) & 3)));
    sig_set(c, layer * 5 + 2);
    STAMPL(layer * 5 + 1, 6);
  } else sig_wait(c, layer * 5 + 2);
  glds3(2 * AT_SLOT + lw * 3072, Lp, v0[11], v0[12], v0[13]);
#pragma unroll
  for (int q = 0; q < 3; ++q) glds1(m0[q], Lp, v0[q]);
#pragma unroll
  for (int q = 3; q < 3 + ATT_LATE; ++q) glds1(m0[q], Lp, v0[q]);
  wait_vm<ATT_LATE>(); BARRIER();
#pragma unroll
  for (int q = 3 + ATT_LATE; q < 11; ++q) glds1(m0[q], Lp, v0[q]);
  wait_vm<0>(); BARRIER();
  if (lw == 0) { poll(c, flg(FL_QKV + layer * 5, (lane & 15) * 16 + kv_cb(lane >> 4))); sig_set(c, layer * 5 + 3); } else sig_wait(c, layer * 5 + 3, M_SIG, true);
  issue(1);
  const int J = (lw - 1) * 256 + blockIdx.x;
  const bool rt = layer == 0 && lw > 0 && J < 366;
  u32x4 v[5][4];
  if (rt) { retile_load(c, J, v, wo, wg, wu, wd); wait_vm<20>(); } else wait_vm<0>();
  BARRIER();
  issue(2);
  if (rt) retile_store(c, J, c.lds + AT_SCR + (lw - 1) * 4096, v);
  if (layer == 0 && (lw == 1 || lw == 2)) publish(c, M_CNT3, 2, flg(FL_RT, blockIdx.x));
  wait_vm<0>(); BARRIER();
  issue(0);
  wait_vm<0>();
  if (layer == 0) {
    if (lw == 0) {
      if (c.j == 0) {
        Poll<2> pl;
#pragma unroll 1
        for (int q = 0; q < 2; ++q) { pl.run(c, flg(FL_RT, q * 128 + lane), flg(FL_RT, q * 128 + 64 + lane)); wait_vm<0>(); pl.keep(); }
        if (lane < 32) __hip_atomic_store((GAS u64*)(c.ws + flg(FL_RT2, lane * 8 + c.xcd)), c.tgt, __ATOMIC_RELAXED, __HIP_MEMORY_SCOPE_AGENT);
      }
      poll(c, flg(FL_RT2, blockIdx.x));
      if (blockIdx.x == 0 && lane == 0) *(u64*)(c.ws + WS_EPOCH) = c.tgt;
    }
  }
  BARRIER();
  u32x4 pk[25];
#pragma unroll
  for (int e = 0; e < 25; ++e) pk[e] = ld16s(c.r, vo, WS_WO + (pr_cb(c) * 5 + e % 5) * 20480 + (lw * 5 + e / 5) * 1024);
#pragma unroll 1
  for (unsigned q = lw; q < 35; q += 4) glds1(AT_SCR + lw * 1024, c.ws + WS_WGU + ((gu_cb(c) * 27) >> 2) * 40960 + ((c.j & 7) * 35 + q) * 1024, vo);
  wait_vm<0>();
  BARRIER(); BARRIER();
  sig_wait(c, layer * 16 + 8, M_CNT);
#pragma unroll
  for (int e = 0; e < 25; ++e) { const int k = lw * 5 + e / 5; ldsw16(c.lds + (k >> 1) * 10240 + (k & 1) * 1024 + (e % 5) * 2048 + vo, pk[e]); }
}

template <int NS, int NB, int WIN>
DI void proj_compute(const Ctx& c, int w, int slot_id, unsigned a_off, const char* res_rm, unsigned res_off, unsigned out_off, unsigned st_off,
                     uint16_t* out, int arr) {
  constexpr bool RES = NS <= NB;
  const int panel = pr_panel(c), cb = pr_cb(c);
  SLOT(slot_id);
  STAMP(0);
  if (w >= 4) {
    if constexpr (RES) { BARRIER(); BARRIER(); }
    else {
      static_assert(RES || (NS == 27 && NB == 15 && WIN == 18), "schedule");
      const int par = (w >> 1) & 1, half = w & 1;
      u32x4 pk[15];
#pragma unroll
      for (int e = 0; e < 15; ++e) {
        const int q = half * 5 + e % 5;
        pk[e] = ld16s(c.r, c.lane * 16, WS_WD + (cb * 5 + (q >> 1)) * 55296 + ((15 + par + (e / 5) * 2) * 2 + (q & 1)) * 1024);
      }
      lds_ptr pw = c.lds + par * 10240 + half * 5120 + c.lane * 16;
      BARRIER(); BARRIER();
#pragma unroll
      for (int e = 0; e < 10; ++e) ldsw16(pw + (e / 5) * 20480 + (e % 5) * 1024, pk[e]);
      BARRIER();
#pragma unroll
      for (int e = 10; e < 15; ++e) ldsw16(pw + 40960 + (e % 5) * 1024, pk[e]);
      BARRIER(); BARRIER(); BARRIER(); BARRIER();
    }
    return;
  }
  const int lane = c.lane, i = lane & 15, g = lane >> 4, mt = panel * 4 + w;
  u32x2 res[5];
#pragma unroll
  for (int t = 0; t < 5; ++t)
    res[t] = res_rm ? *(const u32x2*)(res_rm + (mt * 16 + i) * 1280 + cb * 160 + t * 32 + g * 8)
                    : ld8(c.r, res_off + (mt * 80 + cb * 10 + t * 2 + (g >> 1)) * 256 + i * 16 + (g & 1) * 8);
  f32x4 acc[5];
#pragma unroll
  for (int t = 0; t < 5; ++t) { acc[t] = f32x4{bflo(res[t][0]), bfhi(res[t][0]), bflo(res[t][1]), bfhi(res[t][1])}; PIN(acc[t]); }
  lds_ptr pb = c.lds + lane * 16;
  unsigned av = a_off + mt * (NS * 2048) + lane * 16;
  u32x4 a[WIN];
  bf16x8 f[3][5];
  int sb = 0;
  STAMP(1);
  BARRIER();
  STAMP(2); STAMPC(0); WRESET();
  constexpr int AH = RES ? WO_AH : DN_AH;
  auto lda = [&](int k) {
    if (RES && k >= 16) {
      const unsigned o = a_off + mt * 20480 + 16384 + (k - 16) * 1024 + g * 256 + i * 8;
      const u32x2 lo = ld8(c.r, o), hi = ld8(c.r, o + 128);
      a[k % WIN] = u32x4{lo[0], lo[1], hi[0], hi[1]};
    } else a[k % WIN] = ld16(c.r, av + k * 1024);
  };
#pragma unroll
  for (int k = 0; k < AH; ++k) lda(k);
#pragma unroll
  for (int t = 0; t < 5; ++t) { f[0][t] = ldsr16(pb + t * 2048); f[1][t] = ldsr16(pb + t * 2048 + 1024); }
  SBAR();
  auto window = [&](bool first, bool last) {
#pragma unroll
    for (int k = 0; k < WIN; ++k) {
      const bool ld = !(last && k + 2 >= WIN);
      if ((k & 1) == 0) {
        if (RES) pb += 10240;
        else { pb += sb == NB - 1 ? -(NB - 1) * 10240 : 10240; sb = sb == NB - 1 ? 0 : sb + 1; }
        if (!ld) BARRIER(); else if (!RES && (k == 10 || (k == 0 && !first))) BARRIER_NW();
      }
#pragma unroll
      for (int t = 0; t < 5; ++t) {
        if (ld) f[(k + 2) % 3][t] = ldsr16(pb + t * 2048 + (k & 1) * 1024);
        acc[t] = mfma16(f[k % 3][t], __builtin_bit_cast(bf16x8, a[k]), acc[t]);
        SBAR();
      }
      if (!last || k + AH < WIN) { lda(k + AH); SBAR(); }
    }
    av += WIN * 1024;
  };
  static_assert(2 * NS % WIN == 0 && (2 * NS == WIN || WIN % 6 == 0), "window");
#pragma unroll 1
  for (int b = 1; b < 2 * NS / WIN; ++b) window(b == 1, false);
  window(false, true);
  STAMP(3); STAMPC(1);
  u32x2 o[5];
#pragma unroll
  for (int t = 0; t < 5; ++t) {
    o[t][0] = pk2(acc[t][0], acc[t][1]); o[t][1] = pk2(acc[t][2], acc[t][3]);
  }
  const u32x4 p0 = pair16(o[0][0], o[0][1], o[1][0], o[1][1]), p1 = pair16(o[2][0], o[2][1], o[3][0], o[3][1]),
              p2 = pair16(o[4][0], o[4][1], o[4][0], o[4][1]);
  const int ch = (g & 1) * 2 + (g >> 1);
  if (out) {
    char* po = (char*)out + (mt * 16 + i) * 1280 + cb * 160;
    *(u32x4*)(po + ch * 16) = p0; *(u32x4*)(po + 64 + ch * 16) = p1;
    if (!(g & 1)) *(u32x4*)(po + 128 + (g >> 1) * 16) = p2;
    STAMP(4);
    return;
  }
  const unsigned xo = out_off + (mt * 80 + cb * 10) * 256 + i * 16;
  st16(c.r, xo + ch * 256, p0); st16(c.r, xo + 1024 + ch * 256, p1);
  if (!(g & 1)) st16(c.r, xo + 2048 + (g >> 1) * 256, p2);
  const f32x4 s4 = (acc[0] + acc[1]) + (acc[2] + acc[3]) + acc[4];
  const float sum = rows4((s4[0] + s4[1]) + (s4[2] + s4[3])), mean = sum * (1.f / 80.f);
  f32x4 q4 = {0.f, 0.f, 0.f, 0.f};
#pragma unroll
  for (int t = 0; t < 5; ++t) { const f32x4 d = acc[t] - mean; q4 += d * d; }
  const float m2 = rows4((q4[0] + q4[1]) + (q4[2] + q4[3]));
  if (g == 0) st8(c.r, st_off + (mt >> 3) * 8192 + cb * 1024 + ((mt & 7) * 16 + i) * 8, u32x2{__float_as_uint(sum), __float_as_uint(m2)});
  STAMP(4);
  publish(c, M_CNT4, 4, flg(arr, panel * 8 + cb), true);
  STAMP(5);
}
template <int NF>
DI void gu_consumer(const Ctx& c, int layer, int f0, int mq) {
  const int panel = gu_panel(c), ft0 = (gu_cb(c) * 27) >> 2;
  const int lane = c.lane, i = lane & 15, g = lane >> 4;
  f32x4 acc[2][2 * NF];
#pragma unroll
  for (int m = 0; m < 2; ++m)
#pragma unroll
    for (int t = 0; t < 2 * NF; ++t) acc[m][t] = f32x4{0.f, 0.f, 0.f, 0.f};
  lds_ptr pa = c.lds + GU_A + mq * 2048 + lane * 16, pb = c.lds + f0 * 2048 + lane * 16;
  bf16x8 x0[2], x1[2], wa[NF], wb0[NF], wb1[NF];
  auto step = [&](const bf16x8* xa, const bf16x8* wb, bf16x8* xn, bf16x8* wn_, bool ld) {
#pragma unroll
    for (int q = 0; q < 4 * NF; ++q) {
      const int t = q >> 1, m = q & 1;
      if (ld) {
        if (q < 2) xn[q] = ldsr16(pa + q * 1024);
        else if (q < 2 * NF) { const int e = NF == 4 ? (q == 3 ? 0 : q == 4 ? 1 : q == 6 ? 2 : q == 7 ? 3 : -1) : NF == 3 ? q - 3 : q - 2; if (e >= 0) wn_[e] = ldsr16(pb + (NF + e) * 1024); }
        else if (q & 1) wa[(q - 2 * NF) >> 1] = ldsr16(pb + ((q - 2 * NF) >> 1) * 1024);
      }
      acc[m][t] = mfma16(t < NF ? wa[t] : wb[t - NF], xa[m], acc[m][t]);
      SBAR();
    }
  };
  int sl = 0;
  auto adv = [&]() {
    pa += sl == GU_NA - 1 ? -(GU_NA - 1) * 8192 : 8192; pb += sl == GU_NB - 1 ? -(GU_NB - 1) * 14336 : 14336;
    sl = sl == GU_NA - 1 ? 0 : sl + 1;
  };
  SLOT(layer * 5 + 3);
  STAMP(0);
  STAMP(1);
  BARRIER();
  STAMP(2); STAMPC(0); WRESET();
  x0[0] = ldsr16(pa); x0[1] = ldsr16(pa + 1024);
#pragma unroll
  for (int t = 0; t < NF; ++t) { wa[t] = ldsr16(pb + t * 1024); wb0[t] = ldsr16(pb + (NF + t) * 1024); }
#pragma unroll 1
  for (int st = 0; st < 9; ++st) {
    adv(); BARRIER_NW(); step(x0, wb0, x1, wb1, true);
    adv(); BARRIER_NW(); step(x1, wb1, x0, wb0, true);
  }
  adv(); BARRIER_NW(); step(x0, wb0, x1, wb1, true);
  BARRIER();
  const LAS float* srs = (const LAS float*)(c.lds + M_SST) + mq * 32 + i;
  const LAS float* scs = (const LAS float*)(c.lds + M_SCS) + f0 * 32 + g * 4;
  float rs[2] = {srs[0], srs[16]}; const float nm[2] = {srs[128], srs[144]};
  f32x4 cs[2 * NF];
#pragma unroll
  for (int e = 0; e < 2 * NF; ++e) cs[e] = *(const LAS f32x4*)(scs + e * 16);
  SBAR();
  step(x1, wb1, x0, wb0, false);
  STAMP(3); STAMPC(1);
  rs[0] *= rs[0]; rs[1] *= rs[1];
  const unsigned ho = WS_L + layer * L_SZ + L_H + ((panel * 8 + mq * 2 + (g & 1)) * 216 + (ft0 + f0) * 2 + (g >> 1)) * 256 + i * 16;
#pragma unroll
  for (int f = 0; f < NF; ++f) {
    unsigned p[2][2];
#pragma unroll
    for (int m = 0; m < 2; ++m) {
      f32x4 gt = acc[m][2 * f] + cs[2 * f] * nm[m];
      const f32x4 up = (acc[m][2 * f + 1] + cs[2 * f + 1] * nm[m]) * rs[m];
      gt = __builtin_elementwise_max(gt, f32x4{0.f, 0.f, 0.f, 0.f}) * up;
      p[m][0] = pk2(gt[0], gt[1]); p[m][1] = pk2(gt[2], gt[3]);
    }
    st16(c.r, ho + f * 512, pair16(p[0][0], p[0][1], p[1][0], p[1][1]));
    SBAR();
  }
  STAMP(4);
  publish(c, M_CNT, 8, flg(FL_GU + layer * 5, panel * 16 + gu_cb(c)), true);
  STAMP(5);
}
DI void mlp_load(const Ctx& c0, int lw, int layer, unsigned iss, bool qkv_next, const uint16_t* wqkv) {
  const int half = lw & 1;
  {
    const Ctx c = fresh(c0);
    if (lw == 0) { poll(c, flg(FL_ATT + layer * 5, (pr_panel(c) >> 2) * 32 + (c.lane & 31))); STAMPL(layer * 5 + 2, 6); }
    BARRIER_L(); BARRIER_L();
  }
  { const int w = lw + 8; (void)w; WDUMP(layer * 5 + 2); }
  {
    const Ctx c = fresh(c0);
    const int ft0 = (gu_cb(c) * 27) >> 2;
    u32x4 pk[20];
    auto park = [&](int e0) {
#pragma unroll
      for (int e = e0; e < e0 + 4; ++e) {
        const int q = half * 5 + e % 5;
        pk[e] = ld16s(c.r, c.lane * 16, WS_WD + (pr_cb(c) * 5 + (q >> 1)) * 55296 + (((e / 5) * 2 + (lw >> 1)) * 2 + (q & 1)) * 1024);
      }
    };
    static_assert(GU_NA == 7 && GU_NB == 7, "schedule");
    if (lw >= 2) {
      Ring<7, GU_NB, 20, 2> rg;
#pragma unroll
      for (int p = 0; p < 7; ++p) rg.voff[p] = WS_WGU + (ft0 * 2 + half * 7 + p) * 20480 + c.lane * 16;
      rg.init(c.ws, 1024, 0, half);
      if (half == 0) ln_cols<4>(c, 56, WS_CSG + ft0 * 128, 13824);
      sig_wait(c, iss - layer * 16 + 4, M_CNT4);
      rg.template fill_ahead<1>(c, layer * 5 + 4);
      rg.template range<5, 5>(); rg.steady();
      wait_vm<28>(); BARRIER_L(); park(0);
      wait_vm<21 + 4>(); BARRIER_L(); park(4);
      wait_vm<14 + 8>(); BARRIER_L(); park(8);
      wait_vm<7 + 12>(); BARRIER_L(); park(12);
      wait_vm<16>(); BARRIER_L(); park(16);
    } else {
      Ring<4, GU_NA, 20, 2> rg;
#pragma unroll
      for (int p = 0; p < 4; ++p) rg.voff[p] = WS_L + layer * L_SZ + L_X1 + (gu_panel(c) * 8 + half * 4 + p) * 20480 + c.lane * 16;
      rg.init(c.ws, 1024, GU_A, half);
      Poll<1> pl;
      gate(c, pl, half, flg(FL_WO + layer * 5, gu_panel(c) * 16 + (c.lane & 15)), layer * 5 + 4, iss + 12);
      rg.template fill<3>();
      rg.template head<3>();
      pl.keep();
      f32x2 st[8];
#pragma unroll
      for (int i = 0; i < 8; ++i)
        st[i] = __builtin_bit_cast(f32x2, ld8(c.r, WS_ST + (layer * 2 + 1) * STB + gu_panel(c) * 8192 + i * 1024 + (half * 64 + c.lane) * 8));
      wait_vm<16 + 8>(); BARRIER_L(); park(0);
      wait_vm<12 + 8 + 4>(); BARRIER_L(); park(4);
      wait_vm<8 + 8 + 8>(); BARRIER_L(); ln_rows(c, half * 64 + c.lane, st);
      wait_vm<8>(); BARRIER_L(); park(8); park(12);
      BARRIER_L(); park(16);
    }
    BARRIER_L();
#pragma unroll
    for (int e = 0; e < 20; ++e) PIN(pk[e]);
    { const int w = lw + 8; (void)w; WDUMP(layer * 5 + 3); }
    auto dump = [&]() {
#pragma unroll
      for (int e = 0; e < 20; ++e) ldsw16(c.lds + ((e / 5) * 2 + (lw >> 1)) * 10240 + (half * 5 + e % 5) * 1024 + c.lane * 16, pk[e]);
    };
    if (lw >= 2) {
      Ring<5, DN_NB, 27, 2> rg;
#pragma unroll
      for (int p = 0; p < 5; ++p) { const int q = half * 5 + p; rg.voff[p] = WS_WD + (pr_cb(c) * 5 + (q >> 1)) * 55296 + (q & 1) * 1024 + c.lane * 16; }
      rg.init(c.ws + 8 * 2048, 2048, 0, half);
      rg.slot = 8;
      sig_wait(c, layer * 16 + 16, M_CNT);
      rg.template fill<3>(); dump(); rg.template fill<4>();
      BARRIER_L();
      wait_vm<5 * 5>(); BARRIER_L();
      wait_vm<0>(); BARRIER_L();
      rg.base += 6 * 2048; rg.slot = 6;
      rg.template fill<3>();
      BARRIER_L();
      rg.template fill<3>();
      wait_vm<3 * 5>(); BARRIER_L();
      wait_vm<0>(); BARRIER_L();
      BARRIER_L();
    } else {
      sig_wait(c, layer * 16 + 16, M_CNT);
      dump();
      if (lw == 0) { poll(c, flg(FL_GU + layer * 5, (pr_panel(c) >> 1) * 16 + (c.lane & 15))); STAMPL(layer * 5 + 4, 6); }
#pragma unroll 1
      for (int k = 0; k < 7; ++k) BARRIER_L();
    }
  }
  { const int w = lw + 8; (void)w; WDUMP(layer * 5 + 4); }
  if (qkv_next) qkv_load1(fresh(c0), lw, wqkv);
}

__global__ __launch_bounds__(768) void fused_kernel(const uint16_t* __restrict__ x, const uint16_t* __restrict__ wqkv, const uint16_t* __restrict__ wo,
                                                    const uint16_t* __restrict__ wg, const uint16_t* __restrict__ wu,
                                                    const uint16_t* __restrict__ wd, uint16_t* __restrict__ out, char* __restrict__ ws) {
  extern __shared__ __attribute__((aligned(16))) char smem[];
  Ctx c;
  c.ws = ws; c.lds = (lds_ptr)smem; c.lane = threadIdx.x & 63; c.xcd = blockIdx.x & 7; c.j = blockIdx.x >> 3;
  c.r = __builtin_amdgcn_make_buffer_rsrc(ws, 0, WS_END, 0x00020000);
  const int w = __builtin_amdgcn_readfirstlane(threadIdx.x >> 6);
  SPANST(0);
  c.tgt = 0;
  if (threadIdx.x < 16) *(LAS unsigned*)(c.lds + M_CNT + threadIdx.x * 4) = 0u;
  const bool qkv_on = qkv_cb(c) != 15;
  Ctx c0 = c;
  WDUMP(15);
  if (w >= 8) __builtin_amdgcn_s_setprio(3);
  if (w >= 8) {
    if (qkv_on) qkv_load0(fresh(c0), w - 8, x, wqkv); else BARRIER();
    c0.tgt = epoch(ws);
#pragma unroll 1
    for (int layer = 0; layer < 2; ++layer) {
      const unsigned iss = layer * 24 + (qkv_on ? (layer + 1) * 4 : 0);
      WDUMP(layer * 5);
      att_load(fresh(c0), opq(w - 8), opq(layer), iss, wo, wg, wu, wd);
      WDUMP(layer * 5 + 1);
      mlp_load(c0, opq(w - 8), opq(layer), iss, layer == 0 && qkv_on, wqkv);
    }
  } else {
    if (!qkv_on) BARRIER();
#define TLB_PF 524288
    if (w == 2 || w == 3) {
      const unsigned o = (w - 2) * 64 + c.lane;
      const unsigned t = o * TLB_PF < WS_END ? __builtin_amdgcn_raw_buffer_load_b32(c.r, o * TLB_PF + 2048, 0, 16) : out[(o & 1) * 1048576];
      asm volatile("" ::"v"(t));
    }
    if (w < 2) {
      const char* pc; constexpr unsigned len = 58 * 1024;
      asm volatile("s_getpc_b64 %0" : "=s"(pc));
      const unsigned o = (c.j * 2 + w) * 1024 + c.lane * 16;
      if (o + 16 <= len) { const u32x4 t = *(const GAS u32x4*)(pc + o); asm volatile("" ::"v"(t)); }
    }
#pragma unroll 1
    for (int layer = 0; layer < 2; ++layer) {
      const unsigned L = WS_L + layer * L_SZ;
      qkv_compute(fresh(c0), c0.tgt, opq(w), opq(layer), x, wqkv);
      WDUMP(layer * 5);
      att_compute(fresh(c0), opq(w), opq(layer));
      WDUMP(layer * 5 + 1);
      proj_compute<10, WO_NB, 20>(fresh(c0), opq(w), layer * 5 + 2, L + L_O, layer == 0 ? (const char*)x : nullptr, WS_X2, L + L_X1,
                                  WS_ST + (layer * 2 + 1) * STB, nullptr, FL_WO + layer * 5);
      WDUMP(layer * 5 + 2);
      if (w < 4) gu_consumer<4>(fresh(c0), opq(layer), 0, opq(w));
      else if (GU_SKIP && (gu_cb(c0) & 3) == 0) gu_consumer<2>(fresh(c0), opq(layer), 4, opq(w - 4));
      else gu_consumer<3>(fresh(c0), opq(layer), 4, opq(w - 4));
      WDUMP(layer * 5 + 3);
      proj_compute<27, DN_NB, 18>(fresh(c0), opq(w), layer * 5 + 4, L + L_H, nullptr, L + L_X1, WS_X2, WS_ST + 2 * STB, layer ? out : nullptr,
                                  FL_DN + layer * 5);
      WDUMP(layer * 5 + 4);
    }
  }
}

extern "C" void kernel_launch(void* const* d_in, const int* in_sizes, int n_in, void* d_out, int out_size, void* d_ws, size_t ws_size,
                              hipStream_t stream) {
  (void)hipFuncSetAttribute((const void*)fused_kernel, hipFuncAttributeMaxDynamicSharedMemorySize, LDS_BYTES);
  fused_kernel<<<256, 768, LDS_BYTES, stream>>>((const uint16_t*)d_in[0], (const uint16_t*)d_in[1], (const uint16_t*)d_in[2],
                                                (const uint16_t*)d_in[4], (const uint16_t*)d_in[5], (const uint16_t*)d_in[6],
                                                (uint16_t*)d_out, (char*)d_ws);
}
```

```cpp
#include <hip/hip_runtime.h>
#include <hip/hip_bf16.h>
#include <stdint.h>

typedef __attribute__((ext_vector_type(8))) __bf16 bf16x8;
typedef __attribute__((ext_vector_type(16))) __bf16 bf16x16;
typedef __attribute__((ext_vector_type(4))) short s16x4;
typedef __attribute__((ext_vector_type(8))) short s16x8;
typedef __attribute__((ext_vector_type(2))) __bf16 bf16x2;
typedef __attribute__((ext_vector_type(2))) float f32x2;
typedef __attribute__((ext_vector_type(4))) float f32x4;
typedef __attribute__((ext_vector_type(8))) float f32x8;
typedef __attribute__((ext_vector_type(16))) float f32x16;
typedef __attribute__((ext_vector_type(2))) unsigned u32x2;
typedef __attribute__((ext_vector_type(4))) unsigned u32x4;
typedef unsigned long long u64;
#define LAS __attribute__((address_space(3)))
#define GAS __attribute__((address_space(1)))
typedef LAS char* lds_ptr;
typedef __amdgpu_buffer_rsrc_t rsrc_t;
#define DI __device__ __forceinline__

constexpr unsigned XB = 2048u * 640 * 2;
constexpr unsigned WS_EPOCH = 0;
constexpr unsigned WS_FLAG = 4096;
constexpr int FL_P0 = 0, FL_RT = 1, FL_QKV = 2, FL_ATT = 3, FL_WO = 4, FL_GU = 5, FL_DN = 6, FL_RT2 = 12;
constexpr unsigned WS_WO  = WS_FLAG + 13u * 32768;
constexpr unsigned WS_WGU = WS_WO + 640u * 640 * 2;
constexpr unsigned WS_WD  = WS_WGU + 3456u * 640 * 2;
constexpr unsigned WS_CSQ = WS_WD + 640u * 1728 * 2;
constexpr unsigned WS_CSG = WS_CSQ + 8u * 1920 * 4;
constexpr unsigned WS_ST  = WS_CSG + 4u * 3456 * 4;
constexpr unsigned STB = 2048u * 64;
constexpr unsigned WS_X2  = WS_ST + 4 * STB;
constexpr unsigned WS_L   = WS_X2 + XB;
constexpr unsigned L_Q1 = 0, L_Q2 = L_Q1 + 32u * 65536, L_K1 = L_Q2 + 16u * 32768, L_K2 = L_K1 + 32u * 65536, L_V = L_K2 + 16u * 32768,
                   L_O = L_V + XB, L_X1 = L_O + XB, L_H = L_X1 + XB, L_SZ = L_H + 2048u * 1728 * 2;
constexpr unsigned WS_END = WS_L + 2 * L_SZ;

constexpr int LDS_BYTES = 163840, MISC = 159744, M_SST = MISC, M_SCS = MISC + 1024, M_CNT = MISC + 2048, M_CNT2 = M_CNT + 8, M_CNT3 = M_CNT + 16, M_CNT4 = M_CNT + 24, M_SIG = M_CNT + 32, M_ISS = M_CNT + 40;
constexpr int QKV_NB = 5, QKV_NA = 4, QKV_A = QKV_NB * 16384;
constexpr int WO_NB = 10, DN_NB = 15;
constexpr int GU_NB = 7, GU_NA = 7, GU_A = GU_NB * 14336;
constexpr int AT_SLOT = 45056, AT_CONST = 3 * AT_SLOT, AT_SCR = AT_CONST + 10752, AT_MERGE = 3 * AT_SLOT;
static_assert(QKV_A + QKV_NA * 16384 <= MISC && DN_NB * 10240 <= MISC && GU_A + GU_NA * 8192 <= MISC, "lds");
static_assert(WO_NB * 10240 <= AT_MERGE && AT_MERGE + 24576 <= MISC && AT_SCR + 3 * 4096 <= MISC, "lds");

DI unsigned pk2(float a, float b) { f32x2 v = {a, b}; bf16x2 r = __builtin_convertvector(v, bf16x2); return __builtin_bit_cast(unsigned, r); }
DI float bflo(unsigned u) { return __uint_as_float(u << 16); }
DI float bfhi(unsigned u) { return __uint_as_float(u & 0xffff0000u); }
DI f32x4 mfma16(bf16x8 a, bf16x8 b, f32x4 c) { return __builtin_amdgcn_mfma_f32_16x16x32_bf16(a, b, c, 0, 0, 0); }
DI f32x16 mfma32(bf16x8 a, bf16x8 b, f32x16 c) { return __builtin_amdgcn_mfma_f32_32x32x16_bf16(a, b, c, 0, 0, 0); }
DI bf16x8 ldsr16(lds_ptr p) { return *(const LAS bf16x8*)p; }
DI bf16x8 ldstr(lds_ptr p, int second) {
  const s16x4 a = __builtin_amdgcn_ds_read_tr16_b64_v4i16((LAS s16x4*)p), b = __builtin_amdgcn_ds_read_tr16_b64_v4i16((LAS s16x4*)(p + second));
  return __builtin_bit_cast(bf16x8, (s16x8)__builtin_shufflevector(a, b, 0, 1, 2, 3, 4, 5, 6, 7));
}
#define BARRIER() asm volatile("s_waitcnt lgkmcnt(0)\n\ts_barrier" ::: "memory")
template <int N> DI void wait_vm() { asm volatile("s_waitcnt vmcnt(%0)" ::"n"(N > 63 ? 63 : N) : "memory"); }
#define WDUMP(id)
#define WRESET()
#define BARRIER_L() BARRIER()
#define BARRIER_NW() asm volatile("s_barrier" ::: "memory")
#define SBAR() __builtin_amdgcn_sched_barrier(0)
#define PIN(x) asm volatile("" : "+v"(x))
#define GL_HEAD "s_mov_b32 m0, %0\n\ts_nop 3\n\t"
#define GL(v, off) "global_load_lds_dwordx4 %" #v ", %1 offset:" #off " sc1\n\t"
DI void glds1(unsigned m, const char* sb, unsigned v0) { asm volatile(GL_HEAD GL(2, 0)::"s"(m), "s"(sb), "v"(v0) : "memory"); }
DI void glds2(unsigned m, const char* sb, unsigned v0, unsigned v1) {
  asm volatile(GL_HEAD GL(2, 0) GL(3, 1024)::"s"(m), "s"(sb), "v"(v0), "v"(v1) : "memory");
}
DI void glds3(unsigned m, const char* sb, unsigned v0, unsigned v1, unsigned v2) {
  asm volatile(GL_HEAD GL(2, 0) GL(3, 1024) GL(4, 2048)::"s"(m), "s"(sb), "v"(v0), "v"(v1), "v"(v2) : "memory");
}
DI void glds4(unsigned m, const char* sb, unsigned v0, unsigned v1, unsigned v2, unsigned v3) {
  asm volatile(GL_HEAD GL(2, 0) GL(3, 1024) GL(4, 2048) GL(5, 3072)::"s"(m), "s"(sb), "v"(v0), "v"(v1), "v"(v2), "v"(v3) : "memory");
}
DI void st16(rsrc_t r, unsigned off, u32x4 v) { __builtin_amdgcn_raw_buffer_store_b128(v, r, off, 0, 16); }
DI void st8(rsrc_t r, unsigned off, u32x2 v) { __builtin_amdgcn_raw_buffer_store_b64(v, r, off, 0, 16); }
DI void st4(rsrc_t r, unsigned off, unsigned v) { __builtin_amdgcn_raw_buffer_store_b32(v, r, off, 0, 16); }
DI u32x4 ld16(rsrc_t r, unsigned off) { return __builtin_amdgcn_raw_buffer_load_b128(r, off, 0, 16); }
DI u32x2 ld8(rsrc_t r, unsigned off) { return __builtin_amdgcn_raw_buffer_load_b64(r, off, 0, 16); }
DI u32x4 ld16s(rsrc_t r, unsigned voff, unsigned soff) { return __builtin_amdgcn_raw_buffer_load_b128(r, voff, soff, 16); }
DI void ldsw16(lds_ptr p, u32x4 v) { *(LAS u32x4*)p = v; }
DI f32x2 ld8_async(unsigned voff, const char* sbase) {
  f32x2 v; asm volatile("s_nop 4\n\tglobal_load_dwordx2 %0, %1, %2 sc1" : "=v"(v) : "v"(voff), "s"(sbase) : "memory"); return v;
}
DI u32x4 pair32(unsigned a0, unsigned a1, unsigned b0, unsigned b1) {
  auto x = __builtin_amdgcn_permlane32_swap(a0, b0, false, false);
  auto y = __builtin_amdgcn_permlane32_swap(a1, b1, false, false);
  return u32x4{x[0], y[0], x[1], y[1]};
}
template <int N> DI float ror16(float v) { return __int_as_float(__builtin_amdgcn_update_dpp(0, __float_as_int(v), 0x120 + N, 0xf, 0xf, false)); }
DI float sum32(float v) {
  v += ror16<8>(v); v += ror16<4>(v); v += ror16<2>(v); v += ror16<1>(v);
  auto a = __builtin_amdgcn_permlane16_swap(__float_as_uint(v), __float_as_uint(v), false, false);
  return __uint_as_float(a[0]) + __uint_as_float(a[1]);
}
DI u32x4 pair16(unsigned a0, unsigned a1, unsigned b0, unsigned b1) {
  auto x = __builtin_amdgcn_permlane16_swap(a0, b0, false, false);
  auto y = __builtin_amdgcn_permlane16_swap(a1, b1, false, false);
  return u32x4{x[0], y[0], x[1], y[1]};
}

DI float rows4(float v) {
  auto a = __builtin_amdgcn_permlane16_swap(__float_as_uint(v), __float_as_uint(v), false, false);
  v = __uint_as_float(a[0]) + __uint_as_float(a[1]);
  auto b = __builtin_amdgcn_permlane32_swap(__float_as_uint(v), __float_as_uint(v), false, false);
  return __uint_as_float(b[0]) + __uint_as_float(b[1]);
}

#define STAMP(id)
#define STAMPC(id)
#define STAMPL(sl, id)
#define SLOT(n)

#define WARM_GU 1
#define POLL_DEPTH 1
#define POLL_GATE 1
#define POLL_SLEEP 1
#define ATT_PRIO 3
#define ATT_ORD 0
#define WO_AH 6
#define DN_AH 8
#define ATT_ROT 1
#define GU_SKIP 1
#define ATT_LATE 4
#define GU16 1
#define QKV16 1
#define PACE 16
#define SPANST(id)
struct Ctx {
  char* ws; rsrc_t r; lds_ptr lds; u64 tgt; int lane, xcd, j;
};
DI Ctx fresh(const Ctx& c0) {
  Ctx c = c0;
  asm volatile("v_mbcnt_lo_u32_b32 %0, -1, 0\n\tv_mbcnt_hi_u32_b32 %0, -1, %0" : "=v"(c.lane), "+s"(c.xcd), "+s"(c.j), "+s"(c.ws));
  return c;
}
DI int opq(int v) { asm volatile("" : "+s"(v)); return v; }
constexpr int SPIN_CAP = 1 << 18;

DI u64 epoch(const char* ws) {
  u64 e; asm volatile("s_nop 4\n\ts_load_dwordx2 %0, %1, 0x0\n\ts_waitcnt lgkmcnt(0)" : "=s"(e) : "s"(ws + WS_EPOCH) : "memory"); return e + 1;
}
DI unsigned flg(int arr, int unit) { return WS_FLAG + (arr * 256 + unit) * 128; }
DI void publish(const Ctx& c, int cnt_off, unsigned n, unsigned to, bool mark = false) {
  if (mark && c.lane == 0) __hip_atomic_fetch_add((LAS unsigned*)(c.lds + M_ISS), 1u, __ATOMIC_RELAXED, __HIP_MEMORY_SCOPE_WORKGROUP);
  wait_vm<0>();
  if (c.lane == 0) {
    const unsigned old = __hip_atomic_fetch_add((LAS unsigned*)(c.lds + cnt_off), 1u, __ATOMIC_RELAXED, __HIP_MEMORY_SCOPE_WORKGROUP);
    if (old % n == n - 1) __hip_atomic_store((GAS u64*)(c.ws + to), c.tgt, __ATOMIC_RELAXED, __HIP_MEMORY_SCOPE_AGENT);
  }
}
template <int N> struct Poll {
  u64 v[POLL_DEPTH][N];
  DI void run(const Ctx& c, unsigned o0, unsigned o1 = 0) {

    auto ld = [&](int r) {
      asm volatile("global_load_dwordx2 %0, %1, %2 sc1" : "=v"(v[r][0]) : "v"(o0), "s"(c.ws) : "memory");
      if constexpr (N == 2) asm volatile("global_load_dwordx2 %0, %1, %2 sc1" : "=v"(v[r][N - 1]) : "v"(o1), "s"(c.ws) : "memory");
      __builtin_amdgcn_s_sleep(POLL_SLEEP);
    };
    auto ok = [&](int r) {
      if constexpr (N == 2) asm volatile("s_waitcnt vmcnt(%2)" : "+v"(v[r][0]), "+v"(v[r][N - 1]) : "n"((POLL_DEPTH - 1) * N) : "memory");
      else asm volatile("s_waitcnt vmcnt(%1)" : "+v"(v[r][0]) : "n"(POLL_DEPTH - 1) : "memory");
      return __all(v[r][0] == c.tgt && v[r][N - 1] == c.tgt);
    };
    asm volatile("s_nop 4" ::: "memory");
    ld(0); if (POLL_DEPTH > 1) ld(1);
#pragma unroll 1
    for (int spin = 0; spin < SPIN_CAP; ++spin) {
      if (ok(0)) break; ld(0);
      if (POLL_DEPTH > 1) { if (ok(1)) break; ld(1); }
    }
  }
  DI void keep() {
#pragma unroll
    for (int r = 0; r < POLL_DEPTH; ++r) { asm volatile("" : "+v"(v[r][0])); if constexpr (N == 2) asm volatile("" : "+v"(v[r][N - 1])); }
  }
};
DI void poll(const Ctx& c, unsigned slot) { Poll<1> p; p.run(c, slot); wait_vm<0>(); p.keep(); }
DI void sig_set(const Ctx& c, unsigned v) { if (c.lane == 0) __hip_atomic_store((LAS unsigned*)(c.lds + M_SIG), v, __ATOMIC_RELAXED, __HIP_MEMORY_SCOPE_WORKGROUP); }
DI void sig_wait(const Ctx& c, unsigned v, int off = M_SIG, bool calm = false) {
#pragma unroll 1
  for (int spin = 0; spin < SPIN_CAP * 16; ++spin) {
    if (__builtin_amdgcn_readfirstlane(__hip_atomic_load((LAS unsigned*)(c.lds + off), __ATOMIC_RELAXED, __HIP_MEMORY_SCOPE_WORKGROUP)) >= v) break;
    if (calm) __builtin_amdgcn_s_sleep(8); else if (off != M_SIG) __builtin_amdgcn_s_sleep(1);
  }
  asm volatile("" ::: "memory");
}

DI void gate(const Ctx& c, Poll<1>& p, int half, unsigned slot, unsigned sig, unsigned iss) {
  if (half == 0) { if (POLL_GATE) sig_wait(c, iss, M_ISS); p.run(c, slot); sig_set(c, sig); STAMPL(sig - 1, 6); } else sig_wait(c, sig);
}

template <int PP, int NSLOT, int NS, int LAG = 1>
struct Ring {
  unsigned voff[PP];
  const char* base;
  unsigned adv, l0;
  int slot;
  DI void init(const char* b, unsigned a, unsigned lds0, int half) {
    base = b - 4096; adv = a; l0 = lds0 + half * PP * 1024; slot = 0;
#pragma unroll
    for (int p = 0; p < PP; ++p) voff[p] += 4096 - (p & 3) * 1024;
  }
  DI void issue() {
    const unsigned m = l0 + slot * (2 * PP * 1024);
    glds4(m, base, voff[0], voff[1], voff[2], voff[3]);
    if constexpr (PP == 5) glds1(m + 4096, base, voff[4]);
    if constexpr (PP == 7) glds3(m + 4096, base, voff[4], voff[5], voff[6]);
    if constexpr (PP == 8) glds4(m + 4096, base, voff[4], voff[5], voff[6], voff[7]);
    base += adv; slot = slot == NSLOT - 1 ? 0 : slot + 1;
  }
  template <int N = NSLOT> DI void fill() {
#pragma unroll 1
    for (int s = 0; s < N; ++s) issue();
  }
  template <int NEED> DI void fill_ahead(const Ctx& c, unsigned sig) {
    int n = 0;
#pragma unroll 1
    while (n < NSLOT) {
      issue(); wait_vm<PACE>(); ++n;
      if (n * PP >= NEED * PP + PACE &&
          __builtin_amdgcn_readfirstlane(__hip_atomic_load((LAS unsigned*)(c.lds + M_SIG), __ATOMIC_RELAXED, __HIP_MEMORY_SCOPE_WORKGROUP)) >= sig) break;
    }
    if (n == NSLOT) wait_vm<(NSLOT - NEED) * PP>();
    BARRIER_L(); WRESET();
#pragma unroll 1
    for (; n < NSLOT; ++n) issue();
  }
  DI void rest() { range<NSLOT - 2, NSLOT - LAG>(); steady(); range<NSLOT - 1 - LAG, 0>(); BARRIER_L(); }
  template <int R, int E> DI void range() { if constexpr (R >= E) { wait_vm<R * PP>(); BARRIER_L(); range<R - 1, E>(); } }
  template <int D0 = NSLOT> DI void head() {
    wait_vm<(D0 - 1) * PP>(); BARRIER_L(); WRESET();
    if constexpr (D0 < NSLOT) fill<NSLOT - D0>();
    range<NSLOT - 2, NSLOT - LAG>();
    steady();
  }
  DI void steady() {
#pragma unroll 1
    for (int k = LAG; k < NS - NSLOT + LAG; ++k) { wait_vm<(NSLOT - 1 - LAG) * PP>(); BARRIER_L(); issue(); }
  }
  template <int D0 = NSLOT> DI void run() { head<D0>(); range<NSLOT - 1 - LAG, 0>(); BARRIER_L(); }
};

DI void ln_rows(const Ctx& c, int row, const f32x2 (&p)[8]) {
  f32x2 t = p[0];
#pragma unroll
  for (int i = 1; i < 8; ++i) t += p[i];
  const float m8 = t[0] * 0.125f; float q = 0.f;
#pragma unroll
  for (int i = 0; i < 8; ++i) { const float a = p[i][0] - m8; q += a * a; }
  const float rstd = __builtin_amdgcn_rsqf((t[1] + q * (1.f / 80.f)) * (1.f / 640.f) + 1e-5f);
  *(LAS float*)(c.lds + M_SST + row * 4) = rstd;
  *(LAS float*)(c.lds + M_SST + 512 + row * 4) = -t[0] * (1.f / 640.f);
}
template <int NP> DI void ln_cols_sum(const Ctx& c, int n4, const u32x4 (&q)[NP]) {
  f32x4 cs = __builtin_bit_cast(f32x4, q[0]);
#pragma unroll
  for (int i = 1; i < NP; ++i) cs += __builtin_bit_cast(f32x4, q[i]);
  if (c.lane < n4) *(LAS f32x4*)(c.lds + M_SCS + c.lane * 16) = cs;
}
template <int NP> DI void ln_cols(const Ctx& c, int n4, unsigned off, unsigned pitch) {
  u32x4 q[NP];
#pragma unroll
  for (int i = 0; i < NP; ++i) q[i] = ld16(c.r, off + i * pitch + (c.lane < n4 ? c.lane : 0) * 16);
  ln_cols_sum<NP>(c, n4, q);
}

#define MAP 0
DI int qkv_panel(const Ctx& c) { return (c.xcd & 3) * 4 + (c.j & 3); }
DI int qkv_cb(const Ctx& c) { return (c.xcd >> 2) * 8 + (c.j >> 2); }
DI int pr_panel(const Ctx& c) { return (c.xcd & 3) * 8 + (c.j & 7); }
DI int pr_cb(const Ctx& c) { return (c.xcd >> 2) * 4 + (c.j >> 3); }
DI int gu_panel(const Ctx& c) { return (c.xcd & 1) * 8 + (c.j & 7); }
DI int gu_cb(const Ctx& c) { return (c.xcd >> 1) * 4 + (c.j >> 3); }
DI int att_head(const Ctx& c) { return c.xcd * 4 + (c.j & 3); }
DI int att_qb(const Ctx& c) { return c.j >> 2; }

struct Phase0 {
  u32x4 v[3], t[10];
  DI void load(const Ctx& c, int hw, const uint16_t* x, const uint16_t* wqkv) {
    const int lane = c.lane, r32 = lane & 31, b = blockIdx.x, row = b * 8 + hw * 2 + (lane >> 5);
    v[0] = *(const u32x4*)(x + row * 640 + r32 * 8); v[1] = *(const u32x4*)(x + row * 640 + 256 + r32 * 8);
    v[2] = *(const u32x4*)(x + row * 640 + 512 + (r32 & 15) * 8);
    if (hw == 3 && b < 240) {
      const rsrc_t r = __builtin_amdgcn_make_buffer_rsrc((void*)wqkv, 0, 640 * 3840, 0x00020000);
#pragma unroll
      for (int i = 0; i < 10; ++i)
        t[i] = __builtin_amdgcn_raw_buffer_load_b128(r, (lane >> 3) * 3840 + (lane & 7) * 16, ((b / 30) * 80 + i * 8) * 3840 + (b % 30) * 128, 0);
    }
  }
  DI void rows(const Ctx& c, int hw) {
    const int lane = c.lane, r32 = lane & 31, row = blockIdx.x * 8 + hw * 2 + (lane >> 5);
    const float w2 = r32 < 16 ? 1.f : 0.f;
    float sum = 0.f, s2 = 0.f;
#pragma unroll
    for (int e = 0; e < 4; ++e) { sum += (bflo(v[0][e]) + bfhi(v[0][e])) + (bflo(v[1][e]) + bfhi(v[1][e])); s2 += bflo(v[2][e]) + bfhi(v[2][e]); }
    sum = sum32(sum + w2 * s2);
    const float mean = sum * (1.f / 640.f); float m2 = 0.f; s2 = 0.f;
#pragma unroll
    for (int e = 0; e < 4; ++e) {
      const float a0 = bflo(v[0][e]) - mean, a1 = bfhi(v[0][e]) - mean, a2 = bflo(v[1][e]) - mean, a3 = bfhi(v[1][e]) - mean;
      const float a4 = bflo(v[2][e]) - mean, a5 = bfhi(v[2][e]) - mean;
      m2 += a0 * a0 + a1 * a1 + a2 * a2 + a3 * a3; s2 += a4 * a4 + a5 * a5;
    }
    m2 = sum32(m2 + w2 * s2);
    if (r32 < 8) st8(c.r, WS_ST + (row >> 7) * 8192 + r32 * 1024 + (row & 127) * 8, u32x2{__float_as_uint(sum * 0.125f), __float_as_uint(m2 * 0.125f)});
  }
  float s[8];
  DI void cols1(int hw) {
    if (hw == 3 && blockIdx.x < 240) {
      unsigned ul = 0x00003f80u, uh = 0x3f800000u;
      asm volatile("" : "+v"(ul), "+v"(uh));
      const bf16x2 lo = __builtin_bit_cast(bf16x2, ul), hi = __builtin_bit_cast(bf16x2, uh);
#pragma unroll
      for (int e = 0; e < 4; ++e) {
        s[2 * e] = 0.f; s[2 * e + 1] = 0.f;
#pragma unroll
        for (int i = 0; i < 10; ++i) {
          const unsigned u = t[i][e];
          s[2 * e] = __builtin_amdgcn_fdot2_f32_bf16(__builtin_bit_cast(bf16x2, u), lo, s[2 * e], false);
          s[2 * e + 1] = __builtin_amdgcn_fdot2_f32_bf16(__builtin_bit_cast(bf16x2, u), hi, s[2 * e + 1], false);
        }
      }
#pragma unroll
      for (int e = 0; e < 8; ++e) PIN(s[e]);
    }
  }
  DI void cols2(const Ctx& c, int hw) {
    const int lane = c.lane, b = blockIdx.x;
    if (hw == 3 && b < 240) {
#pragma unroll
      for (int e = 0; e < 8; ++e) s[e] = rows4(s[e] + ror16<8>(s[e]));
      const int hi = (lane >> 3) & 1;
      const f32x4 o = hi ? f32x4{s[4], s[5], s[6], s[7]} : f32x4{s[0], s[1], s[2], s[3]};
      if (lane < 16) st16(c.r, WS_CSQ + ((b / 30) * 1920 + (b % 30) * 64 + (lane & 7) * 8 + hi * 4) * 4, __builtin_bit_cast(u32x4, o));
    }
  }
};

template <bool SWAP, class PRE>
DI void qkv_loop(const Ctx& c, int w, int layer, f32x4 (&acc)[4][4], PRE pre) {
  const int lane = c.lane, i = lane & 15, g = lane >> 4, wm = w & 1, wn = w >> 1;
  const int rm = (i >> 3) * 1024 + (i & 7) * 128 + ((g ^ ((i >> 1) & 7)) << 4);
  lds_ptr pa0 = c.lds + QKV_A + wm * 8192 + (layer == 0 ? rm : lane * 16);
  lds_ptr pa1 = c.lds + QKV_A + wm * 8192 + (layer == 0 ? rm ^ 64 : lane * 16 + 1024);
  const int f = (i >> 2) | ((g & 1) << 2), rb = (g * 8 + (i >> 2)) * 256 + (i & 3) * 8;
  lds_ptr pb[4];
#pragma unroll
  for (int t = 0; t < 4; ++t) pb[t] = c.lds + rb + (((wn * 4 + t) ^ f) << 5);
  bf16x8 x0[4], x1[4], w0[4], w1[4];
  auto step = [&](const bf16x8* xa, const bf16x8* wb, bf16x8* xn, bf16x8* wn_, lds_ptr pa, int bo, bool ld) {
#pragma unroll
    for (int q = 0; q < 16; ++q) {
      if (ld) {
        if (q == 0 || q == 9 || q == 11 || q == 13) { const int t = q == 0 ? 0 : (q - 7) >> 1; wn_[t] = ldstr(pb[t] + bo, 1024); }
        if (q == 2 || q == 4 || q == 6 || q == 8) xn[(q - 2) >> 1] = ldsr16(pa + ((q - 2) >> 1) * 2048);
      }
      const int t = q >> 2, m = q & 3;
      acc[m][t] = SWAP ? mfma16(wb[t], xa[m], acc[m][t]) : mfma16(xa[m], wb[t], acc[m][t]);
      SBAR();
    }
  };
  SLOT(layer * 5);
  if (layer) STAMP(0);
  STAMP(1);
  BARRIER();
  STAMP(2); STAMPC(0); WRESET();
#pragma unroll
  for (int e = 0; e < 4; ++e) { w0[e] = ldstr(pb[e], 1024); x0[e] = ldsr16(pa0 + e * 2048); }
  int sa = 0, sb = 0;
#pragma unroll 1
  for (int st = 0; st < 9; ++st) {
    step(x0, w0, x1, w1, pa1, 8192, true);
    const int da = sa == QKV_NA - 1 ? -(QKV_NA - 1) * 16384 : 16384, db = sb == QKV_NB - 1 ? -(QKV_NB - 1) * 16384 : 16384;
    sa = sa == QKV_NA - 1 ? 0 : sa + 1; sb = sb == QKV_NB - 1 ? 0 : sb + 1;
    pa0 += da; pa1 += da; pb[0] += db; pb[1] += db; pb[2] += db; pb[3] += db;
    BARRIER_NW();
    step(x1, w1, x0, w0, pa0, 0, true);
  }
  step(x0, w0, x1, w1, pa1, 8192, true);
  BARRIER();
  pre(); SBAR();
  step(x1, w1, x0, w0, pa0, 0, false);
  STAMP(3); STAMPC(1);
}
DI unsigned qkv_w_off(const Ctx& c, int q) {
  const int row = q * 4 + (c.lane >> 4);
  return row * 3840 + qkv_cb(c) * 256 + (((c.lane & 15) ^ (QKV16 ? ((row & 3) << 1) | (row & 8) : (row & 3) << 2)) << 4);
}
DI void qkv_helper(Ctx c, u64& tgt, int hw, int layer, const uint16_t* x, const uint16_t* wqkv) {
  const int panel = qkv_panel(c), cb = qkv_cb(c), l = c.lane;
  const bool on = cb != 15;
  if (layer == 0) {
    Phase0 p0;
    p0.load(c, hw, x, wqkv);
    c.tgt = tgt = epoch(c.ws);
    if (on) BARRIER();
    p0.cols1(hw);
    if (on && hw == 3) BARRIER();
    p0.rows(c, hw);
    if (on && hw != 3) BARRIER();
    p0.cols2(c, hw);
    if (on) { BARRIER(); BARRIER(); }
    publish(c, M_CNT2, 4, flg(FL_P0, blockIdx.x));
    if (!on) return;
    BARRIER(); BARRIER();
    const unsigned at = flg(FL_P0, hw < 2 ? panel * 16 + hw * 8 + (l & 7) : (l & 7) * 30 + cb * 2 + ((l >> 3) & 1));
    const u64 v = __hip_atomic_load((const GAS u64*)(c.ws + at), __ATOMIC_RELAXED, __HIP_MEMORY_SCOPE_AGENT);
    BARRIER();
    if (!__all(v == c.tgt)) poll(c, at);
    if (hw < 2) {
      f32x2 p[8];
#pragma unroll
      for (int i = 0; i < 8; ++i) p[i] = __builtin_bit_cast(f32x2, ld8(c.r, WS_ST + panel * 8192 + i * 1024 + (hw * 64 + l) * 8));
      BARRIER(); BARRIER();
      ln_rows(c, hw * 64 + l, p);
    } else if (hw == 2) {
      u32x4 q[8];
#pragma unroll
      for (int i = 0; i < 8; ++i) q[i] = ld16(c.r, WS_CSQ + cb * 512 + i * 7680 + (l & 31) * 16);
      BARRIER(); BARRIER();
      ln_cols_sum<8>(c, 32, q);
    } else { BARRIER(); BARRIER(); }
    BARRIER(); BARRIER();
  } else if (on) {
    if (hw == 2) ln_cols<8>(c, 32, WS_CSQ + cb * 512, 7680);
    BARRIER(); BARRIER(); BARRIER(); BARRIER(); BARRIER(); BARRIER();
    if (hw < 2) {
      f32x2 p[8];
#pragma unroll
      for (int i = 0; i < 8; ++i) p[i] = __builtin_bit_cast(f32x2, ld8(c.r, WS_ST + 2 * STB + panel * 8192 + i * 1024 + (hw * 64 + l) * 8));
      BARRIER(); BARRIER();
      ln_rows(c, hw * 64 + l, p);
    } else { BARRIER(); BARRIER(); }
    BARRIER(); BARRIER(); BARRIER();
  }
}
DI void qkv_compute(Ctx c, u64& tgt, int w, int layer, const uint16_t* x, const uint16_t* wqkv) {
  const int panel = qkv_panel(c), cb = qkv_cb(c);
  if (w >= 4) { qkv_helper(c, tgt, w - 4, layer, x, wqkv); return; }
  if (layer == 0) c.tgt = tgt = epoch(c.ws);
  if (cb == 15) return;
  const int lane = c.lane, i = lane & 15, g = lane >> 4, wm = w & 1, wn = w >> 1;
  const unsigned L = WS_L + layer * L_SZ;
  f32x4 acc[4][4];
#pragma unroll
  for (int e = 0; e < 16; ++e) acc[e >> 2][e & 3] = f32x4{0.f, 0.f, 0.f, 0.f};
  const LAS float* srs = (const LAS float*)(c.lds + M_SST) + wm * 64;
  const LAS float* scs = (const LAS float*)(c.lds + M_SCS) + wn * 64;
  if (cb < 10) {
    float rs[4], nm[4]; f32x4 cs[4];
    qkv_loop<true>(c, w, layer, acc, [&]() {
#pragma unroll
      for (int e = 0; e < 4; ++e) { rs[e] = srs[e * 16 + i]; nm[e] = srs[128 + e * 16 + i]; cs[e] = *(const LAS f32x4*)(scs + e * 16 + g * 4); }
    });
    const float qs = cb < 5 ? 0.22360679774997896f * 1.4426950408889634f : 1.f;
    const unsigned i1 = L + (cb < 5 ? L_Q1 : L_K1), i2 = L + (cb < 5 ? L_Q2 : L_K2);
#pragma unroll
    for (int e = 0; e < 4; ++e) rs[e] *= qs;
#pragma unroll
    for (int t = 0; t < 4; ++t) {
      const int o = (cb % 5) * 16 + wn * 8 + t * 2 + (g >> 1), hp = o / 5, pos = o - hp * 5;
      const unsigned ts = pos == 2 ? 512 : 1024;
      const unsigned a0 = (pos == 2 ? i2 + hp * 32768 : i1 + (2 * hp + (pos > 2)) * 65536 + (pos > 2 ? pos - 3 : pos) * 512) +
                          (panel * 4 + wm * 2) * ts + ((g & 1) * 16 + i) * 16;
#pragma unroll
      for (int mp = 0; mp < 2; ++mp) {
        const f32x4 u = (acc[mp * 2][t] + cs[t] * nm[mp * 2]) * rs[mp * 2], v = (acc[mp * 2 + 1][t] + cs[t] * nm[mp * 2 + 1]) * rs[mp * 2 + 1];
        st16(c.r, a0 + mp * ts, pair16(pk2(u[0], u[1]), pk2(u[2], u[3]), pk2(v[0], v[1]), pk2(v[2], v[3])));
      }
    }
  } else {
    float cs[4]; f32x4 rs[4], nm[4];
    qkv_loop<false>(c, w, layer, acc, [&]() {
#pragma unroll
      for (int e = 0; e < 4; ++e) { cs[e] = scs[e * 16 + i]; rs[e] = *(const LAS f32x4*)(srs + e * 16 + g * 4); nm[e] = *(const LAS f32x4*)(srs + 128 + e * 16 + g * 4); }
    });
#pragma unroll
    for (int t = 0; t < 4; ++t) {
      const int cv = (cb - 10) * 128 + wn * 64 + t * 16 + i, head = cv / 20, dim = cv - head * 20;
      const int d = (head & 1) ? (dim < 4 ? dim + 16 : dim - 4) : dim;
      const unsigned a0 = L + L_V + head * 81920 + (panel * 8 + wm * 4 + (g & 1)) * 640 + (g >> 1) * 320 + d * 16;
#pragma unroll
      for (int mp = 0; mp < 2; ++mp) {
        const f32x4 u = (acc[mp * 2][t] + nm[mp * 2] * cs[t]) * rs[mp * 2], v = (acc[mp * 2 + 1][t] + nm[mp * 2 + 1] * cs[t]) * rs[mp * 2 + 1];
        st16(c.r, a0 + mp * 1280, pair16(pk2(u[0], u[1]), pk2(u[2], u[3]), pk2(v[0], v[1]), pk2(v[2], v[3])));
      }
    }
  }
  SLOT(layer * 5);
  STAMP(4);
  publish(c, M_CNT4, 4, flg(FL_QKV + layer * 5, panel * 16 + cb), true);
  STAMP(5);
}
typedef Ring<8, QKV_NB, 10, 2> QkvB;
DI void qkv_load0(const Ctx& c, int lw, const uint16_t* x, const uint16_t* wqkv) {
  const int half = lw & 1;
  if (lw >= 2) {
    QkvB rg;
#pragma unroll
    for (int p = 0; p < 8; ++p) rg.voff[p] = qkv_w_off(c, half * 8 + p);
    rg.init((const char*)wqkv, 64 * 3840, 0, half);
    rg.template fill<3>(); rg.template run<3>();
  } else {
    Ring<8, QKV_NA, 10, 2> rg;
#pragma unroll
    for (int p = 0; p < 8; ++p) {
      const int mt = qkv_panel(c) * 8 + half * 4 + (p >> 1), r16 = (p & 1) * 8 + (c.lane >> 3);
      rg.voff[p] = (mt * 16 + r16) * 1280 + (((c.lane & 7) ^ ((r16 >> 1) & 7)) << 4);
    }
    rg.init((const char*)x, 128, QKV_A, half);
    rg.template fill<3>(); rg.template run<3>();
  }
}
DI void qkv_load1(const Ctx& c, int lw, const uint16_t* wqkv) {
  const int half = lw & 1;
  static_assert(QKV_NA == 4 && QKV_NB == 5, "schedule");
  if (lw >= 2) {
    QkvB rg;
#pragma unroll
    for (int p = 0; p < 8; ++p) rg.voff[p] = qkv_w_off(c, half * 8 + p);
    rg.init((const char*)wqkv, 64 * 3840, 0, half);
    sig_wait(c, 12, M_CNT4);
    rg.template fill_ahead<1>(c, 6);
    rg.rest();
  } else {
    Ring<8, QKV_NA, 10, 2> rg;
#pragma unroll
    for (int p = 0; p < 8; ++p) rg.voff[p] = WS_X2 + (qkv_panel(c) * 8 + half * 4 + (p >> 1)) * 20480 + (p & 1) * 1024 + c.lane * 16;
    rg.init(c.ws, 2048, QKV_A, half);
    Poll<1> pl;
    gate(c, pl, half, flg(FL_DN, qkv_panel(c) * 16 + (c.lane & 15)), 6, 28);
    rg.template fill<2>(); rg.template run<2>();
    pl.keep();
  }
}

DI f32x16 qk8(s16x4 k2, s16x4 q2) {
 return __builtin_amdgcn_mfma_f32_32x32x8bf16_1k(k2, q2, f32x16{}, 0, 0, 0); }
DI f32x16 qk16(bf16x8 k1, bf16x8 q1, f32x16 s) { return __builtin_amdgcn_mfma_f32_32x32x16_bf16(k1, q1, s, 0, 0, 0); }
DI f32x16 pv_lo(bf16x8 v, f32x16 p, f32x16 o) {
  return __builtin_amdgcn_smfmac_f32_32x32x32_bf16(v, __builtin_bit_cast(bf16x16, (f32x8)__builtin_shufflevector(p, p, 0, 1, 2, 3, 4, 5, 6, 7)), o, 0xDDDD, 0, 0);
}
DI f32x16 pv_hi(bf16x8 v, f32x16 p, f32x16 o) {
  return __builtin_amdgcn_smfmac_f32_32x32x32_bf16(v, __builtin_bit_cast(bf16x16, (f32x8)__builtin_shufflevector(p, p, 8, 9, 10, 11, 12, 13, 14, 15)), o, 0xDDDD, 0, 0);
}
DI float swap32(float v, int h) {
  auto r = __builtin_amdgcn_permlane32_swap(__float_as_uint(v), __float_as_uint(v), false, false);
  return __uint_as_float(h ? r[0] : r[1]);
}
#define EXP4(X, i) do { X[i] = __builtin_amdgcn_exp2f(X[i]); X[i + 1] = __builtin_amdgcn_exp2f(X[i + 1]); X[i + 2] = __builtin_amdgcn_exp2f(X[i + 2]); \
                        X[i + 3] = __builtin_amdgcn_exp2f(X[i + 3]); PIN(X); SBAR(); } while (0)
DI void att_compute(const Ctx& c, int w, int layer) {
  const int lane = c.lane, head = att_head(c), qb = att_qb(c);
  const int qp = w & 3, kp = w >> 2, r32 = lane & 31, h = lane >> 5;
  lds_ptr lds = c.lds;
  const unsigned L = WS_L + layer * L_SZ;
  SLOT(layer * 5 + 1);
  STAMP(0);
  for (int s = w * 64 + lane; s < 672; s += 512) {
    const unsigned v = s % 20 == 0 ? 0x3f803f80u : 0u;
    *(LAS u32x4*)(lds + AT_CONST + s * 16) = u32x4{v, v, v, v};
  }
  lds_ptr pk1 = lds + kp * 8192 + lane * 16;
  lds_ptr pk2_ = h ? lds + AT_CONST + 16 : lds + 16384 + kp * 4096 + r32 * 16 + (head & 1) * 8;
  lds_ptr pv = (r32 >= 20 ? lds + AT_CONST : lds + 24576 + kp * 10240) + h * 320 + r32 * 16;
  const int vmask = r32 >= 20 ? 0 : -1, kmask = h ? 0 : -1;
  f32x16 o0 = f32x16{}, o1 = f32x16{};
  f32x16 s0, s1, s2, s3 = f32x16{};
  STAMP(1);
  BARRIER();
  STAMP(2); STAMPC(0); WRESET();
  const bf16x8 q1a = ldsr16(lds + 2 * AT_SLOT + qp * 2048 + lane * 16), q1b = ldsr16(lds + 2 * AT_SLOT + qp * 2048 + 1024 + lane * 16);
  lds_ptr pq2 = h ? lds + AT_CONST + 16 : lds + 2 * AT_SLOT + 8192 + qp * 1024 + r32 * 16 + (head & 1) * 8;
  const s16x4 q2a = *(const LAS s16x4*)pq2, q2b = *(const LAS s16x4*)(pq2 + 512);
  bf16x8 k1 = ldsr16(pk1), vlo = ldsr16(pv), vhi = bf16x8{};
  s16x4 k2 = *(const LAS s16x4*)pk2_;
  s0 = qk16(k1, q1a, qk8(k2, q2a));
#define PH_EVEN(SC, SN, SP, KLOAD, VOFF) do { \
    SN = qk8(k2, q2b); SBAR(); EXP4(SC, 0); SN = qk16(k1, q1b, SN); SBAR(); KLOAD; SBAR(); EXP4(SC, 4); \
    o1 = pv_hi(vhi, SP, o1); SBAR(); vhi = ldsr16(pv + (VOFF) + 640); SBAR(); EXP4(SC, 8); o0 = pv_lo(vlo, SC, o0); SBAR(); EXP4(SC, 12); } while (0)
#define PH_ODD(SC, SN, SP, VLOAD) do { \
    SN = qk8(k2, q2a); SBAR(); EXP4(SC, 0); SN = qk16(k1, q1a, SN); SBAR(); EXP4(SC, 4); \
    o0 = pv_hi(vhi, SP, o0); SBAR(); EXP4(SC, 8); o1 = pv_lo(vlo, SC, o1); SBAR(); VLOAD; SBAR(); EXP4(SC, 12); } while (0)
#define KLD(t) do { k1 = ldsr16(pk1 + (t) * 1024); k2 = *(const LAS s16x4*)(pk2_ + (t) * 512); } while (0)
#define PRI(v) do { asm volatile("s_cmp_eq_u32 %0, 0\n\ts_cbranch_scc1 1f\n\ts_setprio " #v "\n1:" :: "s"(kp) : "scc"); SBAR(); } while (0)
#define HI(n) if (ATT_PRIO == (n)) PRI(1);
#define LO(n) if (ATT_PRIO == (n)) PRI(0);
#define TWO(t) HI(3) HI(4) HI(5) PH_EVEN(s0, s1, s3, KLD((t) + 1), (t) * 1280); PH_ODD(s1, s2, s0, vlo = ldsr16(pv + ((t) + 1) * 1280)); LO(4)
  for (int grp = 0; grp < 4; ++grp) {
    TWO(0) PH_EVEN(s2, s3, s1, if (grp == 0) BARRIER_NW(); KLD(2), 1280); LO(3) PH_ODD(s3, s0, s2, vlo = ldsr16(pv + 2 * 1280));
    TWO(2) PH_EVEN(s2, s3, s1, KLD(4), 3 * 1280); LO(3) LO(5) PH_ODD(s3, s0, s2, vlo = ldsr16(pv + 4 * 1280));
    TWO(4) PH_EVEN(s2, s3, s1, KLD(6), 5 * 1280); LO(3) PH_ODD(s3, s0, s2, vlo = ldsr16(pv + 6 * 1280));
    TWO(6)
    const int d = grp == 2 ? -2 * AT_SLOT : grp == 3 ? 0 : AT_SLOT;
    PH_EVEN(s2, s3, s1, pk1 += d; pk2_ += (d & kmask); if (grp != 3) BARRIER(); KLD(0), 7 * 1280);
    LO(3) LO(5) PH_ODD(s3, s0, s2, pv += (d & vmask); vlo = ldsr16(pv));
  }
  o1 = pv_hi(vhi, s3, o1);
  STAMP(3); STAMPC(1);
  __builtin_amdgcn_s_setprio(0);
  BARRIER();
  const f32x16 give = kp ? o0 : o1, keep = kp ? o1 : o0;
#pragma unroll
  for (int e = 0; e < 3; ++e)
    *(LAS f32x4*)(lds + AT_MERGE + (w * 3 + e) * 1024 + lane * 16) = f32x4{give[4 * e], give[4 * e + 1], give[4 * e + 2], give[4 * e + 3]};
  BARRIER();
  f32x4 a[3];
#pragma unroll
  for (int e = 0; e < 3; ++e)
    a[e] = f32x4{keep[4 * e], keep[4 * e + 1], keep[4 * e + 2], keep[4 * e + 3]} + *(const LAS f32x4*)(lds + AT_MERGE + ((w ^ 4) * 3 + e) * 1024 + lane * 16);
  const int qt = qb * 8 + qp * 2 + kp;
  float l = h ? a[2][0] : 0.f;
  l += swap32(l, h);
  if (__builtin_expect(__any(!(l >= 5.421e-20f && l <= 1.8447e19f)), 0)) {
    const unsigned o2 = (head >> 1) * 32768 + r32 * 16 + (head & 1) * 8;
    const bf16x8 fq1 = __builtin_bit_cast(bf16x8, ld16(c.r, L + L_Q1 + head * 65536 + qt * 1024 + lane * 16));
    u32x2 fq2 = ld8(c.r, L + L_Q2 + o2 + qt * 512);
    if (h) fq2 = u32x2{0u, 0u};
    const unsigned vi = L + L_V + head * 81920 + h * 320 + (r32 < 20 ? r32 : 0) * 16;
    f32x16 oo = f32x16{};
    float m = -INFINITY;
    const u32x4 one8 = {0x3f803f80u, 0x3f803f80u, 0x3f803f80u, 0x3f803f80u};
    for (int t = 0; t < 64; ++t) {
      const u32x4 fk1 = ld16(c.r, L + L_K1 + head * 65536 + t * 1024 + lane * 16);
      const u32x2 fk2 = ld8(c.r, L + L_K2 + o2 + t * 512);
      u32x4 v0 = ld16(c.r, vi + t * 1280), v1 = ld16(c.r, vi + t * 1280 + 640);
      if (r32 >= 20) { v0 = one8; v1 = one8; }
      f32x16 s = qk16(__builtin_bit_cast(bf16x8, fk1), fq1, qk8(__builtin_bit_cast(s16x4, fk2), __builtin_bit_cast(s16x4, fq2)));
      float tm = s[0];
#pragma unroll
      for (int r = 1; r < 16; ++r) tm = fmaxf(tm, s[r]);
      tm = fmaxf(tm, swap32(tm, h));
      const float mn = fmaxf(m, tm), al = __builtin_amdgcn_exp2f(m - mn);
      m = mn;
#pragma unroll
      for (int r = 0; r < 16; ++r) { s[r] = __builtin_amdgcn_exp2f(s[r] - mn); oo[r] *= al; }
      oo = pv_hi(__builtin_bit_cast(bf16x8, v1), s, pv_lo(__builtin_bit_cast(bf16x8, v0), s, oo));
    }
#pragma unroll
    for (int e = 0; e < 3; ++e) a[e] = f32x4{oo[4 * e], oo[4 * e + 1], oo[4 * e + 2], oo[4 * e + 3]};
    l = h ? a[2][0] : 0.f;
    l += swap32(l, h);
  }
  const float inv = 1.f / l;
  const unsigned A0 = pk2(a[0][0] * inv, a[0][1] * inv), A1 = pk2(a[0][2] * inv, a[0][3] * inv);
  const unsigned B0 = pk2(a[1][0] * inv, a[1][1] * inv), B1 = pk2(a[1][2] * inv, a[1][3] * inv);
  const u32x2 C = {pk2(a[2][0] * inv, a[2][1] * inv), pk2(a[2][2] * inv, a[2][3] * inv)};
  const int tok = qt * 32 + r32;
  const unsigned ob = L + L_O + (tok >> 4) * 20480;
  st16(c.r, ob + (head * 2 + h) * 256 + (tok & 15) * 16, pair32(A0, A1, B0, B1));
  if (!h) st8(c.r, ob + 16384 + head * 128 + (tok & 15) * 8, C);
  STAMP(4);
  publish(c, M_CNT, 8, flg(FL_ATT + layer * 5, qb * 32 + head), true);
  STAMP(5);
}
DI void retile_load(const Ctx& c, int J, u32x4 (&v)[5][4], const uint16_t* wo, const uint16_t* wg, const uint16_t* wu, const uint16_t* wd) {
  const int e = J < 216 ? J : J < 256 ? J - 216 : J - 256;
  const int strip = J < 216 ? (e >> 2) % 27 : e % 10, k0 = J < 216 ? (e & 3) * 5 : min((e / 10) * 5, 49), ld = J < 216 ? 3456 : 1280;
  const rsrc_t r = __builtin_amdgcn_make_buffer_rsrc((void*)(J < 108 ? wg : J < 216 ? wu : J < 256 ? wo : wd), 0,
                                                     J < 216 ? 640 * 3456 : J < 256 ? 640 * 1280 : 1728 * 1280, 0x00020000);
  const unsigned vo = (c.lane >> 3) * ld + (c.lane & 7) * 16;
#pragma unroll
  for (int u = 0; u < 5; ++u)
#pragma unroll
    for (int q = 0; q < 4; ++q) {
      const int ch = (k0 + u) * 4 + q;
      const int row = J < 216 || J >= 256 ? ch * 8 : ch < 64 ? (ch >> 1) * 20 + (ch & 2) * 2 + (ch & 1) * 8 : (ch - 64) * 40 + 16;
      v[u][q] = __builtin_amdgcn_raw_buffer_load_b128(r, vo, row * ld + strip * 128, 0);
    }
}
DI void retile_store(const Ctx& c, int J, lds_ptr scr, const u32x4 (&v)[5][4]) {
  const int e = J < 216 ? J : J < 256 ? J - 216 : J - 256, mat = J >= 108;
  const int strip = J < 216 ? (e >> 2) % 27 : e % 10, k0 = J < 216 ? (e & 3) * 5 : min((e / 10) * 5, 49);
  const unsigned ts = J < 216 ? 40960 : J < 256 ? 20480 : 55296;
  const unsigned dst = (J < 216 ? WS_WGU + mat * 20480 : J < 256 ? WS_WO : WS_WD) + strip * 4 * ts + k0 * 1024;
  const int i = c.lane & 15, g = c.lane >> 4;
  const bf16x8 ones = __builtin_bit_cast(bf16x8, u32x4{0x3f803f80u, 0x3f803f80u, 0x3f803f80u, 0x3f803f80u});
  f32x4 cs[4] = {f32x4{}, f32x4{}, f32x4{}, f32x4{}};
#pragma unroll
  for (int u = 0; u < 5; ++u) {
#pragma unroll
    for (int p = 0; p < 4; ++p) *(LAS u32x4*)(scr + p * 1024 + c.lane * 16) = v[u][p];
#pragma unroll
    for (int t = 0; t < 4; ++t) {
      const bf16x8 f = ldstr(scr + (g * 8 + (i >> 2)) * 128 + t * 32 + (i & 3) * 8, 512);
      __builtin_amdgcn_raw_buffer_store_b128(__builtin_bit_cast(u32x4, f), c.r, c.lane * 16, dst + u * 1024 + t * ts, 16);
      cs[t] = mfma16(f, ones, cs[t]);
    }
  }
  if (J < 216 && i == 0) {
#pragma unroll
    for (int t = 0; t < 4; ++t)
      __builtin_amdgcn_raw_buffer_store_b128(__builtin_bit_cast(u32x4, cs[t]), c.r, g * 16, WS_CSG + ((e & 3) * 3456 + (strip * 8 + t * 2 + mat) * 16) * 4, 16);
  }
}
DI int early_piece(int e) { return e < 4 ? (e & 1) + (e >> 1) * 8 : e < 6 ? 16 + (e - 4) * 4 : e < 9 ? 18 + e : 25 + e; }
DI int late_piece(int e) { return e < 6 ? 2 + e : e < 12 ? 4 + e : e < 15 ? 5 + e : e < 18 ? 6 + e : e < 25 ? 9 + e : 12 + e; }
DI void att_load(const Ctx& c, int lw, int layer, unsigned iss, const uint16_t* wo, const uint16_t* wg, const uint16_t* wu, const uint16_t* wd) {
  const int head = att_head(c), qb = att_qb(c), lane = c.lane;
  const char* Lp = c.ws + WS_L + layer * L_SZ;
  const unsigned vo = lane * 16;
  int grp = ATT_ROT ? qb >> 1 : 0;
  auto src_of = [&](int p) {
    return p < 16 ? L_K1 + head * 65536 + grp * 16384 + p * 1024
                  : p < 24 ? L_K2 + (head >> 1) * 32768 + grp * 8192 + (p - 16) * 1024 : L_V + head * 81920 + grp * 20480 + (p - 24) * 1024;
  };
  unsigned v0[14], m0[11], vg[11], sg[11];
#pragma unroll
  for (int q = 0; q < 11; ++q) {
    const int p = q < 3 ? early_piece(lw * 3 + q) : late_piece(lw * 8 + q - 3), pg = lw * 11 + q;
    v0[q] = src_of(p) + vo; m0[q] = p * 1024;
    vg[q] = src_of(pg) + vo; sg[q] = pg < 16 ? 16384 : pg < 24 ? 8192 : 20480;
    asm volatile("" : "+v"(v0[q]), "+s"(m0[q]));
  }
#pragma unroll
  for (int q = 0; q < 3; ++q) {
    const int p = lw * 3 + q;
    v0[11 + q] = (p < 8 ? L_Q1 + head * 65536 + qb * 8192 + p * 1024 : L_Q2 + (head >> 1) * 32768 + qb * 4096 + (p - 8) * 1024) + vo - q * 1024;
    PIN(v0[11 + q]);
  }
  auto issue = [&](int slot) {
#pragma unroll
    for (int q = 0; q < 11; ++q) { vg[q] += grp == 3 ? -3 * sg[q] : sg[q]; glds1(slot * AT_SLOT + (lw * 11 + q) * 1024, Lp, vg[q]); }
    grp = (grp + 1) & 3;
  };
  const int o0 = (head >> 1) * 5 + (head & 1) * 2, c0 = head * 20;
  auto kv_cb = [&](int sel) { return sel == 0 ? 5 + (o0 >> 4) : sel == 1 ? 5 + ((o0 + 2) >> 4) : sel == 2 ? 10 + (c0 >> 7) : 10 + ((c0 + 19) >> 7); };
  if (lw == 0) {
    sig_wait(c, iss, M_ISS);
    poll(c, flg(FL_QKV + layer * 5, lane & 16 ? (qb * 2 + (lane & 1)) * 16 + ((o0 + (lane & 2)) >> 4) : (grp * 4 + (lane & 3)) * 16 + kv_cb((lane >> 2) & 3)));
    sig_set(c, layer * 5 + 2);
    STAMPL(layer * 5 + 1, 6);
  } else sig_wait(c, layer * 5 + 2);
  glds3(2 * AT_SLOT + lw * 3072, Lp, v0[11], v0[12], v0[13]);
#pragma unroll
  for (int q = 0; q < 3; ++q) glds1(m0[q], Lp, v0[q]);
#pragma unroll
  for (int q = 3; q < 3 + ATT_LATE; ++q) glds1(m0[q], Lp, v0[q]);
  wait_vm<ATT_LATE>(); BARRIER();
#pragma unroll
  for (int q = 3 + ATT_LATE; q < 11; ++q) glds1(m0[q], Lp, v0[q]);
  wait_vm<0>(); BARRIER();
  if (lw == 0) { poll(c, flg(FL_QKV + layer * 5, (lane & 15) * 16 + kv_cb(lane >> 4))); sig_set(c, layer * 5 + 3); } else sig_wait(c, layer * 5 + 3, M_SIG, true);
  issue(1);
  const int J = (lw - 1) * 256 + blockIdx.x;
  const bool rt = layer == 0 && lw > 0 && J < 366;
  u32x4 v[5][4];
  if (rt) { retile_load(c, J, v, wo, wg, wu, wd); wait_vm<20>(); } else wait_vm<0>();
  BARRIER();
  issue(2);
  if (rt) retile_store(c, J, c.lds + AT_SCR + (lw - 1) * 4096, v);
  if (layer == 0 && (lw == 1 || lw == 2)) publish(c, M_CNT3, 2, flg(FL_RT, blockIdx.x));
  wait_vm<0>(); BARRIER();
  issue(0);
  wait_vm<0>();
  if (layer == 0) {
    if (lw == 0) {
      if (c.j == 0) {
        Poll<2> pl;
#pragma unroll 1
        for (int q = 0; q < 2; ++q) { pl.run(c, flg(FL_RT, q * 128 + lane), flg(FL_RT, q * 128 + 64 + lane)); wait_vm<0>(); pl.keep(); }
        if (lane < 32) __hip_atomic_store((GAS u64*)(c.ws + flg(FL_RT2, lane * 8 + c.xcd)), c.tgt, __ATOMIC_RELAXED, __HIP_MEMORY_SCOPE_AGENT);
      }
      poll(c, flg(FL_RT2, blockIdx.x));
      if (blockIdx.x == 0 && lane == 0) *(u64*)(c.ws + WS_EPOCH) = c.tgt;
    }
  }
  BARRIER();
  u32x4 pk[25];
#pragma unroll
  for (int e = 0; e < 25; ++e) pk[e] = ld16s(c.r, vo, WS_WO + (pr_cb(c) * 5 + e % 5) * 20480 + (lw * 5 + e / 5) * 1024);
#pragma unroll 1
  for (unsigned q = lw; q < 35; q += 4) glds1(AT_SCR + lw * 1024, c.ws + WS_WGU + ((gu_cb(c) * 27) >> 2) * 40960 + ((c.j & 7) * 35 + q) * 1024, vo);
  wait_vm<0>();
  BARRIER(); BARRIER();
  sig_wait(c, layer * 16 + 8, M_CNT);
#pragma unroll
  for (int e = 0; e < 25; ++e) { const int k = lw * 5 + e / 5; ldsw16(c.lds + (k >> 1) * 10240 + (k & 1) * 1024 + (e % 5) * 2048 + vo, pk[e]); }
}

template <int NS, int NB, int WIN>
DI void proj_compute(const Ctx& c, int w, int slot_id, unsigned a_off, const char* res_rm, unsigned res_off, unsigned out_off, unsigned st_off,
                     uint16_t* out, int arr) {
  constexpr bool RES = NS <= NB;
  const int panel = pr_panel(c), cb = pr_cb(c);
  SLOT(slot_id);
  STAMP(0);
  if (w >= 4) {
    if constexpr (RES) { BARRIER(); BARRIER(); }
    else {
      static_assert(RES || (NS == 27 && NB == 15 && WIN == 18), "schedule");
      const int par = (w >> 1) & 1, half = w & 1;
      u32x4 pk[15];
#pragma unroll
      for (int e = 0; e < 15; ++e) {
        const int q = half * 5 + e % 5;
        pk[e] = ld16s(c.r, c.lane * 16, WS_WD + (cb * 5 + (q >> 1)) * 55296 + ((15 + par + (e / 5) * 2) * 2 + (q & 1)) * 1024);
      }
      lds_ptr pw = c.lds + par * 10240 + half * 5120 + c.lane * 16;
      BARRIER(); BARRIER();
#pragma unroll
      for (int e = 0; e < 10; ++e) ldsw16(pw + (e / 5) * 20480 + (e % 5) * 1024, pk[e]);
      BARRIER();
#pragma unroll
      for (int e = 10; e < 15; ++e) ldsw16(pw + 40960 + (e % 5) * 1024, pk[e]);
      BARRIER(); BARRIER(); BARRIER(); BARRIER();
    }
    return;
  }
  const int lane = c.lane, i = lane & 15, g = lane >> 4, mt = panel * 4 + w;
  u32x2 res[5];
#pragma unroll
  for (int t = 0; t < 5; ++t)
    res[t] = res_rm ? *(const u32x2*)(res_rm + (mt * 16 + i) * 1280 + cb * 160 + t * 32 + g * 8)
                    : ld8(c.r, res_off + (mt * 80 + cb * 10 + t * 2 + (g >> 1)) * 256 + i * 16 + (g & 1) * 8);
  f32x4 acc[5];
#pragma unroll
  for (int t = 0; t < 5; ++t) { acc[t] = f32x4{bflo(res[t][0]), bfhi(res[t][0]), bflo(res[t][1]), bfhi(res[t][1])}; PIN(acc[t]); }
  lds_ptr pb = c.lds + lane * 16;
  unsigned av = a_off + mt * (NS * 2048) + lane * 16;
  u32x4 a[WIN];
  bf16x8 f[3][5];
  int sb = 0;
  STAMP(1);
  BARRIER();
  STAMP(2); STAMPC(0); WRESET();
  constexpr int AH = RES ? WO_AH : DN_AH;
  auto lda = [&](int k) {
    if (RES && k >= 16) {
      const unsigned o = a_off + mt * 20480 + 16384 + (k - 16) * 1024 + g * 256 + i * 8;
      const u32x2 lo = ld8(c.r, o), hi = ld8(c.r, o + 128);
      a[k % WIN] = u32x4{lo[0], lo[1], hi[0], hi[1]};
    } else a[k % WIN] = ld16(c.r, av + k * 1024);
  };
#pragma unroll
  for (int k = 0; k < AH; ++k) lda(k);
#pragma unroll
  for (int t = 0; t < 5; ++t) { f[0][t] = ldsr16(pb + t * 2048); f[1][t] = ldsr16(pb + t * 2048 + 1024); }
  SBAR();
  auto window = [&](bool first, bool last) {
#pragma unroll
    for (int k = 0; k < WIN; ++k) {
      const bool ld = !(last && k + 2 >= WIN);
      if ((k & 1) == 0) {
        if (RES) pb += 10240;
        else { pb += sb == NB - 1 ? -(NB - 1) * 10240 : 10240; sb = sb == NB - 1 ? 0 : sb + 1; }
        if (!ld) BARRIER(); else if (!RES && (k == 10 || (k == 0 && !first))) BARRIER_NW();
      }
#pragma unroll
      for (int t = 0; t < 5; ++t) {
        if (ld) f[(k + 2) % 3][t] = ldsr16(pb + t * 2048 + (k & 1) * 1024);
        acc[t] = mfma16(f[k % 3][t], __builtin_bit_cast(bf16x8, a[k]), acc[t]);
        SBAR();
      }
      if (!last || k + AH < WIN) { lda(k + AH); SBAR(); }
    }
    av += WIN * 1024;
  };
  static_assert(2 * NS % WIN == 0 && (2 * NS == WIN || WIN % 6 == 0), "window");
#pragma unroll 1
  for (int b = 1; b < 2 * NS / WIN; ++b) window(b == 1, false);
  window(false, true);
  STAMP(3); STAMPC(1);
  u32x2 o[5];
#pragma unroll
  for (int t = 0; t < 5; ++t) {
    o[t][0] = pk2(acc[t][0], acc[t][1]); o[t][1] = pk2(acc[t][2], acc[t][3]);
  }
  const u32x4 p0 = pair16(o[0][0], o[0][1], o[1][0], o[1][1]), p1 = pair16(o[2][0], o[2][1], o[3][0], o[3][1]),
              p2 = pair16(o[4][0], o[4][1], o[4][0], o[4][1]);
  const int ch = (g & 1) * 2 + (g >> 1);
  if (out) {
    char* po = (char*)out + (mt * 16 + i) * 1280 + cb * 160;
    *(u32x4*)(po + ch * 16) = p0; *(u32x4*)(po + 64 + ch * 16) = p1;
    if (!(g & 1)) *(u32x4*)(po + 128 + (g >> 1) * 16) = p2;
    STAMP(4);
    return;
  }
  const unsigned xo = out_off + (mt * 80 + cb * 10) * 256 + i * 16;
  st16(c.r, xo + ch * 256, p0); st16(c.r, xo + 1024 + ch * 256, p1);
  if (!(g & 1)) st16(c.r, xo + 2048 + (g >> 1) * 256, p2);
  const f32x4 s4 = (acc[0] + acc[1]) + (acc[2] + acc[3]) + acc[4];
  const float sum = rows4((s4[0] + s4[1]) + (s4[2] + s4[3])), mean = sum * (1.f / 80.f);
  f32x4 q4 = {0.f, 0.f, 0.f, 0.f};
#pragma unroll
  for (int t = 0; t < 5; ++t) { const f32x4 d = acc[t] - mean; q4 += d * d; }
  const float m2 = rows4((q4[0] + q4[1]) + (q4[2] + q4[3]));
  if (g == 0) st8(c.r, st_off + (mt >> 3) * 8192 + cb * 1024 + ((mt & 7) * 16 + i) * 8, u32x2{__float_as_uint(sum), __float_as_uint(m2)});
  STAMP(4);
  publish(c, M_CNT4, 4, flg(arr, panel * 8 + cb), true);
  STAMP(5);
}
template <int NF>
DI void gu_consumer(const Ctx& c, int layer, int f0, int mq) {
  const int panel = gu_panel(c), ft0 = (gu_cb(c) * 27) >> 2;
  const int lane = c.lane, i = lane & 15, g = lane >> 4;
  f32x4 acc[2][2 * NF];
#pragma unroll
  for (int m = 0; m < 2; ++m)
#pragma unroll
    for (int t = 0; t < 2 * NF; ++t) acc[m][t] = f32x4{0.f, 0.f, 0.f, 0.f};
  lds_ptr pa = c.lds + GU_A + mq * 2048 + lane * 16, pb = c.lds + f0 * 2048 + lane * 16;
  bf16x8 x0[2], x1[2], wa[NF], wb0[NF], wb1[NF];
  auto step = [&](const bf16x8* xa, const bf16x8* wb, bf16x8* xn, bf16x8* wn_, bool ld) {
#pragma unroll
    for (int q = 0; q < 4 * NF; ++q) {
      const int t = q >> 1, m = q & 1;
      if (ld) {
        if (q < 2) xn[q] = ldsr16(pa + q * 1024);
        else if (q < 2 * NF) { const int e = NF == 4 ? (q == 3 ? 0 : q == 4 ? 1 : q == 6 ? 2 : q == 7 ? 3 : -1) : NF == 3 ? q - 3 : q - 2; if (e >= 0) wn_[e] = ldsr16(pb + (NF + e) * 1024); }
        else if (q & 1) wa[(q - 2 * NF) >> 1] = ldsr16(pb + ((q - 2 * NF) >> 1) * 1024);
      }
      acc[m][t] = mfma16(t < NF ? wa[t] : wb[t - NF], xa[m], acc[m][t]);
      SBAR();
    }
  };
  int sl = 0;
  auto adv = [&]() {
    pa += sl == GU_NA - 1 ? -(GU_NA - 1) * 8192 : 8192; pb += sl == GU_NB - 1 ? -(GU_NB - 1) * 14336 : 14336;
    sl = sl == GU_NA - 1 ? 0 : sl + 1;
  };
  SLOT(layer * 5 + 3);
  STAMP(0);
  STAMP(1);
  BARRIER();
  STAMP(2); STAMPC(0); WRESET();
  x0[0] = ldsr16(pa); x0[1] = ldsr16(pa + 1024);
#pragma unroll
  for (int t = 0; t < NF; ++t) { wa[t] = ldsr16(pb + t * 1024); wb0[t] = ldsr16(pb + (NF + t) * 1024); }
#pragma unroll 1
  for (int st = 0; st < 9; ++st) {
    adv(); BARRIER_NW(); step(x0, wb0, x1, wb1, true);
    adv(); BARRIER_NW(); step(x1, wb1, x0, wb0, true);
  }
  adv(); BARRIER_NW(); step(x0, wb0, x1, wb1, true);
  BARRIER();
  const LAS float* srs = (const LAS float*)(c.lds + M_SST) + mq * 32 + i;
  const LAS float* scs = (const LAS float*)(c.lds + M_SCS) + f0 * 32 + g * 4;
  float rs[2] = {srs[0], srs[16]}; const float nm[2] = {srs[128], srs[144]};
  f32x4 cs[2 * NF];
#pragma unroll
  for (int e = 0; e < 2 * NF; ++e) cs[e] = *(const LAS f32x4*)(scs + e * 16);
  SBAR();
  step(x1, wb1, x0, wb0, false);
  STAMP(3); STAMPC(1);
  rs[0] *= rs[0]; rs[1] *= rs[1];
  const unsigned ho = WS_L + layer * L_SZ + L_H + ((panel * 8 + mq * 2 + (g & 1)) * 216 + (ft0 + f0) * 2 + (g >> 1)) * 256 + i * 16;
#pragma unroll
  for (int f = 0; f < NF; ++f) {
    unsigned p[2][2];
#pragma unroll
    for (int m = 0; m < 2; ++m) {
      f32x4 gt = acc[m][2 * f] + cs[2 * f] * nm[m];
      const f32x4 up = (acc[m][2 * f + 1] + cs[2 * f + 1] * nm[m]) * rs[m];
      gt = __builtin_elementwise_max(gt, f32x4{0.f, 0.f, 0.f, 0.f}) * up;
      p[m][0] = pk2(gt[0], gt[1]); p[m][1] = pk2(gt[2], gt[3]);
    }
    st16(c.r, ho + f * 512, pair16(p[0][0], p[0][1], p[1][0], p[1][1]));
    SBAR();
  }
  STAMP(4);
  publish(c, M_CNT, 8, flg(FL_GU + layer * 5, panel * 16 + gu_cb(c)), true);
  STAMP(5);
}
DI void mlp_load(const Ctx& c0, int lw, int layer, unsigned iss, bool qkv_next, const uint16_t* wqkv) {
  const int half = lw & 1;
  {
    const Ctx c = fresh(c0);
    if (lw == 0) { poll(c, flg(FL_ATT + layer * 5, (pr_panel(c) >> 2) * 32 + (c.lane & 31))); STAMPL(layer * 5 + 2, 6); }
    BARRIER_L(); BARRIER_L();
  }
  { const int w = lw + 8; (void)w; WDUMP(layer * 5 + 2); }
  {
    const Ctx c = fresh(c0);
    const int ft0 = (gu_cb(c) * 27) >> 2;
    u32x4 pk[20];
    auto park = [&](int e0) {
#pragma unroll
      for (int e = e0; e < e0 + 4; ++e) {
        const int q = half * 5 + e % 5;
        pk[e] = ld16s(c.r, c.lane * 16, WS_WD + (pr_cb(c) * 5 + (q >> 1)) * 55296 + (((e / 5) * 2 + (lw >> 1)) * 2 + (q & 1)) * 1024);
      }
    };
    static_assert(GU_NA == 7 && GU_NB == 7, "schedule");
    if (lw >= 2) {
      Ring<7, GU_NB, 20, 2> rg;
#pragma unroll
      for (int p = 0; p < 7; ++p) rg.voff[p] = WS_WGU + (ft0 * 2 + half * 7 + p) * 20480 + c.lane * 16;
      rg.init(c.ws, 1024, 0, half);
      if (half == 0) ln_cols<4>(c, 56, WS_CSG + ft0 * 128, 13824);
      sig_wait(c, iss - layer * 16 + 4, M_CNT4);
      rg.template fill_ahead<1>(c, layer * 5 + 4);
      rg.template range<5, 5>(); rg.steady();
      wait_vm<28>(); BARRIER_L(); park(0);
      wait_vm<21 + 4>(); BARRIER_L(); park(4);
      wait_vm<14 + 8>(); BARRIER_L(); park(8);
      wait_vm<7 + 12>(); BARRIER_L(); park(12);
      wait_vm<16>(); BARRIER_L(); park(16);
    } else {
      Ring<4, GU_NA, 20, 2> rg;
#pragma unroll
      for (int p = 0; p < 4; ++p) rg.voff[p] = WS_L + layer * L_SZ + L_X1 + (gu_panel(c) * 8 + half * 4 + p) * 20480 + c.lane * 16;
      rg.init(c.ws, 1024, GU_A, half);
      Poll<1> pl;
      gate(c, pl, half, flg(FL_WO + layer * 5, gu_panel(c) * 16 + (c.lane & 15)), layer * 5 + 4, iss + 12);
      rg.template fill<3>();
      rg.template head<3>();
      pl.keep();
      f32x2 st[8];
#pragma unroll
      for (int i = 0; i < 8; ++i)
        st[i] = __builtin_bit_cast(f32x2, ld8(c.r, WS_ST + (layer * 2 + 1) * STB + gu_panel(c) * 8192 + i * 1024 + (half * 64 + c.lane) * 8));
      wait_vm<16 + 8>(); BARRIER_L(); park(0);
      wait_vm<12 + 8 + 4>(); BARRIER_L(); park(4);
      wait_vm<8 + 8 + 8>(); BARRIER_L(); ln_rows(c, half * 64 + c.lane, st);
      wait_vm<8>(); BARRIER_L(); park(8); park(12);
      BARRIER_L(); park(16);
    }
    BARRIER_L();
#pragma unroll
    for (int e = 0; e < 20; ++e) PIN(pk[e]);
    { const int w = lw + 8; (void)w; WDUMP(layer * 5 + 3); }
    auto dump = [&]() {
#pragma unroll
      for (int e = 0; e < 20; ++e) ldsw16(c.lds + ((e / 5) * 2 + (lw >> 1)) * 10240 + (half * 5 + e % 5) * 1024 + c.lane * 16, pk[e]);
    };
    if (lw >= 2) {
      Ring<5, DN_NB, 27, 2> rg;
#pragma unroll
      for (int p = 0; p < 5; ++p) { const int q = half * 5 + p; rg.voff[p] = WS_WD + (pr_cb(c) * 5 + (q >> 1)) * 55296 + (q & 1) * 1024 + c.lane * 16; }
      rg.init(c.ws + 8 * 2048, 2048, 0, half);
      rg.slot = 8;
      sig_wait(c, layer * 16 + 16, M_CNT);
      rg.template fill<3>(); dump(); rg.template fill<4>();
      BARRIER_L();
      wait_vm<5 * 5>(); BARRIER_L();
      wait_vm<0>(); BARRIER_L();
      rg.base += 6 * 2048; rg.slot = 6;
      rg.template fill<3>();
      BARRIER_L();
      rg.template fill<3>();
      wait_vm<3 * 5>(); BARRIER_L();
      wait_vm<0>(); BARRIER_L();
      BARRIER_L();
    } else {
      sig_wait(c, layer * 16 + 16, M_CNT);
      dump();
      if (lw == 0) { poll(c, flg(FL_GU + layer * 5, (pr_panel(c) >> 1) * 16 + (c.lane & 15))); STAMPL(layer * 5 + 4, 6); }
#pragma unroll 1
      for (int k = 0; k < 7; ++k) BARRIER_L();
    }
  }
  { const int w = lw + 8; (void)w; WDUMP(layer * 5 + 4); }
  if (qkv_next) qkv_load1(fresh(c0), lw, wqkv);
}

__global__ __launch_bounds__(768) void fused_kernel(const uint16_t* __restrict__ x, const uint16_t* __restrict__ wqkv, const uint16_t* __restrict__ wo,
                                                    const uint16_t* __restrict__ wg, const uint16_t* __restrict__ wu,
                                                    const uint16_t* __restrict__ wd, uint16_t* __restrict__ out, char* __restrict__ ws) {
  extern __shared__ __attribute__((aligned(16))) char smem[];
  Ctx c;
  c.ws = ws; c.lds = (lds_ptr)smem; c.lane = threadIdx.x & 63; c.xcd = blockIdx.x & 7; c.j = blockIdx.x >> 3;
  c.r = __builtin_amdgcn_make_buffer_rsrc(ws, 0, WS_END, 0x00020000);
  const int w = __builtin_amdgcn_readfirstlane(threadIdx.x >> 6);
  SPANST(0);
  c.tgt = 0;
  if (threadIdx.x < 16) *(LAS unsigned*)(c.lds + M_CNT + threadIdx.x * 4) = 0u;
  const bool qkv_on = qkv_cb(c) != 15;
  Ctx c0 = c;
  WDUMP(15);
  if (w >= 8) __builtin_amdgcn_s_setprio(3);
  if (w >= 8) {
    if (qkv_on) qkv_load0(fresh(c0), w - 8, x, wqkv); else BARRIER();
    c0.tgt = epoch(ws);
#pragma unroll 1
    for (int layer = 0; layer < 2; ++layer) {
      const unsigned iss = layer * 24 + (qkv_on ? (layer + 1) * 4 : 0);
      WDUMP(layer * 5);
      att_load(fresh(c0), opq(w - 8), opq(layer), iss, wo, wg, wu, wd);
      WDUMP(layer * 5 + 1);
      mlp_load(c0, opq(w - 8), opq(layer), iss, layer == 0 && qkv_on, wqkv);
    }
  } else {
    if (!qkv_on) BARRIER();
#define TLB_PF 524288
    if (w == 2 || w == 3) {
      const unsigned o = (w - 2) * 64 + c.lane;
      const unsigned t = o * TLB_PF < WS_END ? __builtin_amdgcn_raw_buffer_load_b32(c.r, o * TLB_PF + 2048, 0, 16) : out[(o & 1) * 1048576];
      asm volatile("" ::"v"(t));
    }
    if (w < 2) {
      const char* pc; constexpr unsigned len = 58 * 1024;
      asm volatile("s_getpc_b64 %0" : "=s"(pc));
      const unsigned o = (c.j * 2 + w) * 1024 + c.lane * 16;
      if (o + 16 <= len) { const u32x4 t = *(const GAS u32x4*)(pc + o); asm volatile("" ::"v"(t)); }
    }
#pragma unroll 1
    for (int layer = 0; layer < 2; ++layer) {
      const unsigned L = WS_L + layer * L_SZ;
      qkv_compute(fresh(c0), c0.tgt, opq(w), opq(layer), x, wqkv);
      WDUMP(layer * 5);
      att_compute(fresh(c0), opq(w), opq(layer));
      WDUMP(layer * 5 + 1);
      proj_compute<10, WO_NB, 20>(fresh(c0), opq(w), layer * 5 + 2, L + L_O, layer == 0 ? (const char*)x : nullptr, WS_X2, L + L_X1,
                                  WS_ST + (layer * 2 + 1) * STB, nullptr, FL_WO + layer * 5);
      WDUMP(layer * 5 + 2);
      if (w < 4) gu_consumer<4>(fresh(c0), opq(layer), 0, opq(w));
      else if (GU_SKIP && (gu_cb(c0) & 3) == 0) gu_consumer<2>(fresh(c0), opq(layer), 4, opq(w - 4));
      else gu_consumer<3>(fresh(c0), opq(layer), 4, opq(w - 4));
      WDUMP(layer * 5 + 3);
      proj_compute<27, DN_NB, 18>(fresh(c0), opq(w), layer * 5 + 4, L + L_H, nullptr, L + L_X1, WS_X2, WS_ST + 2 * STB, layer ? out : nullptr,
                                  FL_DN + layer * 5);
      WDUMP(layer * 5 + 4);
    }
  }
}

extern "C" void kernel_launch(void* const* d_in, const int* in_sizes, int n_in, void* d_out, int out_size, void* d_ws, size_t ws_size,
                              hipStream_t stream) {
  (void)hipFuncSetAttribute((const void*)fused_kernel, hipFuncAttributeMaxDynamicSharedMemorySize, LDS_BYTES);
  fused_kernel<<<256, 768, LDS_BYTES, stream>>>((const uint16_t*)d_in[0], (const uint16_t*)d_in[1], (const uint16_t*)d_in[2],
                                                (const uint16_t*)d_in[4], (const uint16_t*)d_in[5], (const uint16_t*)d_in[6],
                                                (uint16_t*)d_out, (char*)d_ws);
}
```

```cpp
#include <hip/hip_runtime.h>
#include <hip/hip_bf16.h>
#include <stdint.h>

typedef __attribute__((ext_vector_type(8))) __bf16 bf16x8;
typedef __attribute__((ext_vector_type(16))) __bf16 bf16x16;
typedef __attribute__((ext_vector_type(4))) short s16x4;
typedef __attribute__((ext_vector_type(8))) short s16x8;
typedef __attribute__((ext_vector_type(2))) __bf16 bf16x2;
typedef __attribute__((ext_vector_type(2))) float f32x2;
typedef __attribute__((ext_vector_type(4))) float f32x4;
typedef __attribute__((ext_vector_type(8))) float f32x8;
typedef __attribute__((ext_vector_type(16))) float f32x16;
typedef __attribute__((ext_vector_type(2))) unsigned u32x2;
typedef __attribute__((ext_vector_type(4))) unsigned u32x4;
typedef unsigned long long u64;
#define LAS __attribute__((address_space(3)))
#define GAS __attribute__((address_space(1)))
typedef LAS char* lds_ptr;
typedef __amdgpu_buffer_rsrc_t rsrc_t;
#define DI __device__ __forceinline__

constexpr unsigned XB = 2048u * 640 * 2;
constexpr unsigned WS_EPOCH = 0;
constexpr unsigned WS_FLAG = 4096;
constexpr int FL_P0 = 0, FL_RT = 1, FL_QKV = 2, FL_ATT = 3, FL_WO = 4, FL_GU = 5, FL_DN = 6, FL_RT2 = 12;
constexpr unsigned WS_WO  = WS_FLAG + 13u * 32768;
constexpr unsigned WS_WGU = WS_WO + 640u * 640 * 2;
constexpr unsigned WS_WD  = WS_WGU + 3456u * 640 * 2;
constexpr unsigned WS_CSQ = WS_WD + 640u * 1728 * 2;
constexpr unsigned WS_CSG = WS_CSQ + 8u * 1920 * 4;
constexpr unsigned WS_ST  = WS_CSG + 4u * 3456 * 4;
constexpr unsigned STB = 2048u * 64;
constexpr unsigned WS_X2  = WS_ST + 4 * STB;
constexpr unsigned WS_L   = WS_X2 + XB;
constexpr unsigned L_Q1 = 0, L_Q2 = L_Q1 + 32u * 65536, L_K1 = L_Q2 + 16u * 32768, L_K2 = L_K1 + 32u * 65536, L_V = L_K2 + 16u * 32768,
                   L_O = L_V + XB, L_X1 = L_O + XB, L_H = L_X1 + XB, L_SZ = L_H + 2048u * 1728 * 2;
constexpr unsigned WS_END = WS_L + 2 * L_SZ;

constexpr int LDS_BYTES = 163840, MISC = 159744, M_SST = MISC, M_SCS = MISC + 1024, M_CNT = MISC + 2048, M_CNT2 = M_CNT + 8, M_CNT3 = M_CNT + 16, M_CNT4 = M_CNT + 24, M_SIG = M_CNT + 32, M_ISS = M_CNT + 40, M_TGT = M_CNT + 48;
constexpr int QKV_NB = 5, QKV_NA = 4, QKV_A = QKV_NB * 16384;
constexpr int WO_NB = 10, DN_NB = 15;
constexpr int GU_NB = 7, GU_NA = 7, GU_A = GU_NB * 14336;
constexpr int AT_SLOT = 45056, AT_CONST = 3 * AT_SLOT, AT_SCR = AT_CONST + 10752, AT_MERGE = AT_SLOT;
static_assert(QKV_A + QKV_NA * 16384 <= MISC && DN_NB * 10240 <= MISC && GU_A + GU_NA * 8192 <= MISC, "lds");
static_assert(AT_SCR + 3 * 4096 <= MISC, "lds");

DI unsigned pk2(float a, float b) { f32x2 v = {a, b}; bf16x2 r = __builtin_convertvector(v, bf16x2); return __builtin_bit_cast(unsigned, r); }
DI float bflo(unsigned u) { return __uint_as_float(u << 16); }
DI float bfhi(unsigned u) { return __uint_as_float(u & 0xffff0000u); }
DI f32x4 mfma16(bf16x8 a, bf16x8 b, f32x4 c) { return __builtin_amdgcn_mfma_f32_16x16x32_bf16(a, b, c, 0, 0, 0); }
DI f32x16 mfma32(bf16x8 a, bf16x8 b, f32x16 c) { return __builtin_amdgcn_mfma_f32_32x32x16_bf16(a, b, c, 0, 0, 0); }
DI bf16x8 ldsr16(lds_ptr p) { return *(const LAS bf16x8*)p; }
DI bf16x8 ldstr(lds_ptr p, int second) {
  const s16x4 a = __builtin_amdgcn_ds_read_tr16_b64_v4i16((LAS s16x4*)p), b = __builtin_amdgcn_ds_read_tr16_b64_v4i16((LAS s16x4*)(p + second));
  return __builtin_bit_cast(bf16x8, (s16x8)__builtin_shufflevector(a, b, 0, 1, 2, 3, 4, 5, 6, 7));
}
#define BARRIER() asm volatile("s_waitcnt lgkmcnt(0)\n\ts_barrier" ::: "memory")
template <int N> DI void wait_vm() { asm volatile("s_waitcnt vmcnt(%0)" ::"n"(N > 63 ? 63 : N) : "memory"); }
#define WDUMP(id)
#define WRESET()
#define BARRIER_L() BARRIER()
#define BARRIER_NW() asm volatile("s_barrier" ::: "memory")
#define SBAR() __builtin_amdgcn_sched_barrier(0)
#define PIN(x) asm volatile("" : "+v"(x))
#define GL_HEAD "s_mov_b32 m0, %0\n\ts_nop 3\n\t"
#define GL(v, off) "global_load_lds_dwordx4 %" #v ", %1 offset:" #off " sc1\n\t"
DI void glds1(unsigned m, const char* sb, unsigned v0) { asm volatile(GL_HEAD GL(2, 0)::"s"(m), "s"(sb), "v"(v0) : "memory"); }
DI void glds2(unsigned m, const char* sb, unsigned v0, unsigned v1) {
  asm volatile(GL_HEAD GL(2, 0) GL(3, 1024)::"s"(m), "s"(sb), "v"(v0), "v"(v1) : "memory");
}
DI void glds3(unsigned m, const char* sb, unsigned v0, unsigned v1, unsigned v2) {
  asm volatile(GL_HEAD GL(2, 0) GL(3, 1024) GL(4, 2048)::"s"(m), "s"(sb), "v"(v0), "v"(v1), "v"(v2) : "memory");
}
DI void glds4(unsigned m, const char* sb, unsigned v0, unsigned v1, unsigned v2, unsigned v3) {
  asm volatile(GL_HEAD GL(2, 0) GL(3, 1024) GL(4, 2048) GL(5, 3072)::"s"(m), "s"(sb), "v"(v0), "v"(v1), "v"(v2), "v"(v3) : "memory");
}
DI void st16(rsrc_t r, unsigned off, u32x4 v) { __builtin_amdgcn_raw_buffer_store_b128(v, r, off, 0, 16); }
DI void st8(rsrc_t r, unsigned off, u32x2 v) { __builtin_amdgcn_raw_buffer_store_b64(v, r, off, 0, 16); }
DI void st4(rsrc_t r, unsigned off, unsigned v) { __builtin_amdgcn_raw_buffer_store_b32(v, r, off, 0, 16); }
DI u32x4 ld16(rsrc_t r, unsigned off) { return __builtin_amdgcn_raw_buffer_load_b128(r, off, 0, 16); }
DI u32x2 ld8(rsrc_t r, unsigned off) { return __builtin_amdgcn_raw_buffer_load_b64(r, off, 0, 16); }
DI u32x4 ld16s(rsrc_t r, unsigned voff, unsigned soff) { return __builtin_amdgcn_raw_buffer_load_b128(r, voff, soff, 16); }
DI void ldsw16(lds_ptr p, u32x4 v) { *(LAS u32x4*)p = v; }
DI f32x2 ld8_async(unsigned voff, const char* sbase) {
  f32x2 v; asm volatile("s_nop 4\n\tglobal_load_dwordx2 %0, %1, %2 sc1" : "=v"(v) : "v"(voff), "s"(sbase) : "memory"); return v;
}
DI u32x4 pair32(unsigned a0, unsigned a1, unsigned b0, unsigned b1) {
  auto x = __builtin_amdgcn_permlane32_swap(a0, b0, false, false);
  auto y = __builtin_amdgcn_permlane32_swap(a1, b1, false, false);
  return u32x4{x[0], y[0], x[1], y[1]};
}
template <int N> DI float ror16(float v) { return __int_as_float(__builtin_amdgcn_update_dpp(0, __float_as_int(v), 0x120 + N, 0xf, 0xf, false)); }
DI float sum32(float v) {
  v += ror16<8>(v); v += ror16<4>(v); v += ror16<2>(v); v += ror16<1>(v);
  auto a = __builtin_amdgcn_permlane16_swap(__float_as_uint(v), __float_as_uint(v), false, false);
  return __uint_as_float(a[0]) + __uint_as_float(a[1]);
}
DI u32x4 pair16(unsigned a0, unsigned a1, unsigned b0, unsigned b1) {
  auto x = __builtin_amdgcn_permlane16_swap(a0, b0, false, false);
  auto y = __builtin_amdgcn_permlane16_swap(a1, b1, false, false);
  return u32x4{x[0], y[0], x[1], y[1]};
}

DI float rows4(float v) {
  auto a = __builtin_amdgcn_permlane16_swap(__float_as_uint(v), __float_as_uint(v), false, false);
  v = __uint_as_float(a[0]) + __uint_as_float(a[1]);
  auto b = __builtin_amdgcn_permlane32_swap(__float_as_uint(v), __float_as_uint(v), false, false);
  return __uint_as_float(b[0]) + __uint_as_float(b[1]);
}

#define STAMP(id)
#define STAMPC(id)
#define STAMPL(sl, id)
#define SLOT(n)

#define POLL_DEPTH 1
#define POLL_GATE 1
#define POLL_SLEEP 1
#define ATT_PRIO 3
#define ATT_ORD 0
#define WO_AH 6
#define DN_AH 8
#define ATT_ROT 1
#define GU_SKIP 1
#define ATT_LATE 4
#define GU16 1
#define QKV16 1
#define LD_FIRST 1
#define LAG0 2
#define PACE 16
#define SPANST(id)
struct Ctx {
  char* ws; rsrc_t r; lds_ptr lds; u64 tgt; int lane, xcd, j;
};
DI Ctx fresh(const Ctx& c0) {
  Ctx c = c0;
  asm volatile("v_mbcnt_lo_u32_b32 %0, -1, 0\n\tv_mbcnt_hi_u32_b32 %0, -1, %0" : "=v"(c.lane), "+s"(c.xcd), "+s"(c.j), "+s"(c.ws));
  return c;
}
DI int opq(int v) { asm volatile("" : "+s"(v)); return v; }
constexpr int SPIN_CAP = 1 << 18;

DI u64 epoch(const char* ws) {
  u64 e; asm volatile("s_nop 4\n\ts_load_dwordx2 %0, %1, 0x0\n\ts_waitcnt lgkmcnt(0)" : "=s"(e) : "s"(ws + WS_EPOCH) : "memory"); return e + 1;
}
DI unsigned flg(int arr, int unit) { return WS_FLAG + (arr * 256 + unit) * 128; }
DI void publish(const Ctx& c, int cnt_off, unsigned n, unsigned to, bool mark = false) {
  unsigned one = 1, iss = M_ISS, cnt = cnt_off, old, so;
  u64 tgt = c.tgt, ex;
  asm volatile("" : "+v"(one), "+v"(iss), "+v"(cnt), "+v"(to), "+v"(tgt));
  if (mark) asm volatile("s_mov_b64 %0, exec\n\ts_mov_b64 exec, 1\n\tds_add_u32 %1, %2\n\ts_mov_b64 exec, %0" : "=&s"(ex) : "v"(iss), "v"(one) : "memory");
  asm volatile("s_mov_b64 %2, exec\n\ts_waitcnt vmcnt(0)\n\ts_mov_b64 exec, 1\n\tds_add_rtn_u32 %0, %3, %4\n\ts_waitcnt lgkmcnt(0)\n\t"
               "v_readfirstlane_b32 %1, %0\n\ts_mov_b64 exec, %2" : "=&v"(old), "=&s"(so), "=&s"(ex) : "v"(cnt), "v"(one) : "memory");
  if (so % n == n - 1)
    asm volatile("s_mov_b64 %0, exec\n\ts_mov_b64 exec, 1\n\ts_nop 2\n\tglobal_store_dwordx2 %1, %2, %3 sc1\n\ts_mov_b64 exec, %0"
                 : "=&s"(ex) : "v"(to), "v"(tgt), "s"(c.ws) : "memory");
}
template <int N> struct Poll {
  u64 v[POLL_DEPTH][N];
  DI void run(const Ctx& c, unsigned o0, unsigned o1 = 0) {

    auto ld = [&](int r) {
      asm volatile("global_load_dwordx2 %0, %1, %2 sc1" : "=v"(v[r][0]) : "v"(o0), "s"(c.ws) : "memory");
      if constexpr (N == 2) asm volatile("global_load_dwordx2 %0, %1, %2 sc1" : "=v"(v[r][N - 1]) : "v"(o1), "s"(c.ws) : "memory");
      __builtin_amdgcn_s_sleep(POLL_SLEEP);
    };
    auto ok = [&](int r) {
      if constexpr (N == 2) asm volatile("s_waitcnt vmcnt(%2)" : "+v"(v[r][0]), "+v"(v[r][N - 1]) : "n"((POLL_DEPTH - 1) * N) : "memory");
      else asm volatile("s_waitcnt vmcnt(%1)" : "+v"(v[r][0]) : "n"(POLL_DEPTH - 1) : "memory");
      return __all(v[r][0] == c.tgt && v[r][N - 1] == c.tgt);
    };
    asm volatile("s_nop 4" ::: "memory");
    ld(0); if (POLL_DEPTH > 1) ld(1);
#pragma unroll 1
    for (int spin = 0; spin < SPIN_CAP; ++spin) {
      if (ok(0)) break; ld(0);
      if (POLL_DEPTH > 1) { if (ok(1)) break; ld(1); }
    }
  }
  DI void keep() {
#pragma unroll
    for (int r = 0; r < POLL_DEPTH; ++r) { asm volatile("" : "+v"(v[r][0])); if constexpr (N == 2) asm volatile("" : "+v"(v[r][N - 1])); }
  }
};
DI void poll(const Ctx& c, unsigned slot) { Poll<1> p; p.run(c, slot); wait_vm<0>(); p.keep(); }
DI void sig_set(const Ctx& c, unsigned v) { if (c.lane == 0) __hip_atomic_store((LAS unsigned*)(c.lds + M_SIG), v, __ATOMIC_RELAXED, __HIP_MEMORY_SCOPE_WORKGROUP); }
DI void sig_wait(const Ctx& c, unsigned v, int off = M_SIG, bool calm = false) {
#pragma unroll 1
  for (int spin = 0; spin < SPIN_CAP * 16; ++spin) {
    if (__builtin_amdgcn_readfirstlane(__hip_atomic_load((LAS unsigned*)(c.lds + off), __ATOMIC_RELAXED, __HIP_MEMORY_SCOPE_WORKGROUP)) >= v) break;
    if (calm) __builtin_amdgcn_s_sleep(8); else if (off != M_SIG) __builtin_amdgcn_s_sleep(1);
  }
  asm volatile("" ::: "memory");
}

DI void gate(const Ctx& c, Poll<1>& p, int half, unsigned slot, unsigned sig, unsigned iss) {
  if (half == 0) { if (POLL_GATE) sig_wait(c, iss, M_ISS); p.run(c, slot); sig_set(c, sig); STAMPL(sig - 1, 6); } else sig_wait(c, sig);
}

template <int PP, int NSLOT, int NS, int LAG = 1>
struct Ring {
  unsigned voff[PP];
  const char* base;
  unsigned adv, l0;
  int slot;
  DI void init(const char* b, unsigned a, unsigned lds0, int half) {
    base = b - 4096; adv = a; l0 = lds0 + half * PP * 1024; slot = 0;
#pragma unroll
    for (int p = 0; p < PP; ++p) voff[p] += 4096 - (p & 3) * 1024;
  }
  DI void issue() {
    const unsigned m = l0 + slot * (2 * PP * 1024);
    glds4(m, base, voff[0], voff[1], voff[2], voff[3]);
    if constexpr (PP == 5) glds1(m + 4096, base, voff[4]);
    if constexpr (PP == 7) glds3(m + 4096, base, voff[4], voff[5], voff[6]);
    if constexpr (PP == 8) glds4(m + 4096, base, voff[4], voff[5], voff[6], voff[7]);
    base += adv; slot = slot == NSLOT - 1 ? 0 : slot + 1;
  }
  template <int N = NSLOT> DI void fill() {
#pragma unroll 1
    for (int s = 0; s < N; ++s) issue();
  }
  template <int NEED> DI void fill_ahead(const Ctx& c, unsigned sig) {
    int n = 0;
#pragma unroll 1
    while (n < NSLOT) {
      issue(); wait_vm<PACE>(); ++n;
      if (n * PP >= NEED * PP + PACE &&
          __builtin_amdgcn_readfirstlane(__hip_atomic_load((LAS unsigned*)(c.lds + M_SIG), __ATOMIC_RELAXED, __HIP_MEMORY_SCOPE_WORKGROUP)) >= sig) break;
    }
    if (n == NSLOT) wait_vm<(NSLOT - NEED) * PP>();
    BARRIER_L(); WRESET();
#pragma unroll 1
    for (; n < NSLOT; ++n) issue();
  }
  DI void rest() { range<NSLOT - 2, NSLOT - LAG>(); steady(); range<NSLOT - 1 - LAG, 0>(); BARRIER_L(); }
  template <int R, int E> DI void range() { if constexpr (R >= E) { wait_vm<R * PP>(); BARRIER_L(); range<R - 1, E>(); } }
  template <int D0 = NSLOT> DI void head() {
    wait_vm<(D0 - 1) * PP>(); BARRIER_L(); WRESET();
    if constexpr (D0 < NSLOT) fill<NSLOT - D0>();
    range<NSLOT - 2, NSLOT - LAG>();
    steady();
  }
  DI void steady() {
#pragma unroll 1
    for (int k = LAG; k < NS - NSLOT + LAG; ++k) { wait_vm<(NSLOT - 1 - LAG) * PP>(); BARRIER_L(); issue(); }
  }
  template <int D0 = NSLOT> DI void run() { head<D0>(); range<NSLOT - 1 - LAG, 0>(); BARRIER_L(); }
};

DI void ln_load(const Ctx& c, unsigned off, int row, f32x2 (&p)[8]) {
#pragma unroll
  for (int i = 0; i < 8; ++i) p[i] = __builtin_bit_cast(f32x2, __builtin_amdgcn_raw_buffer_load_b64(c.r, row * 8, off + i * 1024, 16));
}
DI f32x2 ln_sum(const f32x2 (&p)[8]) {
  f32x2 t = ((p[0] + p[1]) + (p[2] + p[3])) + ((p[4] + p[5]) + (p[6] + p[7]));
  const float m8 = t[0] * 0.125f; float q0 = 0.f, q1 = 0.f;
#pragma unroll
  for (int i = 0; i < 8; i += 2) { const float a = p[i][0] - m8, b = p[i + 1][0] - m8; q0 += a * a; q1 += b * b; }
  t[1] += (q0 + q1) * (1.f / 80.f);
  PIN(t);
  return t;
}
DI void ln_put(const Ctx& c, int row, f32x2 t) {
  *(LAS float*)(c.lds + M_SST + row * 4) = __builtin_amdgcn_rsqf(t[1] * (1.f / 640.f) + 1e-5f);
  *(LAS float*)(c.lds + M_SST + 512 + row * 4) = -t[0] * (1.f / 640.f);
}
template <int NP> DI void ln_cols_sum(const Ctx& c, int n4, const u32x4 (&q)[NP]) {
  f32x4 cs = __builtin_bit_cast(f32x4, q[0]);
#pragma unroll
  for (int i = 1; i < NP; ++i) cs += __builtin_bit_cast(f32x4, q[i]);
  if (c.lane < n4) *(LAS f32x4*)(c.lds + M_SCS + c.lane * 16) = cs;
}
template <int NP> DI void ln_cols(const Ctx& c, int n4, unsigned off, unsigned pitch) {
  u32x4 q[NP];
#pragma unroll
  for (int i = 0; i < NP; ++i) q[i] = ld16(c.r, off + i * pitch + (c.lane < n4 ? c.lane : 0) * 16);
  ln_cols_sum<NP>(c, n4, q);
}

DI int qkv_panel(const Ctx& c) { return (c.xcd & 3) * 4 + (c.j & 3); }
DI int qkv_cb(const Ctx& c) { return (c.xcd >> 2) * 8 + (c.j >> 2); }
DI int pr_panel(const Ctx& c) { return (c.xcd & 3) * 8 + (c.j & 7); }
DI int pr_cb(const Ctx& c) { return (c.xcd >> 2) * 4 + (c.j >> 3); }
DI int gu_panel(const Ctx& c) { return (c.xcd & 1) * 8 + (c.j & 7); }
DI int gu_cb(const Ctx& c) { return (c.xcd >> 1) * 4 + (c.j >> 3); }
DI int att_head(const Ctx& c) { return c.xcd * 4 + (c.j & 3); }
DI int att_qb(const Ctx& c) { return c.j >> 2; }

constexpr int P0_SCR = QKV_A + QKV_NA * 16384;
static_assert(P0_SCR + 10240 <= MISC, "lds");
struct Phase0 {
  u32x4 v[3]; float sum, m2;
  DI void load(const Ctx& c, int hw, const uint16_t* x, const uint16_t* wqkv) {
    const int lane = c.lane, r32 = lane & 31, b = blockIdx.x, row = b * 8 + hw * 2 + (lane >> 5);
    v[0] = *(const u32x4*)(x + row * 640 + r32 * 8); v[1] = *(const u32x4*)(x + row * 640 + 256 + r32 * 8);
    v[2] = *(const u32x4*)(x + row * 640 + 512 + (r32 & 15) * 8);
    if (b < 240) {
      const char* src = (const char*)wqkv + ((b / 30) * 80 + hw * 8) * 3840 + (b % 30) * 128;
      const unsigned vo = (lane >> 3) * 3840 + (lane & 7) * 16;
      glds1(P0_SCR + hw * 1024, src, vo); glds1(P0_SCR + 4096 + hw * 1024, src + 32 * 3840, vo);
      if (hw < 2) glds1(P0_SCR + 8192 + hw * 1024, src + 64 * 3840, vo);
    }
  }
  DI float dev2(int q) const {
    const float mean = sum * (1.f / 640.f); float a = 0.f, b = 0.f;
#pragma unroll
    for (int e = 0; e < 4; ++e) { const float d0 = bflo(v[q][e]) - mean, d1 = bfhi(v[q][e]) - mean; a += d0 * d0; b += d1 * d1; }
    return a + b;
  }
  DI void part1(const Ctx& c) {
    unsigned uo = 0x3f803f80u; PIN(uo);
    const bf16x2 ones = __builtin_bit_cast(bf16x2, uo);
    float s[3];
#pragma unroll
    for (int q = 0; q < 3; ++q) {
      s[q] = 0.f;
#pragma unroll
      for (int e = 0; e < 4; ++e) { const unsigned u = v[q][e]; s[q] = __builtin_amdgcn_fdot2_f32_bf16(__builtin_bit_cast(bf16x2, u), ones, s[q], false); }
    }
    sum = sum32(s[0] + s[1] + ((c.lane & 16) ? 0.f : s[2]));
    m2 = dev2(0);
    asm volatile("" : "+v"(sum), "+v"(m2));
    wait_vm<0>();
  }
  DI void part2(const Ctx& c, int hw) {
    const int lane = c.lane, r32 = lane & 31, b = blockIdx.x, row = b * 8 + hw * 2 + (lane >> 5);
    const float t = dev2(2);
    m2 = sum32(m2 + dev2(1) + ((lane & 16) ? 0.f : t));
    if (r32 < 8) st8(c.r, WS_ST + (row >> 7) * 8192 + r32 * 1024 + (row & 127) * 8, u32x2{__float_as_uint(sum * 0.125f), __float_as_uint(m2 * 0.125f)});
  }
  DI void cols(const Ctx& c, int hw) {
    const int i = c.lane & 15, g = c.lane >> 4, b = blockIdx.x;
    if (b < 240) {
      const unsigned o = 0x3f803f80u, z = g < 2 ? o : 0u;
      lds_ptr p = c.lds + P0_SCR + (g * 8 + (i >> 2)) * 128 + hw * 32 + (i & 3) * 8;
      f32x4 cs = mfma16(ldstr(p, 512), __builtin_bit_cast(bf16x8, u32x4{o, o, o, o}), f32x4{});
      cs = mfma16(ldstr(p + 4096, 512), __builtin_bit_cast(bf16x8, u32x4{o, o, o, o}), cs);
      cs = mfma16(ldstr(p + (g < 2 ? 8192 : 0), 512), __builtin_bit_cast(bf16x8, u32x4{z, z, z, z}), cs);
      if (i == 0) st16(c.r, WS_CSQ + ((b / 30) * 1920 + (b % 30) * 64 + hw * 16 + g * 4) * 4, __builtin_bit_cast(u32x4, cs));
    }
  }
};

template <bool SWAP, class PRE, class MID>
DI void qkv_loop(const Ctx& c, int w, int layer, f32x4 (&acc)[4][4], PRE pre, MID mid) {
  const int lane = c.lane, i = lane & 15, g = lane >> 4, wm = w & 1, wn = w >> 1;
  const int rm = (i >> 3) * 1024 + (i & 7) * 128 + ((g ^ ((i >> 1) & 7)) << 4);
  lds_ptr pa0 = c.lds + QKV_A + wm * 8192 + (layer == 0 ? rm : lane * 16);
  lds_ptr pa1 = c.lds + QKV_A + wm * 8192 + (layer == 0 ? rm ^ 64 : lane * 16 + 1024);
  const int f = (i >> 2) | ((g & 1) << 2), rb = (g * 8 + (i >> 2)) * 256 + (i & 3) * 8;
  lds_ptr pb[4];
#pragma unroll
  for (int t = 0; t < 4; ++t) pb[t] = c.lds + rb + (((wn * 4 + t) ^ f) << 5);
  bf16x8 x0[4], x1[4], w0[4], w1[4];
  auto step = [&](const bf16x8* xa, const bf16x8* wb, bf16x8* xn, bf16x8* wn_, lds_ptr pa, int bo, bool ld) {
#pragma unroll
    for (int q = 0; q < 16; ++q) {
      if (ld) {
        if ((q & 3) == 0) wn_[q >> 2] = ldstr(pb[q >> 2] + bo, 1024);
        if ((q & 3) == 2) xn[q >> 2] = ldsr16(pa + (q >> 2) * 2048);
      }
      const int t = q >> 2, m = q & 3;
      acc[m][t] = SWAP ? mfma16(wb[t], xa[m], acc[m][t]) : mfma16(xa[m], wb[t], acc[m][t]);
      SBAR();
    }
  };
  SLOT(layer * 5);
  if (layer) STAMP(0);
  mid(0); mid(4); mid(8); mid(12);
  STAMP(1);
  BARRIER();
  STAMP(2); STAMPC(0); WRESET();
#pragma unroll
  for (int e = 0; e < 4; ++e) { w0[e] = ldstr(pb[e], 1024); x0[e] = ldsr16(pa0 + e * 2048); }
  int sa = 0, sb = 0;
#pragma unroll 1
  for (int st = 0; st < 9; ++st) {
    step(x0, w0, x1, w1, pa1, 8192, true);
    const int da = sa == QKV_NA - 1 ? -(QKV_NA - 1) * 16384 : 16384, db = sb == QKV_NB - 1 ? -(QKV_NB - 1) * 16384 : 16384;
    sa = sa == QKV_NA - 1 ? 0 : sa + 1; sb = sb == QKV_NB - 1 ? 0 : sb + 1;
    pa0 += da; pa1 += da; pb[0] += db; pb[1] += db; pb[2] += db; pb[3] += db;
    if (LAG0 == 1 && layer == 0) asm volatile("s_waitcnt lgkmcnt(0)" ::: "memory");
    BARRIER_NW();
    step(x1, w1, x0, w0, pa0, 0, true);
  }
  step(x0, w0, x1, w1, pa1, 8192, true);
  BARRIER();
  pre(); SBAR();
  step(x1, w1, x0, w0, pa0, 0, false);
  STAMP(3); STAMPC(1);
}
DI unsigned qkv_w_off(const Ctx& c, int q) {
  const int row = q * 4 + (c.lane >> 4);
  return row * 3840 + qkv_cb(c) * 256 + (((c.lane & 15) ^ (QKV16 ? ((row & 3) << 1) | (row & 8) : (row & 3) << 2)) << 4);
}
DI void qkv_helper(Ctx c, u64& tgt, int hw, int layer, const uint16_t* x, const uint16_t* wqkv) {
  const int panel = qkv_panel(c), cb = qkv_cb(c), l = c.lane;
  const bool on = cb != 15;
  if (layer == 0) {
    Phase0 p0;
    p0.load(c, hw, x, wqkv);
    if (on) BARRIER_L();
    p0.part1(c);
    BARRIER_L();
    p0.part2(c, hw);
    if (on) BARRIER_L();
    p0.cols(c, hw);
    c.tgt = tgt = epoch(c.ws);
    if (hw == 0 && l == 0) *(LAS u64*)(c.lds + M_TGT) = tgt;
    if (on) BARRIER_L();
    publish(c, M_CNT2, 4, flg(FL_P0, blockIdx.x));
    if (!on) return;
    BARRIER_L(); BARRIER_L(); BARRIER_L();
    const unsigned at = flg(FL_P0, hw < 2 ? panel * 16 + hw * 8 + (l & 7) : (l & 7) * 30 + cb * 2 + ((l >> 3) & 1));
    const u64 v = __hip_atomic_load((const GAS u64*)(c.ws + at), __ATOMIC_RELAXED, __HIP_MEMORY_SCOPE_AGENT);
    BARRIER_L();
    if (!__all(v == c.tgt)) poll(c, at);
    if (hw < 2) {
      f32x2 p[8];
      ln_load(c, WS_ST + panel * 8192, hw * 64 + l, p);
      BARRIER_L(); BARRIER_L();
      ln_put(c, hw * 64 + l, ln_sum(p));
    } else if (hw == 2) {
      u32x4 q[8];
#pragma unroll
      for (int i = 0; i < 8; ++i) q[i] = ld16(c.r, WS_CSQ + cb * 512 + i * 7680 + (l & 31) * 16);
      BARRIER_L(); BARRIER_L();
      ln_cols_sum<8>(c, 32, q);
    } else { BARRIER_L(); BARRIER_L(); }
    BARRIER_L();
  } else if (on) {
    if (hw == 2) ln_cols<8>(c, 32, WS_CSQ + cb * 512, 7680);
    BARRIER_L(); BARRIER_L(); BARRIER_L(); BARRIER_L(); BARRIER_L(); BARRIER_L();
    if (hw < 2) {
      f32x2 p[8];
      ln_load(c, WS_ST + 2 * STB + panel * 8192, hw * 64 + l, p);
      BARRIER_L(); BARRIER_L();
      ln_put(c, hw * 64 + l, ln_sum(p));
    } else { BARRIER_L(); BARRIER_L(); }
    BARRIER_L(); BARRIER_L(); BARRIER_L();
  }
}
DI void qkv_compute(Ctx c, u64& tgt, int w, int layer, const uint16_t* x, const uint16_t* wqkv) {
  const int panel = qkv_panel(c), cb = qkv_cb(c);
  if (w >= 4) { qkv_helper(c, tgt, w - 4, layer, x, wqkv); return; }
  if (cb == 15) { if (layer == 0) { tgt = epoch(c.ws); wait_vm<0>(); BARRIER(); } return; }
  const int lane = c.lane, i = lane & 15, g = lane >> 4, wm = w & 1, wn = w >> 1;
  const unsigned L = WS_L + layer * L_SZ;
  f32x4 acc[4][4];
#pragma unroll
  for (int e = 0; e < 16; ++e) acc[e >> 2][e & 3] = f32x4{0.f, 0.f, 0.f, 0.f};
  const LAS float* srs = (const LAS float*)(c.lds + M_SST) + wm * 64;
  const LAS float* scs = (const LAS float*)(c.lds + M_SCS) + wn * 64;
  if (cb < 10) {
    float rs[4], nm[4]; f32x4 cs[4]; unsigned ad[4][2];
    const float qs = cb < 5 ? 0.22360679774997896f * 1.4426950408889634f : 1.f;
    const unsigned i1 = L + (cb < 5 ? L_Q1 : L_K1), i2 = L + (cb < 5 ? L_Q2 : L_K2);
    qkv_loop<true>(c, w, layer, acc, [&]() {
#pragma unroll
      for (int e = 0; e < 4; ++e) { rs[e] = srs[e * 16 + i]; nm[e] = srs[128 + e * 16 + i]; cs[e] = *(const LAS f32x4*)(scs + e * 16 + g * 4); }
    }, [&](int q) {
      const int t = q >> 2, o = (cb % 5) * 16 + wn * 8 + t * 2 + (g >> 1), hp = o / 5, pos = o - hp * 5;
      const unsigned ts = pos == 2 ? 512 : 1024;
      ad[t][0] = (pos == 2 ? i2 + hp * 32768 : i1 + (2 * hp + (pos > 2)) * 65536 + (pos > 2 ? pos - 3 : pos) * 512) +
                 (panel * 4 + wm * 2) * ts + ((g & 1) * 16 + i) * 16;
      ad[t][1] = ad[t][0] + ts;
      asm volatile("" : "+v"(ad[t][0]), "+v"(ad[t][1]));
    });
#pragma unroll
    for (int e = 0; e < 4; ++e) rs[e] *= qs;
#pragma unroll
    for (int t = 0; t < 4; ++t) {
#pragma unroll
      for (int mp = 0; mp < 2; ++mp) {
        const f32x4 u = (acc[mp * 2][t] + cs[t] * nm[mp * 2]) * rs[mp * 2], v = (acc[mp * 2 + 1][t] + cs[t] * nm[mp * 2 + 1]) * rs[mp * 2 + 1];
        st16(c.r, ad[t][mp], pair16(pk2(u[0], u[1]), pk2(u[2], u[3]), pk2(v[0], v[1]), pk2(v[2], v[3])));
      }
    }
  } else {
    float cs[4]; f32x4 rs[4], nm[4]; unsigned ad[4];
    qkv_loop<false>(c, w, layer, acc, [&]() {
#pragma unroll
      for (int e = 0; e < 4; ++e) { cs[e] = scs[e * 16 + i]; rs[e] = *(const LAS f32x4*)(srs + e * 16 + g * 4); nm[e] = *(const LAS f32x4*)(srs + 128 + e * 16 + g * 4); }
    }, [&](int q) {
      const int t = q >> 2, cv = (cb - 10) * 128 + wn * 64 + t * 16 + i, head = cv / 20, dim = cv - head * 20;
      const int d = (head & 1) ? (dim < 4 ? dim + 16 : dim - 4) : dim;
      ad[t] = L + L_V + head * 81920 + (panel * 8 + wm * 4 + (g & 1)) * 640 + (g >> 1) * 320 + d * 16;
      PIN(ad[t]);
    });
#pragma unroll
    for (int t = 0; t < 4; ++t) {
      const unsigned a0 = ad[t];
#pragma unroll
      for (int mp = 0; mp < 2; ++mp) {
        const f32x4 u = (acc[mp * 2][t] + nm[mp * 2] * cs[t]) * rs[mp * 2], v = (acc[mp * 2 + 1][t] + nm[mp * 2 + 1] * cs[t]) * rs[mp * 2 + 1];
        st16(c.r, a0 + mp * 1280, pair16(pk2(u[0], u[1]), pk2(u[2], u[3]), pk2(v[0], v[1]), pk2(v[2], v[3])));
      }
    }
  }
  if (layer == 0) {
    const u32x2 t = *(const LAS u32x2*)(c.lds + M_TGT);
    c.tgt = tgt = (u64)(unsigned)__builtin_amdgcn_readfirstlane(t[1]) << 32 | (unsigned)__builtin_amdgcn_readfirstlane(t[0]);
  }
  SLOT(layer * 5);
  STAMP(4);
  publish(c, M_CNT4, 4, flg(FL_QKV + layer * 5, panel * 16 + cb), true);
  STAMP(5);
}
typedef Ring<8, QKV_NB, 10, 2> QkvB;
DI void qkv_load0(const Ctx& c, int lw, const uint16_t* x, const uint16_t* wqkv) {
  const int half = lw & 1;
  if (lw >= 2) {
    Ring<8, QKV_NB, 10, LAG0> rg;
#pragma unroll
    for (int p = 0; p < 8; ++p) rg.voff[p] = qkv_w_off(c, half * 8 + p);
    rg.init((const char*)wqkv, 64 * 3840, 0, half);
    rg.template fill<3>(); rg.template run<3>();
  } else {
    Ring<8, QKV_NA, 10, LAG0> rg;
#pragma unroll
    for (int p = 0; p < 8; ++p) {
      const int mt = qkv_panel(c) * 8 + half * 4 + (p >> 1), r16 = (p & 1) * 8 + (c.lane >> 3);
      rg.voff[p] = (mt * 16 + r16) * 1280 + (((c.lane & 7) ^ ((r16 >> 1) & 7)) << 4);
    }
    rg.init((const char*)x, 128, QKV_A, half);
    rg.template fill<3>(); rg.template run<3>();
  }
}
DI void qkv_load1(const Ctx& c, int lw, const uint16_t* wqkv) {
  const int half = lw & 1;
  static_assert(QKV_NA == 4 && QKV_NB == 5, "schedule");
  if (lw >= 2) {
    QkvB rg;
#pragma unroll
    for (int p = 0; p < 8; ++p) rg.voff[p] = qkv_w_off(c, half * 8 + p);
    rg.init((const char*)wqkv, 64 * 3840, 0, half);
    sig_wait(c, 12, M_CNT4);
    rg.template fill_ahead<1>(c, 6);
    rg.rest();
  } else {
    Ring<8, QKV_NA, 10, 2> rg;
#pragma unroll
    for (int p = 0; p < 8; ++p) rg.voff[p] = WS_X2 + (qkv_panel(c) * 8 + half * 4 + (p >> 1)) * 20480 + (p & 1) * 1024 + c.lane * 16;
    rg.init(c.ws, 2048, QKV_A, half);
    Poll<1> pl;
    gate(c, pl, half, flg(FL_DN, qkv_panel(c) * 16 + (c.lane & 15)), 6, 28);
    rg.template fill<2>(); rg.template run<2>();
    pl.keep();
  }
}

DI f32x16 qk8(s16x4 k2, s16x4 q2) {
 return __builtin_amdgcn_mfma_f32_32x32x8bf16_1k(k2, q2, f32x16{}, 0, 0, 0); }
DI f32x16 qk16(bf16x8 k1, bf16x8 q1, f32x16 s) { return __builtin_amdgcn_mfma_f32_32x32x16_bf16(k1, q1, s, 0, 0, 0); }
DI f32x16 pv_lo(bf16x8 v, f32x16 p, f32x16 o) {
  return __builtin_amdgcn_smfmac_f32_32x32x32_bf16(v, __builtin_bit_cast(bf16x16, (f32x8)__builtin_shufflevector(p, p, 0, 1, 2, 3, 4, 5, 6, 7)), o, 0xDDDD, 0, 0);
}
DI f32x16 pv_hi(bf16x8 v, f32x16 p, f32x16 o) {
  return __builtin_amdgcn_smfmac_f32_32x32x32_bf16(v, __builtin_bit_cast(bf16x16, (f32x8)__builtin_shufflevector(p, p, 8, 9, 10, 11, 12, 13, 14, 15)), o, 0xDDDD, 0, 0);
}
DI float swap32(float v, int h) {
  auto r = __builtin_amdgcn_permlane32_swap(__float_as_uint(v), __float_as_uint(v), false, false);
  return __uint_as_float(h ? r[0] : r[1]);
}
#define EXP4(X, i) do { X[i] = __builtin_amdgcn_exp2f(X[i]); X[i + 1] = __builtin_amdgcn_exp2f(X[i + 1]); X[i + 2] = __builtin_amdgcn_exp2f(X[i + 2]); \
                        X[i + 3] = __builtin_amdgcn_exp2f(X[i + 3]); PIN(X); SBAR(); } while (0)
DI void att_compute(const Ctx& c, int w, int layer) {
  const int lane = c.lane, head = att_head(c), qb = att_qb(c);
  const int qp = w & 3, kp = w >> 2, r32 = lane & 31, h = lane >> 5;
  lds_ptr lds = c.lds;
  const unsigned L = WS_L + layer * L_SZ;
  SLOT(layer * 5 + 1);
  STAMP(0);
  for (int s = w * 64 + lane; s < 672; s += 512) {
    const unsigned v = s % 20 == 0 ? 0x3f803f80u : 0u;
    *(LAS u32x4*)(lds + AT_CONST + s * 16) = u32x4{v, v, v, v};
  }
  lds_ptr pk1 = lds + kp * 8192 + lane * 16;
  lds_ptr pk2_ = h ? lds + AT_CONST + 16 : lds + 16384 + kp * 4096 + r32 * 16 + (head & 1) * 8;
  lds_ptr pv = (r32 >= 20 ? lds + AT_CONST : lds + 24576 + kp * 10240) + h * 320 + r32 * 16;
  const int vmask = r32 >= 20 ? 0 : -1, kmask = h ? 0 : -1;
  const int qt = qb * 8 + qp * 2 + kp, tok = qt * 32 + r32;
  unsigned ob16 = L + L_O + (tok >> 4) * 20480 + (head * 2 + h) * 256 + (tok & 15) * 16, ob8 = L + L_O + (tok >> 4) * 20480 + 16384 + head * 128 + (tok & 15) * 8;
  asm volatile("" : "+v"(ob16), "+v"(ob8));
  f32x16 o0 = f32x16{}, o1 = f32x16{};
  f32x16 s0, s1, s2, s3 = f32x16{};
  STAMP(1);
  BARRIER();
  STAMP(2); STAMPC(0); WRESET();
  const bf16x8 q1a = ldsr16(lds + 2 * AT_SLOT + (qp * 2 + kp) * 1024 + lane * 16), q1b = ldsr16(lds + 2 * AT_SLOT + (qp * 2 + 1 - kp) * 1024 + lane * 16);
  lds_ptr pq2 = h ? lds + AT_CONST + 16 : lds + 2 * AT_SLOT + 8192 + qp * 1024 + r32 * 16 + (head & 1) * 8;
  const s16x4 q2a = *(const LAS s16x4*)(pq2 + kp * 512), q2b = *(const LAS s16x4*)(pq2 + 512 - kp * 512);
  bf16x8 k1 = ldsr16(pk1), vlo = ldsr16(pv), vhi = bf16x8{};
  s16x4 k2 = *(const LAS s16x4*)pk2_;
  s0 = qk16(k1, q1a, qk8(k2, q2a));
#define PH_EVEN(SC, SN, SP, KLOAD, VOFF) do { \
    SN = qk8(k2, q2b); SBAR(); EXP4(SC, 0); SN = qk16(k1, q1b, SN); SBAR(); KLOAD; SBAR(); EXP4(SC, 4); \
    o1 = pv_hi(vhi, SP, o1); SBAR(); vhi = ldsr16(pv + (VOFF) + 640); SBAR(); EXP4(SC, 8); o0 = pv_lo(vlo, SC, o0); SBAR(); EXP4(SC, 12); } while (0)
#define PH_ODD(SC, SN, SP, VLOAD) do { \
    SN = qk8(k2, q2a); SBAR(); EXP4(SC, 0); SN = qk16(k1, q1a, SN); SBAR(); EXP4(SC, 4); \
    o0 = pv_hi(vhi, SP, o0); SBAR(); EXP4(SC, 8); o1 = pv_lo(vlo, SC, o1); SBAR(); VLOAD; SBAR(); EXP4(SC, 12); } while (0)
#define KLD(t) do { k1 = ldsr16(pk1 + (t) * 1024); k2 = *(const LAS s16x4*)(pk2_ + (t) * 512); } while (0)
#define PRI(v) do { asm volatile("s_cmp_eq_u32 %0, 0\n\ts_cbranch_scc1 1f\n\ts_setprio " #v "\n1:" :: "s"(kp) : "scc"); SBAR(); } while (0)
#define HI(n) if (ATT_PRIO == (n)) PRI(1);
#define LO(n) if (ATT_PRIO == (n)) PRI(0);
#define TWO(t) HI(3) HI(4) HI(5) PH_EVEN(s0, s1, s3, KLD((t) + 1), (t) * 1280); PH_ODD(s1, s2, s0, vlo = ldsr16(pv + ((t) + 1) * 1280)); LO(4)
  for (int grp = 0; grp < 4; ++grp) {
    TWO(0) PH_EVEN(s2, s3, s1, if (grp == 0) BARRIER_NW(); KLD(2), 1280); LO(3) PH_ODD(s3, s0, s2, vlo = ldsr16(pv + 2 * 1280));
    TWO(2) PH_EVEN(s2, s3, s1, KLD(4), 3 * 1280); LO(3) LO(5) PH_ODD(s3, s0, s2, vlo = ldsr16(pv + 4 * 1280));
    TWO(4) PH_EVEN(s2, s3, s1, KLD(6), 5 * 1280); LO(3) PH_ODD(s3, s0, s2, vlo = ldsr16(pv + 6 * 1280));
    TWO(6)
    const int d = grp == 2 ? -2 * AT_SLOT : grp == 3 ? 0 : AT_SLOT;
    PH_EVEN(s2, s3, s1, pk1 += d; pk2_ += (d & kmask); if (grp != 3) BARRIER(); KLD(0), 7 * 1280);
    LO(3) LO(5) PH_ODD(s3, s0, s2, pv += (d & vmask); vlo = ldsr16(pv));
  }
  o1 = pv_hi(vhi, s3, o1);
  STAMP(3); STAMPC(1);
  __builtin_amdgcn_s_setprio(0);
  const f32x16 give = o1, keep = o0;
#pragma unroll
  for (int e = 0; e < 3; ++e)
    *(LAS f32x4*)(lds + AT_MERGE + (w * 3 + e) * 1024 + lane * 16) = f32x4{give[4 * e], give[4 * e + 1], give[4 * e + 2], give[4 * e + 3]};
  BARRIER();
  f32x4 a[3];
#pragma unroll
  for (int e = 0; e < 3; ++e)
    a[e] = f32x4{keep[4 * e], keep[4 * e + 1], keep[4 * e + 2], keep[4 * e + 3]} + *(const LAS f32x4*)(lds + AT_MERGE + ((w ^ 4) * 3 + e) * 1024 + lane * 16);
  float l = h ? a[2][0] : 0.f;
  l += swap32(l, h);
  if (__builtin_expect(__any(!(l >= 5.421e-20f && l <= 1.8447e19f)), 0)) {
    const unsigned o2 = (head >> 1) * 32768 + r32 * 16 + (head & 1) * 8;
    const bf16x8 fq1 = __builtin_bit_cast(bf16x8, ld16(c.r, L + L_Q1 + head * 65536 + qt * 1024 + lane * 16));
    u32x2 fq2 = ld8(c.r, L + L_Q2 + o2 + qt * 512);
    if (h) fq2 = u32x2{0u, 0u};
    const unsigned vi = L + L_V + head * 81920 + h * 320 + (r32 < 20 ? r32 : 0) * 16;
    f32x16 oo = f32x16{};
    float m = -INFINITY;
    const u32x4 one8 = {0x3f803f80u, 0x3f803f80u, 0x3f803f80u, 0x3f803f80u};
    for (int t = 0; t < 64; ++t) {
      const u32x4 fk1 = ld16(c.r, L + L_K1 + head * 65536 + t * 1024 + lane * 16);
      const u32x2 fk2 = ld8(c.r, L + L_K2 + o2 + t * 512);
      u32x4 v0 = ld16(c.r, vi + t * 1280), v1 = ld16(c.r, vi + t * 1280 + 640);
      if (r32 >= 20) { v0 = one8; v1 = one8; }
      f32x16 s = qk16(__builtin_bit_cast(bf16x8, fk1), fq1, qk8(__builtin_bit_cast(s16x4, fk2), __builtin_bit_cast(s16x4, fq2)));
      float tm = s[0];
#pragma unroll
      for (int r = 1; r < 16; ++r) tm = fmaxf(tm, s[r]);
      tm = fmaxf(tm, swap32(tm, h));
      const float mn = fmaxf(m, tm), al = __builtin_amdgcn_exp2f(m - mn);
      m = mn;
#pragma unroll
      for (int r = 0; r < 16; ++r) { s[r] = __builtin_amdgcn_exp2f(s[r] - mn); oo[r] *= al; }
      oo = pv_hi(__builtin_bit_cast(bf16x8, v1), s, pv_lo(__builtin_bit_cast(bf16x8, v0), s, oo));
    }
#pragma unroll
    for (int e = 0; e < 3; ++e) a[e] = f32x4{oo[4 * e], oo[4 * e + 1], oo[4 * e + 2], oo[4 * e + 3]};
    l = h ? a[2][0] : 0.f;
    l += swap32(l, h);
  }
  const float inv = __builtin_amdgcn_rcpf(l);
  const unsigned A0 = pk2(a[0][0] * inv, a[0][1] * inv), A1 = pk2(a[0][2] * inv, a[0][3] * inv);
  const unsigned B0 = pk2(a[1][0] * inv, a[1][1] * inv), B1 = pk2(a[1][2] * inv, a[1][3] * inv);
  const u32x2 C = {pk2(a[2][0] * inv, a[2][1] * inv), pk2(a[2][2] * inv, a[2][3] * inv)};
  st16(c.r, ob16, pair32(A0, A1, B0, B1));
  if (!h) st8(c.r, ob8, C);
  STAMP(4);
  publish(c, M_CNT, 8, flg(FL_ATT + layer * 5, qb * 32 + head), true);
  STAMP(5);
}
DI void retile_load(const Ctx& c, int J, u32x4 (&v)[5][4], const uint16_t* wo, const uint16_t* wg, const uint16_t* wu, const uint16_t* wd) {
  const int e = J < 216 ? J : J < 256 ? J - 216 : J - 256;
  const int strip = J < 216 ? (e >> 2) % 27 : e % 10, k0 = J < 216 ? (e & 3) * 5 : min((e / 10) * 5, 49), ld = J < 216 ? 3456 : 1280;
  const rsrc_t r = __builtin_amdgcn_make_buffer_rsrc((void*)(J < 108 ? wg : J < 216 ? wu : J < 256 ? wo : wd), 0,
                                                     J < 216 ? 640 * 3456 : J < 256 ? 640 * 1280 : 1728 * 1280, 0x00020000);
  const unsigned vo = (c.lane >> 3) * ld + (c.lane & 7) * 16;
#pragma unroll
  for (int u = 0; u < 5; ++u)
#pragma unroll
    for (int q = 0; q < 4; ++q) {
      const int ch = (k0 + u) * 4 + q;
      const int row = J < 216 || J >= 256 ? ch * 8 : ch < 64 ? (ch >> 1) * 20 + (ch & 2) * 2 + (ch & 1) * 8 : (ch - 64) * 40 + 16;
      v[u][q] = __builtin_amdgcn_raw_buffer_load_b128(r, vo, row * ld + strip * 128, 0);
    }
}
DI void retile_store(const Ctx& c, int J, lds_ptr scr, const u32x4 (&v)[5][4]) {
  const int e = J < 216 ? J : J < 256 ? J - 216 : J - 256, mat = J >= 108;
  const int strip = J < 216 ? (e >> 2) % 27 : e % 10, k0 = J < 216 ? (e & 3) * 5 : min((e / 10) * 5, 49);
  const unsigned ts = J < 216 ? 40960 : J < 256 ? 20480 : 55296;
  const unsigned dst = (J < 216 ? WS_WGU + mat * 20480 : J < 256 ? WS_WO : WS_WD) + strip * 4 * ts + k0 * 1024;
  const int i = c.lane & 15, g = c.lane >> 4;
  const bf16x8 ones = __builtin_bit_cast(bf16x8, u32x4{0x3f803f80u, 0x3f803f80u, 0x3f803f80u, 0x3f803f80u});
  f32x4 cs[4] = {f32x4{}, f32x4{}, f32x4{}, f32x4{}};
#pragma unroll
  for (int u = 0; u < 5; ++u) {
#pragma unroll
    for (int p = 0; p < 4; ++p) *(LAS u32x4*)(scr + p * 1024 + c.lane * 16) = v[u][p];
#pragma unroll
    for (int t = 0; t < 4; ++t) {
      const bf16x8 f = ldstr(scr + (g * 8 + (i >> 2)) * 128 + t * 32 + (i & 3) * 8, 512);
      __builtin_amdgcn_raw_buffer_store_b128(__builtin_bit_cast(u32x4, f), c.r, c.lane * 16, dst + u * 1024 + t * ts, 16);
      cs[t] = mfma16(f, ones, cs[t]);
    }
  }
  if (J < 216 && i == 0) {
#pragma unroll
    for (int t = 0; t < 4; ++t)
      __builtin_amdgcn_raw_buffer_store_b128(__builtin_bit_cast(u32x4, cs[t]), c.r, g * 16, WS_CSG + ((e & 3) * 3456 + (strip * 8 + t * 2 + mat) * 16) * 4, 16);
  }
}
DI int early_piece(int e) { return e < 4 ? (e & 1) + (e >> 1) * 8 : e < 6 ? 16 + (e - 4) * 4 : e < 9 ? 18 + e : 25 + e; }
DI int late_piece(int e) { return e < 6 ? 2 + e : e < 12 ? 4 + e : e < 15 ? 5 + e : e < 18 ? 6 + e : e < 25 ? 9 + e : 12 + e; }
DI void att_load(const Ctx& c, int lw, int layer, unsigned iss, const uint16_t* wo, const uint16_t* wg, const uint16_t* wu, const uint16_t* wd) {
  const int head = att_head(c), qb = att_qb(c), lane = c.lane;
  const char* Lp = c.ws + WS_L + layer * L_SZ;
  const unsigned vo = lane * 16;
  int grp = ATT_ROT ? qb >> 1 : 0;
  auto src_of = [&](int p) {
    return p < 16 ? L_K1 + head * 65536 + grp * 16384 + p * 1024
                  : p < 24 ? L_K2 + (head >> 1) * 32768 + grp * 8192 + (p - 16) * 1024 : L_V + head * 81920 + grp * 20480 + (p - 24) * 1024;
  };
  unsigned v0[14], m0[11], vg[11], sg[11];
#pragma unroll
  for (int q = 0; q < 11; ++q) {
    const int p = q < 3 ? early_piece(lw * 3 + q) : late_piece(lw * 8 + q - 3), pg = lw * 11 + q;
    v0[q] = src_of(p) + vo; m0[q] = p * 1024;
    vg[q] = src_of(pg) + vo; sg[q] = pg < 16 ? 16384 : pg < 24 ? 8192 : 20480;
    asm volatile("" : "+v"(v0[q]), "+s"(m0[q]));
  }
#pragma unroll
  for (int q = 0; q < 3; ++q) {
    const int p = lw * 3 + q;
    v0[11 + q] = (p < 8 ? L_Q1 + head * 65536 + qb * 8192 + p * 1024 : L_Q2 + (head >> 1) * 32768 + qb * 4096 + (p - 8) * 1024) + vo - q * 1024;
    PIN(v0[11 + q]);
  }
  auto issue = [&](int slot) {
#pragma unroll
    for (int q = 0; q < 11; ++q) { vg[q] += grp == 3 ? -3 * sg[q] : sg[q]; glds1(slot * AT_SLOT + (lw * 11 + q) * 1024, Lp, vg[q]); }
    grp = (grp + 1) & 3;
  };
  const int o0 = (head >> 1) * 5 + (head & 1) * 2, c0 = head * 20;
  auto kv_cb = [&](int sel) { return sel == 0 ? 5 + (o0 >> 4) : sel == 1 ? 5 + ((o0 + 2) >> 4) : sel == 2 ? 10 + (c0 >> 7) : 10 + ((c0 + 19) >> 7); };
  if (lw == 0) {
    sig_wait(c, iss, M_ISS);
    poll(c, flg(FL_QKV + layer * 5, lane & 16 ? (qb * 2 + (lane & 1)) * 16 + ((o0 + (lane & 2)) >> 4) : (grp * 4 + (lane & 3)) * 16 + kv_cb((lane >> 2) & 3)));
    sig_set(c, layer * 5 + 2);
    STAMPL(layer * 5 + 1, 6);
  } else sig_wait(c, layer * 5 + 2);
  glds3(2 * AT_SLOT + lw * 3072, Lp, v0[11], v0[12], v0[13]);
#pragma unroll
  for (int q = 0; q < 3; ++q) glds1(m0[q], Lp, v0[q]);
#pragma unroll
  for (int q = 3; q < 3 + ATT_LATE; ++q) glds1(m0[q], Lp, v0[q]);
  wait_vm<ATT_LATE>(); BARRIER();
#pragma unroll
  for (int q = 3 + ATT_LATE; q < 11; ++q) glds1(m0[q], Lp, v0[q]);
  wait_vm<0>(); BARRIER();
  if (lw == 0) { poll(c, flg(FL_QKV + layer * 5, (lane & 15) * 16 + kv_cb(lane >> 4))); sig_set(c, layer * 5 + 3); } else sig_wait(c, layer * 5 + 3, M_SIG, true);
  issue(1);
  const int J = (lw - 1) * 128 + c.j * 4 + (c.xcd >> 1);
  const bool rt = layer == 0 && lw > 0 && J < 366 && !(c.xcd & 1);
  u32x4 v[5][4];
  if (rt) { retile_load(c, J, v, wo, wg, wu, wd); wait_vm<20>(); } else wait_vm<0>();
  BARRIER();
  issue(2);
  if (rt) retile_store(c, J, c.lds + AT_SCR + (lw - 1) * 4096, v);
  if (layer == 0 && lw > 0) publish(c, M_CNT3, 3, flg(FL_RT, blockIdx.x));
  wait_vm<0>(); BARRIER();
  issue(0);
  wait_vm<0>();
  if (layer == 0) {
    if (lw == 0) {
      if (c.j == 0) {
        Poll<2> pl;
#pragma unroll 1
        for (int q = 0; q < 2; ++q) { pl.run(c, flg(FL_RT, q * 128 + lane), flg(FL_RT, q * 128 + 64 + lane)); wait_vm<0>(); pl.keep(); }
        if (lane < 32) __hip_atomic_store((GAS u64*)(c.ws + flg(FL_RT2, lane * 8 + c.xcd)), c.tgt, __ATOMIC_RELAXED, __HIP_MEMORY_SCOPE_AGENT);
      }
      poll(c, flg(FL_RT2, blockIdx.x));
      if (blockIdx.x == 0 && lane == 0) *(u64*)(c.ws + WS_EPOCH) = c.tgt;
    }
  }
  BARRIER();
  u32x4 pk[25];
#pragma unroll
  for (int e = 0; e < 25; ++e) pk[e] = ld16s(c.r, vo, WS_WO + (pr_cb(c) * 5 + e % 5) * 20480 + (lw * 5 + e / 5) * 1024);
#pragma unroll 1
  for (unsigned q = lw; q < 35; q += 4) glds1(AT_SCR + lw * 1024, c.ws + WS_WGU + ((gu_cb(c) * 27) >> 2) * 40960 + ((c.j & 7) * 35 + q) * 1024, vo);
  wait_vm<0>();
  BARRIER();
  sig_wait(c, layer * 16 + 8, M_CNT);
#pragma unroll
  for (int e = 0; e < 25; ++e) { const int k = lw * 5 + e / 5; ldsw16(c.lds + (k >> 1) * 10240 + (k & 1) * 1024 + (e % 5) * 2048 + vo, pk[e]); }
}

template <int NS, int NB, int WIN>
DI void proj_compute(const Ctx& c, int w, int slot_id, unsigned a_off, const char* res_rm, unsigned res_off, unsigned out_off, unsigned st_off,
                     uint16_t* out, int arr) {
  constexpr bool RES = NS <= NB;
  const int panel = pr_panel(c), cb = pr_cb(c);
  SLOT(slot_id);
  STAMP(0);
  if (w >= 4) {
    if constexpr (RES) { BARRIER(); BARRIER(); }
    else {
      static_assert(RES || (NS == 27 && NB == 15 && WIN == 18), "schedule");
      const int par = (w >> 1) & 1, half = w & 1;
      u32x4 pk[15];
#pragma unroll
      for (int e = 0; e < 15; ++e) {
        const int q = half * 5 + e % 5;
        pk[e] = ld16s(c.r, c.lane * 16, WS_WD + (cb * 5 + (q >> 1)) * 55296 + ((15 + par + (e / 5) * 2) * 2 + (q & 1)) * 1024);
      }
      lds_ptr pw = c.lds + par * 10240 + half * 5120 + c.lane * 16;
      BARRIER(); BARRIER();
#pragma unroll
      for (int e = 0; e < 10; ++e) ldsw16(pw + (e / 5) * 20480 + (e % 5) * 1024, pk[e]);
      BARRIER();
#pragma unroll
      for (int e = 10; e < 15; ++e) ldsw16(pw + 40960 + (e % 5) * 1024, pk[e]);
      BARRIER(); BARRIER(); BARRIER(); BARRIER();
    }
    return;
  }
  const int lane = c.lane, i = lane & 15, g = lane >> 4, mt = panel * 4 + w;
  u32x2 res[5];
#pragma unroll
  for (int t = 0; t < 5; ++t)
    res[t] = res_rm ? *(const u32x2*)(res_rm + (mt * 16 + i) * 1280 + cb * 160 + t * 32 + g * 8)
                    : ld8(c.r, res_off + (mt * 80 + cb * 10 + t * 2 + (g >> 1)) * 256 + i * 16 + (g & 1) * 8);
  f32x4 acc[5];
#pragma unroll
  for (int t = 0; t < 5; ++t) { acc[t] = f32x4{bflo(res[t][0]), bfhi(res[t][0]), bflo(res[t][1]), bfhi(res[t][1])}; PIN(acc[t]); }
  float piv;
  { const f32x4 s4 = (acc[0] + acc[1]) + (acc[2] + acc[3]) + acc[4]; piv = rows4((s4[0] + s4[1]) + (s4[2] + s4[3])) * (1.f / 80.f); }
  const int ch = (g & 1) * 2 + (g >> 1);
  unsigned xo = out ? (mt * 16 + i) * 1280 + cb * 160 + ch * 16 : out_off + (mt * 80 + cb * 10 + ch) * 256 + i * 16;
  unsigned so = st_off + (mt >> 3) * 8192 + cb * 1024 + ((mt & 7) * 16 + i) * 8;
  asm volatile("" : "+v"(piv), "+v"(xo), "+v"(so));
  lds_ptr pb = c.lds + lane * 16;
  unsigned av = a_off + mt * (NS * 2048) + lane * 16;
  u32x4 a[WIN];
  bf16x8 f[3][5];
  int sb = 0;
  STAMP(1);
  BARRIER();
  STAMP(2); STAMPC(0); WRESET();
  constexpr int AH = RES ? WO_AH : DN_AH;
  auto lda = [&](int k) {
    if (RES && k >= 16) {
      const unsigned o = a_off + mt * 20480 + 16384 + (k - 16) * 1024 + g * 256 + i * 8;
      const u32x2 lo = ld8(c.r, o), hi = ld8(c.r, o + 128);
      a[k % WIN] = u32x4{lo[0], lo[1], hi[0], hi[1]};
    } else a[k % WIN] = ld16(c.r, av + k * 1024);
  };
#pragma unroll
  for (int k = 0; k < AH; ++k) lda(k);
#pragma unroll
  for (int t = 0; t < 5; ++t) { f[0][t] = ldsr16(pb + t * 2048); f[1][t] = ldsr16(pb + t * 2048 + 1024); }
  SBAR();
  auto window = [&](bool first, bool last) {
#pragma unroll
    for (int k = 0; k < WIN; ++k) {
      const bool ld = !(last && k + 2 >= WIN);
      if ((k & 1) == 0) {
        if (RES) pb += 10240;
        else { pb += sb == NB - 1 ? -(NB - 1) * 10240 : 10240; sb = sb == NB - 1 ? 0 : sb + 1; }
        if (!ld) BARRIER(); else if (!RES && (k == 10 || (k == 0 && !first))) BARRIER_NW();
      }
#pragma unroll
      for (int t = 0; t < 5; ++t) {
        if (ld) f[(k + 2) % 3][t] = ldsr16(pb + t * 2048 + (k & 1) * 1024);
        acc[t] = mfma16(f[k % 3][t], __builtin_bit_cast(bf16x8, a[k]), acc[t]);
        SBAR();
      }
      if (!last || k + AH < WIN) { lda(k + AH); SBAR(); }
    }
    av += WIN * 1024;
  };
  static_assert(2 * NS % WIN == 0 && (2 * NS == WIN || WIN % 6 == 0), "window");
#pragma unroll 1
  for (int b = 1; b < 2 * NS / WIN; ++b) window(b == 1, false);
  window(false, true);
  STAMP(3); STAMPC(1);
  u32x2 o[5];
#pragma unroll
  for (int t = 0; t < 5; ++t) {
    o[t][0] = pk2(acc[t][0], acc[t][1]); o[t][1] = pk2(acc[t][2], acc[t][3]);
  }
  const u32x4 p0 = pair16(o[0][0], o[0][1], o[1][0], o[1][1]), p1 = pair16(o[2][0], o[2][1], o[3][0], o[3][1]),
              p2 = pair16(o[4][0], o[4][1], o[4][0], o[4][1]);
  if (out) {
    char* po = (char*)out + xo;
    *(u32x4*)po = p0; *(u32x4*)(po + 64) = p1;
    if (!(g & 1)) *(u32x4*)(po + 128) = p2;
    STAMP(4);
    return;
  }
  st16(c.r, xo, p0); st16(c.r, xo + 1024, p1);
  if (!(g & 1)) st16(c.r, xo + 2048, p2);
  f32x4 s4 = acc[0] - piv, q4 = s4 * s4;
#pragma unroll
  for (int t = 1; t < 5; ++t) { const f32x4 d = acc[t] - piv; s4 += d; q4 += d * d; }
  const float sd = rows4((s4[0] + s4[1]) + (s4[2] + s4[3])), sq = rows4((q4[0] + q4[1]) + (q4[2] + q4[3]));
  if (g == 0) st8(c.r, so, u32x2{__float_as_uint(sd + 80.f * piv), __float_as_uint(fmaxf(sq - sd * sd * (1.f / 80.f), 0.f))});
  STAMP(4);
  publish(c, M_CNT4, 4, flg(arr, panel * 8 + cb), true);
  STAMP(5);
}
template <int NF>
DI void gu_consumer16(const Ctx& c, int layer, int f0, int mq) {
  const int panel = gu_panel(c), ft0 = (gu_cb(c) * 27) >> 2;
  const int lane = c.lane, i = lane & 15, g = lane >> 4;
  f32x4 acc[2][2 * NF];
#pragma unroll
  for (int m = 0; m < 2; ++m)
#pragma unroll
    for (int t = 0; t < 2 * NF; ++t) acc[m][t] = f32x4{0.f, 0.f, 0.f, 0.f};
  lds_ptr pa = c.lds + GU_A + mq * 2048 + lane * 16, pb = c.lds + f0 * 2048 + lane * 16;
  bf16x8 x0[2], x1[2], wa[NF], wb0[NF], wb1[NF];
  auto step = [&](const bf16x8* xa, const bf16x8* wb, bf16x8* xn, bf16x8* wn_, bool ld) {
#pragma unroll
    for (int q = 0; q < 4 * NF; ++q) {
      const int t = q >> 1, m = q & 1;
      if (ld) {
        if (q < 2) xn[q] = ldsr16(pa + q * 1024);
        else if (q < 2 * NF) { const int e = NF == 4 ? (q == 3 ? 0 : q == 4 ? 1 : q == 6 ? 2 : q == 7 ? 3 : -1) : NF == 3 ? q - 3 : q - 2; if (e >= 0) wn_[e] = ldsr16(pb + (NF + e) * 1024); }
        else if (q & 1) wa[(q - 2 * NF) >> 1] = ldsr16(pb + ((q - 2 * NF) >> 1) * 1024);
      }
      acc[m][t] = mfma16(t < NF ? wa[t] : wb[t - NF], xa[m], acc[m][t]);
      SBAR();
    }
  };
  int sl = 0;
  auto adv = [&]() {
    pa += sl == GU_NA - 1 ? -(GU_NA - 1) * 8192 : 8192; pb += sl == GU_NB - 1 ? -(GU_NB - 1) * 14336 : 14336;
    sl = sl == GU_NA - 1 ? 0 : sl + 1;
  };
  unsigned ho = WS_L + layer * L_SZ + L_H + ((panel * 8 + mq * 2 + (g & 1)) * 216 + (ft0 + f0) * 2 + (g >> 1)) * 256 + i * 16;
  PIN(ho);
  SLOT(layer * 5 + 3);
  STAMP(0);
  STAMP(1);
  BARRIER();
  STAMP(2); STAMPC(0); WRESET();
  x0[0] = ldsr16(pa); x0[1] = ldsr16(pa + 1024);
#pragma unroll
  for (int t = 0; t < NF; ++t) { wa[t] = ldsr16(pb + t * 1024); wb0[t] = ldsr16(pb + (NF + t) * 1024); }
#pragma unroll 1
  for (int st = 0; st < 9; ++st) {
    adv(); BARRIER_NW(); step(x0, wb0, x1, wb1, true);
    adv(); BARRIER_NW(); step(x1, wb1, x0, wb0, true);
  }
  adv(); BARRIER_NW(); step(x0, wb0, x1, wb1, true);
  BARRIER();
  const LAS float* srs = (const LAS float*)(c.lds + M_SST) + mq * 32 + i;
  const LAS float* scs = (const LAS float*)(c.lds + M_SCS) + f0 * 32 + g * 4;
  float rs[2] = {srs[0], srs[16]}; const float nm[2] = {srs[128], srs[144]};
  f32x4 cs[2 * NF];
#pragma unroll
  for (int e = 0; e < 2 * NF; ++e) cs[e] = *(const LAS f32x4*)(scs + e * 16);
  SBAR();
  step(x1, wb1, x0, wb0, false);
  STAMP(3); STAMPC(1);
  rs[0] *= rs[0]; rs[1] *= rs[1];
#pragma unroll
  for (int f = 0; f < NF; ++f) {
    unsigned p[2][2];
#pragma unroll
    for (int m = 0; m < 2; ++m) {
      f32x4 gt = acc[m][2 * f] + cs[2 * f] * nm[m];
      const f32x4 up = (acc[m][2 * f + 1] + cs[2 * f + 1] * nm[m]) * rs[m];
      gt = __builtin_elementwise_max(gt, f32x4{0.f, 0.f, 0.f, 0.f}) * up;
      p[m][0] = pk2(gt[0], gt[1]); p[m][1] = pk2(gt[2], gt[3]);
    }
    st16(c.r, ho + f * 512, pair16(p[0][0], p[0][1], p[1][0], p[1][1]));
    SBAR();
  }
  STAMP(4);
  publish(c, M_CNT, 8, flg(FL_GU + layer * 5, panel * 16 + gu_cb(c)), true);
  STAMP(5);
}
template <int NF>
DI void gu_consumer32(const Ctx& c, int layer, int f0, int mq) {
  const int panel = gu_panel(c), ft0 = (gu_cb(c) * 27) >> 2;
  const int lane = c.lane, r32 = lane & 31, h = lane >> 5;
  f32x16 acc[NF];
#pragma unroll
  for (int t = 0; t < NF; ++t) acc[t] = f32x16{};
  const int lo = (r32 >> 4) * 1024 + h * 256 + (r32 & 15) * 16;
  lds_ptr pa = c.lds + GU_A + mq * 2048 + lo, pb = c.lds + f0 * 2048 + lo;
  bf16x8 x0[2], x1[2], wa[NF], wb0[NF], wb1[NF];
  auto step = [&](const bf16x8* xa, const bf16x8* wb, bf16x8* xn, bf16x8* wn_, bool ld) {
#pragma unroll
    for (int q = 0; q < 2 * NF; ++q) {
      if (ld) {
        if (q == 0) { xn[0] = ldsr16(pa); xn[1] = ldsr16(pa + 512); }
        if (q >= 1 && q <= NF) wn_[q - 1] = ldsr16(pb + (q - 1) * 2048 + 512);
        if (q >= NF) wa[q - NF] = ldsr16(pb + (q - NF) * 2048);
      }
      if (q < NF) acc[q] = mfma32(wa[q], xa[0], acc[q]); else acc[q - NF] = mfma32(wb[q - NF], xa[1], acc[q - NF]);
      SBAR();
    }
  };
  static_assert(GU_NA == GU_NB, "one slot counter");
  int sl = 0;
  auto adv = [&]() {
    pa += sl == GU_NA - 1 ? -(GU_NA - 1) * 8192 : 8192; pb += sl == GU_NB - 1 ? -(GU_NB - 1) * 14336 : 14336;
    sl = sl == GU_NA - 1 ? 0 : sl + 1;
  };
  SLOT(layer * 5 + 3);
  STAMP(0);
  STAMP(1);
  BARRIER();
  STAMP(2); STAMPC(0); WRESET();
  x0[0] = ldsr16(pa); x0[1] = ldsr16(pa + 512);
#pragma unroll
  for (int t = 0; t < NF; ++t) { wa[t] = ldsr16(pb + t * 2048); wb0[t] = ldsr16(pb + t * 2048 + 512); }
#pragma unroll 1
  for (int st = 0; st < 9; ++st) {
    adv(); BARRIER_NW(); step(x0, wb0, x1, wb1, true);
    adv(); BARRIER_NW(); step(x1, wb1, x0, wb0, true);
  }
  adv(); BARRIER_NW(); step(x0, wb0, x1, wb1, true);
  BARRIER();
  const LAS float* srs = (const LAS float*)(c.lds + M_SST) + mq * 32 + r32;
  const LAS float* scs = (const LAS float*)(c.lds + M_SCS) + f0 * 32 + h * 4;
  float rs = srs[0], nm = srs[128];
  f32x4 cs[2][4];
#pragma unroll
  for (int e = 0; e < 8; ++e) cs[e >> 2][e & 3] = *(const LAS f32x4*)(scs + e * 8);
  SBAR();
  step(x1, wb1, x0, wb0, false);
  STAMP(3); STAMPC(1);
  rs *= rs;
  const unsigned ho = WS_L + layer * L_SZ + L_H + ((panel * 8 + mq * 2 + (r32 >> 4)) * 216 + (ft0 + f0) * 2 + h) * 256 + (r32 & 15) * 16;
#pragma unroll
  for (int f = 0; f < NF; ++f) {
    unsigned p[2][2];
#pragma unroll
    for (int e = 0; e < 2; ++e) {
      const f32x16 a = acc[f];
      f32x4 gt = f32x4{a[e * 4], a[e * 4 + 1], a[e * 4 + 2], a[e * 4 + 3]} + cs[f & 1][e] * nm;
      const f32x4 up = (f32x4{a[8 + e * 4], a[9 + e * 4], a[10 + e * 4], a[11 + e * 4]} + cs[f & 1][2 + e] * nm) * rs;
      gt = __builtin_elementwise_max(gt, f32x4{0.f, 0.f, 0.f, 0.f}) * up;
      p[e][0] = pk2(gt[0], gt[1]); p[e][1] = pk2(gt[2], gt[3]);
    }
    if (f + 2 < NF) {
#pragma unroll
      for (int e = 0; e < 4; ++e) cs[f & 1][e] = *(const LAS f32x4*)(scs + (f + 2) * 32 + e * 8);
    }
    st16(c.r, ho + f * 512, pair32(p[0][0], p[0][1], p[1][0], p[1][1]));
    SBAR();
  }
  STAMP(4);
  publish(c, M_CNT, 8, flg(FL_GU + layer * 5, panel * 16 + gu_cb(c)), true);
  STAMP(5);
}
template <int NF> DI void gu_consumer(const Ctx& c, int layer, int f0, int mq) {
  if constexpr (GU16 == 1 || (GU16 == 2 && NF < 4) || (GU16 == 3 && NF == 4)) gu_consumer16<NF>(c, layer, f0, mq); else gu_consumer32<NF>(c, layer, f0, mq);
}
DI void mlp_load(const Ctx& c0, int lw, int layer, unsigned iss, bool qkv_next, const uint16_t* wqkv) {
  const int half = lw & 1;
  {
    const Ctx c = fresh(c0);
    if (lw == 0) { poll(c, flg(FL_ATT + layer * 5, (pr_panel(c) >> 2) * 32 + (c.lane & 31))); STAMPL(layer * 5 + 2, 6); }
    BARRIER_L(); BARRIER_L();
  }
  { const int w = lw + 8; (void)w; WDUMP(layer * 5 + 2); }
  {
    const Ctx c = fresh(c0);
    const int ft0 = (gu_cb(c) * 27) >> 2;
    u32x4 pk[20];
    auto park = [&](int e0) {
#pragma unroll
      for (int e = e0; e < e0 + 4; ++e) {
        const int q = half * 5 + e % 5;
        pk[e] = ld16s(c.r, c.lane * 16, WS_WD + (pr_cb(c) * 5 + (q >> 1)) * 55296 + (((e / 5) * 2 + (lw >> 1)) * 2 + (q & 1)) * 1024);
      }
    };
    static_assert(GU_NA == 7 && GU_NB == 7, "schedule");
    if (lw >= 2) {
      Ring<7, GU_NB, 20, 2> rg;
#pragma unroll
      for (int p = 0; p < 7; ++p) rg.voff[p] = WS_WGU + (ft0 * 2 + half * 7 + p) * 20480 + c.lane * 16;
      rg.init(c.ws, 1024, 0, half);
      if (half == 0) ln_cols<4>(c, 56, WS_CSG + ft0 * 128, 13824);
      sig_wait(c, iss - layer * 16 + 4, M_CNT4);
      rg.template fill_ahead<1>(c, layer * 5 + 4);
      rg.template range<5, 5>(); rg.steady();
      wait_vm<28>(); BARRIER_L(); park(0);
      wait_vm<21 + 4>(); BARRIER_L(); park(4);
      wait_vm<14 + 8>(); BARRIER_L(); park(8);
      wait_vm<7 + 12>(); BARRIER_L(); park(12);
      wait_vm<16>(); BARRIER_L(); park(16);
    } else {
      Ring<4, GU_NA, 20, 2> rg;
#pragma unroll
      for (int p = 0; p < 4; ++p) rg.voff[p] = WS_L + layer * L_SZ + L_X1 + (gu_panel(c) * 8 + half * 4 + p) * 20480 + c.lane * 16;
      rg.init(c.ws, 1024, GU_A, half);
      Poll<1> pl;
      gate(c, pl, half, flg(FL_WO + layer * 5, gu_panel(c) * 16 + (c.lane & 15)), layer * 5 + 4, iss + 12);
      rg.template fill<3>();
      rg.template head<3>();
      pl.keep();
      f32x2 st[8];
      wait_vm<16>(); BARRIER_L();
      ln_load(c, WS_ST + (layer * 2 + 1) * STB + gu_panel(c) * 8192, half * 64 + c.lane, st);
      wait_vm<12 + 8>(); BARRIER_L(); park(0); park(4);
      wait_vm<8 + 8 + 8>(); BARRIER_L();
      const f32x2 t = ln_sum(st);
      wait_vm<8>(); BARRIER_L(); ln_put(c, half * 64 + c.lane, t); park(8);
      BARRIER_L(); park(12); park(16);
    }
    BARRIER_L();
#pragma unroll
    for (int e = 0; e < 20; ++e) PIN(pk[e]);
    { const int w = lw + 8; (void)w; WDUMP(layer * 5 + 3); }
    auto dump = [&]() {
#pragma unroll
      for (int e = 0; e < 20; ++e) ldsw16(c.lds + ((e / 5) * 2 + (lw >> 1)) * 10240 + (half * 5 + e % 5) * 1024 + c.lane * 16, pk[e]);
    };
    if (lw >= 2) {
      Ring<5, DN_NB, 27, 2> rg;
#pragma unroll
      for (int p = 0; p < 5; ++p) { const int q = half * 5 + p; rg.voff[p] = WS_WD + (pr_cb(c) * 5 + (q >> 1)) * 55296 + (q & 1) * 1024 + c.lane * 16; }
      rg.init(c.ws + 8 * 2048, 2048, 0, half);
      rg.slot = 8;
      sig_wait(c, layer * 16 + 16, M_CNT);
      rg.template fill<3>(); dump(); rg.template fill<4>();
      BARRIER_L();
      wait_vm<5 * 5>(); BARRIER_L();
      wait_vm<0>(); BARRIER_L();
      rg.base += 6 * 2048; rg.slot = 6;
      rg.template fill<3>();
      BARRIER_L();
      rg.template fill<3>();
      wait_vm<3 * 5>(); BARRIER_L();
      wait_vm<0>(); BARRIER_L();
      BARRIER_L();
    } else {
      sig_wait(c, layer * 16 + 16, M_CNT);
      dump();
      if (lw == 0) { poll(c, flg(FL_GU + layer * 5, (pr_panel(c) >> 1) * 16 + (c.lane & 15))); STAMPL(layer * 5 + 4, 6); }
#pragma unroll 1
      for (int k = 0; k < 7; ++k) BARRIER_L();
    }
  }
  { const int w = lw + 8; (void)w; WDUMP(layer * 5 + 4); }
  if (qkv_next) qkv_load1(fresh(c0), lw, wqkv);
}

__global__ __launch_bounds__(768) void fused_kernel(const uint16_t* __restrict__ x, const uint16_t* __restrict__ wqkv, const uint16_t* __restrict__ wo,
                                                    const uint16_t* __restrict__ wg, const uint16_t* __restrict__ wu,
                                                    const uint16_t* __restrict__ wd, uint16_t* __restrict__ out, char* __restrict__ ws) {
  extern __shared__ __attribute__((aligned(16))) char smem[];
  const char* pc;
  asm volatile("s_getpc_b64 %0" : "=s"(pc));
  Ctx c;
  c.ws = ws; c.lds = (lds_ptr)smem; c.lane = threadIdx.x & 63; c.xcd = blockIdx.x & 7; c.j = blockIdx.x >> 3;
  c.r = __builtin_amdgcn_make_buffer_rsrc(ws, 0, WS_END, 0x00020000);
  const int w = __builtin_amdgcn_readfirstlane(threadIdx.x >> 6);
  SPANST(0);
  c.tgt = 0;
  if (threadIdx.x < 16) *(LAS unsigned*)(c.lds + M_CNT + threadIdx.x * 4) = 0u;
  const bool qkv_on = qkv_cb(c) != 15;
  Ctx c0 = c;
  WDUMP(15);
  if (__builtin_expect(w >= 8, LD_FIRST)) {
    __builtin_amdgcn_s_setprio(3);
    if (qkv_on) qkv_load0(fresh(c0), w - 8, x, wqkv); else BARRIER();
    c0.tgt = epoch(ws);
#pragma unroll 1
    for (int layer = 0; layer < 2; ++layer) {
      const unsigned iss = layer * 24 + (qkv_on ? (layer + 1) * 4 : 0);
      WDUMP(layer * 5);
      att_load(fresh(c0), opq(w - 8), opq(layer), iss, wo, wg, wu, wd);
      WDUMP(layer * 5 + 1);
      mlp_load(c0, opq(w - 8), opq(layer), iss, layer == 0 && qkv_on, wqkv);
    }
  } else {
    if (w < 4) {
      const Ctx c = fresh(c0);
      constexpr unsigned len = 58 * 1024;
      if (w < 2) glds1(P0_SCR + 10240 + w * 1024, (const char*)((u64)pc & ~15ull), min((c.j * 2 + w) * 1024 + c.lane * 16, len));
      else asm volatile(GL_HEAD "global_load_lds_dword %2, %1 sc1" ::"s"(P0_SCR + 10240), "s"(c.ws), "v"(min(((w - 2) * 64 + c.lane) * 524288u + 2048, WS_END)) : "memory");
    }
#pragma unroll 1
    for (int layer = 0; layer < 2; ++layer) {
      const unsigned L = WS_L + layer * L_SZ;
      qkv_compute(fresh(c0), c0.tgt, opq(w), opq(layer), x, wqkv);
      WDUMP(layer * 5);
      att_compute(fresh(c0), opq(w), opq(layer));
      WDUMP(layer * 5 + 1);
      proj_compute<10, WO_NB, 20>(fresh(c0), opq(w), layer * 5 + 2, L + L_O, layer == 0 ? (const char*)x : nullptr, WS_X2, L + L_X1,
                                  WS_ST + (layer * 2 + 1) * STB, nullptr, FL_WO + layer * 5);
      WDUMP(layer * 5 + 2);
      if (w < 4) gu_consumer<4>(fresh(c0), opq(layer), 0, opq(w));
      else if (GU_SKIP && (gu_cb(c0) & 3) == 0) gu_consumer<2>(fresh(c0), opq(layer), 4, opq(w - 4));
      else gu_consumer<3>(fresh(c0), opq(layer), 4, opq(w - 4));
      WDUMP(layer * 5 + 3);
      proj_compute<27, DN_NB, 18>(fresh(c0), opq(w), layer * 5 + 4, L + L_H, nullptr, L + L_X1, WS_X2, WS_ST + 2 * STB, layer ? out : nullptr,
                                  FL_DN + layer * 5);
      WDUMP(layer * 5 + 4);
    }
  }
}

extern "C" void kernel_launch(void* const* d_in, const int* in_sizes, int n_in, void* d_out, int out_size, void* d_ws, size_t ws_size,
                              hipStream_t stream) {
  (void)hipFuncSetAttribute((const void*)fused_kernel, hipFuncAttributeMaxDynamicSharedMemorySize, LDS_BYTES);
  fused_kernel<<<256, 768, LDS_BYTES, stream>>>((const uint16_t*)d_in[0], (const uint16_t*)d_in[1], (const uint16_t*)d_in[2],
                                                (const uint16_t*)d_in[4], (const uint16_t*)d_in[5], (const uint16_t*)d_in[6],
                                                (uint16_t*)d_out, (char*)d_ws);
}
```

```cpp
#include <hip/hip_runtime.h>
#include <hip/hip_bf16.h>
#include <stdint.h>

typedef __attribute__((ext_vector_type(8))) __bf16 bf16x8;
typedef __attribute__((ext_vector_type(16))) __bf16 bf16x16;
typedef __attribute__((ext_vector_type(4))) short s16x4;
typedef __attribute__((ext_vector_type(8))) short s16x8;
typedef __attribute__((ext_vector_type(2))) __bf16 bf16x2;
typedef __attribute__((ext_vector_type(2))) float f32x2;
typedef __attribute__((ext_vector_type(4))) float f32x4;
typedef __attribute__((ext_vector_type(8))) float f32x8;
typedef __attribute__((ext_vector_type(16))) float f32x16;
typedef __attribute__((ext_vector_type(2))) unsigned u32x2;
typedef __attribute__((ext_vector_type(4))) unsigned u32x4;
typedef unsigned long long u64;
#define LAS __attribute__((address_space(3)))
#define GAS __attribute__((address_space(1)))
typedef LAS char* lds_ptr;
typedef __amdgpu_buffer_rsrc_t rsrc_t;
#define DI __device__ __forceinline__

constexpr unsigned XB = 2048u * 640 * 2;
constexpr unsigned WS_EPOCH = 0;
constexpr unsigned WS_FLAG = 4096;
constexpr int FL_P0 = 0, FL_RT = 1, FL_QKV = 2, FL_ATT = 3, FL_WO = 4, FL_GU = 5, FL_DN = 6, FL_RT2 = 12;
constexpr unsigned WS_WO  = WS_FLAG + 13u * 32768;
constexpr unsigned WS_WGU = WS_WO + 640u * 640 * 2;
constexpr unsigned WS_WD  = WS_WGU + 3456u * 640 * 2;
constexpr unsigned WS_CSQ = WS_WD + 640u * 1728 * 2;
constexpr unsigned WS_CSG = WS_CSQ + 8u * 1920 * 4;
constexpr unsigned WS_ST  = WS_CSG + 4u * 3456 * 4;
constexpr unsigned STB = 2048u * 64;
constexpr unsigned WS_X2  = WS_ST + 4 * STB;
constexpr unsigned WS_L   = WS_X2 + XB;
constexpr unsigned L_Q1 = 0, L_Q2 = L_Q1 + 32u * 65536, L_K1 = L_Q2 + 16u * 32768, L_K2 = L_K1 + 32u * 65536, L_V = L_K2 + 16u * 32768,
                   L_O = L_V + XB, L_X1 = L_O + XB, L_H = L_X1 + XB, L_SZ = L_H + 2048u * 1728 * 2;
constexpr unsigned WS_END = WS_L + 2 * L_SZ;

constexpr int LDS_BYTES = 163840, MISC = 159744, M_SST = MISC, M_SCS = MISC + 1024, M_CNT = MISC + 2048, M_CNT2 = M_CNT + 8, M_CNT3 = M_CNT + 16, M_CNT4 = M_CNT + 24, M_SIG = M_CNT + 32, M_ISS = M_CNT + 40, M_TGT = M_CNT + 48;
constexpr int QKV_NB = 5, QKV_NA = 4, QKV_A = QKV_NB * 16384;
constexpr int WO_NB = 10, DN_NB = 15;
constexpr int GU_NB = 7, GU_NA = 7, GU_A = GU_NB * 14336;
constexpr int AT_SLOT = 45056, AT_CONST = 3 * AT_SLOT, AT_SCR = AT_CONST + 10752, AT_MERGE = AT_SLOT;
static_assert(QKV_A + QKV_NA * 16384 <= MISC && DN_NB * 10240 <= MISC && GU_A + GU_NA * 8192 <= MISC, "lds");
static_assert(AT_SCR + 3 * 4096 <= MISC, "lds");

DI unsigned pk2(float a, float b) { f32x2 v = {a, b}; bf16x2 r = __builtin_convertvector(v, bf16x2); return __builtin_bit_cast(unsigned, r); }
DI float bflo(unsigned u) { return __uint_as_float(u << 16); }
DI float bfhi(unsigned u) { return __uint_as_float(u & 0xffff0000u); }
DI f32x4 mfma16(bf16x8 a, bf16x8 b, f32x4 c) { return __builtin_amdgcn_mfma_f32_16x16x32_bf16(a, b, c, 0, 0, 0); }
DI f32x16 mfma32(bf16x8 a, bf16x8 b, f32x16 c) { return __builtin_amdgcn_mfma_f32_32x32x16_bf16(a, b, c, 0, 0, 0); }
DI bf16x8 ldsr16(lds_ptr p) { return *(const LAS bf16x8*)p; }
DI bf16x8 ldstr(lds_ptr p, int second) {
  const s16x4 a = __builtin_amdgcn_ds_read_tr16_b64_v4i16((LAS s16x4*)p), b = __builtin_amdgcn_ds_read_tr16_b64_v4i16((LAS s16x4*)(p + second));
  return __builtin_bit_cast(bf16x8, (s16x8)__builtin_shufflevector(a, b, 0, 1, 2, 3, 4, 5, 6, 7));
}
#define BARRIER() asm volatile("s_waitcnt lgkmcnt(0)\n\ts_barrier" ::: "memory")
template <int N> DI void wait_vm() { asm volatile("s_waitcnt vmcnt(%0)" ::"n"(N > 63 ? 63 : N) : "memory"); }
#define WDUMP(id)
#define WRESET()
#define BARRIER_L() BARRIER()
#define BARRIER_NW() asm volatile("s_barrier" ::: "memory")
#define SBAR() __builtin_amdgcn_sched_barrier(0)
#define PIN(x) asm volatile("" : "+v"(x))
#define GL_HEAD "s_mov_b32 m0, %0\n\ts_nop 3\n\t"
#define GL(v, off) "global_load_lds_dwordx4 %" #v ", %1 offset:" #off " sc1\n\t"
DI void glds1(unsigned m, const char* sb, unsigned v0) { asm volatile(GL_HEAD GL(2, 0)::"s"(m), "s"(sb), "v"(v0) : "memory"); }
DI void glds2(unsigned m, const char* sb, unsigned v0, unsigned v1) {
  asm volatile(GL_HEAD GL(2, 0) GL(3, 1024)::"s"(m), "s"(sb), "v"(v0), "v"(v1) : "memory");
}
DI void glds3(unsigned m, const char* sb, unsigned v0, unsigned v1, unsigned v2) {
  asm volatile(GL_HEAD GL(2, 0) GL(3, 1024) GL(4, 2048)::"s"(m), "s"(sb), "v"(v0), "v"(v1), "v"(v2) : "memory");
}
DI void glds4(unsigned m, const char* sb, unsigned v0, unsigned v1, unsigned v2, unsigned v3) {
  asm volatile(GL_HEAD GL(2, 0) GL(3, 1024) GL(4, 2048) GL(5, 3072)::"s"(m), "s"(sb), "v"(v0), "v"(v1), "v"(v2), "v"(v3) : "memory");
}
DI void st16(rsrc_t r, unsigned off, u32x4 v) { __builtin_amdgcn_raw_buffer_store_b128(v, r, off, 0, 16); }
DI void st8(rsrc_t r, unsigned off, u32x2 v) { __builtin_amdgcn_raw_buffer_store_b64(v, r, off, 0, 16); }
DI void st4(rsrc_t r, unsigned off, unsigned v) { __builtin_amdgcn_raw_buffer_store_b32(v, r, off, 0, 16); }
DI u32x4 ld16(rsrc_t r, unsigned off) { return __builtin_amdgcn_raw_buffer_load_b128(r, off, 0, 16); }
DI u32x2 ld8(rsrc_t r, unsigned off) { return __builtin_amdgcn_raw_buffer_load_b64(r, off, 0, 16); }
DI u32x4 ld16s(rsrc_t r, unsigned voff, unsigned soff) { return __builtin_amdgcn_raw_buffer_load_b128(r, voff, soff, 16); }
DI void ldsw16(lds_ptr p, u32x4 v) { *(LAS u32x4*)p = v; }
DI f32x2 ld8_async(unsigned voff, const char* sbase) {
  f32x2 v; asm volatile("s_nop 4\n\tglobal_load_dwordx2 %0, %1, %2 sc1" : "=v"(v) : "v"(voff), "s"(sbase) : "memory"); return v;
}
DI u32x4 pair32(unsigned a0, unsigned a1, unsigned b0, unsigned b1) {
  auto x = __builtin_amdgcn_permlane32_swap(a0, b0, false, false);
  auto y = __builtin_amdgcn_permlane32_swap(a1, b1, false, false);
  return u32x4{x[0], y[0], x[1], y[1]};
}
template <int N> DI float ror16(float v) { return __int_as_float(__builtin_amdgcn_update_dpp(0, __float_as_int(v), 0x120 + N, 0xf, 0xf, false)); }
DI float sum32(float v) {
  v += ror16<8>(v); v += ror16<4>(v); v += ror16<2>(v); v += ror16<1>(v);
  auto a = __builtin_amdgcn_permlane16_swap(__float_as_uint(v), __float_as_uint(v), false, false);
  return __uint_as_float(a[0]) + __uint_as_float(a[1]);
}
DI u32x4 pair16(unsigned a0, unsigned a1, unsigned b0, unsigned b1) {
  auto x = __builtin_amdgcn_permlane16_swap(a0, b0, false, false);
  auto y = __builtin_amdgcn_permlane16_swap(a1, b1, false, false);
  return u32x4{x[0], y[0], x[1], y[1]};
}

DI float rows4(float v) {
  auto a = __builtin_amdgcn_permlane16_swap(__float_as_uint(v), __float_as_uint(v), false, false);
  v = __uint_as_float(a[0]) + __uint_as_float(a[1]);
  auto b = __builtin_amdgcn_permlane32_swap(__float_as_uint(v), __float_as_uint(v), false, false);
  return __uint_as_float(b[0]) + __uint_as_float(b[1]);
}

#define STAMP(id)
#define STAMPC(id)
#define STAMPL(sl, id)
#define SLOT(n)

#define POLL_DEPTH 1
#define POLL_GATE 1
#define POLL_SLEEP 1
#define ATT_PRIO 3
#define ATT_ORD 0
#define WO_AH 6
#define DN_AH 8
#define ATT_ROT 1
#define GU_SKIP 1
#define ATT_LATE 4
#define GU16 1
#define QKV16 1
#define LD_FIRST 1
#define LAG0 2
#define PF_KB 56
#define PROJ2 0
#define PRI_ALL 1
#define RG 0
#define Q0_D0 2
#define PACE 16
#define SPANST(id)
struct Ctx {
  char* ws; rsrc_t r; lds_ptr lds; u64 tgt; int lane, xcd, j;
};
DI Ctx fresh(const Ctx& c0) {
  Ctx c = c0;
  asm volatile("v_mbcnt_lo_u32_b32 %0, -1, 0\n\tv_mbcnt_hi_u32_b32 %0, -1, %0" : "=v"(c.lane), "+s"(c.xcd), "+s"(c.j), "+s"(c.ws));
  return c;
}
DI int opq(int v) { asm volatile("" : "+s"(v)); return v; }
constexpr int SPIN_CAP = 1 << 18;

DI u64 epoch(const char* ws) {
  u64 e; asm volatile("s_nop 4\n\ts_load_dwordx2 %0, %1, 0x0\n\ts_waitcnt lgkmcnt(0)" : "=s"(e) : "s"(ws + WS_EPOCH) : "memory"); return e + 1;
}
DI unsigned flg(int arr, int unit) { return WS_FLAG + (arr * 256 + unit) * 128; }
DI void publish(const Ctx& c, int cnt_off, unsigned n, unsigned to, bool mark = false) {
  unsigned one = 1, iss = M_ISS, cnt = cnt_off, old, so;
  u64 tgt = c.tgt, ex;
  asm volatile("" : "+v"(one), "+v"(iss), "+v"(cnt), "+v"(to), "+v"(tgt));
  if (mark) asm volatile("s_mov_b64 %0, exec\n\ts_mov_b64 exec, 1\n\tds_add_u32 %1, %2\n\ts_mov_b64 exec, %0" : "=&s"(ex) : "v"(iss), "v"(one) : "memory");
  asm volatile("s_mov_b64 %2, exec\n\ts_waitcnt vmcnt(0)\n\ts_mov_b64 exec, 1\n\tds_add_rtn_u32 %0, %3, %4\n\ts_waitcnt lgkmcnt(0)\n\t"
               "v_readfirstlane_b32 %1, %0\n\ts_mov_b64 exec, %2" : "=&v"(old), "=&s"(so), "=&s"(ex) : "v"(cnt), "v"(one) : "memory");
  if (so % n == n - 1)
    asm volatile("s_mov_b64 %0, exec\n\ts_mov_b64 exec, 1\n\ts_nop 2\n\tglobal_store_dwordx2 %1, %2, %3 sc1\n\ts_mov_b64 exec, %0"
                 : "=&s"(ex) : "v"(to), "v"(tgt), "s"(c.ws) : "memory");
}
template <int N> struct Poll {
  u64 v[POLL_DEPTH][N];
  DI void run(const Ctx& c, unsigned o0, unsigned o1 = 0) {

    auto ld = [&](int r) {
      asm volatile("global_load_dwordx2 %0, %1, %2 sc1" : "=v"(v[r][0]) : "v"(o0), "s"(c.ws) : "memory");
      if constexpr (N == 2) asm volatile("global_load_dwordx2 %0, %1, %2 sc1" : "=v"(v[r][N - 1]) : "v"(o1), "s"(c.ws) : "memory");
      __builtin_amdgcn_s_sleep(POLL_SLEEP);
    };
    auto ok = [&](int r) {
      if constexpr (N == 2) asm volatile("s_waitcnt vmcnt(%2)" : "+v"(v[r][0]), "+v"(v[r][N - 1]) : "n"((POLL_DEPTH - 1) * N) : "memory");
      else asm volatile("s_waitcnt vmcnt(%1)" : "+v"(v[r][0]) : "n"(POLL_DEPTH - 1) : "memory");
      return __all(v[r][0] == c.tgt && v[r][N - 1] == c.tgt);
    };
    asm volatile("s_nop 4" ::: "memory");
    ld(0); if (POLL_DEPTH > 1) ld(1);
#pragma unroll 1
    for (int spin = 0; spin < SPIN_CAP; ++spin) {
      if (ok(0)) break; ld(0);
      if (POLL_DEPTH > 1) { if (ok(1)) break; ld(1); }
    }
  }
  DI void keep() {
#pragma unroll
    for (int r = 0; r < POLL_DEPTH; ++r) { asm volatile("" : "+v"(v[r][0])); if constexpr (N == 2) asm volatile("" : "+v"(v[r][N - 1])); }
  }
};
DI void poll(const Ctx& c, unsigned slot) { Poll<1> p; p.run(c, slot); wait_vm<0>(); p.keep(); }
DI void sig_set(const Ctx& c, unsigned v) { if (c.lane == 0) __hip_atomic_store((LAS unsigned*)(c.lds + M_SIG), v, __ATOMIC_RELAXED, __HIP_MEMORY_SCOPE_WORKGROUP); }
DI void sig_wait(const Ctx& c, unsigned v, int off = M_SIG, bool calm = false) {
#pragma unroll 1
  for (int spin = 0; spin < SPIN_CAP * 16; ++spin) {
    if (__builtin_amdgcn_readfirstlane(__hip_atomic_load((LAS unsigned*)(c.lds + off), __ATOMIC_RELAXED, __HIP_MEMORY_SCOPE_WORKGROUP)) >= v) break;
    if (calm) __builtin_amdgcn_s_sleep(8); else if (off != M_SIG) __builtin_amdgcn_s_sleep(1);
  }
  asm volatile("" ::: "memory");
}

DI void gate(const Ctx& c, Poll<1>& p, int half, unsigned slot, unsigned sig, unsigned iss) {
  if (half == 0) { if (POLL_GATE) sig_wait(c, iss, M_ISS); p.run(c, slot); sig_set(c, sig); STAMPL(sig - 1, 6); } else sig_wait(c, sig);
}

template <int PP, int NSLOT, int NS, int LAG = 1>
struct Ring {
  unsigned voff[PP];
  const char* base;
  unsigned adv, l0;
  int slot;
  DI void init(const char* b, unsigned a, unsigned lds0, int half) {
    base = b - 4096; adv = a; l0 = lds0 + half * PP * 1024; slot = 0;
#pragma unroll
    for (int p = 0; p < PP; ++p) voff[p] += 4096 - (p & 3) * 1024;
  }
  DI void issue() {
    const unsigned m = l0 + slot * (2 * PP * 1024);
    glds4(m, base, voff[0], voff[1], voff[2], voff[3]);
    if constexpr (PP == 5) glds1(m + 4096, base, voff[4]);
    if constexpr (PP == 7) glds3(m + 4096, base, voff[4], voff[5], voff[6]);
    if constexpr (PP == 8) glds4(m + 4096, base, voff[4], voff[5], voff[6], voff[7]);
    base += adv; slot = slot == NSLOT - 1 ? 0 : slot + 1;
  }
  template <int N = NSLOT> DI void fill() {
#pragma unroll 1
    for (int s = 0; s < N; ++s) issue();
  }
  template <int NEED> DI void fill_ahead(const Ctx& c, unsigned sig) {
    int n = 0;
#pragma unroll 1
    while (n < NSLOT) {
      issue(); wait_vm<PACE>(); ++n;
      if (n * PP >= NEED * PP + PACE &&
          __builtin_amdgcn_readfirstlane(__hip_atomic_load((LAS unsigned*)(c.lds + M_SIG), __ATOMIC_RELAXED, __HIP_MEMORY_SCOPE_WORKGROUP)) >= sig) break;
    }
    if (n == NSLOT) wait_vm<(NSLOT - NEED) * PP>();
    BARRIER_L(); WRESET();
#pragma unroll 1
    for (; n < NSLOT; ++n) issue();
  }
  DI void rest() { range<NSLOT - 2, NSLOT - LAG>(); steady(); range<NSLOT - 1 - LAG, 0>(); BARRIER_L(); }
  template <int R, int E> DI void range() { if constexpr (R >= E) { wait_vm<R * PP>(); BARRIER_L(); range<R - 1, E>(); } }
  template <int D0 = NSLOT> DI void head() {
    wait_vm<(D0 - 1) * PP>(); BARRIER_L(); WRESET();
    if constexpr (D0 < NSLOT) fill<NSLOT - D0>();
    range<NSLOT - 2, NSLOT - LAG>();
    steady();
  }
  DI void steady() {
#pragma unroll 1
    for (int k = LAG; k < NS - NSLOT + LAG; ++k) { wait_vm<(NSLOT - 1 - LAG) * PP>(); BARRIER_L(); issue(); }
  }
  template <int D0 = NSLOT> DI void run() { head<D0>(); range<NSLOT - 1 - LAG, 0>(); BARRIER_L(); }
};

DI void ln_load(const Ctx& c, unsigned off, int row, f32x2 (&p)[8]) {
#pragma unroll
  for (int i = 0; i < 8; ++i) p[i] = __builtin_bit_cast(f32x2, __builtin_amdgcn_raw_buffer_load_b64(c.r, row * 8, off + i * 1024, 16));
}
DI f32x2 ln_sum(const f32x2 (&p)[8]) {
  f32x2 t = ((p[0] + p[1]) + (p[2] + p[3])) + ((p[4] + p[5]) + (p[6] + p[7]));
  const float m8 = t[0] * 0.125f; float q0 = 0.f, q1 = 0.f;
#pragma unroll
  for (int i = 0; i < 8; i += 2) { const float a = p[i][0] - m8, b = p[i + 1][0] - m8; q0 += a * a; q1 += b * b; }
  t[1] += (q0 + q1) * (1.f / 80.f);
  PIN(t);
  return t;
}
DI void ln_put(const Ctx& c, int row, f32x2 t) {
  *(LAS float*)(c.lds + M_SST + row * 4) = __builtin_amdgcn_rsqf(t[1] * (1.f / 640.f) + 1e-5f);
  *(LAS float*)(c.lds + M_SST + 512 + row * 4) = -t[0] * (1.f / 640.f);
}
template <int NP> DI void ln_cols_sum(const Ctx& c, int n4, const u32x4 (&q)[NP]) {
  f32x4 cs = __builtin_bit_cast(f32x4, q[0]);
#pragma unroll
  for (int i = 1; i < NP; ++i) cs += __builtin_bit_cast(f32x4, q[i]);
  if (c.lane < n4) *(LAS f32x4*)(c.lds + M_SCS + c.lane * 16) = cs;
}
template <int NP> DI void ln_cols(const Ctx& c, int n4, unsigned off, unsigned pitch) {
  u32x4 q[NP];
#pragma unroll
  for (int i = 0; i < NP; ++i) q[i] = ld16(c.r, off + i * pitch + (c.lane < n4 ? c.lane : 0) * 16);
  ln_cols_sum<NP>(c, n4, q);
}

DI int row_group(const Ctx& c) { return RG == 0 ? c.xcd & 3 : RG == 1 ? ((c.xcd & 1) ^ 1) * 2 + ((c.xcd >> 1) & 1) : (c.xcd & 1) * 2 + ((c.xcd >> 1) & 1); }
DI int qkv_panel(const Ctx& c) { return row_group(c) * 4 + (c.j & 3); }
DI int qkv_cb(const Ctx& c) { return (c.xcd >> 2) * 8 + (c.j >> 2); }
DI int pr_panel(const Ctx& c) { return row_group(c) * 8 + (c.j & 7); }
DI int pr_cb(const Ctx& c) { return (c.xcd >> 2) * 4 + (c.j >> 3); }
DI int gu_panel(const Ctx& c) { return (c.xcd & 1) * 8 + (c.j & 7); }
DI int gu_cb(const Ctx& c) { return (c.xcd >> 1) * 4 + (c.j >> 3); }
DI int att_head(const Ctx& c) { return c.xcd * 4 + (c.j & 3); }
DI int att_qb(const Ctx& c) { return c.j >> 2; }

constexpr int P0_SCR = QKV_A + QKV_NA * 16384;
static_assert(P0_SCR + 10240 <= MISC, "lds");
struct Phase0 {
  u32x4 v[3]; float sum, m2;
  DI void load(const Ctx& c, int hw, const uint16_t* x, const uint16_t* wqkv) {
    const int lane = c.lane, r32 = lane & 31, b = blockIdx.x, row = b * 8 + hw * 2 + (lane >> 5);
    v[0] = *(const u32x4*)(x + row * 640 + r32 * 8); v[1] = *(const u32x4*)(x + row * 640 + 256 + r32 * 8);
    v[2] = *(const u32x4*)(x + row * 640 + 512 + (r32 & 15) * 8);
    if (b < 240) {
      const char* src = (const char*)wqkv + ((b / 30) * 80 + hw * 8) * 3840 + (b % 30) * 128;
      const unsigned vo = (lane >> 3) * 3840 + (lane & 7) * 16;
      glds1(P0_SCR + hw * 1024, src, vo); glds1(P0_SCR + 4096 + hw * 1024, src + 32 * 3840, vo);
      if (hw < 2) glds1(P0_SCR + 8192 + hw * 1024, src + 64 * 3840, vo);
    }
  }
  DI float dev2(int q) const {
    const float mean = sum * (1.f / 640.f); float a = 0.f, b = 0.f;
#pragma unroll
    for (int e = 0; e < 4; ++e) { const float d0 = bflo(v[q][e]) - mean, d1 = bfhi(v[q][e]) - mean; a += d0 * d0; b += d1 * d1; }
    return a + b;
  }
  DI void part1(const Ctx& c) {
    unsigned uo = 0x3f803f80u; PIN(uo);
    const bf16x2 ones = __builtin_bit_cast(bf16x2, uo);
    float s[3];
#pragma unroll
    for (int q = 0; q < 3; ++q) {
      s[q] = 0.f;
#pragma unroll
      for (int e = 0; e < 4; ++e) { const unsigned u = v[q][e]; s[q] = __builtin_amdgcn_fdot2_f32_bf16(__builtin_bit_cast(bf16x2, u), ones, s[q], false); }
    }
    sum = sum32(s[0] + s[1] + ((c.lane & 16) ? 0.f : s[2]));
    m2 = dev2(0);
    asm volatile("" : "+v"(sum), "+v"(m2));
    wait_vm<0>();
  }
  DI void part2(const Ctx& c, int hw) {
    const int lane = c.lane, r32 = lane & 31, b = blockIdx.x, row = b * 8 + hw * 2 + (lane >> 5);
    const float t = dev2(2);
    m2 = sum32(m2 + dev2(1) + ((lane & 16) ? 0.f : t));
    if (r32 < 8) st8(c.r, WS_ST + (row >> 7) * 8192 + r32 * 1024 + (row & 127) * 8, u32x2{__float_as_uint(sum * 0.125f), __float_as_uint(m2 * 0.125f)});
  }
  DI void cols(const Ctx& c, int hw) {
    const int i = c.lane & 15, g = c.lane >> 4, b = blockIdx.x;
    if (b < 240) {
      const unsigned o = 0x3f803f80u, z = g < 2 ? o : 0u;
      lds_ptr p = c.lds + P0_SCR + (g * 8 + (i >> 2)) * 128 + hw * 32 + (i & 3) * 8;
      f32x4 cs = mfma16(ldstr(p, 512), __builtin_bit_cast(bf16x8, u32x4{o, o, o, o}), f32x4{});
      cs = mfma16(ldstr(p + 4096, 512), __builtin_bit_cast(bf16x8, u32x4{o, o, o, o}), cs);
      cs = mfma16(ldstr(p + (g < 2 ? 8192 : 0), 512), __builtin_bit_cast(bf16x8, u32x4{z, z, z, z}), cs);
      if (i == 0) st16(c.r, WS_CSQ + ((b / 30) * 1920 + (b % 30) * 64 + hw * 16 + g * 4) * 4, __builtin_bit_cast(u32x4, cs));
    }
  }
};

template <bool SWAP, class PRE, class MID>
DI void qkv_loop(const Ctx& c, int w, int layer, f32x4 (&acc)[4][4], PRE pre, MID mid) {
  const int lane = c.lane, i = lane & 15, g = lane >> 4, wm = w & 1, wn = w >> 1;
  const int rm = (i >> 3) * 1024 + (i & 7) * 128 + ((g ^ ((i >> 1) & 7)) << 4);
  lds_ptr pa0 = c.lds + QKV_A + wm * 8192 + (layer == 0 ? rm : lane * 16);
  lds_ptr pa1 = c.lds + QKV_A + wm * 8192 + (layer == 0 ? rm ^ 64 : lane * 16 + 1024);
  const int f = (i >> 2) | ((g & 1) << 2), rb = (g * 8 + (i >> 2)) * 256 + (i & 3) * 8;
  lds_ptr pb[4];
#pragma unroll
  for (int t = 0; t < 4; ++t) pb[t] = c.lds + rb + (((wn * 4 + t) ^ f) << 5);
  bf16x8 x0[4], x1[4], w0[4], w1[4];
  auto step = [&](const bf16x8* xa, const bf16x8* wb, bf16x8* xn, bf16x8* wn_, lds_ptr pa, int bo, bool ld) {
#pragma unroll
    for (int q = 0; q < 16; ++q) {
      if (ld) {
        if ((q & 3) == 0) wn_[q >> 2] = ldstr(pb[q >> 2] + bo, 1024);
        if ((q & 3) == 2) xn[q >> 2] = ldsr16(pa + (q >> 2) * 2048);
      }
      const int t = q >> 2, m = q & 3;
      acc[m][t] = SWAP ? mfma16(wb[t], xa[m], acc[m][t]) : mfma16(xa[m], wb[t], acc[m][t]);
      SBAR();
    }
  };
  SLOT(layer * 5);
  if (layer) STAMP(0);
  mid(0); mid(4); mid(8); mid(12);
  STAMP(1);
  BARRIER();
  STAMP(2); STAMPC(0); WRESET();
#pragma unroll
  for (int e = 0; e < 4; ++e) { w0[e] = ldstr(pb[e], 1024); x0[e] = ldsr16(pa0 + e * 2048); }
  int sa = 0, sb = 0;
#pragma unroll 1
  for (int st = 0; st < 9; ++st) {
    step(x0, w0, x1, w1, pa1, 8192, true);
    const int da = sa == QKV_NA - 1 ? -(QKV_NA - 1) * 16384 : 16384, db = sb == QKV_NB - 1 ? -(QKV_NB - 1) * 16384 : 16384;
    sa = sa == QKV_NA - 1 ? 0 : sa + 1; sb = sb == QKV_NB - 1 ? 0 : sb + 1;
    pa0 += da; pa1 += da; pb[0] += db; pb[1] += db; pb[2] += db; pb[3] += db;
    if (LAG0 == 1 && layer == 0) asm volatile("s_waitcnt lgkmcnt(0)" ::: "memory");
    BARRIER_NW();
    step(x1, w1, x0, w0, pa0, 0, true);
  }
  step(x0, w0, x1, w1, pa1, 8192, true);
  BARRIER();
  pre(); SBAR();
  step(x1, w1, x0, w0, pa0, 0, false);
  STAMP(3); STAMPC(1);
}
DI unsigned qkv_w_off(const Ctx& c, int q) {
  const int row = q * 4 + (c.lane >> 4);
  return row * 3840 + qkv_cb(c) * 256 + (((c.lane & 15) ^ (QKV16 ? ((row & 3) << 1) | (row & 8) : (row & 3) << 2)) << 4);
}
DI void qkv_helper(Ctx c, u64& tgt, int hw, int layer, const uint16_t* x, const uint16_t* wqkv) {
  const int panel = qkv_panel(c), cb = qkv_cb(c), l = c.lane;
  const bool on = cb != 15;
  if (layer == 0) {
    Phase0 p0;
    p0.load(c, hw, x, wqkv);
    if (on) BARRIER_L();
    p0.part1(c);
    BARRIER_L();
    p0.part2(c, hw);
    if (on) BARRIER_L();
    p0.cols(c, hw);
    c.tgt = tgt = epoch(c.ws);
    if (hw == 0 && l == 0) *(LAS u64*)(c.lds + M_TGT) = tgt;
    if (on) BARRIER_L();
    publish(c, M_CNT2, 4, flg(FL_P0, blockIdx.x));
    if (!on) return;
    BARRIER_L(); BARRIER_L(); BARRIER_L();
    const unsigned at = flg(FL_P0, hw < 2 ? panel * 16 + hw * 8 + (l & 7) : (l & 7) * 30 + cb * 2 + ((l >> 3) & 1));
    const u64 v = __hip_atomic_load((const GAS u64*)(c.ws + at), __ATOMIC_RELAXED, __HIP_MEMORY_SCOPE_AGENT);
    BARRIER_L();
    if (!__all(v == c.tgt)) poll(c, at);
    if (hw < 2) {
      f32x2 p[8];
      ln_load(c, WS_ST + panel * 8192, hw * 64 + l, p);
      BARRIER_L(); BARRIER_L();
      ln_put(c, hw * 64 + l, ln_sum(p));
    } else if (hw == 2) {
      u32x4 q[8];
#pragma unroll
      for (int i = 0; i < 8; ++i) q[i] = ld16(c.r, WS_CSQ + cb * 512 + i * 7680 + (l & 31) * 16);
      BARRIER_L(); BARRIER_L();
      ln_cols_sum<8>(c, 32, q);
    } else { BARRIER_L(); BARRIER_L(); }
    BARRIER_L();
  } else if (on) {
    if (hw == 2) ln_cols<8>(c, 32, WS_CSQ + cb * 512, 7680);
    BARRIER_L(); BARRIER_L(); BARRIER_L(); BARRIER_L(); BARRIER_L(); BARRIER_L();
    if (hw < 2) {
      f32x2 p[8];
      ln_load(c, WS_ST + 2 * STB + panel * 8192, hw * 64 + l, p);
      BARRIER_L(); BARRIER_L();
      ln_put(c, hw * 64 + l, ln_sum(p));
    } else { BARRIER_L(); BARRIER_L(); }
    BARRIER_L(); BARRIER_L(); BARRIER_L();
  }
}
DI void qkv_compute(Ctx c, u64& tgt, int w, int layer, const uint16_t* x, const uint16_t* wqkv) {
  const int panel = qkv_panel(c), cb = qkv_cb(c);
  if (w >= 4) { qkv_helper(c, tgt, w - 4, layer, x, wqkv); return; }
  if (cb == 15) { if (layer == 0) { tgt = epoch(c.ws); wait_vm<0>(); BARRIER(); } return; }
  const int lane = c.lane, i = lane & 15, g = lane >> 4, wm = w & 1, wn = w >> 1;
  const unsigned L = WS_L + layer * L_SZ;
  f32x4 acc[4][4];
#pragma unroll
  for (int e = 0; e < 16; ++e) acc[e >> 2][e & 3] = f32x4{0.f, 0.f, 0.f, 0.f};
  const LAS float* srs = (const LAS float*)(c.lds + M_SST) + wm * 64;
  const LAS float* scs = (const LAS float*)(c.lds + M_SCS) + wn * 64;
  if (cb < 10) {
    float rs[4], nm[4]; f32x4 cs[4]; unsigned ad[4][2];
    const float qs = cb < 5 ? 0.22360679774997896f * 1.4426950408889634f : 1.f;
    const unsigned i1 = L + (cb < 5 ? L_Q1 : L_K1), i2 = L + (cb < 5 ? L_Q2 : L_K2);
    qkv_loop<true>(c, w, layer, acc, [&]() {
#pragma unroll
      for (int e = 0; e < 4; ++e) { rs[e] = srs[e * 16 + i]; nm[e] = srs[128 + e * 16 + i]; cs[e] = *(const LAS f32x4*)(scs + e * 16 + g * 4); }
    }, [&](int q) {
      const int t = q >> 2, o = (cb % 5) * 16 + wn * 8 + t * 2 + (g >> 1), hp = o / 5, pos = o - hp * 5;
      const unsigned ts = pos == 2 ? 512 : 1024;
      ad[t][0] = (pos == 2 ? i2 + hp * 32768 : i1 + (2 * hp + (pos > 2)) * 65536 + (pos > 2 ? pos - 3 : pos) * 512) +
                 (panel * 4 + wm * 2) * ts + ((g & 1) * 16 + i) * 16;
      ad[t][1] = ad[t][0] + ts;
      asm volatile("" : "+v"(ad[t][0]), "+v"(ad[t][1]));
    });
#pragma unroll
    for (int e = 0; e < 4; ++e) rs[e] *= qs;
#pragma unroll
    for (int t = 0; t < 4; ++t) {
#pragma unroll
      for (int mp = 0; mp < 2; ++mp) {
        const f32x4 u = (acc[mp * 2][t] + cs[t] * nm[mp * 2]) * rs[mp * 2], v = (acc[mp * 2 + 1][t] + cs[t] * nm[mp * 2 + 1]) * rs[mp * 2 + 1];
        st16(c.r, ad[t][mp], pair16(pk2(u[0], u[1]), pk2(u[2], u[3]), pk2(v[0], v[1]), pk2(v[2], v[3])));
      }
    }
  } else {
    float cs[4]; f32x4 rs[4], nm[4]; unsigned ad[4];
    qkv_loop<false>(c, w, layer, acc, [&]() {
#pragma unroll
      for (int e = 0; e < 4; ++e) { cs[e] = scs[e * 16 + i]; rs[e] = *(const LAS f32x4*)(srs + e * 16 + g * 4); nm[e] = *(const LAS f32x4*)(srs + 128 + e * 16 + g * 4); }
    }, [&](int q) {
      const int t = q >> 2, cv = (cb - 10) * 128 + wn * 64 + t * 16 + i, head = cv / 20, dim = cv - head * 20;
      const int d = (head & 1) ? (dim < 4 ? dim + 16 : dim - 4) : dim;
      ad[t] = L + L_V + head * 81920 + (panel * 8 + wm * 4 + (g & 1)) * 640 + (g >> 1) * 320 + d * 16;
      PIN(ad[t]);
    });
#pragma unroll
    for (int t = 0; t < 4; ++t) {
      const unsigned a0 = ad[t];
#pragma unroll
      for (int mp = 0; mp < 2; ++mp) {
        const f32x4 u = (acc[mp * 2][t] + nm[mp * 2] * cs[t]) * rs[mp * 2], v = (acc[mp * 2 + 1][t] + nm[mp * 2 + 1] * cs[t]) * rs[mp * 2 + 1];
        st16(c.r, a0 + mp * 1280, pair16(pk2(u[0], u[1]), pk2(u[2], u[3]), pk2(v[0], v[1]), pk2(v[2], v[3])));
      }
    }
  }
  if (layer == 0) {
    const u32x2 t = *(const LAS u32x2*)(c.lds + M_TGT);
    c.tgt = tgt = (u64)(unsigned)__builtin_amdgcn_readfirstlane(t[1]) << 32 | (unsigned)__builtin_amdgcn_readfirstlane(t[0]);
  }
  SLOT(layer * 5);
  STAMP(4);
  publish(c, M_CNT4, 4, flg(FL_QKV + layer * 5, panel * 16 + cb), true);
  STAMP(5);
}
typedef Ring<8, QKV_NB, 10, 2> QkvB;
DI void qkv_load0(const Ctx& c, int lw, const uint16_t* x, const uint16_t* wqkv) {
  const int half = lw & 1;
  if (lw >= 2) {
    Ring<8, QKV_NB, 10, LAG0> rg;
#pragma unroll
    for (int p = 0; p < 8; ++p) rg.voff[p] = qkv_w_off(c, half * 8 + p);
    rg.init((const char*)wqkv, 64 * 3840, 0, half);
    rg.template fill<Q0_D0>(); rg.template run<Q0_D0>();
  } else {
    Ring<8, QKV_NA, 10, LAG0> rg;
#pragma unroll
    for (int p = 0; p < 8; ++p) {
      const int mt = qkv_panel(c) * 8 + half * 4 + (p >> 1), r16 = (p & 1) * 8 + (c.lane >> 3);
      rg.voff[p] = (mt * 16 + r16) * 1280 + (((c.lane & 7) ^ ((r16 >> 1) & 7)) << 4);
    }
    rg.init((const char*)x, 128, QKV_A, half);
    rg.template fill<Q0_D0>(); rg.template run<Q0_D0>();
  }
}
DI void qkv_load1(const Ctx& c, int lw, const uint16_t* wqkv) {
  const int half = lw & 1;
  static_assert(QKV_NA == 4 && QKV_NB == 5, "schedule");
  if (lw >= 2) {
    QkvB rg;
#pragma unroll
    for (int p = 0; p < 8; ++p) rg.voff[p] = qkv_w_off(c, half * 8 + p);
    rg.init((const char*)wqkv, 64 * 3840, 0, half);
    sig_wait(c, 12, M_CNT4);
    rg.template fill_ahead<1>(c, 6);
    rg.rest();
  } else {
    Ring<8, QKV_NA, 10, 2> rg;
#pragma unroll
    for (int p = 0; p < 8; ++p) rg.voff[p] = WS_X2 + (qkv_panel(c) * 8 + half * 4 + (p >> 1)) * 20480 + (p & 1) * 1024 + c.lane * 16;
    rg.init(c.ws, 2048, QKV_A, half);
    Poll<1> pl;
    gate(c, pl, half, flg(FL_DN, qkv_panel(c) * 16 + (c.lane & 15)), 6, 28);
    rg.template fill<2>(); rg.template run<2>();
    pl.keep();
  }
}

DI f32x16 qk8(s16x4 k2, s16x4 q2) {
 return __builtin_amdgcn_mfma_f32_32x32x8bf16_1k(k2, q2, f32x16{}, 0, 0, 0); }
DI f32x16 qk16(bf16x8 k1, bf16x8 q1, f32x16 s) { return __builtin_amdgcn_mfma_f32_32x32x16_bf16(k1, q1, s, 0, 0, 0); }
DI f32x16 pv_lo(bf16x8 v, f32x16 p, f32x16 o) {
  return __builtin_amdgcn_smfmac_f32_32x32x32_bf16(v, __builtin_bit_cast(bf16x16, (f32x8)__builtin_shufflevector(p, p, 0, 1, 2, 3, 4, 5, 6, 7)), o, 0xDDDD, 0, 0);
}
DI f32x16 pv_hi(bf16x8 v, f32x16 p, f32x16 o) {
  return __builtin_amdgcn_smfmac_f32_32x32x32_bf16(v, __builtin_bit_cast(bf16x16, (f32x8)__builtin_shufflevector(p, p, 8, 9, 10, 11, 12, 13, 14, 15)), o, 0xDDDD, 0, 0);
}
DI float swap32(float v, int h) {
  auto r = __builtin_amdgcn_permlane32_swap(__float_as_uint(v), __float_as_uint(v), false, false);
  return __uint_as_float(h ? r[0] : r[1]);
}
#define EXP4(X, i) do { X[i] = __builtin_amdgcn_exp2f(X[i]); X[i + 1] = __builtin_amdgcn_exp2f(X[i + 1]); X[i + 2] = __builtin_amdgcn_exp2f(X[i + 2]); \
                        X[i + 3] = __builtin_amdgcn_exp2f(X[i + 3]); PIN(X); SBAR(); } while (0)
DI void att_compute(const Ctx& c, int w, int layer) {
  const int lane = c.lane, head = att_head(c), qb = att_qb(c);
  const int qp = w & 3, kp = w >> 2, r32 = lane & 31, h = lane >> 5;
  lds_ptr lds = c.lds;
  const unsigned L = WS_L + layer * L_SZ;
  SLOT(layer * 5 + 1);
  STAMP(0);
  for (int s = w * 64 + lane; s < 672; s += 512) {
    const unsigned v = s % 20 == 0 ? 0x3f803f80u : 0u;
    *(LAS u32x4*)(lds + AT_CONST + s * 16) = u32x4{v, v, v, v};
  }
  lds_ptr pk1 = lds + kp * 8192 + lane * 16;
  lds_ptr pk2_ = h ? lds + AT_CONST + 16 : lds + 16384 + kp * 4096 + r32 * 16 + (head & 1) * 8;
  lds_ptr pv = (r32 >= 20 ? lds + AT_CONST : lds + 24576 + kp * 10240) + h * 320 + r32 * 16;
  const int vmask = r32 >= 20 ? 0 : -1, kmask = h ? 0 : -1;
  const int qt = qb * 8 + qp * 2 + kp, tok = qt * 32 + r32;
  unsigned ob16 = L + L_O + (tok >> 4) * 20480 + (head * 2 + h) * 256 + (tok & 15) * 16, ob8 = L + L_O + (tok >> 4) * 20480 + 16384 + head * 128 + (tok & 15) * 8;
  asm volatile("" : "+v"(ob16), "+v"(ob8));
  f32x16 o0 = f32x16{}, o1 = f32x16{};
  f32x16 s0, s1, s2, s3 = f32x16{};
  STAMP(1);
  BARRIER();
  STAMP(2); STAMPC(0); WRESET();
  const bf16x8 q1a = ldsr16(lds + 2 * AT_SLOT + (qp * 2 + kp) * 1024 + lane * 16), q1b = ldsr16(lds + 2 * AT_SLOT + (qp * 2 + 1 - kp) * 1024 + lane * 16);
  lds_ptr pq2 = h ? lds + AT_CONST + 16 : lds + 2 * AT_SLOT + 8192 + qp * 1024 + r32 * 16 + (head & 1) * 8;
  const s16x4 q2a = *(const LAS s16x4*)(pq2 + kp * 512), q2b = *(const LAS s16x4*)(pq2 + 512 - kp * 512);
  bf16x8 k1 = ldsr16(pk1), vlo = ldsr16(pv), vhi = bf16x8{};
  s16x4 k2 = *(const LAS s16x4*)pk2_;
  s0 = qk16(k1, q1a, qk8(k2, q2a));
#define PH_EVEN(SC, SN, SP, KLOAD, VOFF) do { \
    SN = qk8(k2, q2b); SBAR(); EXP4(SC, 0); SN = qk16(k1, q1b, SN); SBAR(); KLOAD; SBAR(); EXP4(SC, 4); \
    o1 = pv_hi(vhi, SP, o1); SBAR(); vhi = ldsr16(pv + (VOFF) + 640); SBAR(); EXP4(SC, 8); o0 = pv_lo(vlo, SC, o0); SBAR(); EXP4(SC, 12); } while (0)
#define PH_ODD(SC, SN, SP, VLOAD) do { \
    SN = qk8(k2, q2a); SBAR(); EXP4(SC, 0); SN = qk16(k1, q1a, SN); SBAR(); EXP4(SC, 4); \
    o0 = pv_hi(vhi, SP, o0); SBAR(); EXP4(SC, 8); o1 = pv_lo(vlo, SC, o1); SBAR(); VLOAD; SBAR(); EXP4(SC, 12); } while (0)
#define KLD(t) do { k1 = ldsr16(pk1 + (t) * 1024); k2 = *(const LAS s16x4*)(pk2_ + (t) * 512); } while (0)
#define PRI(v) do { __builtin_amdgcn_s_setprio(v); SBAR(); } while (0)
#define HI(n) if (ATT_PRIO == (n)) PRI(1);
#define LO(n) if (ATT_PRIO == (n)) PRI(0);
#define TWO(t) HI(3) HI(4) HI(5) PH_EVEN(s0, s1, s3, KLD((t) + 1), (t) * 1280); PH_ODD(s1, s2, s0, vlo = ldsr16(pv + ((t) + 1) * 1280)); LO(4)
  for (int grp = 0; grp < 4; ++grp) {
    TWO(0) PH_EVEN(s2, s3, s1, if (grp == 0) BARRIER_NW(); KLD(2), 1280); LO(3) PH_ODD(s3, s0, s2, vlo = ldsr16(pv + 2 * 1280));
    TWO(2) PH_EVEN(s2, s3, s1, KLD(4), 3 * 1280); LO(3) LO(5) PH_ODD(s3, s0, s2, vlo = ldsr16(pv + 4 * 1280));
    TWO(4) PH_EVEN(s2, s3, s1, KLD(6), 5 * 1280); LO(3) PH_ODD(s3, s0, s2, vlo = ldsr16(pv + 6 * 1280));
    TWO(6)
    const int d = grp == 2 ? -2 * AT_SLOT : grp == 3 ? 0 : AT_SLOT;
    PH_EVEN(s2, s3, s1, pk1 += d; pk2_ += (d & kmask); if (grp != 3) BARRIER(); KLD(0), 7 * 1280);
    LO(3) LO(5) PH_ODD(s3, s0, s2, pv += (d & vmask); vlo = ldsr16(pv));
  }
  o1 = pv_hi(vhi, s3, o1);
  STAMP(3); STAMPC(1);
  __builtin_amdgcn_s_setprio(0);
  const f32x16 give = o1, keep = o0;
#pragma unroll
  for (int e = 0; e < 3; ++e)
    *(LAS f32x4*)(lds + AT_MERGE + (w * 3 + e) * 1024 + lane * 16) = f32x4{give[4 * e], give[4 * e + 1], give[4 * e + 2], give[4 * e + 3]};
  BARRIER();
  f32x4 a[3];
#pragma unroll
  for (int e = 0; e < 3; ++e)
    a[e] = f32x4{keep[4 * e], keep[4 * e + 1], keep[4 * e + 2], keep[4 * e + 3]} + *(const LAS f32x4*)(lds + AT_MERGE + ((w ^ 4) * 3 + e) * 1024 + lane * 16);
  float l = h ? a[2][0] : 0.f;
  l += swap32(l, h);
  if (__builtin_expect(__any(!(l >= 5.421e-20f && l <= 1.8447e19f)), 0)) {
    const unsigned o2 = (head >> 1) * 32768 + r32 * 16 + (head & 1) * 8;
    const bf16x8 fq1 = __builtin_bit_cast(bf16x8, ld16(c.r, L + L_Q1 + head * 65536 + qt * 1024 + lane * 16));
    u32x2 fq2 = ld8(c.r, L + L_Q2 + o2 + qt * 512);
    if (h) fq2 = u32x2{0u, 0u};
    const unsigned vi = L + L_V + head * 81920 + h * 320 + (r32 < 20 ? r32 : 0) * 16;
    f32x16 oo = f32x16{};
    float m = -INFINITY;
    const u32x4 one8 = {0x3f803f80u, 0x3f803f80u, 0x3f803f80u, 0x3f803f80u};
    for (int t = 0; t < 64; ++t) {
      const u32x4 fk1 = ld16(c.r, L + L_K1 + head * 65536 + t * 1024 + lane * 16);
      const u32x2 fk2 = ld8(c.r, L + L_K2 + o2 + t * 512);
      u32x4 v0 = ld16(c.r, vi + t * 1280), v1 = ld16(c.r, vi + t * 1280 + 640);
      if (r32 >= 20) { v0 = one8; v1 = one8; }
      f32x16 s = qk16(__builtin_bit_cast(bf16x8, fk1), fq1, qk8(__builtin_bit_cast(s16x4, fk2), __builtin_bit_cast(s16x4, fq2)));
      float tm = s[0];
#pragma unroll
      for (int r = 1; r < 16; ++r) tm = fmaxf(tm, s[r]);
      tm = fmaxf(tm, swap32(tm, h));
      const float mn = fmaxf(m, tm), al = __builtin_amdgcn_exp2f(m - mn);
      m = mn;
#pragma unroll
      for (int r = 0; r < 16; ++r) { s[r] = __builtin_amdgcn_exp2f(s[r] - mn); oo[r] *= al; }
      oo = pv_hi(__builtin_bit_cast(bf16x8, v1), s, pv_lo(__builtin_bit_cast(bf16x8, v0), s, oo));
    }
#pragma unroll
    for (int e = 0; e < 3; ++e) a[e] = f32x4{oo[4 * e], oo[4 * e + 1], oo[4 * e + 2], oo[4 * e + 3]};
    l = h ? a[2][0] : 0.f;
    l += swap32(l, h);
  }
  const float inv = __builtin_amdgcn_rcpf(l);
  const unsigned A0 = pk2(a[0][0] * inv, a[0][1] * inv), A1 = pk2(a[0][2] * inv, a[0][3] * inv);
  const unsigned B0 = pk2(a[1][0] * inv, a[1][1] * inv), B1 = pk2(a[1][2] * inv, a[1][3] * inv);
  const u32x2 C = {pk2(a[2][0] * inv, a[2][1] * inv), pk2(a[2][2] * inv, a[2][3] * inv)};
  st16(c.r, ob16, pair32(A0, A1, B0, B1));
  if (!h) st8(c.r, ob8, C);
  STAMP(4);
  publish(c, M_CNT, 8, flg(FL_ATT + layer * 5, qb * 32 + head), true);
  STAMP(5);
}
DI void retile_load(const Ctx& c, int J, u32x4 (&v)[5][4], const uint16_t* wo, const uint16_t* wg, const uint16_t* wu, const uint16_t* wd) {
  const int e = J < 216 ? J : J < 256 ? J - 216 : J - 256;
  const int strip = J < 216 ? (e >> 2) % 27 : e % 10, k0 = J < 216 ? (e & 3) * 5 : min((e / 10) * 5, 49), ld = J < 216 ? 3456 : 1280;
  const rsrc_t r = __builtin_amdgcn_make_buffer_rsrc((void*)(J < 108 ? wg : J < 216 ? wu : J < 256 ? wo : wd), 0,
                                                     J < 216 ? 640 * 3456 : J < 256 ? 640 * 1280 : 1728 * 1280, 0x00020000);
  const unsigned vo = (c.lane >> 3) * ld + (c.lane & 7) * 16;
#pragma unroll
  for (int u = 0; u < 5; ++u)
#pragma unroll
    for (int q = 0; q < 4; ++q) {
      const int ch = (k0 + u) * 4 + q;
      const int row = J < 216 || J >= 256 ? ch * 8 : ch < 64 ? (ch >> 1) * 20 + (ch & 2) * 2 + (ch & 1) * 8 : (ch - 64) * 40 + 16;
      v[u][q] = __builtin_amdgcn_raw_buffer_load_b128(r, vo, row * ld + strip * 128, 0);
    }
}
DI void retile_store(const Ctx& c, int J, lds_ptr scr, const u32x4 (&v)[5][4]) {
  const int e = J < 216 ? J : J < 256 ? J - 216 : J - 256, mat = J >= 108;
  const int strip = J < 216 ? (e >> 2) % 27 : e % 10, k0 = J < 216 ? (e & 3) * 5 : min((e / 10) * 5, 49);
  const unsigned ts = J < 216 ? 40960 : J < 256 ? 20480 : 55296;
  const unsigned dst = (J < 216 ? WS_WGU + mat * 20480 : J < 256 ? WS_WO : WS_WD) + strip * 4 * ts + k0 * 1024;
  const int i = c.lane & 15, g = c.lane >> 4;
  const bf16x8 ones = __builtin_bit_cast(bf16x8, u32x4{0x3f803f80u, 0x3f803f80u, 0x3f803f80u, 0x3f803f80u});
  f32x4 cs[4] = {f32x4{}, f32x4{}, f32x4{}, f32x4{}};
#pragma unroll
  for (int u = 0; u < 5; ++u) {
#pragma unroll
    for (int p = 0; p < 4; ++p) *(LAS u32x4*)(scr + p * 1024 + c.lane * 16) = v[u][p];
#pragma unroll
    for (int t = 0; t < 4; ++t) {
      const bf16x8 f = ldstr(scr + (g * 8 + (i >> 2)) * 128 + t * 32 + (i & 3) * 8, 512);
      __builtin_amdgcn_raw_buffer_store_b128(__builtin_bit_cast(u32x4, f), c.r, c.lane * 16, dst + u * 1024 + t * ts, 16);
      cs[t] = mfma16(f, ones, cs[t]);
    }
  }
  if (J < 216 && i == 0) {
#pragma unroll
    for (int t = 0; t < 4; ++t)
      __builtin_amdgcn_raw_buffer_store_b128(__builtin_bit_cast(u32x4, cs[t]), c.r, g * 16, WS_CSG + ((e & 3) * 3456 + (strip * 8 + t * 2 + mat) * 16) * 4, 16);
  }
}
DI int early_piece(int e) { return e < 4 ? (e & 1) + (e >> 1) * 8 : e < 6 ? 16 + (e - 4) * 4 : e < 9 ? 18 + e : 25 + e; }
DI int late_piece(int e) { return e < 6 ? 2 + e : e < 12 ? 4 + e : e < 15 ? 5 + e : e < 18 ? 6 + e : e < 25 ? 9 + e : 12 + e; }
DI void att_load(const Ctx& c, int lw, int layer, unsigned iss, const uint16_t* wo, const uint16_t* wg, const uint16_t* wu, const uint16_t* wd) {
  const int head = att_head(c), qb = att_qb(c), lane = c.lane;
  const char* Lp = c.ws + WS_L + layer * L_SZ;
  const unsigned vo = lane * 16;
  int grp = ATT_ROT ? qb >> 1 : 0;
  auto src_of = [&](int p) {
    return p < 16 ? L_K1 + head * 65536 + grp * 16384 + p * 1024
                  : p < 24 ? L_K2 + (head >> 1) * 32768 + grp * 8192 + (p - 16) * 1024 : L_V + head * 81920 + grp * 20480 + (p - 24) * 1024;
  };
  unsigned v0[14], m0[11], vg[11], sg[11];
#pragma unroll
  for (int q = 0; q < 11; ++q) {
    const int p = q < 3 ? early_piece(lw * 3 + q) : late_piece(lw * 8 + q - 3), pg = lw * 11 + q;
    v0[q] = src_of(p) + vo; m0[q] = p * 1024;
    vg[q] = src_of(pg) + vo; sg[q] = pg < 16 ? 16384 : pg < 24 ? 8192 : 20480;
    asm volatile("" : "+v"(v0[q]), "+s"(m0[q]));
  }
#pragma unroll
  for (int q = 0; q < 3; ++q) {
    const int p = lw * 3 + q;
    v0[11 + q] = (p < 8 ? L_Q1 + head * 65536 + qb * 8192 + p * 1024 : L_Q2 + (head >> 1) * 32768 + qb * 4096 + (p - 8) * 1024) + vo - q * 1024;
    PIN(v0[11 + q]);
  }
  auto issue = [&](int slot) {
#pragma unroll
    for (int q = 0; q < 11; ++q) { vg[q] += grp == 3 ? -3 * sg[q] : sg[q]; glds1(slot * AT_SLOT + (lw * 11 + q) * 1024, Lp, vg[q]); }
    grp = (grp + 1) & 3;
  };
  const int o0 = (head >> 1) * 5 + (head & 1) * 2, c0 = head * 20;
  auto kv_cb = [&](int sel) { return sel == 0 ? 5 + (o0 >> 4) : sel == 1 ? 5 + ((o0 + 2) >> 4) : sel == 2 ? 10 + (c0 >> 7) : 10 + ((c0 + 19) >> 7); };
  if (lw == 0) {
    sig_wait(c, iss, M_ISS);
    poll(c, flg(FL_QKV + layer * 5, lane & 16 ? (qb * 2 + (lane & 1)) * 16 + ((o0 + (lane & 2)) >> 4) : (grp * 4 + (lane & 3)) * 16 + kv_cb((lane >> 2) & 3)));
    sig_set(c, layer * 5 + 2);
    STAMPL(layer * 5 + 1, 6);
  } else sig_wait(c, layer * 5 + 2);
  glds3(2 * AT_SLOT + lw * 3072, Lp, v0[11], v0[12], v0[13]);
#pragma unroll
  for (int q = 0; q < 3; ++q) glds1(m0[q], Lp, v0[q]);
#pragma unroll
  for (int q = 3; q < 3 + ATT_LATE; ++q) glds1(m0[q], Lp, v0[q]);
  wait_vm<ATT_LATE>(); BARRIER();
#pragma unroll
  for (int q = 3 + ATT_LATE; q < 11; ++q) glds1(m0[q], Lp, v0[q]);
  wait_vm<0>(); BARRIER();
  if (lw == 0) { poll(c, flg(FL_QKV + layer * 5, (lane & 15) * 16 + kv_cb(lane >> 4))); sig_set(c, layer * 5 + 3); } else sig_wait(c, layer * 5 + 3, M_SIG, true);
  issue(1);
  const int J = (lw - 1) * 128 + c.j * 4 + (c.xcd >> 1);
  const bool rt = layer == 0 && lw > 0 && J < 366 && !(c.xcd & 1);
  u32x4 v[5][4];
  if (rt) { retile_load(c, J, v, wo, wg, wu, wd); wait_vm<20>(); } else wait_vm<0>();
  BARRIER();
  issue(2);
  if (rt) retile_store(c, J, c.lds + AT_SCR + (lw - 1) * 4096, v);
  if (layer == 0 && lw > 0) publish(c, M_CNT3, 3, flg(FL_RT, blockIdx.x));
  wait_vm<0>(); BARRIER();
  issue(0);
  wait_vm<0>();
  if (layer == 0) {
    if (lw == 0) {
      if (c.j == 0) {
        Poll<2> pl;
#pragma unroll 1
        for (int q = 0; q < 2; ++q) { pl.run(c, flg(FL_RT, q * 128 + lane), flg(FL_RT, q * 128 + 64 + lane)); wait_vm<0>(); pl.keep(); }
        if (lane < 32) __hip_atomic_store((GAS u64*)(c.ws + flg(FL_RT2, lane * 8 + c.xcd)), c.tgt, __ATOMIC_RELAXED, __HIP_MEMORY_SCOPE_AGENT);
      }
      poll(c, flg(FL_RT2, blockIdx.x));
      if (blockIdx.x == 0 && lane == 0) *(u64*)(c.ws + WS_EPOCH) = c.tgt;
    }
  }
  BARRIER();
  u32x4 pk[25];
#pragma unroll
  for (int e = 0; e < 25; ++e) pk[e] = ld16s(c.r, vo, WS_WO + (pr_cb(c) * 5 + e % 5) * 20480 + (lw * 5 + e / 5) * 1024);
#pragma unroll 1
  for (unsigned q = lw; q < 35; q += 4) glds1(AT_SCR + lw * 1024, c.ws + WS_WGU + ((gu_cb(c) * 27) >> 2) * 40960 + ((c.j & 7) * 35 + q) * 1024, vo);
  wait_vm<0>();
  BARRIER();
  sig_wait(c, layer * 16 + 8, M_CNT);
#pragma unroll
  for (int e = 0; e < 25; ++e) { const int k = lw * 5 + e / 5; ldsw16(c.lds + (k >> 1) * 10240 + (k & 1) * 1024 + (e % 5) * 2048 + vo, pk[e]); }
}

template <int NS, int NB, int WIN>
DI void proj_compute(const Ctx& c, int w, int slot_id, unsigned a_off, const char* res_rm, unsigned res_off, unsigned out_off, unsigned st_off,
                     uint16_t* out, int arr) {
  constexpr bool RES = NS <= NB;
  const int panel = pr_panel(c), cb = pr_cb(c);
  SLOT(slot_id);
  STAMP(0);
  if (w >= 4) {
    if constexpr (RES) { BARRIER(); BARRIER(); if (PROJ2) BARRIER(); }
    else {
      static_assert(RES || (NS == 27 && NB == 15 && WIN == 18), "schedule");
      const int par = (w >> 1) & 1, half = w & 1;
      u32x4 pk[15];
#pragma unroll
      for (int e = 0; e < 15; ++e) {
        const int q = half * 5 + e % 5;
        pk[e] = ld16s(c.r, c.lane * 16, WS_WD + (cb * 5 + (q >> 1)) * 55296 + ((15 + par + (e / 5) * 2) * 2 + (q & 1)) * 1024);
      }
      lds_ptr pw = c.lds + par * 10240 + half * 5120 + c.lane * 16;
      BARRIER(); BARRIER();
#pragma unroll
      for (int e = 0; e < 10; ++e) ldsw16(pw + (e / 5) * 20480 + (e % 5) * 1024, pk[e]);
      BARRIER();
#pragma unroll
      for (int e = 10; e < 15; ++e) ldsw16(pw + 40960 + (e % 5) * 1024, pk[e]);
      BARRIER(); BARRIER(); BARRIER(); BARRIER();
      if (PROJ2) BARRIER();
    }
    return;
  }
  const int lane = c.lane, i = lane & 15, g = lane >> 4, mt = panel * 4 + w;
  u32x2 res[5];
#pragma unroll
  for (int t = 0; t < 5; ++t)
    res[t] = res_rm ? *(const u32x2*)(res_rm + (mt * 16 + i) * 1280 + cb * 160 + t * 32 + g * 8)
                    : ld8(c.r, res_off + (mt * 80 + cb * 10 + t * 2 + (g >> 1)) * 256 + i * 16 + (g & 1) * 8);
  f32x4 acc[5];
#pragma unroll
  for (int t = 0; t < 5; ++t) { acc[t] = f32x4{bflo(res[t][0]), bfhi(res[t][0]), bflo(res[t][1]), bfhi(res[t][1])}; PIN(acc[t]); }
  float piv;
  { const f32x4 s4 = (acc[0] + acc[1]) + (acc[2] + acc[3]) + acc[4]; piv = rows4((s4[0] + s4[1]) + (s4[2] + s4[3])) * (1.f / 80.f); }
  const int ch = (g & 1) * 2 + (g >> 1);
  unsigned xo = out ? (mt * 16 + i) * 1280 + cb * 160 + ch * 16 : out_off + (mt * 80 + cb * 10 + ch) * 256 + i * 16;
  unsigned so = st_off + (mt >> 3) * 8192 + cb * 1024 + ((mt & 7) * 16 + i) * 8;
  asm volatile("" : "+v"(piv), "+v"(xo), "+v"(so));
  lds_ptr pb = c.lds + lane * 16;
  unsigned av = a_off + mt * (NS * 2048) + lane * 16;
  u32x4 a[WIN];
  bf16x8 f[3][5];
  int sb = 0;
  STAMP(1);
  BARRIER();
  STAMP(2); STAMPC(0); WRESET();
  constexpr int AH = RES ? WO_AH : DN_AH;
  auto lda = [&](int k) {
    if (RES && k >= 16) {
      const unsigned o = a_off + mt * 20480 + 16384 + (k - 16) * 1024 + g * 256 + i * 8;
      const u32x2 lo = ld8(c.r, o), hi = ld8(c.r, o + 128);
      a[k % WIN] = u32x4{lo[0], lo[1], hi[0], hi[1]};
    } else a[k % WIN] = ld16(c.r, av + k * 1024);
  };
#pragma unroll
  for (int k = 0; k < AH; ++k) lda(k);
#pragma unroll
  for (int t = 0; t < 5; ++t) { f[0][t] = ldsr16(pb + t * 2048); f[1][t] = ldsr16(pb + t * 2048 + 1024); }
  SBAR();
  auto window = [&](bool first, bool last) {
#pragma unroll
    for (int k = 0; k < WIN; ++k) {
      const bool ld = !(last && k + 2 >= WIN);
      if ((k & 1) == 0) {
        if (RES) pb += 10240;
        else { pb += sb == NB - 1 ? -(NB - 1) * 10240 : 10240; sb = sb == NB - 1 ? 0 : sb + 1; }
        if (!ld) BARRIER(); else if (!RES && (k == 10 || (k == 0 && !first))) BARRIER_NW();
      }
#pragma unroll
      for (int t = 0; t < 5; ++t) {
        if (ld) f[(k + 2) % 3][t] = ldsr16(pb + t * 2048 + (k & 1) * 1024);
        acc[t] = mfma16(f[k % 3][t], __builtin_bit_cast(bf16x8, a[k]), acc[t]);
        SBAR();
      }
      if (!last || k + AH < WIN) { lda(k + AH); SBAR(); }
    }
    av += WIN * 1024;
  };
  static_assert(2 * NS % WIN == 0 && (2 * NS == WIN || WIN % 6 == 0), "window");
#pragma unroll 1
  for (int b = 1; b < 2 * NS / WIN; ++b) window(b == 1, false);
  window(false, true);
  STAMP(3); STAMPC(1);
  u32x2 o[5];
#pragma unroll
  for (int t = 0; t < 5; ++t) {
    o[t][0] = pk2(acc[t][0], acc[t][1]); o[t][1] = pk2(acc[t][2], acc[t][3]);
  }
  const u32x4 p0 = pair16(o[0][0], o[0][1], o[1][0], o[1][1]), p1 = pair16(o[2][0], o[2][1], o[3][0], o[3][1]),
              p2 = pair16(o[4][0], o[4][1], o[4][0], o[4][1]);
  if (out) {
    char* po = (char*)out + xo;
    *(u32x4*)po = p0; *(u32x4*)(po + 64) = p1;
    if (!(g & 1)) *(u32x4*)(po + 128) = p2;
    STAMP(4);
    return;
  }
  st16(c.r, xo, p0); st16(c.r, xo + 1024, p1);
  if (!(g & 1)) st16(c.r, xo + 2048, p2);
  f32x4 s4 = acc[0] - piv, q4 = s4 * s4;
#pragma unroll
  for (int t = 1; t < 5; ++t) { const f32x4 d = acc[t] - piv; s4 += d; q4 += d * d; }
  const float sd = rows4((s4[0] + s4[1]) + (s4[2] + s4[3])), sq = rows4((q4[0] + q4[1]) + (q4[2] + q4[3]));
  if (g == 0) st8(c.r, so, u32x2{__float_as_uint(sd + 80.f * piv), __float_as_uint(fmaxf(sq - sd * sd * (1.f / 80.f), 0.f))});
  STAMP(4);
  publish(c, M_CNT4, 4, flg(arr, panel * 8 + cb), true);
  STAMP(5);
}
template <int NF>
DI void gu_consumer16(const Ctx& c, int layer, int f0, int mq) {
  const int panel = gu_panel(c), ft0 = (gu_cb(c) * 27) >> 2;
  const int lane = c.lane, i = lane & 15, g = lane >> 4;
  f32x4 acc[2][2 * NF];
#pragma unroll
  for (int m = 0; m < 2; ++m)
#pragma unroll
    for (int t = 0; t < 2 * NF; ++t) acc[m][t] = f32x4{0.f, 0.f, 0.f, 0.f};
  lds_ptr pa = c.lds + GU_A + mq * 2048 + lane * 16, pb = c.lds + f0 * 2048 + lane * 16;
  bf16x8 x0[2], x1[2], wa[NF], wb0[NF], wb1[NF];
  auto step = [&](const bf16x8* xa, const bf16x8* wb, bf16x8* xn, bf16x8* wn_, bool ld) {
#pragma unroll
    for (int q = 0; q < 4 * NF; ++q) {
      const int t = q >> 1, m = q & 1;
      if (ld) {
        if (q < 2) xn[q] = ldsr16(pa + q * 1024);
        else if (q < 2 * NF) { const int e = NF == 4 ? (q == 3 ? 0 : q == 4 ? 1 : q == 6 ? 2 : q == 7 ? 3 : -1) : NF == 3 ? q - 3 : q - 2; if (e >= 0) wn_[e] = ldsr16(pb + (NF + e) * 1024); }
        else if (q & 1) wa[(q - 2 * NF) >> 1] = ldsr16(pb + ((q - 2 * NF) >> 1) * 1024);
      }
      acc[m][t] = mfma16(t < NF ? wa[t] : wb[t - NF], xa[m], acc[m][t]);
      SBAR();
    }
  };
  int sl = 0;
  auto adv = [&]() {
    pa += sl == GU_NA - 1 ? -(GU_NA - 1) * 8192 : 8192; pb += sl == GU_NB - 1 ? -(GU_NB - 1) * 14336 : 14336;
    sl = sl == GU_NA - 1 ? 0 : sl + 1;
  };
  unsigned ho = WS_L + layer * L_SZ + L_H + ((panel * 8 + mq * 2 + (g & 1)) * 216 + (ft0 + f0) * 2 + (g >> 1)) * 256 + i * 16;
  PIN(ho);
  SLOT(layer * 5 + 3);
  STAMP(0);
  STAMP(1);
  BARRIER();
  STAMP(2); STAMPC(0); WRESET();
  x0[0] = ldsr16(pa); x0[1] = ldsr16(pa + 1024);
#pragma unroll
  for (int t = 0; t < NF; ++t) { wa[t] = ldsr16(pb + t * 1024); wb0[t] = ldsr16(pb + (NF + t) * 1024); }
#pragma unroll 1
  for (int st = 0; st < 9; ++st) {
    adv(); BARRIER_NW(); step(x0, wb0, x1, wb1, true);
    adv(); BARRIER_NW(); step(x1, wb1, x0, wb0, true);
  }
  adv(); BARRIER_NW(); step(x0, wb0, x1, wb1, true);
  BARRIER();
  const LAS float* srs = (const LAS float*)(c.lds + M_SST) + mq * 32 + i;
  const LAS float* scs = (const LAS float*)(c.lds + M_SCS) + f0 * 32 + g * 4;
  float rs[2] = {srs[0], srs[16]}; const float nm[2] = {srs[128], srs[144]};
  f32x4 cs[2 * NF];
#pragma unroll
  for (int e = 0; e < 2 * NF; ++e) cs[e] = *(const LAS f32x4*)(scs + e * 16);
  SBAR();
  step(x1, wb1, x0, wb0, false);
  STAMP(3); STAMPC(1);
  rs[0] *= rs[0]; rs[1] *= rs[1];
#pragma unroll
  for (int f = 0; f < NF; ++f) {
    unsigned p[2][2];
#pragma unroll
    for (int m = 0; m < 2; ++m) {
      f32x4 gt = acc[m][2 * f] + cs[2 * f] * nm[m];
      const f32x4 up = (acc[m][2 * f + 1] + cs[2 * f + 1] * nm[m]) * rs[m];
      gt = __builtin_elementwise_max(gt, f32x4{0.f, 0.f, 0.f, 0.f}) * up;
      p[m][0] = pk2(gt[0], gt[1]); p[m][1] = pk2(gt[2], gt[3]);
    }
    st16(c.r, ho + f * 512, pair16(p[0][0], p[0][1], p[1][0], p[1][1]));
    SBAR();
  }
  STAMP(4);
  publish(c, M_CNT, 8, flg(FL_GU + layer * 5, panel * 16 + gu_cb(c)), true);
  STAMP(5);
}
template <int NF>
DI void gu_consumer32(const Ctx& c, int layer, int f0, int mq) {
  const int panel = gu_panel(c), ft0 = (gu_cb(c) * 27) >> 2;
  const int lane = c.lane, r32 = lane & 31, h = lane >> 5;
  f32x16 acc[NF];
#pragma unroll
  for (int t = 0; t < NF; ++t) acc[t] = f32x16{};
  const int lo = (r32 >> 4) * 1024 + h * 256 + (r32 & 15) * 16;
  lds_ptr pa = c.lds + GU_A + mq * 2048 + lo, pb = c.lds + f0 * 2048 + lo;
  bf16x8 x0[2], x1[2], wa[NF], wb0[NF], wb1[NF];
  auto step = [&](const bf16x8* xa, const bf16x8* wb, bf16x8* xn, bf16x8* wn_, bool ld) {
#pragma unroll
    for (int q = 0; q < 2 * NF; ++q) {
      if (ld) {
        if (q == 0) { xn[0] = ldsr16(pa); xn[1] = ldsr16(pa + 512); }
        if (q >= 1 && q <= NF) wn_[q - 1] = ldsr16(pb + (q - 1) * 2048 + 512);
        if (q >= NF) wa[q - NF] = ldsr16(pb + (q - NF) * 2048);
      }
      if (q < NF) acc[q] = mfma32(wa[q], xa[0], acc[q]); else acc[q - NF] = mfma32(wb[q - NF], xa[1], acc[q - NF]);
      SBAR();
    }
  };
  static_assert(GU_NA == GU_NB, "one slot counter");
  int sl = 0;
  auto adv = [&]() {
    pa += sl == GU_NA - 1 ? -(GU_NA - 1) * 8192 : 8192; pb += sl == GU_NB - 1 ? -(GU_NB - 1) * 14336 : 14336;
    sl = sl == GU_NA - 1 ? 0 : sl + 1;
  };
  SLOT(layer * 5 + 3);
  STAMP(0);
  STAMP(1);
  BARRIER();
  STAMP(2); STAMPC(0); WRESET();
  x0[0] = ldsr16(pa); x0[1] = ldsr16(pa + 512);
#pragma unroll
  for (int t = 0; t < NF; ++t) { wa[t] = ldsr16(pb + t * 2048); wb0[t] = ldsr16(pb + t * 2048 + 512); }
#pragma unroll 1
  for (int st = 0; st < 9; ++st) {
    adv(); BARRIER_NW(); step(x0, wb0, x1, wb1, true);
    adv(); BARRIER_NW(); step(x1, wb1, x0, wb0, true);
  }
  adv(); BARRIER_NW(); step(x0, wb0, x1, wb1, true);
  BARRIER();
  const LAS float* srs = (const LAS float*)(c.lds + M_SST) + mq * 32 + r32;
  const LAS float* scs = (const LAS float*)(c.lds + M_SCS) + f0 * 32 + h * 4;
  float rs = srs[0], nm = srs[128];
  f32x4 cs[2][4];
#pragma unroll
  for (int e = 0; e < 8; ++e) cs[e >> 2][e & 3] = *(const LAS f32x4*)(scs + e * 8);
  SBAR();
  step(x1, wb1, x0, wb0, false);
  STAMP(3); STAMPC(1);
  rs *= rs;
  const unsigned ho = WS_L + layer * L_SZ + L_H + ((panel * 8 + mq * 2 + (r32 >> 4)) * 216 + (ft0 + f0) * 2 + h) * 256 + (r32 & 15) * 16;
#pragma unroll
  for (int f = 0; f < NF; ++f) {
    unsigned p[2][2];
#pragma unroll
    for (int e = 0; e < 2; ++e) {
      const f32x16 a = acc[f];
      f32x4 gt = f32x4{a[e * 4], a[e * 4 + 1], a[e * 4 + 2], a[e * 4 + 3]} + cs[f & 1][e] * nm;
      const f32x4 up = (f32x4{a[8 + e * 4], a[9 + e * 4], a[10 + e * 4], a[11 + e * 4]} + cs[f & 1][2 + e] * nm) * rs;
      gt = __builtin_elementwise_max(gt, f32x4{0.f, 0.f, 0.f, 0.f}) * up;
      p[e][0] = pk2(gt[0], gt[1]); p[e][1] = pk2(gt[2], gt[3]);
    }
    if (f + 2 < NF) {
#pragma unroll
      for (int e = 0; e < 4; ++e) cs[f & 1][e] = *(const LAS f32x4*)(scs + (f + 2) * 32 + e * 8);
    }
    st16(c.r, ho + f * 512, pair32(p[0][0], p[0][1], p[1][0], p[1][1]));
    SBAR();
  }
  STAMP(4);
  publish(c, M_CNT, 8, flg(FL_GU + layer * 5, panel * 16 + gu_cb(c)), true);
  STAMP(5);
}
template <int NF> DI void gu_consumer(const Ctx& c, int layer, int f0, int mq) {
  if constexpr (GU16 == 1 || (GU16 == 2 && NF < 4) || (GU16 == 3 && NF == 4)) gu_consumer16<NF>(c, layer, f0, mq); else gu_consumer32<NF>(c, layer, f0, mq);
}
DI void mlp_load(const Ctx& c0, int lw, int layer, unsigned iss, bool qkv_next, const uint16_t* wqkv) {
  const int half = lw & 1;
  {
    const Ctx c = fresh(c0);
    if (lw == 0) { poll(c, flg(FL_ATT + layer * 5, (pr_panel(c) >> 2) * 32 + (c.lane & 31))); STAMPL(layer * 5 + 2, 6); }
    BARRIER_L(); BARRIER_L();
    if (PROJ2) BARRIER_L();
  }
  { const int w = lw + 8; (void)w; WDUMP(layer * 5 + 2); }
  {
    const Ctx c = fresh(c0);
    const int ft0 = (gu_cb(c) * 27) >> 2;
    u32x4 pk[20];
    auto park = [&](int e0) {
#pragma unroll
      for (int e = e0; e < e0 + 4; ++e) {
        const int q = half * 5 + e % 5;
        pk[e] = ld16s(c.r, c.lane * 16, WS_WD + (pr_cb(c) * 5 + (q >> 1)) * 55296 + (((e / 5) * 2 + (lw >> 1)) * 2 + (q & 1)) * 1024);
      }
    };
    static_assert(GU_NA == 7 && GU_NB == 7, "schedule");
    if (lw >= 2) {
      Ring<7, GU_NB, 20, 2> rg;
#pragma unroll
      for (int p = 0; p < 7; ++p) rg.voff[p] = WS_WGU + (ft0 * 2 + half * 7 + p) * 20480 + c.lane * 16;
      rg.init(c.ws, 1024, 0, half);
      if (half == 0) ln_cols<4>(c, 56, WS_CSG + ft0 * 128, 13824);
      sig_wait(c, iss - layer * 16 + 4, M_CNT4);
      rg.template fill_ahead<1>(c, layer * 5 + 4);
      rg.template range<5, 5>(); rg.steady();
      wait_vm<28>(); BARRIER_L(); park(0);
      wait_vm<21 + 4>(); BARRIER_L(); park(4);
      wait_vm<14 + 8>(); BARRIER_L(); park(8);
      wait_vm<7 + 12>(); BARRIER_L(); park(12);
      wait_vm<16>(); BARRIER_L(); park(16);
    } else {
      Ring<4, GU_NA, 20, 2> rg;
#pragma unroll
      for (int p = 0; p < 4; ++p) rg.voff[p] = WS_L + layer * L_SZ + L_X1 + (gu_panel(c) * 8 + half * 4 + p) * 20480 + c.lane * 16;
      rg.init(c.ws, 1024, GU_A, half);
      Poll<1> pl;
      gate(c, pl, half, flg(FL_WO + layer * 5, gu_panel(c) * 16 + (c.lane & 15)), layer * 5 + 4, iss + 12);
      rg.template fill<3>();
      rg.template head<3>();
      pl.keep();
      f32x2 st[8];
      wait_vm<16>(); BARRIER_L();
      ln_load(c, WS_ST + (layer * 2 + 1) * STB + gu_panel(c) * 8192, half * 64 + c.lane, st);
      wait_vm<12 + 8>(); BARRIER_L(); park(0); park(4);
      wait_vm<8 + 8 + 8>(); BARRIER_L();
      const f32x2 t = ln_sum(st);
      wait_vm<8>(); BARRIER_L(); ln_put(c, half * 64 + c.lane, t); park(8);
      BARRIER_L(); park(12); park(16);
    }
    BARRIER_L();
#pragma unroll
    for (int e = 0; e < 20; ++e) PIN(pk[e]);
    { const int w = lw + 8; (void)w; WDUMP(layer * 5 + 3); }
    auto dump = [&]() {
#pragma unroll
      for (int e = 0; e < 20; ++e) ldsw16(c.lds + ((e / 5) * 2 + (lw >> 1)) * 10240 + (half * 5 + e % 5) * 1024 + c.lane * 16, pk[e]);
    };
    if (lw >= 2) {
      Ring<5, DN_NB, 27, 2> rg;
#pragma unroll
      for (int p = 0; p < 5; ++p) { const int q = half * 5 + p; rg.voff[p] = WS_WD + (pr_cb(c) * 5 + (q >> 1)) * 55296 + (q & 1) * 1024 + c.lane * 16; }
      rg.init(c.ws + 8 * 2048, 2048, 0, half);
      rg.slot = 8;
      sig_wait(c, layer * 16 + 16, M_CNT);
      rg.template fill<3>(); dump(); rg.template fill<4>();
      BARRIER_L();
      wait_vm<5 * 5>(); BARRIER_L();
      wait_vm<0>(); BARRIER_L();
      rg.base += 6 * 2048; rg.slot = 6;
      rg.template fill<3>();
      BARRIER_L();
      rg.template fill<3>();
      wait_vm<3 * 5>(); BARRIER_L();
      wait_vm<0>(); BARRIER_L();
      BARRIER_L();
      if (PROJ2) BARRIER_L();
    } else {
      sig_wait(c, layer * 16 + 16, M_CNT);
      dump();
      if (lw == 0) { poll(c, flg(FL_GU + layer * 5, (pr_panel(c) >> 1) * 16 + (c.lane & 15))); STAMPL(layer * 5 + 4, 6); }
#pragma unroll 1
      for (int k = 0; k < 7 + PROJ2; ++k) BARRIER_L();
    }
  }
  { const int w = lw + 8; (void)w; WDUMP(layer * 5 + 4); }
  if (qkv_next) qkv_load1(fresh(c0), lw, wqkv);
}

__global__ __launch_bounds__(768) void fused_kernel(const uint16_t* __restrict__ x, const uint16_t* __restrict__ wqkv, const uint16_t* __restrict__ wo,
                                                    const uint16_t* __restrict__ wg, const uint16_t* __restrict__ wu,
                                                    const uint16_t* __restrict__ wd, uint16_t* __restrict__ out, char* __restrict__ ws) {
  extern __shared__ __attribute__((aligned(16))) char smem[];
  const char* pc;
  asm volatile("s_getpc_b64 %0" : "=s"(pc));
  glds1(P0_SCR + 10240 + 1024, (const char*)((u64)pc & ~15ull), ((blockIdx.x >> 3) * 12 + (threadIdx.x >> 6)) % PF_KB * 1024 + (threadIdx.x & 63) * 16);
  Ctx c;
  c.ws = ws; c.lds = (lds_ptr)smem; c.lane = threadIdx.x & 63; c.xcd = blockIdx.x & 7; c.j = blockIdx.x >> 3;
  c.r = __builtin_amdgcn_make_buffer_rsrc(ws, 0, WS_END, 0x00020000);
  const int w = __builtin_amdgcn_readfirstlane(threadIdx.x >> 6);
  SPANST(0);
  c.tgt = 0;
  if (threadIdx.x < 16) *(LAS unsigned*)(c.lds + M_CNT + threadIdx.x * 4) = 0u;
  const bool qkv_on = qkv_cb(c) != 15;
  Ctx c0 = c;
  WDUMP(15);
  if (__builtin_expect(w >= 8, LD_FIRST)) {
    __builtin_amdgcn_s_setprio(3);
    if (qkv_on) qkv_load0(fresh(c0), w - 8, x, wqkv); else BARRIER();
    c0.tgt = epoch(ws);
#pragma unroll 1
    for (int layer = 0; layer < 2; ++layer) {
      const unsigned iss = layer * 24 + (qkv_on ? (layer + 1) * 4 : 0);
      WDUMP(layer * 5);
      att_load(fresh(c0), opq(w - 8), opq(layer), iss, wo, wg, wu, wd);
      WDUMP(layer * 5 + 1);
      mlp_load(c0, opq(w - 8), opq(layer), iss, layer == 0 && qkv_on, wqkv);
    }
  } else {
    if (w == 2 || w == 3)
      asm volatile(GL_HEAD "global_load_lds_dword %2, %1 sc1" ::"s"(P0_SCR + 10240), "s"(c.ws), "v"(min(((w - 2) * 64 + fresh(c0).lane) * 524288u + 2048, WS_END)) : "memory");
#pragma unroll 1
    for (int layer = 0; layer < 2; ++layer) {
      const unsigned L = WS_L + layer * L_SZ;
      qkv_compute(fresh(c0), c0.tgt, opq(w), opq(layer), x, wqkv);
      WDUMP(layer * 5);
      att_compute(fresh(c0), opq(w), opq(layer));
      WDUMP(layer * 5 + 1);
      proj_compute<10, WO_NB, 20>(fresh(c0), opq(w), layer * 5 + 2, L + L_O, layer == 0 ? (const char*)x : nullptr, WS_X2, L + L_X1,
                                  WS_ST + (layer * 2 + 1) * STB, nullptr, FL_WO + layer * 5);
      WDUMP(layer * 5 + 2);
      if (w < 4) gu_consumer<4>(fresh(c0), opq(layer), 0, opq(w));
      else if (GU_SKIP && (gu_cb(c0) & 3) == 0) gu_consumer<2>(fresh(c0), opq(layer), 4, opq(w - 4));
      else gu_consumer<3>(fresh(c0), opq(layer), 4, opq(w - 4));
      WDUMP(layer * 5 + 3);
      proj_compute<27, DN_NB, 18>(fresh(c0), opq(w), layer * 5 + 4, L + L_H, nullptr, L + L_X1, WS_X2, WS_ST + 2 * STB, layer ? out : nullptr,
                                  FL_DN + layer * 5);
      WDUMP(layer * 5 + 4);
    }
  }
}

extern "C" void kernel_launch(void* const* d_in, const int* in_sizes, int n_in, void* d_out, int out_size, void* d_ws, size_t ws_size,
                              hipStream_t stream) {
  (void)hipFuncSetAttribute((const void*)fused_kernel, hipFuncAttributeMaxDynamicSharedMemorySize, LDS_BYTES);
  fused_kernel<<<256, 768, LDS_BYTES, stream>>>((const uint16_t*)d_in[0], (const uint16_t*)d_in[1], (const uint16_t*)d_in[2],
                                                (const uint16_t*)d_in[4], (const uint16_t*)d_in[5], (const uint16_t*)d_in[6],
                                                (uint16_t*)d_out, (char*)d_ws);
}
```

```cpp
#include <hip/hip_runtime.h>
#include <hip/hip_bf16.h>
#include <stdint.h>

typedef __attribute__((ext_vector_type(8))) __bf16 bf16x8;
typedef __attribute__((ext_vector_type(16))) __bf16 bf16x16;
typedef __attribute__((ext_vector_type(4))) short s16x4;
typedef __attribute__((ext_vector_type(8))) short s16x8;
typedef __attribute__((ext_vector_type(2))) __bf16 bf16x2;
typedef __attribute__((ext_vector_type(2))) float f32x2;
typedef __attribute__((ext_vector_type(4))) float f32x4;
typedef __attribute__((ext_vector_type(8))) float f32x8;
typedef __attribute__((ext_vector_type(16))) float f32x16;
typedef __attribute__((ext_vector_type(2))) unsigned u32x2;
typedef __attribute__((ext_vector_type(4))) unsigned u32x4;
typedef unsigned long long u64;
#define LAS __attribute__((address_space(3)))
#define GAS __attribute__((address_space(1)))
typedef LAS char* lds_ptr;
typedef __amdgpu_buffer_rsrc_t rsrc_t;
#define DI __device__ __forceinline__

constexpr unsigned XB = 2048u * 640 * 2;
constexpr unsigned WS_EPOCH = 0;
constexpr unsigned WS_FLAG = 4096;
constexpr int FL_P0 = 0, FL_RT = 1, FL_QKV = 2, FL_ATT = 3, FL_WO = 4, FL_GU = 5, FL_DN = 6, FL_RT2 = 12;
constexpr unsigned WS_WO  = WS_FLAG + 13u * 32768;
constexpr unsigned WS_WGU = WS_WO + 640u * 640 * 2;
constexpr unsigned WS_WD  = WS_WGU + 3456u * 640 * 2;
constexpr unsigned WS_CSQ = WS_WD + 640u * 1728 * 2;
constexpr unsigned WS_CSG = WS_CSQ + 8u * 1920 * 4;
constexpr unsigned WS_ST  = WS_CSG + 4u * 3456 * 4;
constexpr unsigned STB = 2048u * 64;
constexpr unsigned WS_X2  = WS_ST + 4 * STB;
constexpr unsigned WS_L   = WS_X2 + XB;
constexpr unsigned L_Q1 = 0, L_Q2 = L_Q1 + 32u * 65536, L_K1 = L_Q2 + 16u * 32768, L_K2 = L_K1 + 32u * 65536, L_V = L_K2 + 16u * 32768,
                   L_O = L_V + XB, L_X1 = L_O + XB, L_H = L_X1 + XB, L_SZ = L_H + 2048u * 1728 * 2;
constexpr unsigned WS_END = WS_L + 2 * L_SZ;

constexpr int LDS_BYTES = 163840, MISC = 159744, M_SST = MISC, M_SCS = MISC + 1024, M_CNT = MISC + 2048, M_CNT2 = M_CNT + 8, M_CNT3 = M_CNT + 16, M_CNT4 = M_CNT + 24, M_SIG = M_CNT + 32, M_ISS = M_CNT + 40, M_TGT = M_CNT + 48;
constexpr int QKV_NB = 5, QKV_NA = 4, QKV_A = QKV_NB * 16384;
constexpr int WO_NB = 10, DN_NB = 15;
constexpr int GU_NB = 7, GU_NA = 7, GU_A = GU_NB * 14336;
constexpr int AT_SLOT = 45056, AT_CONST = 3 * AT_SLOT, AT_SCR = AT_CONST + 10752, AT_MERGE = AT_SLOT;
static_assert(QKV_A + QKV_NA * 16384 <= MISC && DN_NB * 10240 <= MISC && GU_A + GU_NA * 8192 <= MISC, "lds");
static_assert(AT_SCR + 3 * 4096 <= MISC, "lds");

DI unsigned pk2(float a, float b) { f32x2 v = {a, b}; bf16x2 r = __builtin_convertvector(v, bf16x2); return __builtin_bit_cast(unsigned, r); }
DI float bflo(unsigned u) { return __uint_as_float(u << 16); }
DI float bfhi(unsigned u) { return __uint_as_float(u & 0xffff0000u); }
DI f32x4 mfma16(bf16x8 a, bf16x8 b, f32x4 c) { return __builtin_amdgcn_mfma_f32_16x16x32_bf16(a, b, c, 0, 0, 0); }
DI f32x16 mfma32(bf16x8 a, bf16x8 b, f32x16 c) { return __builtin_amdgcn_mfma_f32_32x32x16_bf16(a, b, c, 0, 0, 0); }
DI bf16x8 ldsr16(lds_ptr p) { return *(const LAS bf16x8*)p; }
DI bf16x8 ldstr(lds_ptr p, int second) {
  const s16x4 a = __builtin_amdgcn_ds_read_tr16_b64_v4i16((LAS s16x4*)p), b = __builtin_amdgcn_ds_read_tr16_b64_v4i16((LAS s16x4*)(p + second));
  return __builtin_bit_cast(bf16x8, (s16x8)__builtin_shufflevector(a, b, 0, 1, 2, 3, 4, 5, 6, 7));
}
#define BARRIER() asm volatile("s_waitcnt lgkmcnt(0)\n\ts_barrier" ::: "memory")
template <int N> DI void wait_vm() { asm volatile("s_waitcnt vmcnt(%0)" ::"n"(N > 63 ? 63 : N) : "memory"); }
#define WDUMP(id)
#define WRESET()
#define BARRIER_L() BARRIER()
#define BARRIER_NW() asm volatile("s_barrier" ::: "memory")
#define SBAR() __builtin_amdgcn_sched_barrier(0)
#define PIN(x) asm volatile("" : "+v"(x))
#define GL_HEAD "s_mov_b32 m0, %0\n\ts_nop 3\n\t"
#define GL(v, off) "global_load_lds_dwordx4 %" #v ", %1 offset:" #off " sc1\n\t"
DI void glds1(unsigned m, const char* sb, unsigned v0) { asm volatile(GL_HEAD GL(2, 0)::"s"(m), "s"(sb), "v"(v0) : "memory"); }
DI void glds2(unsigned m, const char* sb, unsigned v0, unsigned v1) {
  asm volatile(GL_HEAD GL(2, 0) GL(3, 1024)::"s"(m), "s"(sb), "v"(v0), "v"(v1) : "memory");
}
DI void glds3(unsigned m, const char* sb, unsigned v0, unsigned v1, unsigned v2) {
  asm volatile(GL_HEAD GL(2, 0) GL(3, 1024) GL(4, 2048)::"s"(m), "s"(sb), "v"(v0), "v"(v1), "v"(v2) : "memory");
}
DI void glds4(unsigned m, const char* sb, unsigned v0, unsigned v1, unsigned v2, unsigned v3) {
  asm volatile(GL_HEAD GL(2, 0) GL(3, 1024) GL(4, 2048) GL(5, 3072)::"s"(m), "s"(sb), "v"(v0), "v"(v1), "v"(v2), "v"(v3) : "memory");
}
DI void st16(rsrc_t r, unsigned off, u32x4 v) { __builtin_amdgcn_raw_buffer_store_b128(v, r, off, 0, 16); }
DI void st8(rsrc_t r, unsigned off, u32x2 v) { __builtin_amdgcn_raw_buffer_store_b64(v, r, off, 0, 16); }
DI void st4(rsrc_t r, unsigned off, unsigned v) { __builtin_amdgcn_raw_buffer_store_b32(v, r, off, 0, 16); }
DI u32x4 ld16(rsrc_t r, unsigned off) { return __builtin_amdgcn_raw_buffer_load_b128(r, off, 0, 16); }
DI u32x2 ld8(rsrc_t r, unsigned off) { return __builtin_amdgcn_raw_buffer_load_b64(r, off, 0, 16); }
DI u32x4 ld16s(rsrc_t r, unsigned voff, unsigned soff) { return __builtin_amdgcn_raw_buffer_load_b128(r, voff, soff, 16); }
DI void ldsw16(lds_ptr p, u32x4 v) { *(LAS u32x4*)p = v; }
DI f32x2 ld8_async(unsigned voff, const char* sbase) {
  f32x2 v; asm volatile("s_nop 4\n\tglobal_load_dwordx2 %0, %1, %2 sc1" : "=v"(v) : "v"(voff), "s"(sbase) : "memory"); return v;
}
DI u32x4 pair32(unsigned a0, unsigned a1, unsigned b0, unsigned b1) {
  auto x = __builtin_amdgcn_permlane32_swap(a0, b0, false, false);
  auto y = __builtin_amdgcn_permlane32_swap(a1, b1, false, false);
  return u32x4{x[0], y[0], x[1], y[1]};
}
template <int N> DI float ror16(float v) { return __int_as_float(__builtin_amdgcn_update_dpp(0, __float_as_int(v), 0x120 + N, 0xf, 0xf, false)); }
DI float sum32(float v) {
  v += ror16<8>(v); v += ror16<4>(v); v += ror16<2>(v); v += ror16<1>(v);
  auto a = __builtin_amdgcn_permlane16_swap(__float_as_uint(v), __float_as_uint(v), false, false);
  return __uint_as_float(a[0]) + __uint_as_float(a[1]);
}
DI u32x4 pair16(unsigned a0, unsigned a1, unsigned b0, unsigned b1) {
  auto x = __builtin_amdgcn_permlane16_swap(a0, b0, false, false);
  auto y = __builtin_amdgcn_permlane16_swap(a1, b1, false, false);
  return u32x4{x[0], y[0], x[1], y[1]};
}

DI float rows4(float v) {
  auto a = __builtin_amdgcn_permlane16_swap(__float_as_uint(v), __float_as_uint(v), false, false);
  v = __uint_as_float(a[0]) + __uint_as_float(a[1]);
  auto b = __builtin_amdgcn_permlane32_swap(__float_as_uint(v), __float_as_uint(v), false, false);
  return __uint_as_float(b[0]) + __uint_as_float(b[1]);
}

#define STAMP(id)
#define STAMPC(id)
#define STAMPL(sl, id)
#define SLOT(n)

#define POLL_DEPTH 1
#define POLL_GATE 1
#define POLL_SLEEP 1
#define ATT_PRIO 3
#define ATT_ORD 0
#define WO_AH 6
#define DN_AH 8
#define ATT_ROT 1
#define GU_SKIP 1
#define ATT_LATE 4
#define GU16 1
#define QKV16 1
#define LD_FIRST 1
#define LAG0 2
#define PF_KB 56
#define PROJ2 0
#define PRI_ALL 1
#define RG 0
#define Q0_D0 2
#define TLB_IN 0
#define PACE 16
#define SPANST(id)
struct Ctx {
  char* ws; rsrc_t r; lds_ptr lds; u64 tgt; int lane, xcd, j;
};
DI Ctx fresh(const Ctx& c0) {
  Ctx c = c0;
  asm volatile("v_mbcnt_lo_u32_b32 %0, -1, 0\n\tv_mbcnt_hi_u32_b32 %0, -1, %0" : "=v"(c.lane), "+s"(c.xcd), "+s"(c.j), "+s"(c.ws));
  return c;
}
DI int opq(int v) { asm volatile("" : "+s"(v)); return v; }
constexpr int SPIN_CAP = 1 << 18;

DI u64 epoch(const char* ws) {
  u64 e; asm volatile("s_nop 4\n\ts_load_dwordx2 %0, %1, 0x0\n\ts_waitcnt lgkmcnt(0)" : "=s"(e) : "s"(ws + WS_EPOCH) : "memory"); return e + 1;
}
DI unsigned flg(int arr, int unit) { return WS_FLAG + (arr * 256 + unit) * 128; }
DI void publish(const Ctx& c, int cnt_off, unsigned n, unsigned to, bool mark = false) {
  unsigned one = 1, iss = M_ISS, cnt = cnt_off, old, so;
  u64 tgt = c.tgt, ex;
  asm volatile("" : "+v"(one), "+v"(iss), "+v"(cnt), "+v"(to), "+v"(tgt));
  if (mark) asm volatile("s_mov_b64 %0, exec\n\ts_mov_b64 exec, 1\n\tds_add_u32 %1, %2\n\ts_mov_b64 exec, %0" : "=&s"(ex) : "v"(iss), "v"(one) : "memory");
  asm volatile("s_mov_b64 %2, exec\n\ts_waitcnt vmcnt(0)\n\ts_mov_b64 exec, 1\n\tds_add_rtn_u32 %0, %3, %4\n\ts_waitcnt lgkmcnt(0)\n\t"
               "v_readfirstlane_b32 %1, %0\n\ts_mov_b64 exec, %2" : "=&v"(old), "=&s"(so), "=&s"(ex) : "v"(cnt), "v"(one) : "memory");
  if (so % n == n - 1)
    asm volatile("s_mov_b64 %0, exec\n\ts_mov_b64 exec, 1\n\ts_nop 2\n\tglobal_store_dwordx2 %1, %2, %3 sc1\n\ts_mov_b64 exec, %0"
                 : "=&s"(ex) : "v"(to), "v"(tgt), "s"(c.ws) : "memory");
}
template <int N> struct Poll {
  u64 v[POLL_DEPTH][N];
  DI void run(const Ctx& c, unsigned o0, unsigned o1 = 0) {

    auto ld = [&](int r) {
      asm volatile("global_load_dwordx2 %0, %1, %2 sc1" : "=v"(v[r][0]) : "v"(o0), "s"(c.ws) : "memory");
      if constexpr (N == 2) asm volatile("global_load_dwordx2 %0, %1, %2 sc1" : "=v"(v[r][N - 1]) : "v"(o1), "s"(c.ws) : "memory");
      __builtin_amdgcn_s_sleep(POLL_SLEEP);
    };
    auto ok = [&](int r) {
      if constexpr (N == 2) asm volatile("s_waitcnt vmcnt(%2)" : "+v"(v[r][0]), "+v"(v[r][N - 1]) : "n"((POLL_DEPTH - 1) * N) : "memory");
      else asm volatile("s_waitcnt vmcnt(%1)" : "+v"(v[r][0]) : "n"(POLL_DEPTH - 1) : "memory");
      return __all(v[r][0] == c.tgt && v[r][N - 1] == c.tgt);
    };
    asm volatile("s_nop 4" ::: "memory");
    ld(0); if (POLL_DEPTH > 1) ld(1);
#pragma unroll 1
    for (int spin = 0; spin < SPIN_CAP; ++spin) {
      if (ok(0)) break; ld(0);
      if (POLL_DEPTH > 1) { if (ok(1)) break; ld(1); }
    }
  }
  DI void keep() {
#pragma unroll
    for (int r = 0; r < POLL_DEPTH; ++r) { asm volatile("" : "+v"(v[r][0])); if constexpr (N == 2) asm volatile("" : "+v"(v[r][N - 1])); }
  }
};
DI void poll(const Ctx& c, unsigned slot) { Poll<1> p; p.run(c, slot); wait_vm<0>(); p.keep(); }
DI void sig_set(const Ctx& c, unsigned v) { if (c.lane == 0) __hip_atomic_store((LAS unsigned*)(c.lds + M_SIG), v, __ATOMIC_RELAXED, __HIP_MEMORY_SCOPE_WORKGROUP); }
DI void sig_wait(const Ctx& c, unsigned v, int off = M_SIG, bool calm = false) {
#pragma unroll 1
  for (int spin = 0; spin < SPIN_CAP * 16; ++spin) {
    if (__builtin_amdgcn_readfirstlane(__hip_atomic_load((LAS unsigned*)(c.lds + off), __ATOMIC_RELAXED, __HIP_MEMORY_SCOPE_WORKGROUP)) >= v) break;
    if (calm) __builtin_amdgcn_s_sleep(8); else if (off != M_SIG) __builtin_amdgcn_s_sleep(1);
  }
  asm volatile("" ::: "memory");
}

DI void gate(const Ctx& c, Poll<1>& p, int half, unsigned slot, unsigned sig, unsigned iss) {
  if (half == 0) { if (POLL_GATE) sig_wait(c, iss, M_ISS); p.run(c, slot); sig_set(c, sig); STAMPL(sig - 1, 6); } else sig_wait(c, sig);
}

template <int PP, int NSLOT, int NS, int LAG = 1>
struct Ring {
  unsigned voff[PP];
  const char* base;
  unsigned adv, l0;
  int slot;
  DI void init(const char* b, unsigned a, unsigned lds0, int half) {
    base = b - 4096; adv = a; l0 = lds0 + half * PP * 1024; slot = 0;
#pragma unroll
    for (int p = 0; p < PP; ++p) voff[p] += 4096 - (p & 3) * 1024;
  }
  DI void issue() {
    const unsigned m = l0 + slot * (2 * PP * 1024);
    glds4(m, base, voff[0], voff[1], voff[2], voff[3]);
    if constexpr (PP == 5) glds1(m + 4096, base, voff[4]);
    if constexpr (PP == 7) glds3(m + 4096, base, voff[4], voff[5], voff[6]);
    if constexpr (PP == 8) glds4(m + 4096, base, voff[4], voff[5], voff[6], voff[7]);
    base += adv; slot = slot == NSLOT - 1 ? 0 : slot + 1;
  }
  template <int N = NSLOT> DI void fill() {
#pragma unroll 1
    for (int s = 0; s < N; ++s) issue();
  }
  template <int NEED> DI void fill_ahead(const Ctx& c, unsigned sig) {
    int n = 0;
#pragma unroll 1
    while (n < NSLOT) {
      issue(); wait_vm<PACE>(); ++n;
      if (n * PP >= NEED * PP + PACE &&
          __builtin_amdgcn_readfirstlane(__hip_atomic_load((LAS unsigned*)(c.lds + M_SIG), __ATOMIC_RELAXED, __HIP_MEMORY_SCOPE_WORKGROUP)) >= sig) break;
    }
    if (n == NSLOT) wait_vm<(NSLOT - NEED) * PP>();
    BARRIER_L(); WRESET();
#pragma unroll 1
    for (; n < NSLOT; ++n) issue();
  }
  DI void rest() { range<NSLOT - 2, NSLOT - LAG>(); steady(); range<NSLOT - 1 - LAG, 0>(); BARRIER_L(); }
  template <int R, int E> DI void range() { if constexpr (R >= E) { wait_vm<R * PP>(); BARRIER_L(); range<R - 1, E>(); } }
  template <int D0 = NSLOT> DI void head() {
    wait_vm<(D0 - 1) * PP>(); BARRIER_L(); WRESET();
    if constexpr (D0 < NSLOT) fill<NSLOT - D0>();
    range<NSLOT - 2, NSLOT - LAG>();
    steady();
  }
  DI void steady() {
#pragma unroll 1
    for (int k = LAG; k < NS - NSLOT + LAG; ++k) { wait_vm<(NSLOT - 1 - LAG) * PP>(); BARRIER_L(); issue(); }
  }
  template <int D0 = NSLOT> DI void run() { head<D0>(); range<NSLOT - 1 - LAG, 0>(); BARRIER_L(); }
};

DI void ln_load(const Ctx& c, unsigned off, int row, f32x2 (&p)[8]) {
#pragma unroll
  for (int i = 0; i < 8; ++i) p[i] = __builtin_bit_cast(f32x2, __builtin_amdgcn_raw_buffer_load_b64(c.r, row * 8, off + i * 1024, 16));
}
DI f32x2 ln_sum(const f32x2 (&p)[8]) {
  f32x2 t = ((p[0] + p[1]) + (p[2] + p[3])) + ((p[4] + p[5]) + (p[6] + p[7]));
  const float m8 = t[0] * 0.125f; float q0 = 0.f, q1 = 0.f;
#pragma unroll
  for (int i = 0; i < 8; i += 2) { const float a = p[i][0] - m8, b = p[i + 1][0] - m8; q0 += a * a; q1 += b * b; }
  t[1] += (q0 + q1) * (1.f / 80.f);
  PIN(t);
  return t;
}
DI void ln_put(const Ctx& c, int row, f32x2 t) {
  *(LAS float*)(c.lds + M_SST + row * 4) = __builtin_amdgcn_rsqf(t[1] * (1.f / 640.f) + 1e-5f);
  *(LAS float*)(c.lds + M_SST + 512 + row * 4) = -t[0] * (1.f / 640.f);
}
template <int NP> DI void ln_cols_sum(const Ctx& c, int n4, const u32x4 (&q)[NP]) {
  f32x4 cs = __builtin_bit_cast(f32x4, q[0]);
#pragma unroll
  for (int i = 1; i < NP; ++i) cs += __builtin_bit_cast(f32x4, q[i]);
  if (c.lane < n4) *(LAS f32x4*)(c.lds + M_SCS + c.lane * 16) = cs;
}
template <int NP> DI void ln_cols(const Ctx& c, int n4, unsigned off, unsigned pitch) {
  u32x4 q[NP];
#pragma unroll
  for (int i = 0; i < NP; ++i) q[i] = ld16(c.r, off + i * pitch + (c.lane < n4 ? c.lane : 0) * 16);
  ln_cols_sum<NP>(c, n4, q);
}

DI int row_group(const Ctx& c) { return RG == 0 ? c.xcd & 3 : RG == 1 ? ((c.xcd & 1) ^ 1) * 2 + ((c.xcd >> 1) & 1) : (c.xcd & 1) * 2 + ((c.xcd >> 1) & 1); }
DI int qkv_panel(const Ctx& c) { return row_group(c) * 4 + (c.j & 3); }
DI int qkv_cb(const Ctx& c) { return (c.xcd >> 2) * 8 + (c.j >> 2); }
DI int pr_panel(const Ctx& c) { return row_group(c) * 8 + (c.j & 7); }
DI int pr_cb(const Ctx& c) { return (c.xcd >> 2) * 4 + (c.j >> 3); }
DI int gu_panel(const Ctx& c) { return (c.xcd & 1) * 8 + (c.j & 7); }
DI int gu_cb(const Ctx& c) { return (c.xcd >> 1) * 4 + (c.j >> 3); }
DI int att_head(const Ctx& c) { return c.xcd * 4 + (c.j & 3); }
DI int att_qb(const Ctx& c) { return c.j >> 2; }

constexpr int P0_SCR = QKV_A + QKV_NA * 16384;
static_assert(P0_SCR + 10240 <= MISC, "lds");
struct Phase0 {
  u32x4 v[3]; float sum, m2;
  DI void load(const Ctx& c, int hw, const uint16_t* x, const uint16_t* wqkv) {
    const int lane = c.lane, r32 = lane & 31, b = blockIdx.x, row = b * 8 + hw * 2 + (lane >> 5);
    v[0] = *(const u32x4*)(x + row * 640 + r32 * 8); v[1] = *(const u32x4*)(x + row * 640 + 256 + r32 * 8);
    v[2] = *(const u32x4*)(x + row * 640 + 512 + (r32 & 15) * 8);
    if (b < 240) {
      const char* src = (const char*)wqkv + ((b / 30) * 80 + hw * 8) * 3840 + (b % 30) * 128;
      const unsigned vo = (lane >> 3) * 3840 + (lane & 7) * 16;
      glds1(P0_SCR + hw * 1024, src, vo); glds1(P0_SCR + 4096 + hw * 1024, src + 32 * 3840, vo);
      if (hw < 2) glds1(P0_SCR + 8192 + hw * 1024, src + 64 * 3840, vo);
    }
  }
  DI float dev2(int q) const {
    const float mean = sum * (1.f / 640.f); float a = 0.f, b = 0.f;
#pragma unroll
    for (int e = 0; e < 4; ++e) { const float d0 = bflo(v[q][e]) - mean, d1 = bfhi(v[q][e]) - mean; a += d0 * d0; b += d1 * d1; }
    return a + b;
  }
  DI void part1(const Ctx& c) {
    unsigned uo = 0x3f803f80u; PIN(uo);
    const bf16x2 ones = __builtin_bit_cast(bf16x2, uo);
    float s[3];
#pragma unroll
    for (int q = 0; q < 3; ++q) {
      s[q] = 0.f;
#pragma unroll
      for (int e = 0; e < 4; ++e) { const unsigned u = v[q][e]; s[q] = __builtin_amdgcn_fdot2_f32_bf16(__builtin_bit_cast(bf16x2, u), ones, s[q], false); }
    }
    sum = sum32(s[0] + s[1] + ((c.lane & 16) ? 0.f : s[2]));
    m2 = dev2(0);
    asm volatile("" : "+v"(sum), "+v"(m2));
    wait_vm<0>();
  }
  DI void part2(const Ctx& c, int hw) {
    const int lane = c.lane, r32 = lane & 31, b = blockIdx.x, row = b * 8 + hw * 2 + (lane >> 5);
    const float t = dev2(2);
    m2 = sum32(m2 + dev2(1) + ((lane & 16) ? 0.f : t));
    if (r32 < 8) st8(c.r, WS_ST + (row >> 7) * 8192 + r32 * 1024 + (row & 127) * 8, u32x2{__float_as_uint(sum * 0.125f), __float_as_uint(m2 * 0.125f)});
  }
  DI void cols(const Ctx& c, int hw) {
    const int i = c.lane & 15, g = c.lane >> 4, b = blockIdx.x;
    if (b < 240) {
      const unsigned o = 0x3f803f80u, z = g < 2 ? o : 0u;
      lds_ptr p = c.lds + P0_SCR + (g * 8 + (i >> 2)) * 128 + hw * 32 + (i & 3) * 8;
      f32x4 cs = mfma16(ldstr(p, 512), __builtin_bit_cast(bf16x8, u32x4{o, o, o, o}), f32x4{});
      cs = mfma16(ldstr(p + 4096, 512), __builtin_bit_cast(bf16x8, u32x4{o, o, o, o}), cs);
      cs = mfma16(ldstr(p + (g < 2 ? 8192 : 0), 512), __builtin_bit_cast(bf16x8, u32x4{z, z, z, z}), cs);
      if (i == 0) st16(c.r, WS_CSQ + ((b / 30) * 1920 + (b % 30) * 64 + hw * 16 + g * 4) * 4, __builtin_bit_cast(u32x4, cs));
    }
  }
};

template <bool SWAP, class PRE, class MID>
DI void qkv_loop(const Ctx& c, int w, int layer, f32x4 (&acc)[4][4], PRE pre, MID mid) {
  const int lane = c.lane, i = lane & 15, g = lane >> 4, wm = w & 1, wn = w >> 1;
  const int rm = (i >> 3) * 1024 + (i & 7) * 128 + ((g ^ ((i >> 1) & 7)) << 4);
  lds_ptr pa0 = c.lds + QKV_A + wm * 8192 + (layer == 0 ? rm : lane * 16);
  lds_ptr pa1 = c.lds + QKV_A + wm * 8192 + (layer == 0 ? rm ^ 64 : lane * 16 + 1024);
  const int f = (i >> 2) | ((g & 1) << 2), rb = (g * 8 + (i >> 2)) * 256 + (i & 3) * 8;
  lds_ptr pb[4];
#pragma unroll
  for (int t = 0; t < 4; ++t) pb[t] = c.lds + rb + (((wn * 4 + t) ^ f) << 5);
  bf16x8 x0[4], x1[4], w0[4], w1[4];
  auto step = [&](const bf16x8* xa, const bf16x8* wb, bf16x8* xn, bf16x8* wn_, lds_ptr pa, int bo, bool ld) {
#pragma unroll
    for (int q = 0; q < 16; ++q) {
      if (ld) {
        if ((q & 3) == 0) wn_[q >> 2] = ldstr(pb[q >> 2] + bo, 1024);
        if ((q & 3) == 2) xn[q >> 2] = ldsr16(pa + (q >> 2) * 2048);
      }
      const int t = q >> 2, m = q & 3;
      acc[m][t] = SWAP ? mfma16(wb[t], xa[m], acc[m][t]) : mfma16(xa[m], wb[t], acc[m][t]);
      SBAR();
    }
  };
  SLOT(layer * 5);
  if (layer) STAMP(0);
  mid(0); mid(4); mid(8); mid(12);
  STAMP(1);
  BARRIER();
  STAMP(2); STAMPC(0); WRESET();
#pragma unroll
  for (int e = 0; e < 4; ++e) { w0[e] = ldstr(pb[e], 1024); x0[e] = ldsr16(pa0 + e * 2048); }
  int sa = 0, sb = 0;
#pragma unroll 1
  for (int st = 0; st < 9; ++st) {
    step(x0, w0, x1, w1, pa1, 8192, true);
    const int da = sa == QKV_NA - 1 ? -(QKV_NA - 1) * 16384 : 16384, db = sb == QKV_NB - 1 ? -(QKV_NB - 1) * 16384 : 16384;
    sa = sa == QKV_NA - 1 ? 0 : sa + 1; sb = sb == QKV_NB - 1 ? 0 : sb + 1;
    pa0 += da; pa1 += da; pb[0] += db; pb[1] += db; pb[2] += db; pb[3] += db;
    if (LAG0 == 1 && layer == 0) asm volatile("s_waitcnt lgkmcnt(0)" ::: "memory");
    BARRIER_NW();
    step(x1, w1, x0, w0, pa0, 0, true);
  }
  step(x0, w0, x1, w1, pa1, 8192, true);
  BARRIER();
  pre(); SBAR();
  step(x1, w1, x0, w0, pa0, 0, false);
  STAMP(3); STAMPC(1);
}
DI unsigned qkv_w_off(const Ctx& c, int q) {
  const int row = q * 4 + (c.lane >> 4);
  return row * 3840 + qkv_cb(c) * 256 + (((c.lane & 15) ^ (QKV16 ? ((row & 3) << 1) | (row & 8) : (row & 3) << 2)) << 4);
}
DI void qkv_helper(Ctx c, u64& tgt, int hw, int layer, const uint16_t* x, const uint16_t* wqkv) {
  const int panel = qkv_panel(c), cb = qkv_cb(c), l = c.lane;
  const bool on = cb != 15;
  if (layer == 0) {
    Phase0 p0;
    p0.load(c, hw, x, wqkv);
    if (on) BARRIER_L();
    p0.part1(c);
    BARRIER_L();
    p0.part2(c, hw);
    if (on) BARRIER_L();
    p0.cols(c, hw);
    c.tgt = tgt = epoch(c.ws);
    if (hw == 0 && l == 0) *(LAS u64*)(c.lds + M_TGT) = tgt;
    if (on) BARRIER_L();
    publish(c, M_CNT2, 4, flg(FL_P0, blockIdx.x));
    if (!on) return;
    BARRIER_L(); BARRIER_L(); BARRIER_L();
    const unsigned at = flg(FL_P0, hw < 2 ? panel * 16 + hw * 8 + (l & 7) : (l & 7) * 30 + cb * 2 + ((l >> 3) & 1));
    const u64 v = __hip_atomic_load((const GAS u64*)(c.ws + at), __ATOMIC_RELAXED, __HIP_MEMORY_SCOPE_AGENT);
    BARRIER_L();
    if (!__all(v == c.tgt)) poll(c, at);
    if (hw < 2) {
      f32x2 p[8];
      ln_load(c, WS_ST + panel * 8192, hw * 64 + l, p);
      BARRIER_L(); BARRIER_L();
      ln_put(c, hw * 64 + l, ln_sum(p));
    } else if (hw == 2) {
      u32x4 q[8];
#pragma unroll
      for (int i = 0; i < 8; ++i) q[i] = ld16(c.r, WS_CSQ + cb * 512 + i * 7680 + (l & 31) * 16);
      BARRIER_L(); BARRIER_L();
      ln_cols_sum<8>(c, 32, q);
    } else { BARRIER_L(); BARRIER_L(); }
    BARRIER_L();
  } else if (on) {
    if (hw == 2) ln_cols<8>(c, 32, WS_CSQ + cb * 512, 7680);
    BARRIER_L(); BARRIER_L(); BARRIER_L(); BARRIER_L(); BARRIER_L(); BARRIER_L();
    if (hw < 2) {
      f32x2 p[8];
      ln_load(c, WS_ST + 2 * STB + panel * 8192, hw * 64 + l, p);
      BARRIER_L(); BARRIER_L();
      ln_put(c, hw * 64 + l, ln_sum(p));
    } else { BARRIER_L(); BARRIER_L(); }
    BARRIER_L(); BARRIER_L(); BARRIER_L();
  }
}
DI void qkv_compute(Ctx c, u64& tgt, int w, int layer, const uint16_t* x, const uint16_t* wqkv) {
  const int panel = qkv_panel(c), cb = qkv_cb(c);
  if (w >= 4) { qkv_helper(c, tgt, w - 4, layer, x, wqkv); return; }
  if (cb == 15) { if (layer == 0) { tgt = epoch(c.ws); wait_vm<0>(); BARRIER(); } return; }
  const int lane = c.lane, i = lane & 15, g = lane >> 4, wm = w & 1, wn = w >> 1;
  const unsigned L = WS_L + layer * L_SZ;
  f32x4 acc[4][4];
#pragma unroll
  for (int e = 0; e < 16; ++e) acc[e >> 2][e & 3] = f32x4{0.f, 0.f, 0.f, 0.f};
  const LAS float* srs = (const LAS float*)(c.lds + M_SST) + wm * 64;
  const LAS float* scs = (const LAS float*)(c.lds + M_SCS) + wn * 64;
  if (cb < 10) {
    float rs[4], nm[4]; f32x4 cs[4]; unsigned ad[4][2];
    const float qs = cb < 5 ? 0.22360679774997896f * 1.4426950408889634f : 1.f;
    const unsigned i1 = L + (cb < 5 ? L_Q1 : L_K1), i2 = L + (cb < 5 ? L_Q2 : L_K2);
    qkv_loop<true>(c, w, layer, acc, [&]() {
#pragma unroll
      for (int e = 0; e < 4; ++e) { rs[e] = srs[e * 16 + i]; nm[e] = srs[128 + e * 16 + i]; cs[e] = *(const LAS f32x4*)(scs + e * 16 + g * 4); }
    }, [&](int q) {
      const int t = q >> 2, o = (cb % 5) * 16 + wn * 8 + t * 2 + (g >> 1), hp = o / 5, pos = o - hp * 5;
      const unsigned ts = pos == 2 ? 512 : 1024;
      ad[t][0] = (pos == 2 ? i2 + hp * 32768 : i1 + (2 * hp + (pos > 2)) * 65536 + (pos > 2 ? pos - 3 : pos) * 512) +
                 (panel * 4 + wm * 2) * ts + ((g & 1) * 16 + i) * 16;
      ad[t][1] = ad[t][0] + ts;
      asm volatile("" : "+v"(ad[t][0]), "+v"(ad[t][1]));
    });
#pragma unroll
    for (int e = 0; e < 4; ++e) rs[e] *= qs;
#pragma unroll
    for (int t = 0; t < 4; ++t) {
#pragma unroll
      for (int mp = 0; mp < 2; ++mp) {
        const f32x4 u = (acc[mp * 2][t] + cs[t] * nm[mp * 2]) * rs[mp * 2], v = (acc[mp * 2 + 1][t] + cs[t] * nm[mp * 2 + 1]) * rs[mp * 2 + 1];
        st16(c.r, ad[t][mp], pair16(pk2(u[0], u[1]), pk2(u[2], u[3]), pk2(v[0], v[1]), pk2(v[2], v[3])));
      }
    }
  } else {
    float cs[4]; f32x4 rs[4], nm[4]; unsigned ad[4];
    qkv_loop<false>(c, w, layer, acc, [&]() {
#pragma unroll
      for (int e = 0; e < 4; ++e) { cs[e] = scs[e * 16 + i]; rs[e] = *(const LAS f32x4*)(srs + e * 16 + g * 4); nm[e] = *(const LAS f32x4*)(srs + 128 + e * 16 + g * 4); }
    }, [&](int q) {
      const int t = q >> 2, cv = (cb - 10) * 128 + wn * 64 + t * 16 + i, head = cv / 20, dim = cv - head * 20;
      const int d = (head & 1) ? (dim < 4 ? dim + 16 : dim - 4) : dim;
      ad[t] = L + L_V + head * 81920 + (panel * 8 + wm * 4 + (g & 1)) * 640 + (g >> 1) * 320 + d * 16;
      PIN(ad[t]);
    });
#pragma unroll
    for (int t = 0; t < 4; ++t) {
      const unsigned a0 = ad[t];
#pragma unroll
      for (int mp = 0; mp < 2; ++mp) {
        const f32x4 u = (acc[mp * 2][t] + nm[mp * 2] * cs[t]) * rs[mp * 2], v = (acc[mp * 2 + 1][t] + nm[mp * 2 + 1] * cs[t]) * rs[mp * 2 + 1];
        st16(c.r, a0 + mp * 1280, pair16(pk2(u[0], u[1]), pk2(u[2], u[3]), pk2(v[0], v[1]), pk2(v[2], v[3])));
      }
    }
  }
  if (layer == 0) {
    const u32x2 t = *(const LAS u32x2*)(c.lds + M_TGT);
    c.tgt = tgt = (u64)(unsigned)__builtin_amdgcn_readfirstlane(t[1]) << 32 | (unsigned)__builtin_amdgcn_readfirstlane(t[0]);
  }
  SLOT(layer * 5);
  STAMP(4);
  publish(c, M_CNT4, 4, flg(FL_QKV + layer * 5, panel * 16 + cb), true);
  STAMP(5);
}
typedef Ring<8, QKV_NB, 10, 2> QkvB;
DI void qkv_load0(const Ctx& c, int lw, const uint16_t* x, const uint16_t* wqkv) {
  const int half = lw & 1;
  if (lw >= 2) {
    Ring<8, QKV_NB, 10, LAG0> rg;
#pragma unroll
    for (int p = 0; p < 8; ++p) rg.voff[p] = qkv_w_off(c, half * 8 + p);
    rg.init((const char*)wqkv, 64 * 3840, 0, half);
    rg.template fill<Q0_D0>(); rg.template run<Q0_D0>();
  } else {
    Ring<8, QKV_NA, 10, LAG0> rg;
#pragma unroll
    for (int p = 0; p < 8; ++p) {
      const int mt = qkv_panel(c) * 8 + half * 4 + (p >> 1), r16 = (p & 1) * 8 + (c.lane >> 3);
      rg.voff[p] = (mt * 16 + r16) * 1280 + (((c.lane & 7) ^ ((r16 >> 1) & 7)) << 4);
    }
    rg.init((const char*)x, 128, QKV_A, half);
    rg.template fill<Q0_D0>(); rg.template run<Q0_D0>();
  }
}
DI void qkv_load1(const Ctx& c, int lw, const uint16_t* wqkv) {
  const int half = lw & 1;
  static_assert(QKV_NA == 4 && QKV_NB == 5, "schedule");
  if (lw >= 2) {
    QkvB rg;
#pragma unroll
    for (int p = 0; p < 8; ++p) rg.voff[p] = qkv_w_off(c, half * 8 + p);
    rg.init((const char*)wqkv, 64 * 3840, 0, half);
    sig_wait(c, 12, M_CNT4);
    rg.template fill_ahead<1>(c, 6);
    rg.rest();
  } else {
    Ring<8, QKV_NA, 10, 2> rg;
#pragma unroll
    for (int p = 0; p < 8; ++p) rg.voff[p] = WS_X2 + (qkv_panel(c) * 8 + half * 4 + (p >> 1)) * 20480 + (p & 1) * 1024 + c.lane * 16;
    rg.init(c.ws, 2048, QKV_A, half);
    Poll<1> pl;
    gate(c, pl, half, flg(FL_DN, qkv_panel(c) * 16 + (c.lane & 15)), 6, 28);
    rg.template fill<2>(); rg.template run<2>();
    pl.keep();
  }
}

DI f32x16 qk8(s16x4 k2, s16x4 q2) {
 return __builtin_amdgcn_mfma_f32_32x32x8bf16_1k(k2, q2, f32x16{}, 0, 0, 0); }
DI f32x16 qk16(bf16x8 k1, bf16x8 q1, f32x16 s) { return __builtin_amdgcn_mfma_f32_32x32x16_bf16(k1, q1, s, 0, 0, 0); }
DI f32x16 pv_lo(bf16x8 v, f32x16 p, f32x16 o) {
  return __builtin_amdgcn_smfmac_f32_32x32x32_bf16(v, __builtin_bit_cast(bf16x16, (f32x8)__builtin_shufflevector(p, p, 0, 1, 2, 3, 4, 5, 6, 7)), o, 0xDDDD, 0, 0);
}
DI f32x16 pv_hi(bf16x8 v, f32x16 p, f32x16 o) {
  return __builtin_amdgcn_smfmac_f32_32x32x32_bf16(v, __builtin_bit_cast(bf16x16, (f32x8)__builtin_shufflevector(p, p, 8, 9, 10, 11, 12, 13, 14, 15)), o, 0xDDDD, 0, 0);
}
DI float swap32(float v, int h) {
  auto r = __builtin_amdgcn_permlane32_swap(__float_as_uint(v), __float_as_uint(v), false, false);
  return __uint_as_float(h ? r[0] : r[1]);
}
#define EXP4(X, i) do { X[i] = __builtin_amdgcn_exp2f(X[i]); X[i + 1] = __builtin_amdgcn_exp2f(X[i + 1]); X[i + 2] = __builtin_amdgcn_exp2f(X[i + 2]); \
                        X[i + 3] = __builtin_amdgcn_exp2f(X[i + 3]); PIN(X); SBAR(); } while (0)
DI void att_compute(const Ctx& c, int w, int layer) {
  const int lane = c.lane, head = att_head(c), qb = att_qb(c);
  const int qp = w & 3, kp = w >> 2, r32 = lane & 31, h = lane >> 5;
  lds_ptr lds = c.lds;
  const unsigned L = WS_L + layer * L_SZ;
  SLOT(layer * 5 + 1);
  STAMP(0);
  for (int s = w * 64 + lane; s < 672; s += 512) {
    const unsigned v = s % 20 == 0 ? 0x3f803f80u : 0u;
    *(LAS u32x4*)(lds + AT_CONST + s * 16) = u32x4{v, v, v, v};
  }
  lds_ptr pk1 = lds + kp * 8192 + lane * 16;
  lds_ptr pk2_ = h ? lds + AT_CONST + 16 : lds + 16384 + kp * 4096 + r32 * 16 + (head & 1) * 8;
  lds_ptr pv = (r32 >= 20 ? lds + AT_CONST : lds + 24576 + kp * 10240) + h * 320 + r32 * 16;
  const int vmask = r32 >= 20 ? 0 : -1, kmask = h ? 0 : -1;
  const int qt = qb * 8 + qp * 2 + kp, tok = qt * 32 + r32;
  unsigned ob16 = L + L_O + (tok >> 4) * 20480 + (head * 2 + h) * 256 + (tok & 15) * 16, ob8 = L + L_O + (tok >> 4) * 20480 + 16384 + head * 128 + (tok & 15) * 8;
  asm volatile("" : "+v"(ob16), "+v"(ob8));
  f32x16 o0 = f32x16{}, o1 = f32x16{};
  f32x16 s0, s1, s2, s3 = f32x16{};
  STAMP(1);
  BARRIER();
  STAMP(2); STAMPC(0); WRESET();
  const bf16x8 q1a = ldsr16(lds + 2 * AT_SLOT + (qp * 2 + kp) * 1024 + lane * 16), q1b = ldsr16(lds + 2 * AT_SLOT + (qp * 2 + 1 - kp) * 1024 + lane * 16);
  lds_ptr pq2 = h ? lds + AT_CONST + 16 : lds + 2 * AT_SLOT + 8192 + qp * 1024 + r32 * 16 + (head & 1) * 8;
  const s16x4 q2a = *(const LAS s16x4*)(pq2 + kp * 512), q2b = *(const LAS s16x4*)(pq2 + 512 - kp * 512);
  bf16x8 k1 = ldsr16(pk1), vlo = ldsr16(pv), vhi = bf16x8{};
  s16x4 k2 = *(const LAS s16x4*)pk2_;
  s0 = qk16(k1, q1a, qk8(k2, q2a));
#define PH_EVEN(SC, SN, SP, KLOAD, VOFF) do { \
    SN = qk8(k2, q2b); SBAR(); EXP4(SC, 0); SN = qk16(k1, q1b, SN); SBAR(); KLOAD; SBAR(); EXP4(SC, 4); \
    o1 = pv_hi(vhi, SP, o1); SBAR(); vhi = ldsr16(pv + (VOFF) + 640); SBAR(); EXP4(SC, 8); o0 = pv_lo(vlo, SC, o0); SBAR(); EXP4(SC, 12); } while (0)
#define PH_ODD(SC, SN, SP, VLOAD) do { \
    SN = qk8(k2, q2a); SBAR(); EXP4(SC, 0); SN = qk16(k1, q1a, SN); SBAR(); EXP4(SC, 4); \
    o0 = pv_hi(vhi, SP, o0); SBAR(); EXP4(SC, 8); o1 = pv_lo(vlo, SC, o1); SBAR(); VLOAD; SBAR(); EXP4(SC, 12); } while (0)
#define KLD(t) do { k1 = ldsr16(pk1 + (t) * 1024); k2 = *(const LAS s16x4*)(pk2_ + (t) * 512); } while (0)
#define PRI(v) do { __builtin_amdgcn_s_setprio(v); SBAR(); } while (0)
#define HI(n) if (ATT_PRIO == (n)) PRI(1);
#define LO(n) if (ATT_PRIO == (n)) PRI(0);
#define TWO(t) HI(3) HI(4) HI(5) PH_EVEN(s0, s1, s3, KLD((t) + 1), (t) * 1280); PH_ODD(s1, s2, s0, vlo = ldsr16(pv + ((t) + 1) * 1280)); LO(4)
  for (int grp = 0; grp < 4; ++grp) {
    TWO(0) PH_EVEN(s2, s3, s1, if (grp == 0) BARRIER_NW(); KLD(2), 1280); LO(3) PH_ODD(s3, s0, s2, vlo = ldsr16(pv + 2 * 1280));
    TWO(2) PH_EVEN(s2, s3, s1, KLD(4), 3 * 1280); LO(3) LO(5) PH_ODD(s3, s0, s2, vlo = ldsr16(pv + 4 * 1280));
    TWO(4) PH_EVEN(s2, s3, s1, KLD(6), 5 * 1280); LO(3) PH_ODD(s3, s0, s2, vlo = ldsr16(pv + 6 * 1280));
    TWO(6)
    const int d = grp == 2 ? -2 * AT_SLOT : grp == 3 ? 0 : AT_SLOT;
    PH_EVEN(s2, s3, s1, pk1 += d; pk2_ += (d & kmask); if (grp != 3) BARRIER(); KLD(0), 7 * 1280);
    LO(3) LO(5) PH_ODD(s3, s0, s2, pv += (d & vmask); vlo = ldsr16(pv));
  }
  o1 = pv_hi(vhi, s3, o1);
  STAMP(3); STAMPC(1);
  __builtin_amdgcn_s_setprio(0);
  const f32x16 give = o1, keep = o0;
#pragma unroll
  for (int e = 0; e < 3; ++e)
    *(LAS f32x4*)(lds + AT_MERGE + (w * 3 + e) * 1024 + lane * 16) = f32x4{give[4 * e], give[4 * e + 1], give[4 * e + 2], give[4 * e + 3]};
  BARRIER();
  f32x4 a[3];
#pragma unroll
  for (int e = 0; e < 3; ++e)
    a[e] = f32x4{keep[4 * e], keep[4 * e + 1], keep[4 * e + 2], keep[4 * e + 3]} + *(const LAS f32x4*)(lds + AT_MERGE + ((w ^ 4) * 3 + e) * 1024 + lane * 16);
  float l = h ? a[2][0] : 0.f;
  l += swap32(l, h);
  if (__builtin_expect(__any(!(l >= 5.421e-20f && l <= 1.8447e19f)), 0)) {
    const unsigned o2 = (head >> 1) * 32768 + r32 * 16 + (head & 1) * 8;
    const bf16x8 fq1 = __builtin_bit_cast(bf16x8, ld16(c.r, L + L_Q1 + head * 65536 + qt * 1024 + lane * 16));
    u32x2 fq2 = ld8(c.r, L + L_Q2 + o2 + qt * 512);
    if (h) fq2 = u32x2{0u, 0u};
    const unsigned vi = L + L_V + head * 81920 + h * 320 + (r32 < 20 ? r32 : 0) * 16;
    f32x16 oo = f32x16{};
    float m = -INFINITY;
    const u32x4 one8 = {0x3f803f80u, 0x3f803f80u, 0x3f803f80u, 0x3f803f80u};
    for (int t = 0; t < 64; ++t) {
      const u32x4 fk1 = ld16(c.r, L + L_K1 + head * 65536 + t * 1024 + lane * 16);
      const u32x2 fk2 = ld8(c.r, L + L_K2 + o2 + t * 512);
      u32x4 v0 = ld16(c.r, vi + t * 1280), v1 = ld16(c.r, vi + t * 1280 + 640);
      if (r32 >= 20) { v0 = one8; v1 = one8; }
      f32x16 s = qk16(__builtin_bit_cast(bf16x8, fk1), fq1, qk8(__builtin_bit_cast(s16x4, fk2), __builtin_bit_cast(s16x4, fq2)));
      float tm = s[0];
#pragma unroll
      for (int r = 1; r < 16; ++r) tm = fmaxf(tm, s[r]);
      tm = fmaxf(tm, swap32(tm, h));
      const float mn = fmaxf(m, tm), al = __builtin_amdgcn_exp2f(m - mn);
      m = mn;
#pragma unroll
      for (int r = 0; r < 16; ++r) { s[r] = __builtin_amdgcn_exp2f(s[r] - mn); oo[r] *= al; }
      oo = pv_hi(__builtin_bit_cast(bf16x8, v1), s, pv_lo(__builtin_bit_cast(bf16x8, v0), s, oo));
    }
#pragma unroll
    for (int e = 0; e < 3; ++e) a[e] = f32x4{oo[4 * e], oo[4 * e + 1], oo[4 * e + 2], oo[4 * e + 3]};
    l = h ? a[2][0] : 0.f;
    l += swap32(l, h);
  }
  const float inv = __builtin_amdgcn_rcpf(l);
  const unsigned A0 = pk2(a[0][0] * inv, a[0][1] * inv), A1 = pk2(a[0][2] * inv, a[0][3] * inv);
  const unsigned B0 = pk2(a[1][0] * inv, a[1][1] * inv), B1 = pk2(a[1][2] * inv, a[1][3] * inv);
  const u32x2 C = {pk2(a[2][0] * inv, a[2][1] * inv), pk2(a[2][2] * inv, a[2][3] * inv)};
  st16(c.r, ob16, pair32(A0, A1, B0, B1));
  if (!h) st8(c.r, ob8, C);
  STAMP(4);
  publish(c, M_CNT, 8, flg(FL_ATT + layer * 5, qb * 32 + head), true);
  STAMP(5);
}
DI void retile_load(const Ctx& c, int J, u32x4 (&v)[5][4], const uint16_t* wo, const uint16_t* wg, const uint16_t* wu, const uint16_t* wd) {
  const int e = J < 216 ? J : J < 256 ? J - 216 : J - 256;
  const int strip = J < 216 ? (e >> 2) % 27 : e % 10, k0 = J < 216 ? (e & 3) * 5 : min((e / 10) * 5, 49), ld = J < 216 ? 3456 : 1280;
  const rsrc_t r = __builtin_amdgcn_make_buffer_rsrc((void*)(J < 108 ? wg : J < 216 ? wu : J < 256 ? wo : wd), 0,
                                                     J < 216 ? 640 * 3456 : J < 256 ? 640 * 1280 : 1728 * 1280, 0x00020000);
  const unsigned vo = (c.lane >> 3) * ld + (c.lane & 7) * 16;
#pragma unroll
  for (int u = 0; u < 5; ++u)
#pragma unroll
    for (int q = 0; q < 4; ++q) {
      const int ch = (k0 + u) * 4 + q;
      const int row = J < 216 || J >= 256 ? ch * 8 : ch < 64 ? (ch >> 1) * 20 + (ch & 2) * 2 + (ch & 1) * 8 : (ch - 64) * 40 + 16;
      v[u][q] = __builtin_amdgcn_raw_buffer_load_b128(r, vo, row * ld + strip * 128, 0);
    }
}
DI void retile_store(const Ctx& c, int J, lds_ptr scr, const u32x4 (&v)[5][4]) {
  const int e = J < 216 ? J : J < 256 ? J - 216 : J - 256, mat = J >= 108;
  const int strip = J < 216 ? (e >> 2) % 27 : e % 10, k0 = J < 216 ? (e & 3) * 5 : min((e / 10) * 5, 49);
  const unsigned ts = J < 216 ? 40960 : J < 256 ? 20480 : 55296;
  const unsigned dst = (J < 216 ? WS_WGU + mat * 20480 : J < 256 ? WS_WO : WS_WD) + strip * 4 * ts + k0 * 1024;
  const int i = c.lane & 15, g = c.lane >> 4;
  const bf16x8 ones = __builtin_bit_cast(bf16x8, u32x4{0x3f803f80u, 0x3f803f80u, 0x3f803f80u, 0x3f803f80u});
  f32x4 cs[4] = {f32x4{}, f32x4{}, f32x4{}, f32x4{}};
#pragma unroll
  for (int u = 0; u < 5; ++u) {
#pragma unroll
    for (int p = 0; p < 4; ++p) *(LAS u32x4*)(scr + p * 1024 + c.lane * 16) = v[u][p];
#pragma unroll
    for (int t = 0; t < 4; ++t) {
      const bf16x8 f = ldstr(scr + (g * 8 + (i >> 2)) * 128 + t * 32 + (i & 3) * 8, 512);
      __builtin_amdgcn_raw_buffer_store_b128(__builtin_bit_cast(u32x4, f), c.r, c.lane * 16, dst + u * 1024 + t * ts, 16);
      cs[t] = mfma16(f, ones, cs[t]);
    }
  }
  if (J < 216 && i == 0) {
#pragma unroll
    for (int t = 0; t < 4; ++t)
      __builtin_amdgcn_raw_buffer_store_b128(__builtin_bit_cast(u32x4, cs[t]), c.r, g * 16, WS_CSG + ((e & 3) * 3456 + (strip * 8 + t * 2 + mat) * 16) * 4, 16);
  }
}
DI int early_piece(int e) { return e < 4 ? (e & 1) + (e >> 1) * 8 : e < 6 ? 16 + (e - 4) * 4 : e < 9 ? 18 + e : 25 + e; }
DI int late_piece(int e) { return e < 6 ? 2 + e : e < 12 ? 4 + e : e < 15 ? 5 + e : e < 18 ? 6 + e : e < 25 ? 9 + e : 12 + e; }
DI void att_load(const Ctx& c, int lw, int layer, unsigned iss, const uint16_t* wo, const uint16_t* wg, const uint16_t* wu, const uint16_t* wd) {
  const int head = att_head(c), qb = att_qb(c), lane = c.lane;
  const char* Lp = c.ws + WS_L + layer * L_SZ;
  const unsigned vo = lane * 16;
  int grp = ATT_ROT ? qb >> 1 : 0;
  auto src_of = [&](int p) {
    return p < 16 ? L_K1 + head * 65536 + grp * 16384 + p * 1024
                  : p < 24 ? L_K2 + (head >> 1) * 32768 + grp * 8192 + (p - 16) * 1024 : L_V + head * 81920 + grp * 20480 + (p - 24) * 1024;
  };
  unsigned v0[14], m0[11], vg[11], sg[11];
#pragma unroll
  for (int q = 0; q < 11; ++q) {
    const int p = q < 3 ? early_piece(lw * 3 + q) : late_piece(lw * 8 + q - 3), pg = lw * 11 + q;
    v0[q] = src_of(p) + vo; m0[q] = p * 1024;
    vg[q] = src_of(pg) + vo; sg[q] = pg < 16 ? 16384 : pg < 24 ? 8192 : 20480;
    asm volatile("" : "+v"(v0[q]), "+s"(m0[q]));
  }
#pragma unroll
  for (int q = 0; q < 3; ++q) {
    const int p = lw * 3 + q;
    v0[11 + q] = (p < 8 ? L_Q1 + head * 65536 + qb * 8192 + p * 1024 : L_Q2 + (head >> 1) * 32768 + qb * 4096 + (p - 8) * 1024) + vo - q * 1024;
    PIN(v0[11 + q]);
  }
  auto issue = [&](int slot) {
#pragma unroll
    for (int q = 0; q < 11; ++q) { vg[q] += grp == 3 ? -3 * sg[q] : sg[q]; glds1(slot * AT_SLOT + (lw * 11 + q) * 1024, Lp, vg[q]); }
    grp = (grp + 1) & 3;
  };
  const int o0 = (head >> 1) * 5 + (head & 1) * 2, c0 = head * 20;
  auto kv_cb = [&](int sel) { return sel == 0 ? 5 + (o0 >> 4) : sel == 1 ? 5 + ((o0 + 2) >> 4) : sel == 2 ? 10 + (c0 >> 7) : 10 + ((c0 + 19) >> 7); };
  if (lw == 0) {
    sig_wait(c, iss, M_ISS);
    poll(c, flg(FL_QKV + layer * 5, lane & 16 ? (qb * 2 + (lane & 1)) * 16 + ((o0 + (lane & 2)) >> 4) : (grp * 4 + (lane & 3)) * 16 + kv_cb((lane >> 2) & 3)));
    sig_set(c, layer * 5 + 2);
    STAMPL(layer * 5 + 1, 6);
  } else sig_wait(c, layer * 5 + 2);
  glds3(2 * AT_SLOT + lw * 3072, Lp, v0[11], v0[12], v0[13]);
#pragma unroll
  for (int q = 0; q < 3; ++q) glds1(m0[q], Lp, v0[q]);
#pragma unroll
  for (int q = 3; q < 3 + ATT_LATE; ++q) glds1(m0[q], Lp, v0[q]);
  wait_vm<ATT_LATE>(); BARRIER();
#pragma unroll
  for (int q = 3 + ATT_LATE; q < 11; ++q) glds1(m0[q], Lp, v0[q]);
  wait_vm<0>(); BARRIER();
  if (lw == 0) { poll(c, flg(FL_QKV + layer * 5, (lane & 15) * 16 + kv_cb(lane >> 4))); sig_set(c, layer * 5 + 3); } else sig_wait(c, layer * 5 + 3, M_SIG, true);
  issue(1);
  const int J = (lw - 1) * 128 + c.j * 4 + (c.xcd >> 1);
  const bool rt = layer == 0 && lw > 0 && J < 366 && !(c.xcd & 1);
  u32x4 v[5][4];
  if (rt) { retile_load(c, J, v, wo, wg, wu, wd); wait_vm<20>(); } else wait_vm<0>();
  BARRIER();
  issue(2);
  if (rt) retile_store(c, J, c.lds + AT_SCR + (lw - 1) * 4096, v);
  if (layer == 0 && lw > 0) publish(c, M_CNT3, 3, flg(FL_RT, blockIdx.x));
  wait_vm<0>(); BARRIER();
  issue(0);
  wait_vm<0>();
  if (layer == 0) {
    if (lw == 0) {
      if (c.j == 0) {
        Poll<2> pl;
#pragma unroll 1
        for (int q = 0; q < 2; ++q) { pl.run(c, flg(FL_RT, q * 128 + lane), flg(FL_RT, q * 128 + 64 + lane)); wait_vm<0>(); pl.keep(); }
        if (lane < 32) __hip_atomic_store((GAS u64*)(c.ws + flg(FL_RT2, lane * 8 + c.xcd)), c.tgt, __ATOMIC_RELAXED, __HIP_MEMORY_SCOPE_AGENT);
      }
      poll(c, flg(FL_RT2, blockIdx.x));
      if (blockIdx.x == 0 && lane == 0) *(u64*)(c.ws + WS_EPOCH) = c.tgt;
    }
  }
  BARRIER();
  u32x4 pk[25];
#pragma unroll
  for (int e = 0; e < 25; ++e) pk[e] = ld16s(c.r, vo, WS_WO + (pr_cb(c) * 5 + e % 5) * 20480 + (lw * 5 + e / 5) * 1024);
#pragma unroll 1
  for (unsigned q = lw; q < 35; q += 4) glds1(AT_SCR + lw * 1024, c.ws + WS_WGU + ((gu_cb(c) * 27) >> 2) * 40960 + ((c.j & 7) * 35 + q) * 1024, vo);
  wait_vm<0>();
  BARRIER();
  sig_wait(c, layer * 16 + 8, M_CNT);
#pragma unroll
  for (int e = 0; e < 25; ++e) { const int k = lw * 5 + e / 5; ldsw16(c.lds + (k >> 1) * 10240 + (k & 1) * 1024 + (e % 5) * 2048 + vo, pk[e]); }
}

template <int NS, int NB, int WIN>
DI void proj_compute(const Ctx& c, int w, int slot_id, unsigned a_off, const char* res_rm, unsigned res_off, unsigned out_off, unsigned st_off,
                     uint16_t* out, int arr) {
  constexpr bool RES = NS <= NB;
  const int panel = pr_panel(c), cb = pr_cb(c);
  SLOT(slot_id);
  STAMP(0);
  if (w >= 4) {
    if constexpr (RES) { BARRIER(); BARRIER(); if (PROJ2) BARRIER(); }
    else {
      static_assert(RES || (NS == 27 && NB == 15 && WIN == 18), "schedule");
      const int par = (w >> 1) & 1, half = w & 1;
      u32x4 pk[15];
#pragma unroll
      for (int e = 0; e < 15; ++e) {
        const int q = half * 5 + e % 5;
        pk[e] = ld16s(c.r, c.lane * 16, WS_WD + (cb * 5 + (q >> 1)) * 55296 + ((15 + par + (e / 5) * 2) * 2 + (q & 1)) * 1024);
      }
      lds_ptr pw = c.lds + par * 10240 + half * 5120 + c.lane * 16;
      BARRIER(); BARRIER();
#pragma unroll
      for (int e = 0; e < 10; ++e) ldsw16(pw + (e / 5) * 20480 + (e % 5) * 1024, pk[e]);
      BARRIER();
#pragma unroll
      for (int e = 10; e < 15; ++e) ldsw16(pw + 40960 + (e % 5) * 1024, pk[e]);
      BARRIER(); BARRIER(); BARRIER(); BARRIER();
      if (PROJ2) BARRIER();
    }
    return;
  }
  const int lane = c.lane, i = lane & 15, g = lane >> 4, mt = panel * 4 + w;
  u32x2 res[5];
#pragma unroll
  for (int t = 0; t < 5; ++t)
    res[t] = res_rm ? *(const u32x2*)(res_rm + (mt * 16 + i) * 1280 + cb * 160 + t * 32 + g * 8)
                    : ld8(c.r, res_off + (mt * 80 + cb * 10 + t * 2 + (g >> 1)) * 256 + i * 16 + (g & 1) * 8);
  f32x4 acc[5];
#pragma unroll
  for (int t = 0; t < 5; ++t) { acc[t] = f32x4{bflo(res[t][0]), bfhi(res[t][0]), bflo(res[t][1]), bfhi(res[t][1])}; PIN(acc[t]); }
  float piv;
  { const f32x4 s4 = (acc[0] + acc[1]) + (acc[2] + acc[3]) + acc[4]; piv = rows4((s4[0] + s4[1]) + (s4[2] + s4[3])) * (1.f / 80.f); }
  const int ch = (g & 1) * 2 + (g >> 1);
  unsigned xo = out ? (mt * 16 + i) * 1280 + cb * 160 + ch * 16 : out_off + (mt * 80 + cb * 10 + ch) * 256 + i * 16;
  unsigned so = st_off + (mt >> 3) * 8192 + cb * 1024 + ((mt & 7) * 16 + i) * 8;
  asm volatile("" : "+v"(piv), "+v"(xo), "+v"(so));
  lds_ptr pb = c.lds + lane * 16;
  unsigned av = a_off + mt * (NS * 2048) + lane * 16;
  u32x4 a[WIN];
  bf16x8 f[3][5];
  int sb = 0;
  STAMP(1);
  BARRIER();
  STAMP(2); STAMPC(0); WRESET();
  constexpr int AH = RES ? WO_AH : DN_AH;
  auto lda = [&](int k) {
    if (RES && k >= 16) {
      const unsigned o = a_off + mt * 20480 + 16384 + (k - 16) * 1024 + g * 256 + i * 8;
      const u32x2 lo = ld8(c.r, o), hi = ld8(c.r, o + 128);
      a[k % WIN] = u32x4{lo[0], lo[1], hi[0], hi[1]};
    } else a[k % WIN] = ld16(c.r, av + k * 1024);
  };
#pragma unroll
  for (int k = 0; k < AH; ++k) lda(k);
#pragma unroll
  for (int t = 0; t < 5; ++t) { f[0][t] = ldsr16(pb + t * 2048); f[1][t] = ldsr16(pb + t * 2048 + 1024); }
  SBAR();
  auto window = [&](bool first, bool last) {
#pragma unroll
    for (int k = 0; k < WIN; ++k) {
      const bool ld = !(last && k + 2 >= WIN);
      if ((k & 1) == 0) {
        if (RES) pb += 10240;
        else { pb += sb == NB - 1 ? -(NB - 1) * 10240 : 10240; sb = sb == NB - 1 ? 0 : sb + 1; }
        if (!ld) BARRIER(); else if (!RES && (k == 10 || (k == 0 && !first))) BARRIER_NW();
      }
#pragma unroll
      for (int t = 0; t < 5; ++t) {
        if (ld) f[(k + 2) % 3][t] = ldsr16(pb + t * 2048 + (k & 1) * 1024);
        acc[t] = mfma16(f[k % 3][t], __builtin_bit_cast(bf16x8, a[k]), acc[t]);
        SBAR();
      }
      if (!last || k + AH < WIN) { lda(k + AH); SBAR(); }
    }
    av += WIN * 1024;
  };
  static_assert(2 * NS % WIN == 0 && (2 * NS == WIN || WIN % 6 == 0), "window");
#pragma unroll 1
  for (int b = 1; b < 2 * NS / WIN; ++b) window(b == 1, false);
  window(false, true);
  STAMP(3); STAMPC(1);
  u32x2 o[5];
#pragma unroll
  for (int t = 0; t < 5; ++t) {
    o[t][0] = pk2(acc[t][0], acc[t][1]); o[t][1] = pk2(acc[t][2], acc[t][3]);
  }
  const u32x4 p0 = pair16(o[0][0], o[0][1], o[1][0], o[1][1]), p1 = pair16(o[2][0], o[2][1], o[3][0], o[3][1]),
              p2 = pair16(o[4][0], o[4][1], o[4][0], o[4][1]);
  if (out) {
    char* po = (char*)out + xo;
    *(u32x4*)po = p0; *(u32x4*)(po + 64) = p1;
    if (!(g & 1)) *(u32x4*)(po + 128) = p2;
    STAMP(4);
    return;
  }
  st16(c.r, xo, p0); st16(c.r, xo + 1024, p1);
  if (!(g & 1)) st16(c.r, xo + 2048, p2);
  f32x4 s4 = acc[0] - piv, q4 = s4 * s4;
#pragma unroll
  for (int t = 1; t < 5; ++t) { const f32x4 d = acc[t] - piv; s4 += d; q4 += d * d; }
  const float sd = rows4((s4[0] + s4[1]) + (s4[2] + s4[3])), sq = rows4((q4[0] + q4[1]) + (q4[2] + q4[3]));
  if (g == 0) st8(c.r, so, u32x2{__float_as_uint(sd + 80.f * piv), __float_as_uint(fmaxf(sq - sd * sd * (1.f / 80.f), 0.f))});
  STAMP(4);
  publish(c, M_CNT4, 4, flg(arr, panel * 8 + cb), true);
  STAMP(5);
}
template <int NF>
DI void gu_consumer16(const Ctx& c, int layer, int f0, int mq) {
  const int panel = gu_panel(c), ft0 = (gu_cb(c) * 27) >> 2;
  const int lane = c.lane, i = lane & 15, g = lane >> 4;
  f32x4 acc[2][2 * NF];
#pragma unroll
  for (int m = 0; m < 2; ++m)
#pragma unroll
    for (int t = 0; t < 2 * NF; ++t) acc[m][t] = f32x4{0.f, 0.f, 0.f, 0.f};
  lds_ptr pa = c.lds + GU_A + mq * 2048 + lane * 16, pb = c.lds + f0 * 2048 + lane * 16;
  bf16x8 x0[2], x1[2], wa[NF], wb0[NF], wb1[NF];
  auto step = [&](const bf16x8* xa, const bf16x8* wb, bf16x8* xn, bf16x8* wn_, bool ld) {
#pragma unroll
    for (int q = 0; q < 4 * NF; ++q) {
      const int t = q >> 1, m = q & 1;
      if (ld) {
        if (q < 2) xn[q] = ldsr16(pa + q * 1024);
        else if (q < 2 * NF) { const int e = NF == 4 ? (q == 3 ? 0 : q == 4 ? 1 : q == 6 ? 2 : q == 7 ? 3 : -1) : NF == 3 ? q - 3 : q - 2; if (e >= 0) wn_[e] = ldsr16(pb + (NF + e) * 1024); }
        else if (q & 1) wa[(q - 2 * NF) >> 1] = ldsr16(pb + ((q - 2 * NF) >> 1) * 1024);
      }
      acc[m][t] = mfma16(t < NF ? wa[t] : wb[t - NF], xa[m], acc[m][t]);
      SBAR();
    }
  };
  int sl = 0;
  auto adv = [&]() {
    pa += sl == GU_NA - 1 ? -(GU_NA - 1) * 8192 : 8192; pb += sl == GU_NB - 1 ? -(GU_NB - 1) * 14336 : 14336;
    sl = sl == GU_NA - 1 ? 0 : sl + 1;
  };
  unsigned ho = WS_L + layer * L_SZ + L_H + ((panel * 8 + mq * 2 + (g & 1)) * 216 + (ft0 + f0) * 2 + (g >> 1)) * 256 + i * 16;
  PIN(ho);
  SLOT(layer * 5 + 3);
  STAMP(0);
  STAMP(1);
  BARRIER();
  STAMP(2); STAMPC(0); WRESET();
  x0[0] = ldsr16(pa); x0[1] = ldsr16(pa + 1024);
#pragma unroll
  for (int t = 0; t < NF; ++t) { wa[t] = ldsr16(pb + t * 1024); wb0[t] = ldsr16(pb + (NF + t) * 1024); }
#pragma unroll 1
  for (int st = 0; st < 9; ++st) {
    adv(); BARRIER_NW(); step(x0, wb0, x1, wb1, true);
    adv(); BARRIER_NW(); step(x1, wb1, x0, wb0, true);
  }
  adv(); BARRIER_NW(); step(x0, wb0, x1, wb1, true);
  BARRIER();
  const LAS float* srs = (const LAS float*)(c.lds + M_SST) + mq * 32 + i;
  const LAS float* scs = (const LAS float*)(c.lds + M_SCS) + f0 * 32 + g * 4;
  float rs[2] = {srs[0], srs[16]}; const float nm[2] = {srs[128], srs[144]};
  f32x4 cs[2 * NF];
#pragma unroll
  for (int e = 0; e < 2 * NF; ++e) cs[e] = *(const LAS f32x4*)(scs + e * 16);
  SBAR();
  step(x1, wb1, x0, wb0, false);
  STAMP(3); STAMPC(1);
  rs[0] *= rs[0]; rs[1] *= rs[1];
#pragma unroll
  for (int f = 0; f < NF; ++f) {
    unsigned p[2][2];
#pragma unroll
    for (int m = 0; m < 2; ++m) {
      f32x4 gt = acc[m][2 * f] + cs[2 * f] * nm[m];
      const f32x4 up = (acc[m][2 * f + 1] + cs[2 * f + 1] * nm[m]) * rs[m];
      gt = __builtin_elementwise_max(gt, f32x4{0.f, 0.f, 0.f, 0.f}) * up;
      p[m][0] = pk2(gt[0], gt[1]); p[m][1] = pk2(gt[2], gt[3]);
    }
    st16(c.r, ho + f * 512, pair16(p[0][0], p[0][1], p[1][0], p[1][1]));
    SBAR();
  }
  STAMP(4);
  publish(c, M_CNT, 8, flg(FL_GU + layer * 5, panel * 16 + gu_cb(c)), true);
  STAMP(5);
}
template <int NF>
DI void gu_consumer32(const Ctx& c, int layer, int f0, int mq) {
  const int panel = gu_panel(c), ft0 = (gu_cb(c) * 27) >> 2;
  const int lane = c.lane, r32 = lane & 31, h = lane >> 5;
  f32x16 acc[NF];
#pragma unroll
  for (int t = 0; t < NF; ++t) acc[t] = f32x16{};
  const int lo = (r32 >> 4) * 1024 + h * 256 + (r32 & 15) * 16;
  lds_ptr pa = c.lds + GU_A + mq * 2048 + lo, pb = c.lds + f0 * 2048 + lo;
  bf16x8 x0[2], x1[2], wa[NF], wb0[NF], wb1[NF];
  auto step = [&](const bf16x8* xa, const bf16x8* wb, bf16x8* xn, bf16x8* wn_, bool ld) {
#pragma unroll
    for (int q = 0; q < 2 * NF; ++q) {
      if (ld) {
        if (q == 0) { xn[0] = ldsr16(pa); xn[1] = ldsr16(pa + 512); }
        if (q >= 1 && q <= NF) wn_[q - 1] = ldsr16(pb + (q - 1) * 2048 + 512);
        if (q >= NF) wa[q - NF] = ldsr16(pb + (q - NF) * 2048);
      }
      if (q < NF) acc[q] = mfma32(wa[q], xa[0], acc[q]); else acc[q - NF] = mfma32(wb[q - NF], xa[1], acc[q - NF]);
      SBAR();
    }
  };
  static_assert(GU_NA == GU_NB, "one slot counter");
  int sl = 0;
  auto adv = [&]() {
    pa += sl == GU_NA - 1 ? -(GU_NA - 1) * 8192 : 8192; pb += sl == GU_NB - 1 ? -(GU_NB - 1) * 14336 : 14336;
    sl = sl == GU_NA - 1 ? 0 : sl + 1;
  };
  SLOT(layer * 5 + 3);
  STAMP(0);
  STAMP(1);
  BARRIER();
  STAMP(2); STAMPC(0); WRESET();
  x0[0] = ldsr16(pa); x0[1] = ldsr16(pa + 512);
#pragma unroll
  for (int t = 0; t < NF; ++t) { wa[t] = ldsr16(pb + t * 2048); wb0[t] = ldsr16(pb + t * 2048 + 512); }
#pragma unroll 1
  for (int st = 0; st < 9; ++st) {
    adv(); BARRIER_NW(); step(x0, wb0, x1, wb1, true);
    adv(); BARRIER_NW(); step(x1, wb1, x0, wb0, true);
  }
  adv(); BARRIER_NW(); step(x0, wb0, x1, wb1, true);
  BARRIER();
  const LAS float* srs = (const LAS float*)(c.lds + M_SST) + mq * 32 + r32;
  const LAS float* scs = (const LAS float*)(c.lds + M_SCS) + f0 * 32 + h * 4;
  float rs = srs[0], nm = srs[128];
  f32x4 cs[2][4];
#pragma unroll
  for (int e = 0; e < 8; ++e) cs[e >> 2][e & 3] = *(const LAS f32x4*)(scs + e * 8);
  SBAR();
  step(x1, wb1, x0, wb0, false);
  STAMP(3); STAMPC(1);
  rs *= rs;
  const unsigned ho = WS_L + layer * L_SZ + L_H + ((panel * 8 + mq * 2 + (r32 >> 4)) * 216 + (ft0 + f0) * 2 + h) * 256 + (r32 & 15) * 16;
#pragma unroll
  for (int f = 0; f < NF; ++f) {
    unsigned p[2][2];
#pragma unroll
    for (int e = 0; e < 2; ++e) {
      const f32x16 a = acc[f];
      f32x4 gt = f32x4{a[e * 4], a[e * 4 + 1], a[e * 4 + 2], a[e * 4 + 3]} + cs[f & 1][e] * nm;
      const f32x4 up = (f32x4{a[8 + e * 4], a[9 + e * 4], a[10 + e * 4], a[11 + e * 4]} + cs[f & 1][2 + e] * nm) * rs;
      gt = __builtin_elementwise_max(gt, f32x4{0.f, 0.f, 0.f, 0.f}) * up;
      p[e][0] = pk2(gt[0], gt[1]); p[e][1] = pk2(gt[2], gt[3]);
    }
    if (f + 2 < NF) {
#pragma unroll
      for (int e = 0; e < 4; ++e) cs[f & 1][e] = *(const LAS f32x4*)(scs + (f + 2) * 32 + e * 8);
    }
    st16(c.r, ho + f * 512, pair32(p[0][0], p[0][1], p[1][0], p[1][1]));
    SBAR();
  }
  STAMP(4);
  publish(c, M_CNT, 8, flg(FL_GU + layer * 5, panel * 16 + gu_cb(c)), true);
  STAMP(5);
}
template <int NF> DI void gu_consumer(const Ctx& c, int layer, int f0, int mq) {
  if constexpr (GU16 == 1 || (GU16 == 2 && NF < 4) || (GU16 == 3 && NF == 4)) gu_consumer16<NF>(c, layer, f0, mq); else gu_consumer32<NF>(c, layer, f0, mq);
}
DI void mlp_load(const Ctx& c0, int lw, int layer, unsigned iss, bool qkv_next, const uint16_t* wqkv) {
  const int half = lw & 1;
  {
    const Ctx c = fresh(c0);
    if (lw == 0) { poll(c, flg(FL_ATT + layer * 5, (pr_panel(c) >> 2) * 32 + (c.lane & 31))); STAMPL(layer * 5 + 2, 6); }
    BARRIER_L(); BARRIER_L();
    if (PROJ2) BARRIER_L();
  }
  { const int w = lw + 8; (void)w; WDUMP(layer * 5 + 2); }
  {
    const Ctx c = fresh(c0);
    const int ft0 = (gu_cb(c) * 27) >> 2;
    u32x4 pk[20];
    auto park = [&](int e0) {
#pragma unroll
      for (int e = e0; e < e0 + 4; ++e) {
        const int q = half * 5 + e % 5;
        pk[e] = ld16s(c.r, c.lane * 16, WS_WD + (pr_cb(c) * 5 + (q >> 1)) * 55296 + (((e / 5) * 2 + (lw >> 1)) * 2 + (q & 1)) * 1024);
      }
    };
    static_assert(GU_NA == 7 && GU_NB == 7, "schedule");
    if (lw >= 2) {
      Ring<7, GU_NB, 20, 2> rg;
#pragma unroll
      for (int p = 0; p < 7; ++p) rg.voff[p] = WS_WGU + (ft0 * 2 + half * 7 + p) * 20480 + c.lane * 16;
      rg.init(c.ws, 1024, 0, half);
      if (half == 0) ln_cols<4>(c, 56, WS_CSG + ft0 * 128, 13824);
      sig_wait(c, iss - layer * 16 + 4, M_CNT4);
      rg.template fill_ahead<1>(c, layer * 5 + 4);
      rg.template range<5, 5>(); rg.steady();
      wait_vm<28>(); BARRIER_L(); park(0);
      wait_vm<21 + 4>(); BARRIER_L(); park(4);
      wait_vm<14 + 8>(); BARRIER_L(); park(8);
      wait_vm<7 + 12>(); BARRIER_L(); park(12);
      wait_vm<16>(); BARRIER_L(); park(16);
    } else {
      Ring<4, GU_NA, 20, 2> rg;
#pragma unroll
      for (int p = 0; p < 4; ++p) rg.voff[p] = WS_L + layer * L_SZ + L_X1 + (gu_panel(c) * 8 + half * 4 + p) * 20480 + c.lane * 16;
      rg.init(c.ws, 1024, GU_A, half);
      Poll<1> pl;
      gate(c, pl, half, flg(FL_WO + layer * 5, gu_panel(c) * 16 + (c.lane & 15)), layer * 5 + 4, iss + 12);
      rg.template fill<3>();
      rg.template head<3>();
      pl.keep();
      f32x2 st[8];
      wait_vm<16>(); BARRIER_L();
      ln_load(c, WS_ST + (layer * 2 + 1) * STB + gu_panel(c) * 8192, half * 64 + c.lane, st);
      wait_vm<12 + 8>(); BARRIER_L(); park(0); park(4);
      wait_vm<8 + 8 + 8>(); BARRIER_L();
      const f32x2 t = ln_sum(st);
      wait_vm<8>(); BARRIER_L(); ln_put(c, half * 64 + c.lane, t); park(8);
      BARRIER_L(); park(12); park(16);
    }
    BARRIER_L();
#pragma unroll
    for (int e = 0; e < 20; ++e) PIN(pk[e]);
    { const int w = lw + 8; (void)w; WDUMP(layer * 5 + 3); }
    auto dump = [&]() {
#pragma unroll
      for (int e = 0; e < 20; ++e) ldsw16(c.lds + ((e / 5) * 2 + (lw >> 1)) * 10240 + (half * 5 + e % 5) * 1024 + c.lane * 16, pk[e]);
    };
    if (lw >= 2) {
      Ring<5, DN_NB, 27, 2> rg;
#pragma unroll
      for (int p = 0; p < 5; ++p) { const int q = half * 5 + p; rg.voff[p] = WS_WD + (pr_cb(c) * 5 + (q >> 1)) * 55296 + (q & 1) * 1024 + c.lane * 16; }
      rg.init(c.ws + 8 * 2048, 2048, 0, half);
      rg.slot = 8;
      sig_wait(c, layer * 16 + 16, M_CNT);
      rg.template fill<3>(); dump(); rg.template fill<4>();
      BARRIER_L();
      wait_vm<5 * 5>(); BARRIER_L();
      wait_vm<0>(); BARRIER_L();
      rg.base += 6 * 2048; rg.slot = 6;
      rg.template fill<3>();
      BARRIER_L();
      rg.template fill<3>();
      wait_vm<3 * 5>(); BARRIER_L();
      wait_vm<0>(); BARRIER_L();
      BARRIER_L();
      if (PROJ2) BARRIER_L();
    } else {
      sig_wait(c, layer * 16 + 16, M_CNT);
      dump();
      if (lw == 0) { poll(c, flg(FL_GU + layer * 5, (pr_panel(c) >> 1) * 16 + (c.lane & 15))); STAMPL(layer * 5 + 4, 6); }
#pragma unroll 1
      for (int k = 0; k < 7 + PROJ2; ++k) BARRIER_L();
    }
  }
  { const int w = lw + 8; (void)w; WDUMP(layer * 5 + 4); }
  if (qkv_next) qkv_load1(fresh(c0), lw, wqkv);
}

__global__ __launch_bounds__(768) void fused_kernel(const uint16_t* __restrict__ x, const uint16_t* __restrict__ wqkv, const uint16_t* __restrict__ wo,
                                                    const uint16_t* __restrict__ wg, const uint16_t* __restrict__ wu,
                                                    const uint16_t* __restrict__ wd, uint16_t* __restrict__ out, char* __restrict__ ws) {
  extern __shared__ __attribute__((aligned(16))) char smem[];
  const char* pc;
  asm volatile("s_getpc_b64 %0" : "=s"(pc));
  glds1(P0_SCR + 10240 + 1024, (const char*)((u64)pc & ~15ull), ((blockIdx.x >> 3) * 12 + (threadIdx.x >> 6)) % PF_KB * 1024 + (threadIdx.x & 63) * 16);
  Ctx c;
  c.ws = ws; c.lds = (lds_ptr)smem; c.lane = threadIdx.x & 63; c.xcd = blockIdx.x & 7; c.j = blockIdx.x >> 3;
  c.r = __builtin_amdgcn_make_buffer_rsrc(ws, 0, WS_END, 0x00020000);
  const int w = __builtin_amdgcn_readfirstlane(threadIdx.x >> 6);
  SPANST(0);
  c.tgt = 0;
  if (threadIdx.x < 16) *(LAS unsigned*)(c.lds + M_CNT + threadIdx.x * 4) = 0u;
  const bool qkv_on = qkv_cb(c) != 15;
  Ctx c0 = c;
  WDUMP(15);
  if (__builtin_expect(w >= 8, LD_FIRST)) {
    __builtin_amdgcn_s_setprio(3);
    if (qkv_on) qkv_load0(fresh(c0), w - 8, x, wqkv); else BARRIER();
    c0.tgt = epoch(ws);
#pragma unroll 1
    for (int layer = 0; layer < 2; ++layer) {
      const unsigned iss = layer * 24 + (qkv_on ? (layer + 1) * 4 : 0);
      WDUMP(layer * 5);
      att_load(fresh(c0), opq(w - 8), opq(layer), iss, wo, wg, wu, wd);
      WDUMP(layer * 5 + 1);
      mlp_load(c0, opq(w - 8), opq(layer), iss, layer == 0 && qkv_on, wqkv);
    }
  } else {
    if (w < 4) {
      const unsigned l = fresh(c0).lane;
      const char* base = w == 0 ? (const char*)x : w == 1 ? (const char*)wqkv : ws;
      const unsigned off = w < 2 ? l * 38400u : min(((w - 2) * 64 + l) * 524288u + 2048, WS_END);
      if (TLB_IN || w >= 2) asm volatile(GL_HEAD "global_load_lds_dword %2, %1 sc1" ::"s"(P0_SCR + 10240), "s"(base), "v"(off) : "memory");
    }
#pragma unroll 1
    for (int layer = 0; layer < 2; ++layer) {
      const unsigned L = WS_L + layer * L_SZ;
      qkv_compute(fresh(c0), c0.tgt, opq(w), opq(layer), x, wqkv);
      WDUMP(layer * 5);
      att_compute(fresh(c0), opq(w), opq(layer));
      WDUMP(layer * 5 + 1);
      proj_compute<10, WO_NB, 20>(fresh(c0), opq(w), layer * 5 + 2, L + L_O, layer == 0 ? (const char*)x : nullptr, WS_X2, L + L_X1,
                                  WS_ST + (layer * 2 + 1) * STB, nullptr, FL_WO + layer * 5);
      WDUMP(layer * 5 + 2);
      if (w < 4) gu_consumer<4>(fresh(c0), opq(layer), 0, opq(w));
      else if (GU_SKIP && (gu_cb(c0) & 3) == 0) gu_consumer<2>(fresh(c0), opq(layer), 4, opq(w - 4));
      else gu_consumer<3>(fresh(c0), opq(layer), 4, opq(w - 4));
      WDUMP(layer * 5 + 3);
      proj_compute<27, DN_NB, 18>(fresh(c0), opq(w), layer * 5 + 4, L + L_H, nullptr, L + L_X1, WS_X2, WS_ST + 2 * STB, layer ? out : nullptr,
                                  FL_DN + layer * 5);
      WDUMP(layer * 5 + 4);
    }
  }
}

extern "C" void kernel_launch(void* const* d_in, const int* in_sizes, int n_in, void* d_out, int out_size, void* d_ws, size_t ws_size,
                              hipStream_t stream) {
  (void)hipFuncSetAttribute((const void*)fused_kernel, hipFuncAttributeMaxDynamicSharedMemorySize, LDS_BYTES);
  fused_kernel<<<256, 768, LDS_BYTES, stream>>>((const uint16_t*)d_in[0], (const uint16_t*)d_in[1], (const uint16_t*)d_in[2],
                                                (const uint16_t*)d_in[4], (const uint16_t*)d_in[5], (const uint16_t*)d_in[6],
                                                (uint16_t*)d_out, (char*)d_ws);
}
```

```cpp
#include <hip/hip_runtime.h>
#include <hip/hip_bf16.h>
#include <stdint.h>

typedef __attribute__((ext_vector_type(8))) __bf16 bf16x8;
typedef __attribute__((ext_vector_type(16))) __bf16 bf16x16;
typedef __attribute__((ext_vector_type(4))) short s16x4;
typedef __attribute__((ext_vector_type(8))) short s16x8;
typedef __attribute__((ext_vector_type(2))) __bf16 bf16x2;
typedef __attribute__((ext_vector_type(2))) float f32x2;
typedef __attribute__((ext_vector_type(4))) float f32x4;
typedef __attribute__((ext_vector_type(8))) float f32x8;
typedef __attribute__((ext_vector_type(16))) float f32x16;
typedef __attribute__((ext_vector_type(2))) unsigned u32x2;
typedef __attribute__((ext_vector_type(4))) unsigned u32x4;
typedef unsigned long long u64;
#define LAS __attribute__((address_space(3)))
#define GAS __attribute__((address_space(1)))
typedef LAS char* lds_ptr;
typedef __amdgpu_buffer_rsrc_t rsrc_t;
#define DI __device__ __forceinline__

constexpr unsigned XB = 2048u * 640 * 2;
constexpr unsigned WS_EPOCH = 0;
constexpr unsigned WS_FLAG = 4096;
constexpr int FL_P0 = 0, FL_RT = 1, FL_QKV = 2, FL_ATT = 3, FL_WO = 4, FL_GU = 5, FL_DN = 6, FL_RT2 = 12;
constexpr unsigned WS_WO  = WS_FLAG + 13u * 32768;
constexpr unsigned WS_WGU = WS_WO + 640u * 640 * 2;
constexpr unsigned WS_WD  = WS_WGU + 3456u * 640 * 2;
constexpr unsigned WS_CSQ = WS_WD + 640u * 1728 * 2;
constexpr unsigned WS_CSG = WS_CSQ + 8u * 1920 * 4;
constexpr unsigned WS_ST  = WS_CSG + 4u * 3456 * 4;
constexpr unsigned STB = 2048u * 64;
constexpr unsigned WS_X2  = WS_ST + 4 * STB;
constexpr unsigned WS_L   = WS_X2 + XB;
constexpr unsigned L_Q1 = 0, L_Q2 = L_Q1 + 32u * 65536, L_K1 = L_Q2 + 16u * 32768, L_K2 = L_K1 + 32u * 65536, L_V = L_K2 + 16u * 32768,
                   L_O = L_V + XB, L_X1 = L_O + XB, L_H = L_X1 + XB, L_SZ = L_H + 2048u * 1728 * 2;
constexpr unsigned WS_END = WS_L + 2 * L_SZ;

constexpr int LDS_BYTES = 163840, MISC = 159744, M_SST = MISC, M_SCS = MISC + 1024, M_CNT = MISC + 2048, M_CNT2 = M_CNT + 8, M_CNT3 = M_CNT + 16, M_CNT4 = M_CNT + 24, M_SIG = M_CNT + 32, M_ISS = M_CNT + 40, M_TGT = M_CNT + 48;
constexpr int QKV_NB = 5, QKV_NA = 4, QKV_A = QKV_NB * 16384;
constexpr int WO_NB = 10, DN_NB = 15;
constexpr int GU_NB = 7, GU_NA = 7, GU_A = GU_NB * 14336;
constexpr int AT_SLOT = 45056, AT_CONST = 3 * AT_SLOT, AT_SCR = AT_CONST + 10752, AT_MERGE = AT_SLOT;
static_assert(QKV_A + QKV_NA * 16384 <= MISC && DN_NB * 10240 <= MISC && GU_A + GU_NA * 8192 <= MISC, "lds");
static_assert(AT_SCR + 3 * 4096 <= MISC, "lds");

DI unsigned pk2(float a, float b) { f32x2 v = {a, b}; bf16x2 r = __builtin_convertvector(v, bf16x2); return __builtin_bit_cast(unsigned, r); }
DI float bflo(unsigned u) { return __uint_as_float(u << 16); }
DI float bfhi(unsigned u) { return __uint_as_float(u & 0xffff0000u); }
DI f32x4 mfma16(bf16x8 a, bf16x8 b, f32x4 c) { return __builtin_amdgcn_mfma_f32_16x16x32_bf16(a, b, c, 0, 0, 0); }
DI f32x16 mfma32(bf16x8 a, bf16x8 b, f32x16 c) { return __builtin_amdgcn_mfma_f32_32x32x16_bf16(a, b, c, 0, 0, 0); }
DI bf16x8 ldsr16(lds_ptr p) { return *(const LAS bf16x8*)p; }
DI bf16x8 ldstr(lds_ptr p, int second) {
  const s16x4 a = __builtin_amdgcn_ds_read_tr16_b64_v4i16((LAS s16x4*)p), b = __builtin_amdgcn_ds_read_tr16_b64_v4i16((LAS s16x4*)(p + second));
  return __builtin_bit_cast(bf16x8, (s16x8)__builtin_shufflevector(a, b, 0, 1, 2, 3, 4, 5, 6, 7));
}
#define BARRIER() asm volatile("s_waitcnt lgkmcnt(0)\n\ts_barrier" ::: "memory")
template <int N> DI void wait_vm() { asm volatile("s_waitcnt vmcnt(%0)" ::"n"(N > 63 ? 63 : N) : "memory"); }
#define WDUMP(id)
#define WRESET()
#define BARRIER_L() BARRIER()
#define BARRIER_NW() asm volatile("s_barrier" ::: "memory")
#define SBAR() __builtin_amdgcn_sched_barrier(0)
#define PIN(x) asm volatile("" : "+v"(x))
#define GL_HEAD "s_mov_b32 m0, %0\n\ts_nop 3\n\t"
#define GL(v, off) "global_load_lds_dwordx4 %" #v ", %1 offset:" #off " sc1\n\t"
DI void glds1(unsigned m, const char* sb, unsigned v0) { asm volatile(GL_HEAD GL(2, 0)::"s"(m), "s"(sb), "v"(v0) : "memory"); }
DI void glds2(unsigned m, const char* sb, unsigned v0, unsigned v1) {
  asm volatile(GL_HEAD GL(2, 0) GL(3, 1024)::"s"(m), "s"(sb), "v"(v0), "v"(v1) : "memory");
}
DI void glds3(unsigned m, const char* sb, unsigned v0, unsigned v1, unsigned v2) {
  asm volatile(GL_HEAD GL(2, 0) GL(3, 1024) GL(4, 2048)::"s"(m), "s"(sb), "v"(v0), "v"(v1), "v"(v2) : "memory");
}
DI void glds4(unsigned m, const char* sb, unsigned v0, unsigned v1, unsigned v2, unsigned v3) {
  asm volatile(GL_HEAD GL(2, 0) GL(3, 1024) GL(4, 2048) GL(5, 3072)::"s"(m), "s"(sb), "v"(v0), "v"(v1), "v"(v2), "v"(v3) : "memory");
}
DI void st16(rsrc_t r, unsigned off, u32x4 v) { __builtin_amdgcn_raw_buffer_store_b128(v, r, off, 0, 16); }
DI void st8(rsrc_t r, unsigned off, u32x2 v) { __builtin_amdgcn_raw_buffer_store_b64(v, r, off, 0, 16); }
DI void st4(rsrc_t r, unsigned off, unsigned v) { __builtin_amdgcn_raw_buffer_store_b32(v, r, off, 0, 16); }
DI u32x4 ld16(rsrc_t r, unsigned off) { return __builtin_amdgcn_raw_buffer_load_b128(r, off, 0, 16); }
DI u32x2 ld8(rsrc_t r, unsigned off) { return __builtin_amdgcn_raw_buffer_load_b64(r, off, 0, 16); }
DI u32x4 ld16s(rsrc_t r, unsigned voff, unsigned soff) { return __builtin_amdgcn_raw_buffer_load_b128(r, voff, soff, 16); }
DI void ldsw16(lds_ptr p, u32x4 v) { *(LAS u32x4*)p = v; }
DI f32x2 ld8_async(unsigned voff, const char* sbase) {
  f32x2 v; asm volatile("s_nop 4\n\tglobal_load_dwordx2 %0, %1, %2 sc1" : "=v"(v) : "v"(voff), "s"(sbase) : "memory"); return v;
}
DI u32x4 pair32(unsigned a0, unsigned a1, unsigned b0, unsigned b1) {
  auto x = __builtin_amdgcn_permlane32_swap(a0, b0, false, false);
  auto y = __builtin_amdgcn_permlane32_swap(a1, b1, false, false);
  return u32x4{x[0], y[0], x[1], y[1]};
}
template <int N> DI float ror16(float v) { return __int_as_float(__builtin_amdgcn_update_dpp(0, __float_as_int(v), 0x120 + N, 0xf, 0xf, false)); }
DI float sum32(float v) {
  v += ror16<8>(v); v += ror16<4>(v); v += ror16<2>(v); v += ror16<1>(v);
  auto a = __builtin_amdgcn_permlane16_swap(__float_as_uint(v), __float_as_uint(v), false, false);
  return __uint_as_float(a[0]) + __uint_as_float(a[1]);
}
DI u32x4 pair16(unsigned a0, unsigned a1, unsigned b0, unsigned b1) {
  auto x = __builtin_amdgcn_permlane16_swap(a0, b0, false, false);
  auto y = __builtin_amdgcn_permlane16_swap(a1, b1, false, false);
  return u32x4{x[0], y[0], x[1], y[1]};
}

DI float rows4(float v) {
  auto a = __builtin_amdgcn_permlane16_swap(__float_as_uint(v), __float_as_uint(v), false, false);
  v = __uint_as_float(a[0]) + __uint_as_float(a[1]);
  auto b = __builtin_amdgcn_permlane32_swap(__float_as_uint(v), __float_as_uint(v), false, false);
  return __uint_as_float(b[0]) + __uint_as_float(b[1]);
}

#define STAMP(id)
#define STAMPC(id)
#define STAMPL(sl, id)
#define SLOT(n)

#define ATT_PRIO 3
#define ATT_ORD 0
#define WO_AH 6
#define DN_AH 8
#define ATT_ROT 1
#define GU_SKIP 1
#define ATT_LATE 4
#define GU16 1
#define QKV16 1
#define LD_FIRST 1
#define LAG0 2
#define PF_KB 56
#define PROJ2 0
#define PRI_ALL 1
#define RG 0
#define Q0_D0 2
#define TLB_IN 0
#define PACE 16
#define SPANST(id)
struct Ctx {
  char* ws; rsrc_t r; lds_ptr lds; u64 tgt; int lane, xcd, j;
};
DI Ctx fresh(const Ctx& c0) {
  Ctx c = c0;
  asm volatile("v_mbcnt_lo_u32_b32 %0, -1, 0\n\tv_mbcnt_hi_u32_b32 %0, -1, %0" : "=v"(c.lane), "+s"(c.xcd), "+s"(c.j), "+s"(c.ws));
  return c;
}
DI int opq(int v) { asm volatile("" : "+s"(v)); return v; }
constexpr int SPIN_CAP = 1 << 18;

DI u64 epoch(const char* ws) {
  u64 e; asm volatile("s_nop 4\n\ts_load_dwordx2 %0, %1, 0x0\n\ts_waitcnt lgkmcnt(0)" : "=s"(e) : "s"(ws + WS_EPOCH) : "memory"); return e + 1;
}
DI unsigned flg(int arr, int unit) { return WS_FLAG + (arr * 256 + unit) * 128; }
DI void publish(const Ctx& c, int cnt_off, unsigned n, unsigned to, bool mark = false) {
  unsigned one = 1, iss = M_ISS, cnt = cnt_off, old, so;
  u64 tgt = c.tgt, ex;
  asm volatile("" : "+v"(one), "+v"(iss), "+v"(cnt), "+v"(to), "+v"(tgt));
  if (mark) asm volatile("s_mov_b64 %0, exec\n\ts_mov_b64 exec, 1\n\tds_add_u32 %1, %2\n\ts_mov_b64 exec, %0" : "=&s"(ex) : "v"(iss), "v"(one) : "memory");
  asm volatile("s_mov_b64 %2, exec\n\ts_waitcnt vmcnt(0)\n\ts_mov_b64 exec, 1\n\tds_add_rtn_u32 %0, %3, %4\n\ts_waitcnt lgkmcnt(0)\n\t"
               "v_readfirstlane_b32 %1, %0\n\ts_mov_b64 exec, %2" : "=&v"(old), "=&s"(so), "=&s"(ex) : "v"(cnt), "v"(one) : "memory");
  if (so % n == n - 1)
    asm volatile("s_mov_b64 %0, exec\n\ts_mov_b64 exec, 1\n\ts_nop 2\n\tglobal_store_dwordx2 %1, %2, %3 sc1\n\ts_mov_b64 exec, %0"
                 : "=&s"(ex) : "v"(to), "v"(tgt), "s"(c.ws) : "memory");
}
DI void poll(const Ctx& c, unsigned off) {
#pragma unroll 1
  for (int spin = 0; spin < SPIN_CAP; ++spin) {
    u64 v;
    asm volatile("s_nop 4\n\tglobal_load_dwordx2 %0, %1, %2 sc1\n\ts_sleep 1\n\ts_waitcnt vmcnt(0)" : "=&v"(v) : "v"(off), "s"(c.ws) : "memory");
    if (__all(v == c.tgt)) break;
    __builtin_amdgcn_s_sleep(2);
  }
}
DI void sig_set(const Ctx& c, unsigned v) { if (c.lane == 0) __hip_atomic_store((LAS unsigned*)(c.lds + M_SIG), v, __ATOMIC_RELAXED, __HIP_MEMORY_SCOPE_WORKGROUP); }
DI void sig_wait(const Ctx& c, unsigned v, int off = M_SIG, bool calm = false) {
#pragma unroll 1
  for (int spin = 0; spin < SPIN_CAP * 16; ++spin) {
    if (__builtin_amdgcn_readfirstlane(__hip_atomic_load((LAS unsigned*)(c.lds + off), __ATOMIC_RELAXED, __HIP_MEMORY_SCOPE_WORKGROUP)) >= v) break;
    if (calm) __builtin_amdgcn_s_sleep(8); else if (off != M_SIG) __builtin_amdgcn_s_sleep(1);
  }
  asm volatile("" ::: "memory");
}

template <int PP, int NSLOT, int NS, int LAG = 1>
struct Ring {
  unsigned voff[PP];
  const char* base;
  unsigned adv, l0;
  int slot;
  DI void init(const char* b, unsigned a, unsigned lds0, int half) {
    base = b - 4096; adv = a; l0 = lds0 + half * PP * 1024; slot = 0;
#pragma unroll
    for (int p = 0; p < PP; ++p) voff[p] += 4096 - (p & 3) * 1024;
  }
  DI void issue() {
    const unsigned m = l0 + slot * (2 * PP * 1024);
    glds4(m, base, voff[0], voff[1], voff[2], voff[3]);
    if constexpr (PP == 5) glds1(m + 4096, base, voff[4]);
    if constexpr (PP == 7) glds3(m + 4096, base, voff[4], voff[5], voff[6]);
    if constexpr (PP == 8) glds4(m + 4096, base, voff[4], voff[5], voff[6], voff[7]);
    base += adv; slot = slot == NSLOT - 1 ? 0 : slot + 1;
  }
  template <int D0> DI void start(const Ctx& c, int half, unsigned delta, unsigned flag, unsigned sig, unsigned iss) {
    if (half == 0) {
      sig_wait(c, iss, M_ISS);
      poll(c, flag);
      issue(); base += delta - adv; l0 += PP * 1024; slot = 0; issue(); base -= delta; l0 -= PP * 1024;
      sig_set(c, sig);
      STAMPL(sig - 1, 6);
    } else { base += adv; slot = 1; sig_wait(c, sig); }
    fill<D0 - 1>();
  }
  template <int N = NSLOT> DI void fill() {
#pragma unroll 1
    for (int s = 0; s < N; ++s) issue();
  }
  template <int NEED, int N = NSLOT> DI void fill_ahead(const Ctx& c, unsigned sig) {
    int n = 0;
#pragma unroll 1
    while (n < N) {
      if (n * PP >= NEED * PP + (NEED ? PACE : 0) &&
          __builtin_amdgcn_readfirstlane(__hip_atomic_load((LAS unsigned*)(c.lds + M_SIG), __ATOMIC_RELAXED, __HIP_MEMORY_SCOPE_WORKGROUP)) >= sig) break;
      issue(); wait_vm<PACE>(); ++n;
    }
    if (n == N) wait_vm<(N - NEED) * PP>();
    BARRIER_L(); WRESET();
#pragma unroll 1
    for (; n < N; ++n) issue();
  }
  DI void rest() { range<NSLOT - 2, NSLOT - LAG>(); steady(); range<NSLOT - 1 - LAG, 0>(); BARRIER_L(); }
  template <int R, int E> DI void range() { if constexpr (R >= E) { wait_vm<R * PP>(); BARRIER_L(); range<R - 1, E>(); } }
  template <int D0 = NSLOT> DI void head() {
    wait_vm<(D0 - 1) * PP>(); BARRIER_L(); WRESET();
    if constexpr (D0 < NSLOT) fill<NSLOT - D0>();
    range<NSLOT - 2, NSLOT - LAG>();
    steady();
  }
  DI void steady() {
#pragma unroll 1
    for (int k = LAG; k < NS - NSLOT + LAG; ++k) { wait_vm<(NSLOT - 1 - LAG) * PP>(); BARRIER_L(); issue(); }
  }
  template <int D0 = NSLOT> DI void run() { head<D0>(); range<NSLOT - 1 - LAG, 0>(); BARRIER_L(); }
};

DI void ln_load(const Ctx& c, unsigned off, int row, f32x2 (&p)[8]) {
#pragma unroll
  for (int i = 0; i < 8; ++i) p[i] = __builtin_bit_cast(f32x2, __builtin_amdgcn_raw_buffer_load_b64(c.r, row * 8, off + i * 1024, 16));
}
DI f32x2 ln_sum(const f32x2 (&p)[8]) {
  f32x2 t = ((p[0] + p[1]) + (p[2] + p[3])) + ((p[4] + p[5]) + (p[6] + p[7]));
  const float m8 = t[0] * 0.125f; float q0 = 0.f, q1 = 0.f;
#pragma unroll
  for (int i = 0; i < 8; i += 2) { const float a = p[i][0] - m8, b = p[i + 1][0] - m8; q0 += a * a; q1 += b * b; }
  t[1] += (q0 + q1) * (1.f / 80.f);
  PIN(t);
  return t;
}
DI void ln_put(const Ctx& c, int row, f32x2 t) {
  *(LAS float*)(c.lds + M_SST + row * 4) = __builtin_amdgcn_rsqf(t[1] * (1.f / 640.f) + 1e-5f);
  *(LAS float*)(c.lds + M_SST + 512 + row * 4) = -t[0] * (1.f / 640.f);
}
template <int NP> DI void ln_cols_sum(const Ctx& c, int n4, const u32x4 (&q)[NP]) {
  f32x4 cs = __builtin_bit_cast(f32x4, q[0]);
#pragma unroll
  for (int i = 1; i < NP; ++i) cs += __builtin_bit_cast(f32x4, q[i]);
  if (c.lane < n4) *(LAS f32x4*)(c.lds + M_SCS + c.lane * 16) = cs;
}
template <int NP> DI void ln_cols(const Ctx& c, int n4, unsigned off, unsigned pitch) {
  u32x4 q[NP];
#pragma unroll
  for (int i = 0; i < NP; ++i) q[i] = ld16(c.r, off + i * pitch + (c.lane < n4 ? c.lane : 0) * 16);
  ln_cols_sum<NP>(c, n4, q);
}

DI int row_group(const Ctx& c) { return RG == 0 ? c.xcd & 3 : RG == 1 ? ((c.xcd & 1) ^ 1) * 2 + ((c.xcd >> 1) & 1) : (c.xcd & 1) * 2 + ((c.xcd >> 1) & 1); }
DI int qkv_panel(const Ctx& c) { return row_group(c) * 4 + (c.j & 3); }
DI int qkv_cb(const Ctx& c) { return (c.xcd >> 2) * 8 + (c.j >> 2); }
DI int pr_panel(const Ctx& c) { return row_group(c) * 8 + (c.j & 7); }
DI int pr_cb(const Ctx& c) { return (c.xcd >> 2) * 4 + (c.j >> 3); }
DI int gu_panel(const Ctx& c) { return (c.xcd & 1) * 8 + (c.j & 7); }
DI int gu_cb(const Ctx& c) { return (c.xcd >> 1) * 4 + (c.j >> 3); }
DI int att_head(const Ctx& c) { return c.xcd * 4 + (c.j & 3); }
DI int att_qb(const Ctx& c) { return c.j >> 2; }

constexpr int P0_SCR = QKV_A + QKV_NA * 16384;
static_assert(P0_SCR + 10240 <= MISC, "lds");
struct Phase0 {
  u32x4 v[3]; float sum, m2;
  DI void load(const Ctx& c, int hw, const uint16_t* x, const uint16_t* wqkv) {
    const int lane = c.lane, r32 = lane & 31, b = blockIdx.x, row = b * 8 + hw * 2 + (lane >> 5);
    v[0] = *(const u32x4*)(x + row * 640 + r32 * 8); v[1] = *(const u32x4*)(x + row * 640 + 256 + r32 * 8);
    v[2] = *(const u32x4*)(x + row * 640 + 512 + (r32 & 15) * 8);
    if (b < 240) {
      const char* src = (const char*)wqkv + ((b / 30) * 80 + hw * 8) * 3840 + (b % 30) * 128;
      const unsigned vo = (lane >> 3) * 3840 + (lane & 7) * 16;
      glds1(P0_SCR + hw * 1024, src, vo); glds1(P0_SCR + 4096 + hw * 1024, src + 32 * 3840, vo);
      if (hw < 2) glds1(P0_SCR + 8192 + hw * 1024, src + 64 * 3840, vo);
    }
  }
  DI float dev2(int q) const {
    const float mean = sum * (1.f / 640.f); float a = 0.f, b = 0.f;
#pragma unroll
    for (int e = 0; e < 4; ++e) { const float d0 = bflo(v[q][e]) - mean, d1 = bfhi(v[q][e]) - mean; a += d0 * d0; b += d1 * d1; }
    return a + b;
  }
  DI void part1(const Ctx& c) {
    unsigned uo = 0x3f803f80u; PIN(uo);
    const bf16x2 ones = __builtin_bit_cast(bf16x2, uo);
    float s[3];
#pragma unroll
    for (int q = 0; q < 3; ++q) {
      s[q] = 0.f;
#pragma unroll
      for (int e = 0; e < 4; ++e) { const unsigned u = v[q][e]; s[q] = __builtin_amdgcn_fdot2_f32_bf16(__builtin_bit_cast(bf16x2, u), ones, s[q], false); }
    }
    sum = sum32(s[0] + s[1] + ((c.lane & 16) ? 0.f : s[2]));
    m2 = dev2(0);
    asm volatile("" : "+v"(sum), "+v"(m2));
    wait_vm<0>();
  }
  DI void part2(const Ctx& c, int hw) {
    const int lane = c.lane, r32 = lane & 31, b = blockIdx.x, row = b * 8 + hw * 2 + (lane >> 5);
    const float t = dev2(2);
    m2 = sum32(m2 + dev2(1) + ((lane & 16) ? 0.f : t));
    if (r32 < 8) st8(c.r, WS_ST + (row >> 7) * 8192 + r32 * 1024 + (row & 127) * 8, u32x2{__float_as_uint(sum * 0.125f), __float_as_uint(m2 * 0.125f)});
  }
  DI void cols(const Ctx& c, int hw) {
    const int i = c.lane & 15, g = c.lane >> 4, b = blockIdx.x;
    if (b < 240) {
      const unsigned o = 0x3f803f80u, z = g < 2 ? o : 0u;
      lds_ptr p = c.lds + P0_SCR + (g * 8 + (i >> 2)) * 128 + hw * 32 + (i & 3) * 8;
      f32x4 cs = mfma16(ldstr(p, 512), __builtin_bit_cast(bf16x8, u32x4{o, o, o, o}), f32x4{});
      cs = mfma16(ldstr(p + 4096, 512), __builtin_bit_cast(bf16x8, u32x4{o, o, o, o}), cs);
      cs = mfma16(ldstr(p + (g < 2 ? 8192 : 0), 512), __builtin_bit_cast(bf16x8, u32x4{z, z, z, z}), cs);
      if (i == 0) st16(c.r, WS_CSQ + ((b / 30) * 1920 + (b % 30) * 64 + hw * 16 + g * 4) * 4, __builtin_bit_cast(u32x4, cs));
    }
  }
};

template <bool SWAP, class PRE, class MID>
DI void qkv_loop(const Ctx& c, int w, int layer, f32x4 (&acc)[4][4], PRE pre, MID mid) {
  const int lane = c.lane, i = lane & 15, g = lane >> 4, wm = w & 1, wn = w >> 1;
  const int rm = (i >> 3) * 1024 + (i & 7) * 128 + ((g ^ ((i >> 1) & 7)) << 4);
  lds_ptr pa0 = c.lds + QKV_A + wm * 8192 + (layer == 0 ? rm : lane * 16);
  lds_ptr pa1 = c.lds + QKV_A + wm * 8192 + (layer == 0 ? rm ^ 64 : lane * 16 + 1024);
  const int f = (i >> 2) | ((g & 1) << 2), rb = (g * 8 + (i >> 2)) * 256 + (i & 3) * 8;
  lds_ptr pb[4];
#pragma unroll
  for (int t = 0; t < 4; ++t) pb[t] = c.lds + rb + (((wn * 4 + t) ^ f) << 5);
  bf16x8 x0[4], x1[4], w0[4], w1[4];
  auto step = [&](const bf16x8* xa, const bf16x8* wb, bf16x8* xn, bf16x8* wn_, lds_ptr pa, int bo, bool ld) {
#pragma unroll
    for (int q = 0; q < 16; ++q) {
      if (ld) {
        if ((q & 3) == 0) wn_[q >> 2] = ldstr(pb[q >> 2] + bo, 1024);
        if ((q & 3) == 2) xn[q >> 2] = ldsr16(pa + (q >> 2) * 2048);
      }
      const int t = q >> 2, m = q & 3;
      acc[m][t] = SWAP ? mfma16(wb[t], xa[m], acc[m][t]) : mfma16(xa[m], wb[t], acc[m][t]);
      SBAR();
    }
  };
  SLOT(layer * 5);
  if (layer) STAMP(0);
  mid(0); mid(4); mid(8); mid(12);
  STAMP(1);
  BARRIER();
  STAMP(2); STAMPC(0); WRESET();
#pragma unroll
  for (int e = 0; e < 4; ++e) { w0[e] = ldstr(pb[e], 1024); x0[e] = ldsr16(pa0 + e * 2048); }
  int sa = 0, sb = 0;
#pragma unroll 1
  for (int st = 0; st < 9; ++st) {
    step(x0, w0, x1, w1, pa1, 8192, true);
    const int da = sa == QKV_NA - 1 ? -(QKV_NA - 1) * 16384 : 16384, db = sb == QKV_NB - 1 ? -(QKV_NB - 1) * 16384 : 16384;
    sa = sa == QKV_NA - 1 ? 0 : sa + 1; sb = sb == QKV_NB - 1 ? 0 : sb + 1;
    pa0 += da; pa1 += da; pb[0] += db; pb[1] += db; pb[2] += db; pb[3] += db;
    if (LAG0 == 1 && layer == 0) asm volatile("s_waitcnt lgkmcnt(0)" ::: "memory");
    BARRIER_NW();
    step(x1, w1, x0, w0, pa0, 0, true);
  }
  step(x0, w0, x1, w1, pa1, 8192, true);
  BARRIER();
  pre(); SBAR();
  step(x1, w1, x0, w0, pa0, 0, false);
  STAMP(3); STAMPC(1);
}
DI unsigned qkv_w_off(const Ctx& c, int q) {
  const int row = q * 4 + (c.lane >> 4);
  return row * 3840 + qkv_cb(c) * 256 + (((c.lane & 15) ^ (QKV16 ? ((row & 3) << 1) | (row & 8) : (row & 3) << 2)) << 4);
}
DI void qkv_helper(Ctx c, u64& tgt, int hw, int layer, const uint16_t* x, const uint16_t* wqkv) {
  const int panel = qkv_panel(c), cb = qkv_cb(c), l = c.lane;
  const bool on = cb != 15;
  if (layer == 0) {
    Phase0 p0;
    p0.load(c, hw, x, wqkv);
    if (on) BARRIER_L();
    p0.part1(c);
    BARRIER_L();
    p0.part2(c, hw);
    if (on) BARRIER_L();
    p0.cols(c, hw);
    c.tgt = tgt = epoch(c.ws);
    if (hw == 0 && l == 0) *(LAS u64*)(c.lds + M_TGT) = tgt;
    if (on) BARRIER_L();
    publish(c, M_CNT2, 4, flg(FL_P0, blockIdx.x));
    if (!on) return;
    BARRIER_L(); BARRIER_L(); BARRIER_L();
    const unsigned at = flg(FL_P0, hw < 2 ? panel * 16 + hw * 8 + (l & 7) : (l & 7) * 30 + cb * 2 + ((l >> 3) & 1));
    const u64 v = __hip_atomic_load((const GAS u64*)(c.ws + at), __ATOMIC_RELAXED, __HIP_MEMORY_SCOPE_AGENT);
    BARRIER_L();
    if (!__all(v == c.tgt)) poll(c, at);
    if (hw < 2) {
      f32x2 p[8];
      ln_load(c, WS_ST + panel * 8192, hw * 64 + l, p);
      BARRIER_L(); BARRIER_L();
      ln_put(c, hw * 64 + l, ln_sum(p));
    } else if (hw == 2) {
      u32x4 q[8];
#pragma unroll
      for (int i = 0; i < 8; ++i) q[i] = ld16(c.r, WS_CSQ + cb * 512 + i * 7680 + (l & 31) * 16);
      BARRIER_L(); BARRIER_L();
      ln_cols_sum<8>(c, 32, q);
    } else { BARRIER_L(); BARRIER_L(); }
    BARRIER_L();
  } else if (on) {
    if (hw == 2) ln_cols<8>(c, 32, WS_CSQ + cb * 512, 7680);
    BARRIER_L(); BARRIER_L(); BARRIER_L(); BARRIER_L(); BARRIER_L(); BARRIER_L();
    if (hw < 2) {
      f32x2 p[8];
      ln_load(c, WS_ST + 2 * STB + panel * 8192, hw * 64 + l, p);
      BARRIER_L(); BARRIER_L();
      ln_put(c, hw * 64 + l, ln_sum(p));
    } else { BARRIER_L(); BARRIER_L(); }
    BARRIER_L(); BARRIER_L(); BARRIER_L();
  }
}
DI void qkv_compute(Ctx c, u64& tgt, int w, int layer, const uint16_t* x, const uint16_t* wqkv) {
  const int panel = qkv_panel(c), cb = qkv_cb(c);
  if (w >= 4) { qkv_helper(c, tgt, w - 4, layer, x, wqkv); return; }
  if (cb == 15) { if (layer == 0) { tgt = epoch(c.ws); wait_vm<0>(); BARRIER(); } return; }
  const int lane = c.lane, i = lane & 15, g = lane >> 4, wm = w & 1, wn = w >> 1;
  const unsigned L = WS_L + layer * L_SZ;
  f32x4 acc[4][4];
#pragma unroll
  for (int e = 0; e < 16; ++e) acc[e >> 2][e & 3] = f32x4{0.f, 0.f, 0.f, 0.f};
  const LAS float* srs = (const LAS float*)(c.lds + M_SST) + wm * 64;
  const LAS float* scs = (const LAS float*)(c.lds + M_SCS) + wn * 64;
  if (cb < 10) {
    float rs[4], nm[4]; f32x4 cs[4]; unsigned ad[4][2];
    const float qs = cb < 5 ? 0.22360679774997896f * 1.4426950408889634f : 1.f;
    const unsigned i1 = L + (cb < 5 ? L_Q1 : L_K1), i2 = L + (cb < 5 ? L_Q2 : L_K2);
    qkv_loop<true>(c, w, layer, acc, [&]() {
#pragma unroll
      for (int e = 0; e < 4; ++e) { rs[e] = srs[e * 16 + i]; nm[e] = srs[128 + e * 16 + i]; cs[e] = *(const LAS f32x4*)(scs + e * 16 + g * 4); }
    }, [&](int q) {
      const int t = q >> 2, o = (cb % 5) * 16 + wn * 8 + t * 2 + (g >> 1), hp = o / 5, pos = o - hp * 5;
      const unsigned ts = pos == 2 ? 512 : 1024;
      ad[t][0] = (pos == 2 ? i2 + hp * 32768 : i1 + (2 * hp + (pos > 2)) * 65536 + (pos > 2 ? pos - 3 : pos) * 512) +
                 (panel * 4 + wm * 2) * ts + ((g & 1) * 16 + i) * 16;
      ad[t][1] = ad[t][0] + ts;
      asm volatile("" : "+v"(ad[t][0]), "+v"(ad[t][1]));
    });
#pragma unroll
    for (int e = 0; e < 4; ++e) rs[e] *= qs;
#pragma unroll
    for (int t = 0; t < 4; ++t) {
#pragma unroll
      for (int mp = 0; mp < 2; ++mp) {
        const f32x4 u = (acc[mp * 2][t] + cs[t] * nm[mp * 2]) * rs[mp * 2], v = (acc[mp * 2 + 1][t] + cs[t] * nm[mp * 2 + 1]) * rs[mp * 2 + 1];
        st16(c.r, ad[t][mp], pair16(pk2(u[0], u[1]), pk2(u[2], u[3]), pk2(v[0], v[1]), pk2(v[2], v[3])));
      }
    }
  } else {
    float cs[4]; f32x4 rs[4], nm[4]; unsigned ad[4];
    qkv_loop<false>(c, w, layer, acc, [&]() {
#pragma unroll
      for (int e = 0; e < 4; ++e) { cs[e] = scs[e * 16 + i]; rs[e] = *(const LAS f32x4*)(srs + e * 16 + g * 4); nm[e] = *(const LAS f32x4*)(srs + 128 + e * 16 + g * 4); }
    }, [&](int q) {
      const int t = q >> 2, cv = (cb - 10) * 128 + wn * 64 + t * 16 + i, head = cv / 20, dim = cv - head * 20;
      const int d = (head & 1) ? (dim < 4 ? dim + 16 : dim - 4) : dim;
      ad[t] = L + L_V + head * 81920 + (panel * 8 + wm * 4 + (g & 1)) * 640 + (g >> 1) * 320 + d * 16;
      PIN(ad[t]);
    });
#pragma unroll
    for (int t = 0; t < 4; ++t) {
      const unsigned a0 = ad[t];
#pragma unroll
      for (int mp = 0; mp < 2; ++mp) {
        const f32x4 u = (acc[mp * 2][t] + nm[mp * 2] * cs[t]) * rs[mp * 2], v = (acc[mp * 2 + 1][t] + nm[mp * 2 + 1] * cs[t]) * rs[mp * 2 + 1];
        st16(c.r, a0 + mp * 1280, pair16(pk2(u[0], u[1]), pk2(u[2], u[3]), pk2(v[0], v[1]), pk2(v[2], v[3])));
      }
    }
  }
  if (layer == 0) {
    const u32x2 t = *(const LAS u32x2*)(c.lds + M_TGT);
    c.tgt = tgt = (u64)(unsigned)__builtin_amdgcn_readfirstlane(t[1]) << 32 | (unsigned)__builtin_amdgcn_readfirstlane(t[0]);
  }
  SLOT(layer * 5);
  STAMP(4);
  publish(c, M_CNT4, 4, flg(FL_QKV + layer * 5, panel * 16 + cb), true);
  STAMP(5);
}
typedef Ring<8, QKV_NB, 10, 2> QkvB;
DI void qkv_load0(const Ctx& c, int lw, const uint16_t* x, const uint16_t* wqkv) {
  const int half = lw & 1;
  if (lw >= 2) {
    Ring<8, QKV_NB, 10, LAG0> rg;
#pragma unroll
    for (int p = 0; p < 8; ++p) rg.voff[p] = qkv_w_off(c, half * 8 + p);
    rg.init((const char*)wqkv, 64 * 3840, 0, half);
    rg.template fill<Q0_D0>(); rg.template run<Q0_D0>();
  } else {
    Ring<8, QKV_NA, 10, LAG0> rg;
#pragma unroll
    for (int p = 0; p < 8; ++p) {
      const int mt = qkv_panel(c) * 8 + half * 4 + (p >> 1), r16 = (p & 1) * 8 + (c.lane >> 3);
      rg.voff[p] = (mt * 16 + r16) * 1280 + (((c.lane & 7) ^ ((r16 >> 1) & 7)) << 4);
    }
    rg.init((const char*)x, 128, QKV_A, half);
    rg.template fill<Q0_D0>(); rg.template run<Q0_D0>();
  }
}
DI void qkv_load1(const Ctx& c, int lw, const uint16_t* wqkv) {
  const int half = lw & 1;
  static_assert(QKV_NA == 4 && QKV_NB == 5, "schedule");
  if (lw >= 2) {
    QkvB rg;
#pragma unroll
    for (int p = 0; p < 8; ++p) rg.voff[p] = qkv_w_off(c, half * 8 + p);
    rg.init((const char*)wqkv, 64 * 3840, 0, half);
    sig_wait(c, 12, M_CNT4);
    rg.template fill_ahead<1>(c, 6);
    rg.rest();
  } else {
    Ring<8, QKV_NA, 10, 2> rg;
#pragma unroll
    for (int p = 0; p < 8; ++p) rg.voff[p] = WS_X2 + (qkv_panel(c) * 8 + half * 4 + (p >> 1)) * 20480 + (p & 1) * 1024 + c.lane * 16;
    rg.init(c.ws, 2048, QKV_A, half);
    rg.template start<2>(c, half, 4 * 20480, flg(FL_DN, qkv_panel(c) * 16 + (c.lane & 15)), 6, 28);
    rg.template run<2>();
  }
}

DI f32x16 qk8(s16x4 k2, s16x4 q2) {
 return __builtin_amdgcn_mfma_f32_32x32x8bf16_1k(k2, q2, f32x16{}, 0, 0, 0); }
DI f32x16 qk16(bf16x8 k1, bf16x8 q1, f32x16 s) { return __builtin_amdgcn_mfma_f32_32x32x16_bf16(k1, q1, s, 0, 0, 0); }
DI f32x16 pv_lo(bf16x8 v, f32x16 p, f32x16 o) {
  return __builtin_amdgcn_smfmac_f32_32x32x32_bf16(v, __builtin_bit_cast(bf16x16, (f32x8)__builtin_shufflevector(p, p, 0, 1, 2, 3, 4, 5, 6, 7)), o, 0xDDDD, 0, 0);
}
DI f32x16 pv_hi(bf16x8 v, f32x16 p, f32x16 o) {
  return __builtin_amdgcn_smfmac_f32_32x32x32_bf16(v, __builtin_bit_cast(bf16x16, (f32x8)__builtin_shufflevector(p, p, 8, 9, 10, 11, 12, 13, 14, 15)), o, 0xDDDD, 0, 0);
}
DI float swap32(float v, int h) {
  auto r = __builtin_amdgcn_permlane32_swap(__float_as_uint(v), __float_as_uint(v), false, false);
  return __uint_as_float(h ? r[0] : r[1]);
}
#define EXP4(X, i) do { X[i] = __builtin_amdgcn_exp2f(X[i]); X[i + 1] = __builtin_amdgcn_exp2f(X[i + 1]); X[i + 2] = __builtin_amdgcn_exp2f(X[i + 2]); \
                        X[i + 3] = __builtin_amdgcn_exp2f(X[i + 3]); PIN(X); SBAR(); } while (0)
DI void att_compute(const Ctx& c, int w, int layer) {
  const int lane = c.lane, head = att_head(c), qb = att_qb(c);
  const int qp = w & 3, kp = w >> 2, r32 = lane & 31, h = lane >> 5;
  lds_ptr lds = c.lds;
  const unsigned L = WS_L + layer * L_SZ;
  SLOT(layer * 5 + 1);
  STAMP(0);
  for (int s = w * 64 + lane; s < 672; s += 512) {
    const unsigned v = s % 20 == 0 ? 0x3f803f80u : 0u;
    *(LAS u32x4*)(lds + AT_CONST + s * 16) = u32x4{v, v, v, v};
  }
  lds_ptr pk1 = lds + kp * 8192 + lane * 16;
  lds_ptr pk2_ = h ? lds + AT_CONST + 16 : lds + 16384 + kp * 4096 + r32 * 16 + (head & 1) * 8;
  lds_ptr pv = (r32 >= 20 ? lds + AT_CONST : lds + 24576 + kp * 10240) + h * 320 + r32 * 16;
  const int vmask = r32 >= 20 ? 0 : -1, kmask = h ? 0 : -1;
  const int qt = qb * 8 + qp * 2 + kp, tok = qt * 32 + r32;
  unsigned ob16 = L + L_O + (tok >> 4) * 20480 + (head * 2 + h) * 256 + (tok & 15) * 16, ob8 = L + L_O + (tok >> 4) * 20480 + 16384 + head * 128 + (tok & 15) * 8;
  asm volatile("" : "+v"(ob16), "+v"(ob8));
  f32x16 o0 = f32x16{}, o1 = f32x16{};
  f32x16 s0, s1, s2, s3 = f32x16{};
  STAMP(1);
  BARRIER();
  STAMP(2); STAMPC(0); WRESET();
  const bf16x8 q1a = ldsr16(lds + 2 * AT_SLOT + (qp * 2 + kp) * 1024 + lane * 16), q1b = ldsr16(lds + 2 * AT_SLOT + (qp * 2 + 1 - kp) * 1024 + lane * 16);
  lds_ptr pq2 = h ? lds + AT_CONST + 16 : lds + 2 * AT_SLOT + 8192 + qp * 1024 + r32 * 16 + (head & 1) * 8;
  const s16x4 q2a = *(const LAS s16x4*)(pq2 + kp * 512), q2b = *(const LAS s16x4*)(pq2 + 512 - kp * 512);
  bf16x8 k1 = ldsr16(pk1), vlo = ldsr16(pv), vhi = bf16x8{};
  s16x4 k2 = *(const LAS s16x4*)pk2_;
  s0 = qk16(k1, q1a, qk8(k2, q2a));
#define PH_EVEN(SC, SN, SP, KLOAD, VOFF) do { \
    SN = qk8(k2, q2b); SBAR(); EXP4(SC, 0); SN = qk16(k1, q1b, SN); SBAR(); KLOAD; SBAR(); EXP4(SC, 4); \
    o1 = pv_hi(vhi, SP, o1); SBAR(); vhi = ldsr16(pv + (VOFF) + 640); SBAR(); EXP4(SC, 8); o0 = pv_lo(vlo, SC, o0); SBAR(); EXP4(SC, 12); } while (0)
#define PH_ODD(SC, SN, SP, VLOAD) do { \
    SN = qk8(k2, q2a); SBAR(); EXP4(SC, 0); SN = qk16(k1, q1a, SN); SBAR(); EXP4(SC, 4); \
    o0 = pv_hi(vhi, SP, o0); SBAR(); EXP4(SC, 8); o1 = pv_lo(vlo, SC, o1); SBAR(); VLOAD; SBAR(); EXP4(SC, 12); } while (0)
#define KLD(t) do { k1 = ldsr16(pk1 + (t) * 1024); k2 = *(const LAS s16x4*)(pk2_ + (t) * 512); } while (0)
#define PRI(v) do { __builtin_amdgcn_s_setprio(v); SBAR(); } while (0)
#define HI(n) if (ATT_PRIO == (n)) PRI(1);
#define LO(n) if (ATT_PRIO == (n)) PRI(0);
#define TWO(t) HI(3) HI(4) HI(5) PH_EVEN(s0, s1, s3, KLD((t) + 1), (t) * 1280); PH_ODD(s1, s2, s0, vlo = ldsr16(pv + ((t) + 1) * 1280)); LO(4)
  for (int grp = 0; grp < 4; ++grp) {
    TWO(0) PH_EVEN(s2, s3, s1, if (grp == 0) BARRIER_NW(); KLD(2), 1280); LO(3) PH_ODD(s3, s0, s2, vlo = ldsr16(pv + 2 * 1280));
    TWO(2) PH_EVEN(s2, s3, s1, KLD(4), 3 * 1280); LO(3) LO(5) PH_ODD(s3, s0, s2, vlo = ldsr16(pv + 4 * 1280));
    TWO(4) PH_EVEN(s2, s3, s1, KLD(6), 5 * 1280); LO(3) PH_ODD(s3, s0, s2, vlo = ldsr16(pv + 6 * 1280));
    TWO(6)
    const int d = grp == 2 ? -2 * AT_SLOT : grp == 3 ? 0 : AT_SLOT;
    PH_EVEN(s2, s3, s1, pk1 += d; pk2_ += (d & kmask); if (grp != 3) BARRIER(); KLD(0), 7 * 1280);
    LO(3) LO(5) PH_ODD(s3, s0, s2, pv += (d & vmask); vlo = ldsr16(pv));
  }
  o1 = pv_hi(vhi, s3, o1);
  STAMP(3); STAMPC(1);
  __builtin_amdgcn_s_setprio(0);
  const f32x16 give = o1, keep = o0;
#pragma unroll
  for (int e = 0; e < 3; ++e)
    *(LAS f32x4*)(lds + AT_MERGE + (w * 3 + e) * 1024 + lane * 16) = f32x4{give[4 * e], give[4 * e + 1], give[4 * e + 2], give[4 * e + 3]};
  BARRIER();
  f32x4 a[3];
#pragma unroll
  for (int e = 0; e < 3; ++e)
    a[e] = f32x4{keep[4 * e], keep[4 * e + 1], keep[4 * e + 2], keep[4 * e + 3]} + *(const LAS f32x4*)(lds + AT_MERGE + ((w ^ 4) * 3 + e) * 1024 + lane * 16);
  float l = h ? a[2][0] : 0.f;
  l += swap32(l, h);
  if (__builtin_expect(__any(!(l >= 5.421e-20f && l <= 1.8447e19f)), 0)) {
    const unsigned o2 = (head >> 1) * 32768 + r32 * 16 + (head & 1) * 8;
    const bf16x8 fq1 = __builtin_bit_cast(bf16x8, ld16(c.r, L + L_Q1 + head * 65536 + qt * 1024 + lane * 16));
    u32x2 fq2 = ld8(c.r, L + L_Q2 + o2 + qt * 512);
    if (h) fq2 = u32x2{0u, 0u};
    const unsigned vi = L + L_V + head * 81920 + h * 320 + (r32 < 20 ? r32 : 0) * 16;
    f32x16 oo = f32x16{};
    float m = -INFINITY;
    const u32x4 one8 = {0x3f803f80u, 0x3f803f80u, 0x3f803f80u, 0x3f803f80u};
    for (int t = 0; t < 64; ++t) {
      const u32x4 fk1 = ld16(c.r, L + L_K1 + head * 65536 + t * 1024 + lane * 16);
      const u32x2 fk2 = ld8(c.r, L + L_K2 + o2 + t * 512);
      u32x4 v0 = ld16(c.r, vi + t * 1280), v1 = ld16(c.r, vi + t * 1280 + 640);
      if (r32 >= 20) { v0 = one8; v1 = one8; }
      f32x16 s = qk16(__builtin_bit_cast(bf16x8, fk1), fq1, qk8(__builtin_bit_cast(s16x4, fk2), __builtin_bit_cast(s16x4, fq2)));
      float tm = s[0];
#pragma unroll
      for (int r = 1; r < 16; ++r) tm = fmaxf(tm, s[r]);
      tm = fmaxf(tm, swap32(tm, h));
      const float mn = fmaxf(m, tm), al = __builtin_amdgcn_exp2f(m - mn);
      m = mn;
#pragma unroll
      for (int r = 0; r < 16; ++r) { s[r] = __builtin_amdgcn_exp2f(s[r] - mn); oo[r] *= al; }
      oo = pv_hi(__builtin_bit_cast(bf16x8, v1), s, pv_lo(__builtin_bit_cast(bf16x8, v0), s, oo));
    }
#pragma unroll
    for (int e = 0; e < 3; ++e) a[e] = f32x4{oo[4 * e], oo[4 * e + 1], oo[4 * e + 2], oo[4 * e + 3]};
    l = h ? a[2][0] : 0.f;
    l += swap32(l, h);
  }
  const float inv = __builtin_amdgcn_rcpf(l);
  const unsigned A0 = pk2(a[0][0] * inv, a[0][1] * inv), A1 = pk2(a[0][2] * inv, a[0][3] * inv);
  const unsigned B0 = pk2(a[1][0] * inv, a[1][1] * inv), B1 = pk2(a[1][2] * inv, a[1][3] * inv);
  const u32x2 C = {pk2(a[2][0] * inv, a[2][1] * inv), pk2(a[2][2] * inv, a[2][3] * inv)};
  st16(c.r, ob16, pair32(A0, A1, B0, B1));
  if (!h) st8(c.r, ob8, C);
  STAMP(4);
  publish(c, M_CNT, 8, flg(FL_ATT + layer * 5, qb * 32 + head), true);
  STAMP(5);
}
DI void retile_load(const Ctx& c, int J, u32x4 (&v)[5][4], const uint16_t* wo, const uint16_t* wg, const uint16_t* wu, const uint16_t* wd) {
  const int e = J < 216 ? J : J < 256 ? J - 216 : J - 256;
  const int strip = J < 216 ? (e >> 2) % 27 : e % 10, k0 = J < 216 ? (e & 3) * 5 : min((e / 10) * 5, 49), ld = J < 216 ? 3456 : 1280;
  const rsrc_t r = __builtin_amdgcn_make_buffer_rsrc((void*)(J < 108 ? wg : J < 216 ? wu : J < 256 ? wo : wd), 0,
                                                     J < 216 ? 640 * 3456 : J < 256 ? 640 * 1280 : 1728 * 1280, 0x00020000);
  const unsigned vo = (c.lane >> 3) * ld + (c.lane & 7) * 16;
#pragma unroll
  for (int u = 0; u < 5; ++u)
#pragma unroll
    for (int q = 0; q < 4; ++q) {
      const int ch = (k0 + u) * 4 + q;
      const int row = J < 216 || J >= 256 ? ch * 8 : ch < 64 ? (ch >> 1) * 20 + (ch & 2) * 2 + (ch & 1) * 8 : (ch - 64) * 40 + 16;
      v[u][q] = __builtin_amdgcn_raw_buffer_load_b128(r, vo, row * ld + strip * 128, 0);
    }
}
DI void retile_store(const Ctx& c, int J, lds_ptr scr, const u32x4 (&v)[5][4]) {
  const int e = J < 216 ? J : J < 256 ? J - 216 : J - 256, mat = J >= 108;
  const int strip = J < 216 ? (e >> 2) % 27 : e % 10, k0 = J < 216 ? (e & 3) * 5 : min((e / 10) * 5, 49);
  const unsigned ts = J < 216 ? 40960 : J < 256 ? 20480 : 55296;
  const unsigned dst = (J < 216 ? WS_WGU + mat * 20480 : J < 256 ? WS_WO : WS_WD) + strip * 4 * ts + k0 * 1024;
  const int i = c.lane & 15, g = c.lane >> 4;
  const bf16x8 ones = __builtin_bit_cast(bf16x8, u32x4{0x3f803f80u, 0x3f803f80u, 0x3f803f80u, 0x3f803f80u});
  f32x4 cs[4] = {f32x4{}, f32x4{}, f32x4{}, f32x4{}};
#pragma unroll
  for (int u = 0; u < 5; ++u) {
#pragma unroll
    for (int p = 0; p < 4; ++p) *(LAS u32x4*)(scr + p * 1024 + c.lane * 16) = v[u][p];
#pragma unroll
    for (int t = 0; t < 4; ++t) {
      const bf16x8 f = ldstr(scr + (g * 8 + (i >> 2)) * 128 + t * 32 + (i & 3) * 8, 512);
      __builtin_amdgcn_raw_buffer_store_b128(__builtin_bit_cast(u32x4, f), c.r, c.lane * 16, dst + u * 1024 + t * ts, 16);
      cs[t] = mfma16(f, ones, cs[t]);
    }
  }
  if (J < 216 && i == 0) {
#pragma unroll
    for (int t = 0; t < 4; ++t)
      __builtin_amdgcn_raw_buffer_store_b128(__builtin_bit_cast(u32x4, cs[t]), c.r, g * 16, WS_CSG + ((e & 3) * 3456 + (strip * 8 + t * 2 + mat) * 16) * 4, 16);
  }
}
DI int early_piece(int e) { return e < 4 ? (e & 1) + (e >> 1) * 8 : e < 6 ? 16 + (e - 4) * 4 : e < 9 ? 18 + e : 25 + e; }
DI int late_piece(int e) { return e < 6 ? 2 + e : e < 12 ? 4 + e : e < 15 ? 5 + e : e < 18 ? 6 + e : e < 25 ? 9 + e : 12 + e; }
DI void att_load(const Ctx& c, int lw, int layer, unsigned iss, const uint16_t* wo, const uint16_t* wg, const uint16_t* wu, const uint16_t* wd) {
  const int head = att_head(c), qb = att_qb(c), lane = c.lane;
  const char* Lp = c.ws + WS_L + layer * L_SZ;
  const unsigned vo = lane * 16;
  int grp = ATT_ROT ? qb >> 1 : 0;
  auto src_of = [&](int p) {
    return p < 16 ? L_K1 + head * 65536 + grp * 16384 + p * 1024
                  : p < 24 ? L_K2 + (head >> 1) * 32768 + grp * 8192 + (p - 16) * 1024 : L_V + head * 81920 + grp * 20480 + (p - 24) * 1024;
  };
  unsigned v0[14], m0[11], vg[11], sg[11];
#pragma unroll
  for (int q = 0; q < 11; ++q) {
    const int p = q < 3 ? early_piece(lw * 3 + q) : late_piece(lw * 8 + q - 3), pg = lw * 11 + q;
    v0[q] = src_of(p) + vo; m0[q] = p * 1024;
    vg[q] = src_of(pg) + vo; sg[q] = pg < 16 ? 16384 : pg < 24 ? 8192 : 20480;
    asm volatile("" : "+v"(v0[q]), "+s"(m0[q]));
  }
#pragma unroll
  for (int q = 0; q < 3; ++q) {
    const int p = lw * 3 + q;
    v0[11 + q] = (p < 8 ? L_Q1 + head * 65536 + qb * 8192 + p * 1024 : L_Q2 + (head >> 1) * 32768 + qb * 4096 + (p - 8) * 1024) + vo - q * 1024;
    PIN(v0[11 + q]);
  }
  auto issue = [&](int slot) {
#pragma unroll
    for (int q = 0; q < 11; ++q) { vg[q] += grp == 3 ? -3 * sg[q] : sg[q]; glds1(slot * AT_SLOT + (lw * 11 + q) * 1024, Lp, vg[q]); }
    grp = (grp + 1) & 3;
  };
  const int o0 = (head >> 1) * 5 + (head & 1) * 2, c0 = head * 20;
  auto kv_cb = [&](int sel) { return sel == 0 ? 5 + (o0 >> 4) : sel == 1 ? 5 + ((o0 + 2) >> 4) : sel == 2 ? 10 + (c0 >> 7) : 10 + ((c0 + 19) >> 7); };
  if (lw == 0) {
    sig_wait(c, iss, M_ISS);
    poll(c, flg(FL_QKV + layer * 5, lane & 16 ? (qb * 2 + (lane & 1)) * 16 + ((o0 + (lane & 2)) >> 4) : (grp * 4 + (lane & 3)) * 16 + kv_cb((lane >> 2) & 3)));
    sig_set(c, layer * 5 + 2);
    STAMPL(layer * 5 + 1, 6);
  } else sig_wait(c, layer * 5 + 2);
  glds3(2 * AT_SLOT + lw * 3072, Lp, v0[11], v0[12], v0[13]);
#pragma unroll
  for (int q = 0; q < 3; ++q) glds1(m0[q], Lp, v0[q]);
#pragma unroll
  for (int q = 3; q < 3 + ATT_LATE; ++q) glds1(m0[q], Lp, v0[q]);
  wait_vm<ATT_LATE>(); BARRIER();
#pragma unroll
  for (int q = 3 + ATT_LATE; q < 11; ++q) glds1(m0[q], Lp, v0[q]);
  wait_vm<0>(); BARRIER();
  if (lw == 0) { poll(c, flg(FL_QKV + layer * 5, (lane & 15) * 16 + kv_cb(lane >> 4))); sig_set(c, layer * 5 + 3); } else sig_wait(c, layer * 5 + 3, M_SIG, true);
  issue(1);
  const int J = (lw - 1) * 128 + c.j * 4 + (c.xcd >> 1);
  const bool rt = layer == 0 && lw > 0 && J < 366 && !(c.xcd & 1);
  u32x4 v[5][4];
  if (rt) { retile_load(c, J, v, wo, wg, wu, wd); wait_vm<20>(); } else wait_vm<0>();
  BARRIER();
  issue(2);
  if (rt) retile_store(c, J, c.lds + AT_SCR + (lw - 1) * 4096, v);
  if (layer == 0 && lw > 0) publish(c, M_CNT3, 3, flg(FL_RT, blockIdx.x));
  wait_vm<0>(); BARRIER();
  issue(0);
  wait_vm<0>();
  if (layer == 0) {
    if (lw == 0) {
      if (c.j == 0) {
#pragma unroll 1
        for (int q = 0; q < 4; ++q) poll(c, flg(FL_RT, q * 64 + lane));
        if (lane < 32) __hip_atomic_store((GAS u64*)(c.ws + flg(FL_RT2, lane * 8 + c.xcd)), c.tgt, __ATOMIC_RELAXED, __HIP_MEMORY_SCOPE_AGENT);
      }
      poll(c, flg(FL_RT2, blockIdx.x));
      if (blockIdx.x == 0 && lane == 0) *(u64*)(c.ws + WS_EPOCH) = c.tgt;
    }
  }
  BARRIER();
  u32x4 pk[25];
#pragma unroll
  for (int e = 0; e < 25; ++e) pk[e] = ld16s(c.r, vo, WS_WO + (pr_cb(c) * 5 + e % 5) * 20480 + (lw * 5 + e / 5) * 1024);
#pragma unroll 1
  for (unsigned q = lw; q < 35; q += 4) glds1(AT_SCR + lw * 1024, c.ws + WS_WGU + ((gu_cb(c) * 27) >> 2) * 40960 + ((c.j & 7) * 35 + q) * 1024, vo);
  wait_vm<0>();
  BARRIER();
#pragma unroll
  for (int e = 0; e < 25; ++e) PIN(pk[e]);
  auto dump = [&]() {
#pragma unroll
    for (int e = 0; e < 25; ++e) { const int k = lw * 5 + e / 5; ldsw16(c.lds + (k >> 1) * 10240 + (k & 1) * 1024 + (e % 5) * 2048 + vo, pk[e]); }
  };
  sig_wait(c, layer * 16 + 8, M_CNT);
  dump();
  if (lw == 0) { poll(c, flg(FL_ATT + layer * 5, (pr_panel(c) >> 2) * 32 + (lane & 31))); STAMPL(layer * 5 + 2, 6); }
}

template <int NS, int NB, int WIN>
DI void proj_compute(const Ctx& c, int w, int slot_id, unsigned a_off, const char* res_rm, unsigned res_off, unsigned out_off, unsigned st_off,
                     uint16_t* out, int arr) {
  constexpr bool RES = NS <= NB;
  const int panel = pr_panel(c), cb = pr_cb(c);
  SLOT(slot_id);
  STAMP(0);
  if (w >= 4) {
    if constexpr (RES) { BARRIER(); BARRIER(); if (PROJ2) BARRIER(); }
    else {
      static_assert(RES || (NS == 27 && NB == 15 && WIN == 18), "schedule");
      const int par = (w >> 1) & 1, half = w & 1;
      u32x4 pk[15];
#pragma unroll
      for (int e = 0; e < 15; ++e) {
        const int q = half * 5 + e % 5;
        pk[e] = ld16s(c.r, c.lane * 16, WS_WD + (cb * 5 + (q >> 1)) * 55296 + ((15 + par + (e / 5) * 2) * 2 + (q & 1)) * 1024);
      }
      lds_ptr pw = c.lds + par * 10240 + half * 5120 + c.lane * 16;
      BARRIER(); BARRIER();
#pragma unroll
      for (int e = 0; e < 10; ++e) ldsw16(pw + (e / 5) * 20480 + (e % 5) * 1024, pk[e]);
      BARRIER();
#pragma unroll
      for (int e = 10; e < 15; ++e) ldsw16(pw + 40960 + (e % 5) * 1024, pk[e]);
      BARRIER(); BARRIER(); BARRIER(); BARRIER();
      if (PROJ2) BARRIER();
    }
    return;
  }
  const int lane = c.lane, i = lane & 15, g = lane >> 4, mt = panel * 4 + w;
  u32x2 res[5];
#pragma unroll
  for (int t = 0; t < 5; ++t)
    res[t] = res_rm ? *(const u32x2*)(res_rm + (mt * 16 + i) * 1280 + cb * 160 + t * 32 + g * 8)
                    : ld8(c.r, res_off + (mt * 80 + cb * 10 + t * 2 + (g >> 1)) * 256 + i * 16 + (g & 1) * 8);
  f32x4 acc[5];
#pragma unroll
  for (int t = 0; t < 5; ++t) { acc[t] = f32x4{bflo(res[t][0]), bfhi(res[t][0]), bflo(res[t][1]), bfhi(res[t][1])}; PIN(acc[t]); }
  float piv;
  { const f32x4 s4 = (acc[0] + acc[1]) + (acc[2] + acc[3]) + acc[4]; piv = rows4((s4[0] + s4[1]) + (s4[2] + s4[3])) * (1.f / 80.f); }
  const int ch = (g & 1) * 2 + (g >> 1);
  unsigned xo = out ? (mt * 16 + i) * 1280 + cb * 160 + ch * 16 : out_off + (mt * 80 + cb * 10 + ch) * 256 + i * 16;
  unsigned so = st_off + (mt >> 3) * 8192 + cb * 1024 + ((mt & 7) * 16 + i) * 8;
  asm volatile("" : "+v"(piv), "+v"(xo), "+v"(so));
  lds_ptr pb = c.lds + lane * 16;
  unsigned av = a_off + mt * (NS * 2048) + lane * 16;
  u32x4 a[WIN];
  bf16x8 f[3][5];
  int sb = 0;
  STAMP(1);
  BARRIER();
  STAMP(2); STAMPC(0); WRESET();
  constexpr int AH = RES ? WO_AH : DN_AH;
  auto lda = [&](int k) {
    if (RES && k >= 16) {
      const unsigned o = a_off + mt * 20480 + 16384 + (k - 16) * 1024 + g * 256 + i * 8;
      const u32x2 lo = ld8(c.r, o), hi = ld8(c.r, o + 128);
      a[k % WIN] = u32x4{lo[0], lo[1], hi[0], hi[1]};
    } else a[k % WIN] = ld16(c.r, av + k * 1024);
  };
#pragma unroll
  for (int k = 0; k < AH; ++k) lda(k);
#pragma unroll
  for (int t = 0; t < 5; ++t) { f[0][t] = ldsr16(pb + t * 2048); f[1][t] = ldsr16(pb + t * 2048 + 1024); }
  SBAR();
  auto window = [&](bool first, bool last) {
#pragma unroll
    for (int k = 0; k < WIN; ++k) {
      const bool ld = !(last && k + 2 >= WIN);
      if ((k & 1) == 0) {
        if (RES) pb += 10240;
        else { pb += sb == NB - 1 ? -(NB - 1) * 10240 : 10240; sb = sb == NB - 1 ? 0 : sb + 1; }
        if (!ld) BARRIER(); else if (!RES && (k == 10 || (k == 0 && !first))) BARRIER_NW();
      }
#pragma unroll
      for (int t = 0; t < 5; ++t) {
        if (ld) f[(k + 2) % 3][t] = ldsr16(pb + t * 2048 + (k & 1) * 1024);
        acc[t] = mfma16(f[k % 3][t], __builtin_bit_cast(bf16x8, a[k]), acc[t]);
        SBAR();
      }
      if (!last || k + AH < WIN) { lda(k + AH); SBAR(); }
    }
    av += WIN * 1024;
  };
  static_assert(2 * NS % WIN == 0 && (2 * NS == WIN || WIN % 6 == 0), "window");
#pragma unroll 1
  for (int b = 1; b < 2 * NS / WIN; ++b) window(b == 1, false);
  window(false, true);
  STAMP(3); STAMPC(1);
  u32x2 o[5];
#pragma unroll
  for (int t = 0; t < 5; ++t) {
    o[t][0] = pk2(acc[t][0], acc[t][1]); o[t][1] = pk2(acc[t][2], acc[t][3]);
  }
  const u32x4 p0 = pair16(o[0][0], o[0][1], o[1][0], o[1][1]), p1 = pair16(o[2][0], o[2][1], o[3][0], o[3][1]),
              p2 = pair16(o[4][0], o[4][1], o[4][0], o[4][1]);
  if (out) {
    char* po = (char*)out + xo;
    *(u32x4*)po = p0; *(u32x4*)(po + 64) = p1;
    if (!(g & 1)) *(u32x4*)(po + 128) = p2;
    STAMP(4);
    return;
  }
  st16(c.r, xo, p0); st16(c.r, xo + 1024, p1);
  if (!(g & 1)) st16(c.r, xo + 2048, p2);
  f32x4 s4 = acc[0] - piv, q4 = s4 * s4;
#pragma unroll
  for (int t = 1; t < 5; ++t) { const f32x4 d = acc[t] - piv; s4 += d; q4 += d * d; }
  const float sd = rows4((s4[0] + s4[1]) + (s4[2] + s4[3])), sq = rows4((q4[0] + q4[1]) + (q4[2] + q4[3]));
  if (g == 0) st8(c.r, so, u32x2{__float_as_uint(sd + 80.f * piv), __float_as_uint(fmaxf(sq - sd * sd * (1.f / 80.f), 0.f))});
  STAMP(4);
  publish(c, M_CNT4, 4, flg(arr, panel * 8 + cb), true);
  STAMP(5);
}
template <int NF>
DI void gu_consumer16(const Ctx& c, int layer, int f0, int mq) {
  const int panel = gu_panel(c), ft0 = (gu_cb(c) * 27) >> 2;
  const int lane = c.lane, i = lane & 15, g = lane >> 4;
  f32x4 acc[2][2 * NF];
#pragma unroll
  for (int m = 0; m < 2; ++m)
#pragma unroll
    for (int t = 0; t < 2 * NF; ++t) acc[m][t] = f32x4{0.f, 0.f, 0.f, 0.f};
  lds_ptr pa = c.lds + GU_A + mq * 2048 + lane * 16, pb = c.lds + f0 * 2048 + lane * 16;
  bf16x8 x0[2], x1[2], wa[NF], wb0[NF], wb1[NF];
  auto step = [&](const bf16x8* xa, const bf16x8* wb, bf16x8* xn, bf16x8* wn_, bool ld) {
#pragma unroll
    for (int q = 0; q < 4 * NF; ++q) {
      const int t = q >> 1, m = q & 1;
      if (ld) {
        if (q < 2) xn[q] = ldsr16(pa + q * 1024);
        else if (q < 2 * NF) { const int e = NF == 4 ? (q == 3 ? 0 : q == 4 ? 1 : q == 6 ? 2 : q == 7 ? 3 : -1) : NF == 3 ? q - 3 : q - 2; if (e >= 0) wn_[e] = ldsr16(pb + (NF + e) * 1024); }
        else if (q & 1) wa[(q - 2 * NF) >> 1] = ldsr16(pb + ((q - 2 * NF) >> 1) * 1024);
      }
      acc[m][t] = mfma16(t < NF ? wa[t] : wb[t - NF], xa[m], acc[m][t]);
      SBAR();
    }
  };
  int sl = 0;
  auto adv = [&]() {
    pa += sl == GU_NA - 1 ? -(GU_NA - 1) * 8192 : 8192; pb += sl == GU_NB - 1 ? -(GU_NB - 1) * 14336 : 14336;
    sl = sl == GU_NA - 1 ? 0 : sl + 1;
  };
  unsigned ho = WS_L + layer * L_SZ + L_H + ((panel * 8 + mq * 2 + (g & 1)) * 216 + (ft0 + f0) * 2 + (g >> 1)) * 256 + i * 16;
  PIN(ho);
  SLOT(layer * 5 + 3);
  STAMP(0);
  STAMP(1);
  BARRIER();
  STAMP(2); STAMPC(0); WRESET();
  x0[0] = ldsr16(pa); x0[1] = ldsr16(pa + 1024);
#pragma unroll
  for (int t = 0; t < NF; ++t) { wa[t] = ldsr16(pb + t * 1024); wb0[t] = ldsr16(pb + (NF + t) * 1024); }
#pragma unroll 1
  for (int st = 0; st < 9; ++st) {
    adv(); BARRIER_NW(); step(x0, wb0, x1, wb1, true);
    adv(); BARRIER_NW(); step(x1, wb1, x0, wb0, true);
  }
  adv(); BARRIER_NW(); step(x0, wb0, x1, wb1, true);
  BARRIER();
  const LAS float* srs = (const LAS float*)(c.lds + M_SST) + mq * 32 + i;
  const LAS float* scs = (const LAS float*)(c.lds + M_SCS) + f0 * 32 + g * 4;
  float rs[2] = {srs[0], srs[16]}; const float nm[2] = {srs[128], srs[144]};
  f32x4 cs[2 * NF];
#pragma unroll
  for (int e = 0; e < 2 * NF; ++e) cs[e] = *(const LAS f32x4*)(scs + e * 16);
  SBAR();
  step(x1, wb1, x0, wb0, false);
  STAMP(3); STAMPC(1);
  rs[0] *= rs[0]; rs[1] *= rs[1];
#pragma unroll
  for (int f = 0; f < NF; ++f) {
    unsigned p[2][2];
#pragma unroll
    for (int m = 0; m < 2; ++m) {
      f32x4 gt = acc[m][2 * f] + cs[2 * f] * nm[m];
      const f32x4 up = (acc[m][2 * f + 1] + cs[2 * f + 1] * nm[m]) * rs[m];
      gt = __builtin_elementwise_max(gt, f32x4{0.f, 0.f, 0.f, 0.f}) * up;
      p[m][0] = pk2(gt[0], gt[1]); p[m][1] = pk2(gt[2], gt[3]);
    }
    st16(c.r, ho + f * 512, pair16(p[0][0], p[0][1], p[1][0], p[1][1]));
    SBAR();
  }
  STAMP(4);
  publish(c, M_CNT, 8, flg(FL_GU + layer * 5, panel * 16 + gu_cb(c)), true);
  STAMP(5);
}
template <int NF>
DI void gu_consumer32(const Ctx& c, int layer, int f0, int mq) {
  const int panel = gu_panel(c), ft0 = (gu_cb(c) * 27) >> 2;
  const int lane = c.lane, r32 = lane & 31, h = lane >> 5;
  f32x16 acc[NF];
#pragma unroll
  for (int t = 0; t < NF; ++t) acc[t] = f32x16{};
  const int lo = (r32 >> 4) * 1024 + h * 256 + (r32 & 15) * 16;
  lds_ptr pa = c.lds + GU_A + mq * 2048 + lo, pb = c.lds + f0 * 2048 + lo;
  bf16x8 x0[2], x1[2], wa[NF], wb0[NF], wb1[NF];
  auto step = [&](const bf16x8* xa, const bf16x8* wb, bf16x8* xn, bf16x8* wn_, bool ld) {
#pragma unroll
    for (int q = 0; q < 2 * NF; ++q) {
      if (ld) {
        if (q == 0) { xn[0] = ldsr16(pa); xn[1] = ldsr16(pa + 512); }
        if (q >= 1 && q <= NF) wn_[q - 1] = ldsr16(pb + (q - 1) * 2048 + 512);
        if (q >= NF) wa[q - NF] = ldsr16(pb + (q - NF) * 2048);
      }
      if (q < NF) acc[q] = mfma32(wa[q], xa[0], acc[q]); else acc[q - NF] = mfma32(wb[q - NF], xa[1], acc[q - NF]);
      SBAR();
    }
  };
  static_assert(GU_NA == GU_NB, "one slot counter");
  int sl = 0;
  auto adv = [&]() {
    pa += sl == GU_NA - 1 ? -(GU_NA - 1) * 8192 : 8192; pb += sl == GU_NB - 1 ? -(GU_NB - 1) * 14336 : 14336;
    sl = sl == GU_NA - 1 ? 0 : sl + 1;
  };
  SLOT(layer * 5 + 3);
  STAMP(0);
  STAMP(1);
  BARRIER();
  STAMP(2); STAMPC(0); WRESET();
  x0[0] = ldsr16(pa); x0[1] = ldsr16(pa + 512);
#pragma unroll
  for (int t = 0; t < NF; ++t) { wa[t] = ldsr16(pb + t * 2048); wb0[t] = ldsr16(pb + t * 2048 + 512); }
#pragma unroll 1
  for (int st = 0; st < 9; ++st) {
    adv(); BARRIER_NW(); step(x0, wb0, x1, wb1, true);
    adv(); BARRIER_NW(); step(x1, wb1, x0, wb0, true);
  }
  adv(); BARRIER_NW(); step(x0, wb0, x1, wb1, true);
  BARRIER();
  const LAS float* srs = (const LAS float*)(c.lds + M_SST) + mq * 32 + r32;
  const LAS float* scs = (const LAS float*)(c.lds + M_SCS) + f0 * 32 + h * 4;
  float rs = srs[0], nm = srs[128];
  f32x4 cs[2][4];
#pragma unroll
  for (int e = 0; e < 8; ++e) cs[e >> 2][e & 3] = *(const LAS f32x4*)(scs + e * 8);
  SBAR();
  step(x1, wb1, x0, wb0, false);
  STAMP(3); STAMPC(1);
  rs *= rs;
  const unsigned ho = WS_L + layer * L_SZ + L_H + ((panel * 8 + mq * 2 + (r32 >> 4)) * 216 + (ft0 + f0) * 2 + h) * 256 + (r32 & 15) * 16;
#pragma unroll
  for (int f = 0; f < NF; ++f) {
    unsigned p[2][2];
#pragma unroll
    for (int e = 0; e < 2; ++e) {
      const f32x16 a = acc[f];
      f32x4 gt = f32x4{a[e * 4], a[e * 4 + 1], a[e * 4 + 2], a[e * 4 + 3]} + cs[f & 1][e] * nm;
      const f32x4 up = (f32x4{a[8 + e * 4], a[9 + e * 4], a[10 + e * 4], a[11 + e * 4]} + cs[f & 1][2 + e] * nm) * rs;
      gt = __builtin_elementwise_max(gt, f32x4{0.f, 0.f, 0.f, 0.f}) * up;
      p[e][0] = pk2(gt[0], gt[1]); p[e][1] = pk2(gt[2], gt[3]);
    }
    if (f + 2 < NF) {
#pragma unroll
      for (int e = 0; e < 4; ++e) cs[f & 1][e] = *(const LAS f32x4*)(scs + (f + 2) * 32 + e * 8);
    }
    st16(c.r, ho + f * 512, pair32(p[0][0], p[0][1], p[1][0], p[1][1]));
    SBAR();
  }
  STAMP(4);
  publish(c, M_CNT, 8, flg(FL_GU + layer * 5, panel * 16 + gu_cb(c)), true);
  STAMP(5);
}
template <int NF> DI void gu_consumer(const Ctx& c, int layer, int f0, int mq) {
  if constexpr (GU16 == 1 || (GU16 == 2 && NF < 4) || (GU16 == 3 && NF == 4)) gu_consumer16<NF>(c, layer, f0, mq); else gu_consumer32<NF>(c, layer, f0, mq);
}
DI void mlp_load(const Ctx& c0, int lw, int layer, unsigned iss, bool qkv_next, const uint16_t* wqkv) {
  const int half = lw & 1;
  {
    BARRIER_L(); BARRIER_L();
    if (PROJ2) BARRIER_L();
  }
  { const int w = lw + 8; (void)w; WDUMP(layer * 5 + 2); }
  {
    const Ctx c = fresh(c0);
    const int ft0 = (gu_cb(c) * 27) >> 2;
    u32x4 pk[20];
    auto park = [&](int e0) {
#pragma unroll
      for (int e = e0; e < e0 + 4; ++e) {
        const int q = half * 5 + e % 5;
        pk[e] = ld16s(c.r, c.lane * 16, WS_WD + (pr_cb(c) * 5 + (q >> 1)) * 55296 + (((e / 5) * 2 + (lw >> 1)) * 2 + (q & 1)) * 1024);
      }
    };
    static_assert(GU_NA == 7 && GU_NB == 7, "schedule");
    if (lw >= 2) {
      Ring<7, GU_NB, 20, 2> rg;
#pragma unroll
      for (int p = 0; p < 7; ++p) rg.voff[p] = WS_WGU + (ft0 * 2 + half * 7 + p) * 20480 + c.lane * 16;
      rg.init(c.ws, 1024, 0, half);
      if (half == 0) ln_cols<4>(c, 56, WS_CSG + ft0 * 128, 13824);
      sig_wait(c, iss - layer * 16 + 4, M_CNT4);
      rg.template fill_ahead<1>(c, layer * 5 + 4);
      rg.template range<5, 5>(); rg.steady();
      wait_vm<28>(); BARRIER_L(); park(0);
      wait_vm<21 + 4>(); BARRIER_L(); park(4);
      wait_vm<14 + 8>(); BARRIER_L(); park(8);
      wait_vm<7 + 12>(); BARRIER_L(); park(12);
      wait_vm<16>(); BARRIER_L(); park(16);
    } else {
      Ring<4, GU_NA, 20, 2> rg;
#pragma unroll
      for (int p = 0; p < 4; ++p) rg.voff[p] = WS_L + layer * L_SZ + L_X1 + (gu_panel(c) * 8 + half * 4 + p) * 20480 + c.lane * 16;
      rg.init(c.ws, 1024, GU_A, half);
      rg.template start<3>(c, half, 4 * 20480, flg(FL_WO + layer * 5, gu_panel(c) * 16 + (c.lane & 15)), layer * 5 + 4, iss + 12);
      rg.template head<3>();
      f32x2 st[8];
      wait_vm<16>(); BARRIER_L();
      ln_load(c, WS_ST + (layer * 2 + 1) * STB + gu_panel(c) * 8192, half * 64 + c.lane, st);
      wait_vm<12 + 8>(); BARRIER_L(); park(0); park(4);
      wait_vm<8 + 8 + 8>(); BARRIER_L();
      const f32x2 t = ln_sum(st);
      wait_vm<8>(); BARRIER_L(); ln_put(c, half * 64 + c.lane, t); park(8);
      BARRIER_L(); park(12); park(16);
    }
    BARRIER_L();
#pragma unroll
    for (int e = 0; e < 20; ++e) PIN(pk[e]);
    { const int w = lw + 8; (void)w; WDUMP(layer * 5 + 3); }
    auto dump = [&]() {
#pragma unroll
      for (int e = 0; e < 20; ++e) ldsw16(c.lds + ((e / 5) * 2 + (lw >> 1)) * 10240 + (half * 5 + e % 5) * 1024 + c.lane * 16, pk[e]);
    };
    if (lw >= 2) {
      Ring<5, DN_NB, 27, 2> rg;
#pragma unroll
      for (int p = 0; p < 5; ++p) { const int q = half * 5 + p; rg.voff[p] = WS_WD + (pr_cb(c) * 5 + (q >> 1)) * 55296 + (q & 1) * 1024 + c.lane * 16; }
      rg.init(c.ws + 8 * 2048, 2048, 0, half);
      rg.slot = 8;
      sig_wait(c, layer * 16 + 16, M_CNT);
      rg.template fill<3>(); dump(); rg.template fill<4>();
      BARRIER_L();
      wait_vm<5 * 5>(); BARRIER_L();
      wait_vm<0>(); BARRIER_L();
      rg.base += 6 * 2048; rg.slot = 6;
      rg.template fill<3>();
      BARRIER_L();
      rg.template fill<3>();
      wait_vm<3 * 5>(); BARRIER_L();
      wait_vm<0>(); BARRIER_L();
      BARRIER_L();
      if (PROJ2) BARRIER_L();
    } else {
      sig_wait(c, layer * 16 + 16, M_CNT);
      dump();
      if (lw == 0) { poll(c, flg(FL_GU + layer * 5, (pr_panel(c) >> 1) * 16 + (c.lane & 15))); STAMPL(layer * 5 + 4, 6); }
#pragma unroll 1
      for (int k = 0; k < 7 + PROJ2; ++k) BARRIER_L();
    }
  }
  { const int w = lw + 8; (void)w; WDUMP(layer * 5 + 4); }
  if (qkv_next) qkv_load1(fresh(c0), lw, wqkv);
}

__global__ __launch_bounds__(768, 3) void fused_kernel(const uint16_t* __restrict__ x, const uint16_t* __restrict__ wqkv, const uint16_t* __restrict__ wo,
                                                    const uint16_t* __restrict__ wg, const uint16_t* __restrict__ wu,
                                                    const uint16_t* __restrict__ wd, uint16_t* __restrict__ out, char* __restrict__ ws) {
  extern __shared__ __attribute__((aligned(16))) char smem[];
  const char* pc;
  asm volatile("s_getpc_b64 %0" : "=s"(pc));
  glds1(P0_SCR + 10240 + 1024, (const char*)((u64)pc & ~15ull), ((blockIdx.x >> 3) * 12 + (threadIdx.x >> 6)) % PF_KB * 1024 + (threadIdx.x & 63) * 16);
  Ctx c;
  c.ws = ws; c.lds = (lds_ptr)smem; c.lane = threadIdx.x & 63; c.xcd = blockIdx.x & 7; c.j = blockIdx.x >> 3;
  c.r = __builtin_amdgcn_make_buffer_rsrc(ws, 0, WS_END, 0x00020000);
  const int w = __builtin_amdgcn_readfirstlane(threadIdx.x >> 6);
  SPANST(0);
  c.tgt = 0;
  if (threadIdx.x < 16) *(LAS unsigned*)(c.lds + M_CNT + threadIdx.x * 4) = 0u;
  const bool qkv_on = qkv_cb(c) != 15;
  Ctx c0 = c;
  WDUMP(15);
  if (__builtin_expect(w >= 8, LD_FIRST)) {
    __builtin_amdgcn_s_setprio(3);
    if (qkv_on) qkv_load0(fresh(c0), w - 8, x, wqkv); else BARRIER();
    c0.tgt = epoch(ws);
#pragma unroll 1
    for (int layer = 0; layer < 2; ++layer) {
      const unsigned iss = layer * 24 + (qkv_on ? (layer + 1) * 4 : 0);
      WDUMP(layer * 5);
      att_load(fresh(c0), opq(w - 8), opq(layer), iss, wo, wg, wu, wd);
      WDUMP(layer * 5 + 1);
      mlp_load(c0, opq(w - 8), opq(layer), iss, layer == 0 && qkv_on, wqkv);
    }
  } else {
    if (w < 4) {
      const unsigned l = fresh(c0).lane;
      const char* base = w == 0 ? (const char*)x : w == 1 ? (const char*)wqkv : ws;
      const unsigned off = w < 2 ? l * 38400u : min(((w - 2) * 64 + l) * 524288u + 2048, WS_END);
      if (TLB_IN || w >= 2) asm volatile(GL_HEAD "global_load_lds_dword %2, %1 sc1" ::"s"(P0_SCR + 10240), "s"(base), "v"(off) : "memory");
    }
#pragma unroll 1
    for (int layer = 0; layer < 2; ++layer) {
      const unsigned L = WS_L + layer * L_SZ;
      qkv_compute(fresh(c0), c0.tgt, opq(w), opq(layer), x, wqkv);
      WDUMP(layer * 5);
      att_compute(fresh(c0), opq(w), opq(layer));
      WDUMP(layer * 5 + 1);
      proj_compute<10, WO_NB, 20>(fresh(c0), opq(w), layer * 5 + 2, L + L_O, layer == 0 ? (const char*)x : nullptr, WS_X2, L + L_X1,
                                  WS_ST + (layer * 2 + 1) * STB, nullptr, FL_WO + layer * 5);
      WDUMP(layer * 5 + 2);
      if (w < 4) gu_consumer<4>(fresh(c0), opq(layer), 0, opq(w));
      else if (GU_SKIP && (gu_cb(c0) & 3) == 0) gu_consumer<2>(fresh(c0), opq(layer), 4, opq(w - 4));
      else gu_consumer<3>(fresh(c0), opq(layer), 4, opq(w - 4));
      WDUMP(layer * 5 + 3);
      proj_compute<27, DN_NB, 18>(fresh(c0), opq(w), layer * 5 + 4, L + L_H, nullptr, L + L_X1, WS_X2, WS_ST + 2 * STB, layer ? out : nullptr,
                                  FL_DN + layer * 5);
      WDUMP(layer * 5 + 4);
    }
  }
}

extern "C" void kernel_launch(void* const* d_in, const int* in_sizes, int n_in, void* d_out, int out_size, void* d_ws, size_t ws_size,
                              hipStream_t stream) {
  (void)hipFuncSetAttribute((const void*)fused_kernel, hipFuncAttributeMaxDynamicSharedMemorySize, LDS_BYTES);
  fused_kernel<<<256, 768, LDS_BYTES, stream>>>((const uint16_t*)d_in[0], (const uint16_t*)d_in[1], (const uint16_t*)d_in[2],
                                                (const uint16_t*)d_in[4], (const uint16_t*)d_in[5], (const uint16_t*)d_in[6],
                                                (uint16_t*)d_out, (char*)d_ws);
}
```

```cpp
#include <hip/hip_runtime.h>
#include <hip/hip_bf16.h>
#include <stdint.h>

typedef __attribute__((ext_vector_type(8))) __bf16 bf16x8;
typedef __attribute__((ext_vector_type(16))) __bf16 bf16x16;
typedef __attribute__((ext_vector_type(4))) short s16x4;
typedef __attribute__((ext_vector_type(8))) short s16x8;
typedef __attribute__((ext_vector_type(2))) __bf16 bf16x2;
typedef __attribute__((ext_vector_type(2))) float f32x2;
typedef __attribute__((ext_vector_type(4))) float f32x4;
typedef __attribute__((ext_vector_type(8))) float f32x8;
typedef __attribute__((ext_vector_type(16))) float f32x16;
typedef __attribute__((ext_vector_type(2))) unsigned u32x2;
typedef __attribute__((ext_vector_type(4))) unsigned u32x4;
typedef unsigned long long u64;
#define LAS __attribute__((address_space(3)))
#define GAS __attribute__((address_space(1)))
typedef LAS char* lds_ptr;
typedef __amdgpu_buffer_rsrc_t rsrc_t;
#define DI __device__ __forceinline__

constexpr unsigned XB = 2048u * 640 * 2;
constexpr unsigned WS_EPOCH = 0;
constexpr unsigned WS_FLAG = 4096;
constexpr int FL_P0 = 0, FL_RT = 1, FL_QKV = 2, FL_ATT = 3, FL_WO = 4, FL_GU = 5, FL_DN = 6, FL_RT2 = 12;
constexpr unsigned WS_WO  = WS_FLAG + 13u * 32768;
constexpr unsigned WS_WGU = WS_WO + 640u * 640 * 2;
constexpr unsigned WS_WD  = WS_WGU + 3456u * 640 * 2;
constexpr unsigned WS_CSQ = WS_WD + 640u * 1728 * 2;
constexpr unsigned WS_CSG = WS_CSQ + 8u * 1920 * 4;
constexpr unsigned WS_ST  = WS_CSG + 4u * 3456 * 4;
constexpr unsigned STB = 2048u * 64;
constexpr unsigned WS_X2  = WS_ST + 4 * STB;
constexpr unsigned WS_L   = WS_X2 + XB;
constexpr unsigned L_Q1 = 0, L_Q2 = L_Q1 + 32u * 65536, L_K1 = L_Q2 + 16u * 32768, L_K2 = L_K1 + 32u * 65536, L_V = L_K2 + 16u * 32768,
                   L_O = L_V + XB, L_X1 = L_O + XB, L_H = L_X1 + XB, L_SZ = L_H + 2048u * 1728 * 2;
constexpr unsigned WS_END = WS_L + 2 * L_SZ;

constexpr int LDS_BYTES = 163840, MISC = 159744, M_SST = MISC, M_SCS = MISC + 1024, M_CNT = MISC + 2048, M_CNT2 = M_CNT + 8, M_CNT3 = M_CNT + 16, M_CNT4 = M_CNT + 24, M_SIG = M_CNT + 32, M_ISS = M_CNT + 40, M_TGT = M_CNT + 48;
constexpr int QKV_NB = 5, QKV_NA = 4, QKV_A = QKV_NB * 16384;
constexpr int WO_NB = 10, DN_NB = 15;
constexpr int GU_NB = 7, GU_NA = 7, GU_A = GU_NB * 14336;
constexpr int AT_SLOT = 45056, AT_CONST = 3 * AT_SLOT, AT_SCR = AT_CONST + 10752, AT_MERGE = AT_SLOT;
static_assert(QKV_A + QKV_NA * 16384 <= MISC && DN_NB * 10240 <= MISC && GU_A + GU_NA * 8192 <= MISC, "lds");
static_assert(AT_SCR + 3 * 4096 <= MISC, "lds");

DI unsigned pk2(float a, float b) { f32x2 v = {a, b}; bf16x2 r = __builtin_convertvector(v, bf16x2); return __builtin_bit_cast(unsigned, r); }
DI float bflo(unsigned u) { return __uint_as_float(u << 16); }
DI float bfhi(unsigned u) { return __uint_as_float(u & 0xffff0000u); }
DI f32x4 mfma16(bf16x8 a, bf16x8 b, f32x4 c) { return __builtin_amdgcn_mfma_f32_16x16x32_bf16(a, b, c, 0, 0, 0); }
DI f32x16 mfma32(bf16x8 a, bf16x8 b, f32x16 c) { return __builtin_amdgcn_mfma_f32_32x32x16_bf16(a, b, c, 0, 0, 0); }
DI bf16x8 ldsr16(lds_ptr p) { return *(const LAS bf16x8*)p; }
DI bf16x8 ldstr(lds_ptr p, int second) {
  const s16x4 a = __builtin_amdgcn_ds_read_tr16_b64_v4i16((LAS s16x4*)p), b = __builtin_amdgcn_ds_read_tr16_b64_v4i16((LAS s16x4*)(p + second));
  return __builtin_bit_cast(bf16x8, (s16x8)__builtin_shufflevector(a, b, 0, 1, 2, 3, 4, 5, 6, 7));
}
#define BARRIER() asm volatile("s_waitcnt lgkmcnt(0)\n\ts_barrier" ::: "memory")
template <int N> DI void wait_vm() { asm volatile("s_waitcnt vmcnt(%0)" ::"n"(N > 63 ? 63 : N) : "memory"); }
#define WDUMP(id)
#define WRESET()
#define BARRIER_L() BARRIER()
#define BARRIER_NW() asm volatile("s_barrier" ::: "memory")
#define SBAR() __builtin_amdgcn_sched_barrier(0)
#define PIN(x) asm volatile("" : "+v"(x))
#define GL_HEAD "s_mov_b32 m0, %0\n\ts_nop 3\n\t"
#define GL(v, off) "global_load_lds_dwordx4 %" #v ", %1 offset:" #off " sc1\n\t"
DI void glds1(unsigned m, const char* sb, unsigned v0) { asm volatile(GL_HEAD GL(2, 0)::"s"(m), "s"(sb), "v"(v0) : "memory"); }
DI void glds2(unsigned m, const char* sb, unsigned v0, unsigned v1) {
  asm volatile(GL_HEAD GL(2, 0) GL(3, 1024)::"s"(m), "s"(sb), "v"(v0), "v"(v1) : "memory");
}
DI void glds3(unsigned m, const char* sb, unsigned v0, unsigned v1, unsigned v2) {
  asm volatile(GL_HEAD GL(2, 0) GL(3, 1024) GL(4, 2048)::"s"(m), "s"(sb), "v"(v0), "v"(v1), "v"(v2) : "memory");
}
DI void glds4(unsigned m, const char* sb, unsigned v0, unsigned v1, unsigned v2, unsigned v3) {
  asm volatile(GL_HEAD GL(2, 0) GL(3, 1024) GL(4, 2048) GL(5, 3072)::"s"(m), "s"(sb), "v"(v0), "v"(v1), "v"(v2), "v"(v3) : "memory");
}
DI void st16(rsrc_t r, unsigned off, u32x4 v) { __builtin_amdgcn_raw_buffer_store_b128(v, r, off, 0, 16); }
DI void st8(rsrc_t r, unsigned off, u32x2 v) { __builtin_amdgcn_raw_buffer_store_b64(v, r, off, 0, 16); }
DI void st4(rsrc_t r, unsigned off, unsigned v) { __builtin_amdgcn_raw_buffer_store_b32(v, r, off, 0, 16); }
DI u32x4 ld16(rsrc_t r, unsigned off) { return __builtin_amdgcn_raw_buffer_load_b128(r, off, 0, 16); }
DI u32x2 ld8(rsrc_t r, unsigned off) { return __builtin_amdgcn_raw_buffer_load_b64(r, off, 0, 16); }
DI u32x4 ld16s(rsrc_t r, unsigned voff, unsigned soff) { return __builtin_amdgcn_raw_buffer_load_b128(r, voff, soff, 16); }
DI void ldsw16(lds_ptr p, u32x4 v) { *(LAS u32x4*)p = v; }
DI f32x2 ld8_async(unsigned voff, const char* sbase) {
  f32x2 v; asm volatile("s_nop 4\n\tglobal_load_dwordx2 %0, %1, %2 sc1" : "=v"(v) : "v"(voff), "s"(sbase) : "memory"); return v;
}
DI u32x4 pair32(unsigned a0, unsigned a1, unsigned b0, unsigned b1) {
  auto x = __builtin_amdgcn_permlane32_swap(a0, b0, false, false);
  auto y = __builtin_amdgcn_permlane32_swap(a1, b1, false, false);
  return u32x4{x[0], y[0], x[1], y[1]};
}
template <int N> DI float ror16(float v) { return __int_as_float(__builtin_amdgcn_update_dpp(0, __float_as_int(v), 0x120 + N, 0xf, 0xf, false)); }
DI float sum32(float v) {
  v += ror16<8>(v); v += ror16<4>(v); v += ror16<2>(v); v += ror16<1>(v);
  auto a = __builtin_amdgcn_permlane16_swap(__float_as_uint(v), __float_as_uint(v), false, false);
  return __uint_as_float(a[0]) + __uint_as_float(a[1]);
}
DI u32x4 pair16(unsigned a0, unsigned a1, unsigned b0, unsigned b1) {
  auto x = __builtin_amdgcn_permlane16_swap(a0, b0, false, false);
  auto y = __builtin_amdgcn_permlane16_swap(a1, b1, false, false);
  return u32x4{x[0], y[0], x[1], y[1]};
}

DI float rows4(float v) {
  auto a = __builtin_amdgcn_permlane16_swap(__float_as_uint(v), __float_as_uint(v), false, false);
  v = __uint_as_float(a[0]) + __uint_as_float(a[1]);
  auto b = __builtin_amdgcn_permlane32_swap(__float_as_uint(v), __float_as_uint(v), false, false);
  return __uint_as_float(b[0]) + __uint_as_float(b[1]);
}

#define STAMP(id)
#define STAMPC(id)
#define STAMPL(sl, id)
#define SLOT(n)

#define ATT_PRIO 3
#define ATT_ORD 0
#define WO_AH 6
#define DN_AH 8
#define ATT_ROT 0
#define GU_SKIP 1
#define ATT_LATE 0
#define GU16 1
#define QKV16 1
#define LD_FIRST 1
#define PF_KB 56
#define PROJ2 0
#define PRI_ALL 1
#define RG 0
#define TLB_IN 0
#define PACE 16
#define SPANST(id)
struct Ctx {
  char* ws; rsrc_t r; lds_ptr lds; u64 tgt; int lane, xcd, j;
};
DI Ctx fresh(const Ctx& c0) {
  Ctx c = c0;
  asm volatile("v_mbcnt_lo_u32_b32 %0, -1, 0\n\tv_mbcnt_hi_u32_b32 %0, -1, %0" : "=v"(c.lane), "+s"(c.xcd), "+s"(c.j), "+s"(c.ws));
  return c;
}
DI int opq(int v) { asm volatile("" : "+s"(v)); return v; }
constexpr int SPIN_CAP = 1 << 18;

DI u64 epoch(const char* ws) {
  u64 e; asm volatile("s_nop 4\n\ts_load_dwordx2 %0, %1, 0x0\n\ts_waitcnt lgkmcnt(0)" : "=s"(e) : "s"(ws + WS_EPOCH) : "memory"); return e + 1;
}
DI unsigned flg(int arr, int unit) { return WS_FLAG + (arr * 256 + unit) * 128; }
DI void publish(const Ctx& c, int cnt_off, unsigned n, unsigned to, bool mark = false) {
  unsigned one = 1, iss = M_ISS, cnt = cnt_off, old, so;
  u64 tgt = c.tgt, ex;
  asm volatile("" : "+v"(one), "+v"(iss), "+v"(cnt), "+v"(to), "+v"(tgt));
  if (mark) asm volatile("s_mov_b64 %0, exec\n\ts_mov_b64 exec, 1\n\tds_add_u32 %1, %2\n\ts_mov_b64 exec, %0" : "=&s"(ex) : "v"(iss), "v"(one) : "memory");
  asm volatile("s_mov_b64 %2, exec\n\ts_waitcnt vmcnt(0)\n\ts_mov_b64 exec, 1\n\tds_add_rtn_u32 %0, %3, %4\n\ts_waitcnt lgkmcnt(0)\n\t"
               "v_readfirstlane_b32 %1, %0\n\ts_mov_b64 exec, %2" : "=&v"(old), "=&s"(so), "=&s"(ex) : "v"(cnt), "v"(one) : "memory");
  if (so % n == n - 1)
    asm volatile("s_mov_b64 %0, exec\n\ts_mov_b64 exec, 1\n\ts_nop 2\n\tglobal_store_dwordx2 %1, %2, %3 sc1\n\ts_mov_b64 exec, %0"
                 : "=&s"(ex) : "v"(to), "v"(tgt), "s"(c.ws) : "memory");
}
DI void poll(const Ctx& c, unsigned off) {
#pragma unroll 1
  for (int spin = 0; spin < SPIN_CAP; ++spin) {
    u64 v;
    asm volatile("s_nop 4\n\tglobal_load_dwordx2 %0, %1, %2 sc1\n\ts_sleep 1\n\ts_waitcnt vmcnt(0)" : "=&v"(v) : "v"(off), "s"(c.ws) : "memory");
    if (__all(v == c.tgt)) break;
    __builtin_amdgcn_s_sleep(2);
  }
}
DI void sig_set(const Ctx& c, unsigned v) { if (c.lane == 0) __hip_atomic_store((LAS unsigned*)(c.lds + M_SIG), v, __ATOMIC_RELAXED, __HIP_MEMORY_SCOPE_WORKGROUP); }
DI void sig_wait(const Ctx& c, unsigned v, int off = M_SIG, bool calm = false) {
#pragma unroll 1
  for (int spin = 0; spin < SPIN_CAP * 16; ++spin) {
    if (__builtin_amdgcn_readfirstlane(__hip_atomic_load((LAS unsigned*)(c.lds + off), __ATOMIC_RELAXED, __HIP_MEMORY_SCOPE_WORKGROUP)) >= v) break;
    if (calm) __builtin_amdgcn_s_sleep(8); else if (off != M_SIG) __builtin_amdgcn_s_sleep(1);
  }
  asm volatile("" ::: "memory");
}

template <int PP, int NSLOT, int NS, int LAG = 1>
struct Ring {
  unsigned voff[PP];
  const char* base;
  unsigned adv, l0;
  int slot;
  DI void init(const char* b, unsigned a, unsigned lds0, int half) {
    base = b - 4096; adv = a; l0 = lds0 + half * PP * 1024; slot = 0;
#pragma unroll
    for (int p = 0; p < PP; ++p) voff[p] += 4096 - (p & 3) * 1024;
  }
  DI void issue() {
    const unsigned m = l0 + slot * (2 * PP * 1024);
    glds4(m, base, voff[0], voff[1], voff[2], voff[3]);
    if constexpr (PP == 5) glds1(m + 4096, base, voff[4]);
    if constexpr (PP == 7) glds3(m + 4096, base, voff[4], voff[5], voff[6]);
    if constexpr (PP == 8) glds4(m + 4096, base, voff[4], voff[5], voff[6], voff[7]);
    base += adv; slot = slot == NSLOT - 1 ? 0 : slot + 1;
  }
  template <int D0> DI void start(const Ctx& c, int half, unsigned delta, unsigned flag, unsigned sig, unsigned iss) {
    if (half == 0) {
      sig_wait(c, iss, M_ISS);
      poll(c, flag);
      issue(); base += delta - adv; l0 += PP * 1024; slot = 0; issue(); base -= delta; l0 -= PP * 1024;
      sig_set(c, sig);
      STAMPL(sig == 11 ? 5 : sig == 7 ? 3 : 8, 6);
    } else { base += adv; slot = 1; sig_wait(c, sig); }
    fill<D0 - 1>();
  }
  template <int N = NSLOT> DI void fill() {
#pragma unroll 1
    for (int s = 0; s < N; ++s) issue();
  }
  template <int NEED, int N = NSLOT> DI void fill_ahead(const Ctx& c, unsigned sig) {
    int n = 0;
#pragma unroll 1
    while (n < N) {
      if (n * PP >= NEED * PP + (NEED ? PACE : 0) &&
          __builtin_amdgcn_readfirstlane(__hip_atomic_load((LAS unsigned*)(c.lds + M_SIG), __ATOMIC_RELAXED, __HIP_MEMORY_SCOPE_WORKGROUP)) >= sig) break;
      issue(); wait_vm<PACE>(); ++n;
    }
    if (n == N) wait_vm<(N - NEED) * PP>();
    BARRIER_L(); WRESET();
#pragma unroll 1
    for (; n < N; ++n) issue();
  }
  DI void rest() { range<NSLOT - 2, NSLOT - LAG>(); steady(); range<NSLOT - 1 - LAG, 0>(); BARRIER_L(); }
  template <int R, int E> DI void range() { if constexpr (R >= E) { wait_vm<R * PP>(); BARRIER_L(); range<R - 1, E>(); } }
  template <int D0 = NSLOT> DI void head() {
    wait_vm<(D0 - 1) * PP>(); BARRIER_L(); WRESET();
    if constexpr (D0 < NSLOT) fill<NSLOT - D0>();
    range<NSLOT - 2, NSLOT - LAG>();
    steady();
  }
  DI void steady() {
#pragma unroll 1
    for (int k = LAG; k < NS - NSLOT + LAG; ++k) { wait_vm<(NSLOT - 1 - LAG) * PP>(); BARRIER_L(); issue(); }
  }
  template <int D0 = NSLOT> DI void run() { head<D0>(); range<NSLOT - 1 - LAG, 0>(); BARRIER_L(); }
};

DI void ln_load(const Ctx& c, unsigned off, int row, f32x2 (&p)[8]) {
#pragma unroll
  for (int i = 0; i < 8; ++i) p[i] = __builtin_bit_cast(f32x2, __builtin_amdgcn_raw_buffer_load_b64(c.r, row * 8, off + i * 1024, 16));
}
DI f32x2 ln_sum(const f32x2 (&p)[8]) {
  f32x2 t = ((p[0] + p[1]) + (p[2] + p[3])) + ((p[4] + p[5]) + (p[6] + p[7]));
  const float m8 = t[0] * 0.125f; float q0 = 0.f, q1 = 0.f;
#pragma unroll
  for (int i = 0; i < 8; i += 2) { const float a = p[i][0] - m8, b = p[i + 1][0] - m8; q0 += a * a; q1 += b * b; }
  t[1] += (q0 + q1) * (1.f / 80.f);
  PIN(t);
  return t;
}
DI void ln_put(const Ctx& c, int row, f32x2 t) {
  *(LAS float*)(c.lds + M_SST + row * 4) = __builtin_amdgcn_rsqf(t[1] * (1.f / 640.f) + 1e-5f);
  *(LAS float*)(c.lds + M_SST + 512 + row * 4) = -t[0] * (1.f / 640.f);
}
template <int NP> DI void ln_cols_sum(const Ctx& c, int n4, const u32x4 (&q)[NP]) {
  f32x4 cs = __builtin_bit_cast(f32x4, q[0]);
#pragma unroll
  for (int i = 1; i < NP; ++i) cs += __builtin_bit_cast(f32x4, q[i]);
  if (c.lane < n4) *(LAS f32x4*)(c.lds + M_SCS + c.lane * 16) = cs;
}
template <int NP> DI void ln_cols(const Ctx& c, int n4, unsigned off, unsigned pitch) {
  u32x4 q[NP];
#pragma unroll
  for (int i = 0; i < NP; ++i) q[i] = ld16(c.r, off + i * pitch + (c.lane < n4 ? c.lane : 0) * 16);
  ln_cols_sum<NP>(c, n4, q);
}

DI int row_group(const Ctx& c) { return RG == 0 ? c.xcd & 3 : RG == 1 ? ((c.xcd & 1) ^ 1) * 2 + ((c.xcd >> 1) & 1) : (c.xcd & 1) * 2 + ((c.xcd >> 1) & 1); }
DI int qkv_panel(const Ctx& c) { return (c.xcd & 3) + 4 * (c.j & 3); }
DI int qkv_cb(const Ctx& c) { return (c.j >> 2) * 2 + (c.xcd >> 2); }
DI int pr_panel(const Ctx& c) { return row_group(c) * 8 + (c.j & 7); }
DI int pr_cb(const Ctx& c) { return (c.xcd >> 2) * 4 + (c.j >> 3); }
DI int gu_panel(const Ctx& c) { return (c.xcd & 1) * 8 + (c.j & 7); }
DI int gu_cb(const Ctx& c) { return (c.xcd >> 1) * 4 + (c.j >> 3); }
DI int att_head(const Ctx& c) { return c.xcd * 4 + (c.j & 3); }
DI int att_qb(const Ctx& c) { return c.j >> 2; }

constexpr int P0_SCR = QKV_A + QKV_NA * 16384;
static_assert(P0_SCR + 10240 <= MISC, "lds");
struct Phase0 {
  u32x4 v[3]; float sum, m2;
  DI void load(const Ctx& c, int hw, const uint16_t* x, const uint16_t* wqkv) {
    const int lane = c.lane, r32 = lane & 31, b = blockIdx.x, row = b * 8 + hw * 2 + (lane >> 5);
    v[0] = *(const u32x4*)(x + row * 640 + r32 * 8); v[1] = *(const u32x4*)(x + row * 640 + 256 + r32 * 8);
    v[2] = *(const u32x4*)(x + row * 640 + 512 + (r32 & 15) * 8);
    if (b < 240) {
      const char* src = (const char*)wqkv + ((b / 30) * 80 + hw * 8) * 3840 + (b % 30) * 128;
      const unsigned vo = (lane >> 3) * 3840 + (lane & 7) * 16;
      glds1(P0_SCR + hw * 1024, src, vo); glds1(P0_SCR + 4096 + hw * 1024, src + 32 * 3840, vo);
      if (hw < 2) glds1(P0_SCR + 8192 + hw * 1024, src + 64 * 3840, vo);
    }
  }
  DI float dev2(int q) const {
    const float mean = sum * (1.f / 640.f); float a = 0.f, b = 0.f;
#pragma unroll
    for (int e = 0; e < 4; ++e) { const float d0 = bflo(v[q][e]) - mean, d1 = bfhi(v[q][e]) - mean; a += d0 * d0; b += d1 * d1; }
    return a + b;
  }
  DI void part1(const Ctx& c) {
    unsigned uo = 0x3f803f80u; PIN(uo);
    const bf16x2 ones = __builtin_bit_cast(bf16x2, uo);
    float s[3];
#pragma unroll
    for (int q = 0; q < 3; ++q) {
      s[q] = 0.f;
#pragma unroll
      for (int e = 0; e < 4; ++e) { const unsigned u = v[q][e]; s[q] = __builtin_amdgcn_fdot2_f32_bf16(__builtin_bit_cast(bf16x2, u), ones, s[q], false); }
    }
    sum = sum32(s[0] + s[1] + ((c.lane & 16) ? 0.f : s[2]));
    m2 = dev2(0);
    asm volatile("" : "+v"(sum), "+v"(m2));
    wait_vm<0>();
  }
  DI void part2(const Ctx& c, int hw) {
    const int lane = c.lane, r32 = lane & 31, b = blockIdx.x, row = b * 8 + hw * 2 + (lane >> 5);
    const float t = dev2(2);
    m2 = sum32(m2 + dev2(1) + ((lane & 16) ? 0.f : t));
    if (r32 < 8) st8(c.r, WS_ST + (row >> 7) * 8192 + r32 * 1024 + (row & 127) * 8, u32x2{__float_as_uint(sum * 0.125f), __float_as_uint(m2 * 0.125f)});
  }
  DI void cols(const Ctx& c, int hw) {
    const int i = c.lane & 15, g = c.lane >> 4, b = blockIdx.x;
    if (b < 240) {
      const unsigned o = 0x3f803f80u, z = g < 2 ? o : 0u;
      lds_ptr p = c.lds + P0_SCR + (g * 8 + (i >> 2)) * 128 + hw * 32 + (i & 3) * 8;
      f32x4 cs = mfma16(ldstr(p, 512), __builtin_bit_cast(bf16x8, u32x4{o, o, o, o}), f32x4{});
      cs = mfma16(ldstr(p + 4096, 512), __builtin_bit_cast(bf16x8, u32x4{o, o, o, o}), cs);
      cs = mfma16(ldstr(p + (g < 2 ? 8192 : 0), 512), __builtin_bit_cast(bf16x8, u32x4{z, z, z, z}), cs);
      if (i == 0) st16(c.r, WS_CSQ + ((b / 30) * 1920 + (b % 30) * 64 + hw * 16 + g * 4) * 4, __builtin_bit_cast(u32x4, cs));
    }
  }
};

template <bool SWAP, class PRE, class MID>
DI void qkv_loop(const Ctx& c, int w, int layer, f32x4 (&acc)[4][4], PRE pre, MID mid) {
  const int lane = c.lane, i = lane & 15, g = lane >> 4, wm = w & 1, wn = w >> 1;
  const int rm = (i >> 3) * 1024 + (i & 7) * 128 + ((g ^ ((i >> 1) & 7)) << 4);
  lds_ptr pa0 = c.lds + QKV_A + wm * 8192 + (layer == 0 ? rm : lane * 16);
  lds_ptr pa1 = c.lds + QKV_A + wm * 8192 + (layer == 0 ? rm ^ 64 : lane * 16 + 1024);
  const int f = (i >> 2) | ((g & 1) << 2), rb = (g * 8 + (i >> 2)) * 256 + (i & 3) * 8;
  lds_ptr pb[4];
#pragma unroll
  for (int t = 0; t < 4; ++t) pb[t] = c.lds + rb + (((wn * 4 + t) ^ f) << 5);
  bf16x8 x0[4], x1[4], w0[4], w1[4];
  auto step = [&](const bf16x8* xa, const bf16x8* wb, bf16x8* xn, bf16x8* wn_, lds_ptr pa, int bo, bool ld) {
#pragma unroll
    for (int q = 0; q < 16; ++q) {
      if (ld) {
        if ((q & 3) == 0) wn_[q >> 2] = ldstr(pb[q >> 2] + bo, 1024);
        if ((q & 3) == 2) xn[q >> 2] = ldsr16(pa + (q >> 2) * 2048);
      }
      const int t = q >> 2, m = q & 3;
      acc[m][t] = SWAP ? mfma16(wb[t], xa[m], acc[m][t]) : mfma16(xa[m], wb[t], acc[m][t]);
      SBAR();
    }
  };
  SLOT(layer * 5);
  if (layer) STAMP(0);
  mid(0); mid(4); mid(8); mid(12);
  STAMP(1);
  BARRIER();
  STAMP(2); STAMPC(0); WRESET();
#pragma unroll
  for (int e = 0; e < 4; ++e) { w0[e] = ldstr(pb[e], 1024); x0[e] = ldsr16(pa0 + e * 2048); }
  int sa = 0, sb = 0;
#pragma unroll 1
  for (int st = 0; st < 9; ++st) {
    step(x0, w0, x1, w1, pa1, 8192, true);
    const int da = sa == QKV_NA - 1 ? -(QKV_NA - 1) * 16384 : 16384, db = sb == QKV_NB - 1 ? -(QKV_NB - 1) * 16384 : 16384;
    sa = sa == QKV_NA - 1 ? 0 : sa + 1; sb = sb == QKV_NB - 1 ? 0 : sb + 1;
    pa0 += da; pa1 += da; pb[0] += db; pb[1] += db; pb[2] += db; pb[3] += db;
    BARRIER_NW();
    step(x1, w1, x0, w0, pa0, 0, true);
  }
  step(x0, w0, x1, w1, pa1, 8192, true);
  BARRIER();
  pre(); SBAR();
  step(x1, w1, x0, w0, pa0, 0, false);
  STAMP(3); STAMPC(1);
}
DI unsigned qkv_w_off(const Ctx& c, int q) {
  const int row = q * 4 + (c.lane >> 4);
  return row * 3840 + qkv_cb(c) * 256 + (((c.lane & 15) ^ (QKV16 ? ((row & 3) << 1) | (row & 8) : (row & 3) << 2)) << 4);
}
DI void qkv_helper(Ctx c, u64& tgt, int hw, int layer, const uint16_t* x, const uint16_t* wqkv) {
  const int panel = qkv_panel(c), cb = qkv_cb(c), l = c.lane;
  const bool on = cb != 15;
  if (layer == 0) {
    Phase0 p0;
    p0.load(c, hw, x, wqkv);
    if (on) BARRIER_L();
    p0.part1(c);
    BARRIER_L();
    p0.part2(c, hw);
    if (on) BARRIER_L();
    p0.cols(c, hw);
    c.tgt = tgt = epoch(c.ws);
    if (hw == 0 && l == 0) *(LAS u64*)(c.lds + M_TGT) = tgt;
    if (on) BARRIER_L();
    publish(c, M_CNT2, 4, flg(FL_P0, blockIdx.x));
    if (!on) return;
    BARRIER_L(); BARRIER_L(); BARRIER_L();
    const unsigned at = flg(FL_P0, hw < 2 ? panel * 16 + hw * 8 + (l & 7) : (l & 7) * 30 + cb * 2 + ((l >> 3) & 1));
    const u64 v = __hip_atomic_load((const GAS u64*)(c.ws + at), __ATOMIC_RELAXED, __HIP_MEMORY_SCOPE_AGENT);
    BARRIER_L();
    if (!__all(v == c.tgt)) poll(c, at);
    if (hw < 2) {
      f32x2 p[8];
      ln_load(c, WS_ST + panel * 8192, hw * 64 + l, p);
      BARRIER_L(); BARRIER_L();
      ln_put(c, hw * 64 + l, ln_sum(p));
    } else if (hw == 2) {
      u32x4 q[8];
#pragma unroll
      for (int i = 0; i < 8; ++i) q[i] = ld16(c.r, WS_CSQ + cb * 512 + i * 7680 + (l & 31) * 16);
      BARRIER_L(); BARRIER_L();
      ln_cols_sum<8>(c, 32, q);
    } else { BARRIER_L(); BARRIER_L(); }
    BARRIER_L();
  } else if (on) {
    if (hw == 2) ln_cols<8>(c, 32, WS_CSQ + cb * 512, 7680);
    BARRIER_L(); BARRIER_L(); BARRIER_L(); BARRIER_L(); BARRIER_L(); BARRIER_L();
    if (hw < 2) {
      f32x2 p[8];
      ln_load(c, WS_ST + 2 * STB + panel * 8192, hw * 64 + l, p);
      BARRIER_L(); BARRIER_L();
      ln_put(c, hw * 64 + l, ln_sum(p));
    } else { BARRIER_L(); BARRIER_L(); }
    BARRIER_L(); BARRIER_L(); BARRIER_L();
  }
}
DI void qkv_compute(Ctx c, u64& tgt, int w, int layer, const uint16_t* x, const uint16_t* wqkv) {
  const int panel = qkv_panel(c), cb = qkv_cb(c);
  if (w >= 4) { qkv_helper(c, tgt, w - 4, layer, x, wqkv); return; }
  if (cb == 15) { if (layer == 0) { tgt = epoch(c.ws); wait_vm<0>(); BARRIER(); } return; }
  const int lane = c.lane, i = lane & 15, g = lane >> 4, wm = w & 1, wn = w >> 1;
  const unsigned L = WS_L + layer * L_SZ;
  f32x4 acc[4][4];
#pragma unroll
  for (int e = 0; e < 16; ++e) acc[e >> 2][e & 3] = f32x4{0.f, 0.f, 0.f, 0.f};
  const LAS float* srs = (const LAS float*)(c.lds + M_SST) + wm * 64;
  const LAS float* scs = (const LAS float*)(c.lds + M_SCS) + wn * 64;
  if (cb < 10) {
    float rs[4], nm[4]; f32x4 cs[4]; unsigned ad[4][2];
    const float qs = cb < 5 ? 0.22360679774997896f * 1.4426950408889634f : 1.f;
    const unsigned i1 = L + (cb < 5 ? L_Q1 : L_K1), i2 = L + (cb < 5 ? L_Q2 : L_K2);
    qkv_loop<true>(c, w, layer, acc, [&]() {
#pragma unroll
      for (int e = 0; e < 4; ++e) { rs[e] = srs[e * 16 + i]; nm[e] = srs[128 + e * 16 + i]; cs[e] = *(const LAS f32x4*)(scs + e * 16 + g * 4); }
    }, [&](int q) {
      const int t = q >> 2, o = (cb % 5) * 16 + wn * 8 + t * 2 + (g >> 1), hp = o / 5, pos = o - hp * 5;
      const unsigned ts = pos == 2 ? 512 : 1024;
      ad[t][0] = (pos == 2 ? i2 + hp * 32768 : i1 + (2 * hp + (pos > 2)) * 65536 + (pos > 2 ? pos - 3 : pos) * 512) +
                 (panel * 4 + wm * 2) * ts + ((g & 1) * 16 + i) * 16;
      ad[t][1] = ad[t][0] + ts;
      asm volatile("" : "+v"(ad[t][0]), "+v"(ad[t][1]));
    });
#pragma unroll
    for (int e = 0; e < 4; ++e) rs[e] *= qs;
#pragma unroll
    for (int t = 0; t < 4; ++t) {
#pragma unroll
      for (int mp = 0; mp < 2; ++mp) {
        const f32x4 u = (acc[mp * 2][t] + cs[t] * nm[mp * 2]) * rs[mp * 2], v = (acc[mp * 2 + 1][t] + cs[t] * nm[mp * 2 + 1]) * rs[mp * 2 + 1];
        st16(c.r, ad[t][mp], pair16(pk2(u[0], u[1]), pk2(u[2], u[3]), pk2(v[0], v[1]), pk2(v[2], v[3])));
      }
    }
  } else {
    float cs[4]; f32x4 rs[4], nm[4]; unsigned ad[4];
    qkv_loop<false>(c, w, layer, acc, [&]() {
#pragma unroll
      for (int e = 0; e < 4; ++e) { cs[e] = scs[e * 16 + i]; rs[e] = *(const LAS f32x4*)(srs + e * 16 + g * 4); nm[e] = *(const LAS f32x4*)(srs + 128 + e * 16 + g * 4); }
    }, [&](int q) {
      const int t = q >> 2, cv = (cb - 10) * 128 + wn * 64 + t * 16 + i, head = cv / 20, dim = cv - head * 20;
      const int d = (head & 1) ? (dim < 4 ? dim + 16 : dim - 4) : dim;
      ad[t] = L + L_V + head * 81920 + (panel * 8 + wm * 4 + (g & 1)) * 640 + (g >> 1) * 320 + d * 16;
      PIN(ad[t]);
    });
#pragma unroll
    for (int t = 0; t < 4; ++t) {
      const unsigned a0 = ad[t];
#pragma unroll
      for (int mp = 0; mp < 2; ++mp) {
        const f32x4 u = (acc[mp * 2][t] + nm[mp * 2] * cs[t]) * rs[mp * 2], v = (acc[mp * 2 + 1][t] + nm[mp * 2 + 1] * cs[t]) * rs[mp * 2 + 1];
        st16(c.r, a0 + mp * 1280, pair16(pk2(u[0], u[1]), pk2(u[2], u[3]), pk2(v[0], v[1]), pk2(v[2], v[3])));
      }
    }
  }
  if (layer == 0) {
    const u32x2 t = *(const LAS u32x2*)(c.lds + M_TGT);
    c.tgt = tgt = (u64)(unsigned)__builtin_amdgcn_readfirstlane(t[1]) << 32 | (unsigned)__builtin_amdgcn_readfirstlane(t[0]);
  }
  SLOT(layer * 5);
  STAMP(4);
  publish(c, M_CNT4, 4, flg(FL_QKV + layer * 5, panel * 16 + cb), true);
  STAMP(5);
}
typedef Ring<8, QKV_NB, 10, 2> QkvB;
DI void qkv_load0(const Ctx& c, int lw, const uint16_t* x, const uint16_t* wqkv) {
  const int half = lw & 1;
  if (lw >= 2) {
    Ring<8, QKV_NB, 10, 2> rg;
#pragma unroll
    for (int p = 0; p < 8; ++p) rg.voff[p] = qkv_w_off(c, half * 8 + p);
    rg.init((const char*)wqkv, 64 * 3840, 0, half);
    rg.template fill<2>(); wait_vm<8>(); BARRIER_L(); rg.issue(); wait_vm<8>(); BARRIER_L(); rg.template fill<2>();
    rg.steady(); rg.template range<2, 0>(); BARRIER_L();
  } else {
    Ring<8, QKV_NA, 10, 2> rg;
#pragma unroll
    for (int p = 0; p < 8; ++p) {
      const int mt = qkv_panel(c) * 8 + half * 4 + (p >> 1), r16 = (p & 1) * 8 + (c.lane >> 3);
      rg.voff[p] = (mt * 16 + r16) * 1280 + (((c.lane & 7) ^ ((r16 >> 1) & 7)) << 4);
    }
    rg.init((const char*)x, 128, QKV_A, half);
    rg.template fill<2>(); wait_vm<8>(); BARRIER_L(); rg.issue(); wait_vm<8>(); BARRIER_L(); rg.issue();
    rg.steady(); rg.template range<1, 0>(); BARRIER_L();
  }
}
DI void qkv_load1(const Ctx& c, int lw, const uint16_t* wqkv) {
  const int half = lw & 1;
  static_assert(QKV_NA == 4 && QKV_NB == 5, "schedule");
  if (lw >= 2) {
    QkvB rg;
#pragma unroll
    for (int p = 0; p < 8; ++p) rg.voff[p] = qkv_w_off(c, half * 8 + p);
    rg.init((const char*)wqkv, 64 * 3840, 0, half);
    sig_wait(c, 12, M_CNT4);
    rg.template fill_ahead<1>(c, 11);
    rg.rest();
  } else {
    Ring<8, QKV_NA, 10, 2> rg;
#pragma unroll
    for (int p = 0; p < 8; ++p) rg.voff[p] = WS_X2 + (qkv_panel(c) * 8 + half * 4 + (p >> 1)) * 20480 + (p & 1) * 1024 + c.lane * 16;
    rg.init(c.ws, 2048, QKV_A, half);
    rg.template start<2>(c, half, 4 * 20480, flg(FL_DN, qkv_panel(c) * 16 + (c.lane & 15)), 11, 28);
    rg.template run<2>();
  }
}

DI f32x16 qk8(s16x4 k2, s16x4 q2) {
 return __builtin_amdgcn_mfma_f32_32x32x8bf16_1k(k2, q2, f32x16{}, 0, 0, 0); }
DI f32x16 qk16(bf16x8 k1, bf16x8 q1, f32x16 s) { return __builtin_amdgcn_mfma_f32_32x32x16_bf16(k1, q1, s, 0, 0, 0); }
DI f32x16 pv_lo(bf16x8 v, f32x16 p, f32x16 o) {
  return __builtin_amdgcn_smfmac_f32_32x32x32_bf16(v, __builtin_bit_cast(bf16x16, (f32x8)__builtin_shufflevector(p, p, 0, 1, 2, 3, 4, 5, 6, 7)), o, 0xDDDD, 0, 0);
}
DI f32x16 pv_hi(bf16x8 v, f32x16 p, f32x16 o) {
  return __builtin_amdgcn_smfmac_f32_32x32x32_bf16(v, __builtin_bit_cast(bf16x16, (f32x8)__builtin_shufflevector(p, p, 8, 9, 10, 11, 12, 13, 14, 15)), o, 0xDDDD, 0, 0);
}
DI float swap32(float v, int h) {
  auto r = __builtin_amdgcn_permlane32_swap(__float_as_uint(v), __float_as_uint(v), false, false);
  return __uint_as_float(h ? r[0] : r[1]);
}
#define EXP4(X, i) do { X[i] = __builtin_amdgcn_exp2f(X[i]); X[i + 1] = __builtin_amdgcn_exp2f(X[i + 1]); X[i + 2] = __builtin_amdgcn_exp2f(X[i + 2]); \
                        X[i + 3] = __builtin_amdgcn_exp2f(X[i + 3]); PIN(X); SBAR(); } while (0)
DI void att_compute(const Ctx& c, int w, int layer) {
  const int lane = c.lane, head = att_head(c), qb = att_qb(c);
  const int qp = w & 3, kp = w >> 2, r32 = lane & 31, h = lane >> 5;
  lds_ptr lds = c.lds;
  const unsigned L = WS_L + layer * L_SZ;
  SLOT(layer * 5 + 1);
  STAMP(0);
  for (int s = w * 64 + lane; s < 672; s += 512) {
    const unsigned v = s % 20 == 0 ? 0x3f803f80u : 0u;
    *(LAS u32x4*)(lds + AT_CONST + s * 16) = u32x4{v, v, v, v};
  }
  lds_ptr pk1 = lds + kp * 8192 + lane * 16;
  lds_ptr pk2_ = h ? lds + AT_CONST + 16 : lds + 16384 + kp * 4096 + r32 * 16 + (head & 1) * 8;
  lds_ptr pv = (r32 >= 20 ? lds + AT_CONST : lds + 24576 + kp * 10240) + h * 320 + r32 * 16;
  const int vmask = r32 >= 20 ? 0 : -1, kmask = h ? 0 : -1;
  const int qt = qb * 8 + qp * 2 + kp, tok = qt * 32 + r32;
  unsigned ob16 = L + L_O + (tok >> 4) * 20480 + (head * 2 + h) * 256 + (tok & 15) * 16, ob8 = L + L_O + (tok >> 4) * 20480 + 16384 + head * 128 + (tok & 15) * 8;
  asm volatile("" : "+v"(ob16), "+v"(ob8));
  f32x16 o0 = f32x16{}, o1 = f32x16{};
  f32x16 s0, s1, s2, s3 = f32x16{};
  STAMP(1);
  BARRIER();
  STAMP(2); STAMPC(0); WRESET();
  const bf16x8 q1a = ldsr16(lds + 2 * AT_SLOT + (qp * 2 + kp) * 1024 + lane * 16), q1b = ldsr16(lds + 2 * AT_SLOT + (qp * 2 + 1 - kp) * 1024 + lane * 16);
  lds_ptr pq2 = h ? lds + AT_CONST + 16 : lds + 2 * AT_SLOT + 8192 + qp * 1024 + r32 * 16 + (head & 1) * 8;
  const s16x4 q2a = *(const LAS s16x4*)(pq2 + kp * 512), q2b = *(const LAS s16x4*)(pq2 + 512 - kp * 512);
  bf16x8 k1 = ldsr16(pk1), vlo = ldsr16(pv), vhi = bf16x8{};
  s16x4 k2 = *(const LAS s16x4*)pk2_;
  s0 = qk16(k1, q1a, qk8(k2, q2a));
#define PH_EVEN(SC, SN, SP, KLOAD, VOFF) do { \
    SN = qk8(k2, q2b); SBAR(); EXP4(SC, 0); SN = qk16(k1, q1b, SN); SBAR(); KLOAD; SBAR(); EXP4(SC, 4); \
    o1 = pv_hi(vhi, SP, o1); SBAR(); vhi = ldsr16(pv + (VOFF) + 640); SBAR(); EXP4(SC, 8); o0 = pv_lo(vlo, SC, o0); SBAR(); EXP4(SC, 12); } while (0)
#define PH_ODD(SC, SN, SP, VLOAD) do { \
    SN = qk8(k2, q2a); SBAR(); EXP4(SC, 0); SN = qk16(k1, q1a, SN); SBAR(); EXP4(SC, 4); \
    o0 = pv_hi(vhi, SP, o0); SBAR(); EXP4(SC, 8); o1 = pv_lo(vlo, SC, o1); SBAR(); VLOAD; SBAR(); EXP4(SC, 12); } while (0)
#define KLD(t) do { k1 = ldsr16(pk1 + (t) * 1024); k2 = *(const LAS s16x4*)(pk2_ + (t) * 512); } while (0)
#define PRI(v) do { __builtin_amdgcn_s_setprio(v); SBAR(); } while (0)
#define HI(n) if (ATT_PRIO == (n)) PRI(1);
#define LO(n) if (ATT_PRIO == (n)) PRI(0);
#define TWO(t) HI(3) HI(4) HI(5) PH_EVEN(s0, s1, s3, KLD((t) + 1), (t) * 1280); PH_ODD(s1, s2, s0, vlo = ldsr16(pv + ((t) + 1) * 1280)); LO(4)
  for (int grp = 0; grp < 4; ++grp) {
    TWO(0) PH_EVEN(s2, s3, s1, if (grp == 0) BARRIER_NW(); KLD(2), 1280); LO(3) PH_ODD(s3, s0, s2, vlo = ldsr16(pv + 2 * 1280));
    TWO(2) PH_EVEN(s2, s3, s1, KLD(4), 3 * 1280); LO(3) LO(5) PH_ODD(s3, s0, s2, vlo = ldsr16(pv + 4 * 1280));
    TWO(4) PH_EVEN(s2, s3, s1, KLD(6), 5 * 1280); LO(3) PH_ODD(s3, s0, s2, vlo = ldsr16(pv + 6 * 1280));
    TWO(6)
    const int d = grp == 2 ? -2 * AT_SLOT : grp == 3 ? 0 : AT_SLOT;
    PH_EVEN(s2, s3, s1, pk1 += d; pk2_ += (d & kmask); if (grp != 3) BARRIER(); KLD(0), 7 * 1280);
    LO(3) LO(5) PH_ODD(s3, s0, s2, pv += (d & vmask); vlo = ldsr16(pv));
  }
  o1 = pv_hi(vhi, s3, o1);
  STAMP(3); STAMPC(1);
  __builtin_amdgcn_s_setprio(0);
  const f32x16 give = o1, keep = o0;
#pragma unroll
  for (int e = 0; e < 3; ++e)
    *(LAS f32x4*)(lds + AT_MERGE + (w * 3 + e) * 1024 + lane * 16) = f32x4{give[4 * e], give[4 * e + 1], give[4 * e + 2], give[4 * e + 3]};
  BARRIER();
  f32x4 a[3];
#pragma unroll
  for (int e = 0; e < 3; ++e)
    a[e] = f32x4{keep[4 * e], keep[4 * e + 1], keep[4 * e + 2], keep[4 * e + 3]} + *(const LAS f32x4*)(lds + AT_MERGE + ((w ^ 4) * 3 + e) * 1024 + lane * 16);
  float l = h ? a[2][0] : 0.f;
  l += swap32(l, h);
  if (__builtin_expect(__any(!(l >= 5.421e-20f && l <= 1.8447e19f)), 0)) {
    const unsigned o2 = (head >> 1) * 32768 + r32 * 16 + (head & 1) * 8;
    const bf16x8 fq1 = __builtin_bit_cast(bf16x8, ld16(c.r, L + L_Q1 + head * 65536 + qt * 1024 + lane * 16));
    u32x2 fq2 = ld8(c.r, L + L_Q2 + o2 + qt * 512);
    if (h) fq2 = u32x2{0u, 0u};
    const unsigned vi = L + L_V + head * 81920 + h * 320 + (r32 < 20 ? r32 : 0) * 16;
    f32x16 oo = f32x16{};
    float m = -INFINITY;
    const u32x4 one8 = {0x3f803f80u, 0x3f803f80u, 0x3f803f80u, 0x3f803f80u};
    for (int t = 0; t < 64; ++t) {
      const u32x4 fk1 = ld16(c.r, L + L_K1 + head * 65536 + t * 1024 + lane * 16);
      const u32x2 fk2 = ld8(c.r, L + L_K2 + o2 + t * 512);
      u32x4 v0 = ld16(c.r, vi + t * 1280), v1 = ld16(c.r, vi + t * 1280 + 640);
      if (r32 >= 20) { v0 = one8; v1 = one8; }
      f32x16 s = qk16(__builtin_bit_cast(bf16x8, fk1), fq1, qk8(__builtin_bit_cast(s16x4, fk2), __builtin_bit_cast(s16x4, fq2)));
      float tm = s[0];
#pragma unroll
      for (int r = 1; r < 16; ++r) tm = fmaxf(tm, s[r]);
      tm = fmaxf(tm, swap32(tm, h));
      const float mn = fmaxf(m, tm), al = __builtin_amdgcn_exp2f(m - mn);
      m = mn;
#pragma unroll
      for (int r = 0; r < 16; ++r) { s[r] = __builtin_amdgcn_exp2f(s[r] - mn); oo[r] *= al; }
      oo = pv_hi(__builtin_bit_cast(bf16x8, v1), s, pv_lo(__builtin_bit_cast(bf16x8, v0), s, oo));
    }
#pragma unroll
    for (int e = 0; e < 3; ++e) a[e] = f32x4{oo[4 * e], oo[4 * e + 1], oo[4 * e + 2], oo[4 * e + 3]};
    l = h ? a[2][0] : 0.f;
    l += swap32(l, h);
  }
  const float inv = __builtin_amdgcn_rcpf(l);
  const unsigned A0 = pk2(a[0][0] * inv, a[0][1] * inv), A1 = pk2(a[0][2] * inv, a[0][3] * inv);
  const unsigned B0 = pk2(a[1][0] * inv, a[1][1] * inv), B1 = pk2(a[1][2] * inv, a[1][3] * inv);
  const u32x2 C = {pk2(a[2][0] * inv, a[2][1] * inv), pk2(a[2][2] * inv, a[2][3] * inv)};
  st16(c.r, ob16, pair32(A0, A1, B0, B1));
  if (!h) st8(c.r, ob8, C);
  STAMP(4);
  publish(c, M_CNT, 8, flg(FL_ATT + layer * 5, qb * 32 + head), true);
  STAMP(5);
}
DI void retile_load(const Ctx& c, int J, u32x4 (&v)[5][4], const uint16_t* wo, const uint16_t* wg, const uint16_t* wu, const uint16_t* wd) {
  const int e = J < 216 ? J : J < 256 ? J - 216 : J - 256;
  const int strip = J < 216 ? (e >> 2) % 27 : e % 10, k0 = J < 216 ? (e & 3) * 5 : min((e / 10) * 5, 49), ld = J < 216 ? 3456 : 1280;
  const rsrc_t r = __builtin_amdgcn_make_buffer_rsrc((void*)(J < 108 ? wg : J < 216 ? wu : J < 256 ? wo : wd), 0,
                                                     J < 216 ? 640 * 3456 : J < 256 ? 640 * 1280 : 1728 * 1280, 0x00020000);
  const unsigned vo = (c.lane >> 3) * ld + (c.lane & 7) * 16;
#pragma unroll
  for (int u = 0; u < 5; ++u)
#pragma unroll
    for (int q = 0; q < 4; ++q) {
      const int ch = (k0 + u) * 4 + q;
      const int row = J < 216 || J >= 256 ? ch * 8 : ch < 64 ? (ch >> 1) * 20 + (ch & 2) * 2 + (ch & 1) * 8 : (ch - 64) * 40 + 16;
      v[u][q] = __builtin_amdgcn_raw_buffer_load_b128(r, vo, row * ld + strip * 128, 0);
    }
}
DI void retile_store(const Ctx& c, int J, lds_ptr scr, const u32x4 (&v)[5][4]) {
  const int e = J < 216 ? J : J < 256 ? J - 216 : J - 256, mat = J >= 108;
  const int strip = J < 216 ? (e >> 2) % 27 : e % 10, k0 = J < 216 ? (e & 3) * 5 : min((e / 10) * 5, 49);
  const unsigned ts = J < 216 ? 40960 : J < 256 ? 20480 : 55296;
  const unsigned dst = (J < 216 ? WS_WGU + mat * 20480 : J < 256 ? WS_WO : WS_WD) + strip * 4 * ts + k0 * 1024;
  const int i = c.lane & 15, g = c.lane >> 4;
  const bf16x8 ones = __builtin_bit_cast(bf16x8, u32x4{0x3f803f80u, 0x3f803f80u, 0x3f803f80u, 0x3f803f80u});
  f32x4 cs[4] = {f32x4{}, f32x4{}, f32x4{}, f32x4{}};
#pragma unroll
  for (int u = 0; u < 5; ++u) {
#pragma unroll
    for (int p = 0; p < 4; ++p) *(LAS u32x4*)(scr + p * 1024 + c.lane * 16) = v[u][p];
#pragma unroll
    for (int t = 0; t < 4; ++t) {
      const bf16x8 f = ldstr(scr + (g * 8 + (i >> 2)) * 128 + t * 32 + (i & 3) * 8, 512);
      __builtin_amdgcn_raw_buffer_store_b128(__builtin_bit_cast(u32x4, f), c.r, c.lane * 16, dst + u * 1024 + t * ts, 16);
      cs[t] = mfma16(f, ones, cs[t]);
    }
  }
  if (J < 216 && i == 0) {
#pragma unroll
    for (int t = 0; t < 4; ++t)
      __builtin_amdgcn_raw_buffer_store_b128(__builtin_bit_cast(u32x4, cs[t]), c.r, g * 16, WS_CSG + ((e & 3) * 3456 + (strip * 8 + t * 2 + mat) * 16) * 4, 16);
  }
}
DI int early_piece(int e) { return e < 4 ? (e & 1) + (e >> 1) * 8 : e < 6 ? 16 + (e - 4) * 4 : e < 9 ? 18 + e : 25 + e; }
DI int late_piece(int e) { return e < 6 ? 2 + e : e < 12 ? 4 + e : e < 15 ? 5 + e : e < 18 ? 6 + e : e < 25 ? 9 + e : 12 + e; }
DI void att_load(const Ctx& c, int lw, int layer, unsigned iss, const uint16_t* wo, const uint16_t* wg, const uint16_t* wu, const uint16_t* wd) {
  const int head = att_head(c), qb = att_qb(c), lane = c.lane;
  const char* Lp = c.ws + WS_L + layer * L_SZ;
  const unsigned vo = lane * 16;
  int grp = ATT_ROT ? qb >> 1 : 0;
  auto src_of = [&](int p) {
    return p < 16 ? L_K1 + head * 65536 + grp * 16384 + p * 1024
                  : p < 24 ? L_K2 + (head >> 1) * 32768 + grp * 8192 + (p - 16) * 1024 : L_V + head * 81920 + grp * 20480 + (p - 24) * 1024;
  };
  unsigned v0[14], m0[11], vg[11], sg[11];
#pragma unroll
  for (int q = 0; q < 11; ++q) {
    const int p = q < 3 ? early_piece(lw * 3 + q) : late_piece(lw * 8 + q - 3), pg = lw * 11 + q;
    v0[q] = src_of(p) + vo; m0[q] = p * 1024;
    vg[q] = src_of(pg) + vo; sg[q] = pg < 16 ? 16384 : pg < 24 ? 8192 : 20480;
    asm volatile("" : "+v"(v0[q]), "+s"(m0[q]));
  }
#pragma unroll
  for (int q = 0; q < 3; ++q) {
    const int p = lw * 3 + q;
    v0[11 + q] = (p < 8 ? L_Q1 + head * 65536 + qb * 8192 + p * 1024 : L_Q2 + (head >> 1) * 32768 + qb * 4096 + (p - 8) * 1024) + vo - q * 1024;
    PIN(v0[11 + q]);
  }
  auto issue = [&](int slot) {
#pragma unroll
    for (int q = 0; q < 11; ++q) { vg[q] += grp == 3 ? -3 * sg[q] : sg[q]; glds1(slot * AT_SLOT + (lw * 11 + q) * 1024, Lp, vg[q]); }
    grp = (grp + 1) & 3;
  };
  const int o0 = (head >> 1) * 5 + (head & 1) * 2, c0 = head * 20;
  auto kv_cb = [&](int sel) { return sel == 0 ? 5 + (o0 >> 4) : sel == 1 ? 5 + ((o0 + 2) >> 4) : sel == 2 ? 10 + (c0 >> 7) : 10 + ((c0 + 19) >> 7); };
  if (lw == 0) {
    sig_wait(c, iss, M_ISS);
    poll(c, flg(FL_QKV + layer * 5, lane & 16 ? (qb * 2 + (lane & 1)) * 16 + ((o0 + (lane & 2)) >> 4) : (grp * 4 + (lane & 3)) * 16 + kv_cb((lane >> 2) & 3)));
    sig_set(c, layer * 10 + 3);
    STAMPL(layer * 5 + 1, 6);
  } else sig_wait(c, layer * 10 + 3);
  glds3(2 * AT_SLOT + lw * 3072, Lp, v0[11], v0[12], v0[13]);
#pragma unroll
  for (int q = 0; q < 3; ++q) glds1(m0[q], Lp, v0[q]);
#pragma unroll
  for (int q = 3; q < 3 + ATT_LATE; ++q) glds1(m0[q], Lp, v0[q]);
  wait_vm<ATT_LATE>(); BARRIER();
#pragma unroll
  for (int q = 3 + ATT_LATE; q < 11; ++q) glds1(m0[q], Lp, v0[q]);
  wait_vm<0>(); BARRIER();
  if (lw == 0) { poll(c, flg(FL_QKV + layer * 5, (lane & 15) * 16 + kv_cb(lane >> 4))); sig_set(c, layer * 10 + 5); } else sig_wait(c, layer * 10 + 5, M_SIG, true);
  issue(1);
  const int J = (lw - 1) * 128 + c.j * 4 + (c.xcd >> 1);
  const bool rt = layer == 0 && lw > 0 && J < 366 && !(c.xcd & 1);
  u32x4 v[5][4];
  if (rt) { retile_load(c, J, v, wo, wg, wu, wd); wait_vm<20>(); } else wait_vm<0>();
  BARRIER();
  issue(2);
  if (rt) retile_store(c, J, c.lds + AT_SCR + (lw - 1) * 4096, v);
  if (layer == 0 && lw > 0) publish(c, M_CNT3, 3, flg(FL_RT, blockIdx.x));
  wait_vm<0>(); BARRIER();
  issue(0);
  wait_vm<0>();
  if (layer == 0) {
    if (lw == 0) {
      if (c.j == 0) {
#pragma unroll 1
        for (int q = 0; q < 4; ++q) poll(c, flg(FL_RT, q * 64 + lane));
        if (lane < 32) __hip_atomic_store((GAS u64*)(c.ws + flg(FL_RT2, lane * 8 + c.xcd)), c.tgt, __ATOMIC_RELAXED, __HIP_MEMORY_SCOPE_AGENT);
      }
      poll(c, flg(FL_RT2, blockIdx.x));
      if (blockIdx.x == 0 && lane == 0) *(u64*)(c.ws + WS_EPOCH) = c.tgt;
    }
  }
  BARRIER();
  u32x4 pk[25];
#pragma unroll
  for (int e = 0; e < 25; ++e) pk[e] = ld16s(c.r, vo, WS_WO + (pr_cb(c) * 5 + e % 5) * 20480 + (lw * 5 + e / 5) * 1024);
#pragma unroll 1
  for (unsigned q = lw; q < 35; q += 4) glds1(AT_SCR + lw * 1024, c.ws + WS_WGU + ((gu_cb(c) * 27) >> 2) * 40960 + ((c.j & 7) * 35 + q) * 1024, vo);
  wait_vm<0>();
  BARRIER();
#pragma unroll
  for (int e = 0; e < 25; ++e) PIN(pk[e]);
  auto dump = [&]() {
#pragma unroll
    for (int e = 0; e < 25; ++e) { const int k = lw * 5 + e / 5; ldsw16(c.lds + (k >> 1) * 10240 + (k & 1) * 1024 + (e % 5) * 2048 + vo, pk[e]); }
  };
  sig_wait(c, layer * 16 + 8, M_CNT);
  dump();
  if (lw == 0) { poll(c, flg(FL_ATT + layer * 5, (pr_panel(c) >> 2) * 32 + (lane & 31))); STAMPL(layer * 5 + 2, 6); }
}

template <int NS, int NB, int WIN>
DI void proj_compute(const Ctx& c, int w, int slot_id, unsigned a_off, const char* res_rm, unsigned res_off, unsigned out_off, unsigned st_off,
                     uint16_t* out, int arr) {
  constexpr bool RES = NS <= NB;
  const int panel = pr_panel(c), cb = pr_cb(c);
  SLOT(slot_id);
  STAMP(0);
  if (w >= 4) {
    if constexpr (RES) { BARRIER(); BARRIER(); if (PROJ2) BARRIER(); }
    else {
      static_assert(RES || (NS == 27 && NB == 15 && WIN == 18), "schedule");
      const int par = (w >> 1) & 1, half = w & 1;
      u32x4 pk[15];
#pragma unroll
      for (int e = 0; e < 15; ++e) {
        const int q = half * 5 + e % 5;
        pk[e] = ld16s(c.r, c.lane * 16, WS_WD + (cb * 5 + (q >> 1)) * 55296 + ((15 + par + (e / 5) * 2) * 2 + (q & 1)) * 1024);
      }
      lds_ptr pw = c.lds + par * 10240 + half * 5120 + c.lane * 16;
      BARRIER(); BARRIER();
#pragma unroll
      for (int e = 0; e < 10; ++e) ldsw16(pw + (e / 5) * 20480 + (e % 5) * 1024, pk[e]);
      BARRIER();
#pragma unroll
      for (int e = 10; e < 15; ++e) ldsw16(pw + 40960 + (e % 5) * 1024, pk[e]);
      BARRIER(); BARRIER(); BARRIER(); BARRIER();
      if (PROJ2) BARRIER();
    }
    return;
  }
  const int lane = c.lane, i = lane & 15, g = lane >> 4, mt = panel * 4 + w;
  u32x2 res[5];
#pragma unroll
  for (int t = 0; t < 5; ++t)
    res[t] = res_rm ? *(const u32x2*)(res_rm + (mt * 16 + i) * 1280 + cb * 160 + t * 32 + g * 8)
                    : ld8(c.r, res_off + (mt * 80 + cb * 10 + t * 2 + (g >> 1)) * 256 + i * 16 + (g & 1) * 8);
  f32x4 acc[5];
#pragma unroll
  for (int t = 0; t < 5; ++t) { acc[t] = f32x4{bflo(res[t][0]), bfhi(res[t][0]), bflo(res[t][1]), bfhi(res[t][1])}; PIN(acc[t]); }
  float piv;
  { const f32x4 s4 = (acc[0] + acc[1]) + (acc[2] + acc[3]) + acc[4]; piv = rows4((s4[0] + s4[1]) + (s4[2] + s4[3])) * (1.f / 80.f); }
  const int ch = (g & 1) * 2 + (g >> 1);
  unsigned xo = out ? (mt * 16 + i) * 1280 + cb * 160 + ch * 16 : out_off + (mt * 80 + cb * 10 + ch) * 256 + i * 16;
  unsigned so = st_off + (mt >> 3) * 8192 + cb * 1024 + ((mt & 7) * 16 + i) * 8;
  asm volatile("" : "+v"(piv), "+v"(xo), "+v"(so));
  lds_ptr pb = c.lds + lane * 16;
  unsigned av = a_off + mt * (NS * 2048) + lane * 16;
  u32x4 a[WIN];
  bf16x8 f[3][5];
  int sb = 0;
  STAMP(1);
  BARRIER();
  STAMP(2); STAMPC(0); WRESET();
  constexpr int AH = RES ? WO_AH : DN_AH;
  auto lda = [&](int k) {
    if (RES && k >= 16) {
      const unsigned o = a_off + mt * 20480 + 16384 + (k - 16) * 1024 + g * 256 + i * 8;
      const u32x2 lo = ld8(c.r, o), hi = ld8(c.r, o + 128);
      a[k % WIN] = u32x4{lo[0], lo[1], hi[0], hi[1]};
    } else a[k % WIN] = ld16(c.r, av + k * 1024);
  };
#pragma unroll
  for (int k = 0; k < AH; ++k) lda(k);
#pragma unroll
  for (int t = 0; t < 5; ++t) { f[0][t] = ldsr16(pb + t * 2048); f[1][t] = ldsr16(pb + t * 2048 + 1024); }
  SBAR();
  auto window = [&](bool first, bool last) {
#pragma unroll
    for (int k = 0; k < WIN; ++k) {
      const bool ld = !(last && k + 2 >= WIN);
      if ((k & 1) == 0) {
        if (RES) pb += 10240;
        else { pb += sb == NB - 1 ? -(NB - 1) * 10240 : 10240; sb = sb == NB - 1 ? 0 : sb + 1; }
        if (!ld) BARRIER(); else if (!RES && (k == 10 || (k == 0 && !first))) BARRIER_NW();
      }
#pragma unroll
      for (int t = 0; t < 5; ++t) {
        if (ld) f[(k + 2) % 3][t] = ldsr16(pb + t * 2048 + (k & 1) * 1024);
        acc[t] = mfma16(f[k % 3][t], __builtin_bit_cast(bf16x8, a[k]), acc[t]);
        SBAR();
      }
      if (!last || k + AH < WIN) { lda(k + AH); SBAR(); }
    }
    av += WIN * 1024;
  };
  static_assert(2 * NS % WIN == 0 && (2 * NS == WIN || WIN % 6 == 0), "window");
#pragma unroll 1
  for (int b = 1; b < 2 * NS / WIN; ++b) window(b == 1, false);
  window(false, true);
  STAMP(3); STAMPC(1);
  u32x2 o[5];
#pragma unroll
  for (int t = 0; t < 5; ++t) {
    o[t][0] = pk2(acc[t][0], acc[t][1]); o[t][1] = pk2(acc[t][2], acc[t][3]);
  }
  const u32x4 p0 = pair16(o[0][0], o[0][1], o[1][0], o[1][1]), p1 = pair16(o[2][0], o[2][1], o[3][0], o[3][1]),
              p2 = pair16(o[4][0], o[4][1], o[4][0], o[4][1]);
  if (out) {
    char* po = (char*)out + xo;
    *(u32x4*)po = p0; *(u32x4*)(po + 64) = p1;
    if (!(g & 1)) *(u32x4*)(po + 128) = p2;
    STAMP(4);
    return;
  }
  st16(c.r, xo, p0); st16(c.r, xo + 1024, p1);
  if (!(g & 1)) st16(c.r, xo + 2048, p2);
  f32x4 s4 = acc[0] - piv, q4 = s4 * s4;
#pragma unroll
  for (int t = 1; t < 5; ++t) { const f32x4 d = acc[t] - piv; s4 += d; q4 += d * d; }
  const float sd = rows4((s4[0] + s4[1]) + (s4[2] + s4[3])), sq = rows4((q4[0] + q4[1]) + (q4[2] + q4[3]));
  if (g == 0) st8(c.r, so, u32x2{__float_as_uint(sd + 80.f * piv), __float_as_uint(fmaxf(sq - sd * sd * (1.f / 80.f), 0.f))});
  STAMP(4);
  publish(c, M_CNT4, 4, flg(arr, panel * 8 + cb), true);
  STAMP(5);
}
template <int NF>
DI void gu_consumer16(const Ctx& c, int layer, int f0, int mq) {
  const int panel = gu_panel(c), ft0 = (gu_cb(c) * 27) >> 2;
  const int lane = c.lane, i = lane & 15, g = lane >> 4;
  f32x4 acc[2][2 * NF];
#pragma unroll
  for (int m = 0; m < 2; ++m)
#pragma unroll
    for (int t = 0; t < 2 * NF; ++t) acc[m][t] = f32x4{0.f, 0.f, 0.f, 0.f};
  lds_ptr pa = c.lds + GU_A + mq * 2048 + lane * 16, pb = c.lds + f0 * 2048 + lane * 16;
  bf16x8 x0[2], x1[2], wa[NF], wb0[NF], wb1[NF];
  auto step = [&](const bf16x8* xa, const bf16x8* wb, bf16x8* xn, bf16x8* wn_, bool ld) {
#pragma unroll
    for (int q = 0; q < 4 * NF; ++q) {
      const int t = q >> 1, m = q & 1;
      if (ld) {
        if (q < 2) xn[q] = ldsr16(pa + q * 1024);
        else if (q < 2 * NF) { const int e = NF == 4 ? (q == 3 ? 0 : q == 4 ? 1 : q == 6 ? 2 : q == 7 ? 3 : -1) : NF == 3 ? q - 3 : q - 2; if (e >= 0) wn_[e] = ldsr16(pb + (NF + e) * 1024); }
        else if (q & 1) wa[(q - 2 * NF) >> 1] = ldsr16(pb + ((q - 2 * NF) >> 1) * 1024);
      }
      acc[m][t] = mfma16(t < NF ? wa[t] : wb[t - NF], xa[m], acc[m][t]);
      SBAR();
    }
  };
  int sl = 0;
  auto adv = [&]() {
    pa += sl == GU_NA - 1 ? -(GU_NA - 1) * 8192 : 8192; pb += sl == GU_NB - 1 ? -(GU_NB - 1) * 14336 : 14336;
    sl = sl == GU_NA - 1 ? 0 : sl + 1;
  };
  unsigned ho = WS_L + layer * L_SZ + L_H + ((panel * 8 + mq * 2 + (g & 1)) * 216 + (ft0 + f0) * 2 + (g >> 1)) * 256 + i * 16;
  PIN(ho);
  SLOT(layer * 5 + 3);
  STAMP(0);
  STAMP(1);
  BARRIER();
  STAMP(2); STAMPC(0); WRESET();
  x0[0] = ldsr16(pa); x0[1] = ldsr16(pa + 1024);
#pragma unroll
  for (int t = 0; t < NF; ++t) { wa[t] = ldsr16(pb + t * 1024); wb0[t] = ldsr16(pb + (NF + t) * 1024); }
#pragma unroll 1
  for (int st = 0; st < 9; ++st) {
    adv(); BARRIER_NW(); step(x0, wb0, x1, wb1, true);
    adv(); BARRIER_NW(); step(x1, wb1, x0, wb0, true);
  }
  adv(); BARRIER_NW(); step(x0, wb0, x1, wb1, true);
  BARRIER();
  const LAS float* srs = (const LAS float*)(c.lds + M_SST) + mq * 32 + i;
  const LAS float* scs = (const LAS float*)(c.lds + M_SCS) + f0 * 32 + g * 4;
  float rs[2] = {srs[0], srs[16]}; const float nm[2] = {srs[128], srs[144]};
  f32x4 cs[2 * NF];
#pragma unroll
  for (int e = 0; e < 2 * NF; ++e) cs[e] = *(const LAS f32x4*)(scs + e * 16);
  SBAR();
  step(x1, wb1, x0, wb0, false);
  STAMP(3); STAMPC(1);
  rs[0] *= rs[0]; rs[1] *= rs[1];
#pragma unroll
  for (int f = 0; f < NF; ++f) {
    unsigned p[2][2];
#pragma unroll
    for (int m = 0; m < 2; ++m) {
      f32x4 gt = acc[m][2 * f] + cs[2 * f] * nm[m];
      const f32x4 up = (acc[m][2 * f + 1] + cs[2 * f + 1] * nm[m]) * rs[m];
      gt = __builtin_elementwise_max(gt, f32x4{0.f, 0.f, 0.f, 0.f}) * up;
      p[m][0] = pk2(gt[0], gt[1]); p[m][1] = pk2(gt[2], gt[3]);
    }
    st16(c.r, ho + f * 512, pair16(p[0][0], p[0][1], p[1][0], p[1][1]));
    SBAR();
  }
  STAMP(4);
  publish(c, M_CNT, 8, flg(FL_GU + layer * 5, panel * 16 + gu_cb(c)), true);
  STAMP(5);
}
template <int NF>
DI void gu_consumer32(const Ctx& c, int layer, int f0, int mq) {
  const int panel = gu_panel(c), ft0 = (gu_cb(c) * 27) >> 2;
  const int lane = c.lane, r32 = lane & 31, h = lane >> 5;
  f32x16 acc[NF];
#pragma unroll
  for (int t = 0; t < NF; ++t) acc[t] = f32x16{};
  const int lo = (r32 >> 4) * 1024 + h * 256 + (r32 & 15) * 16;
  lds_ptr pa = c.lds + GU_A + mq * 2048 + lo, pb = c.lds + f0 * 2048 + lo;
  bf16x8 x0[2], x1[2], wa[NF], wb0[NF], wb1[NF];
  auto step = [&](const bf16x8* xa, const bf16x8* wb, bf16x8* xn, bf16x8* wn_, bool ld) {
#pragma unroll
    for (int q = 0; q < 2 * NF; ++q) {
      if (ld) {
        if (q == 0) { xn[0] = ldsr16(pa); xn[1] = ldsr16(pa + 512); }
        if (q >= 1 && q <= NF) wn_[q - 1] = ldsr16(pb + (q - 1) * 2048 + 512);
        if (q >= NF) wa[q - NF] = ldsr16(pb + (q - NF) * 2048);
      }
      if (q < NF) acc[q] = mfma32(wa[q], xa[0], acc[q]); else acc[q - NF] = mfma32(wb[q - NF], xa[1], acc[q - NF]);
      SBAR();
    }
  };
  static_assert(GU_NA == GU_NB, "one slot counter");
  int sl = 0;
  auto adv = [&]() {
    pa += sl == GU_NA - 1 ? -(GU_NA - 1) * 8192 : 8192; pb += sl == GU_NB - 1 ? -(GU_NB - 1) * 14336 : 14336;
    sl = sl == GU_NA - 1 ? 0 : sl + 1;
  };
  SLOT(layer * 5 + 3);
  STAMP(0);
  STAMP(1);
  BARRIER();
  STAMP(2); STAMPC(0); WRESET();
  x0[0] = ldsr16(pa); x0[1] = ldsr16(pa + 512);
#pragma unroll
  for (int t = 0; t < NF; ++t) { wa[t] = ldsr16(pb + t * 2048); wb0[t] = ldsr16(pb + t * 2048 + 512); }
#pragma unroll 1
  for (int st = 0; st < 9; ++st) {
    adv(); BARRIER_NW(); step(x0, wb0, x1, wb1, true);
    adv(); BARRIER_NW(); step(x1, wb1, x0, wb0, true);
  }
  adv(); BARRIER_NW(); step(x0, wb0, x1, wb1, true);
  BARRIER();
  const LAS float* srs = (const LAS float*)(c.lds + M_SST) + mq * 32 + r32;
  const LAS float* scs = (const LAS float*)(c.lds + M_SCS) + f0 * 32 + h * 4;
  float rs = srs[0], nm = srs[128];
  f32x4 cs[2][4];
#pragma unroll
  for (int e = 0; e < 8; ++e) cs[e >> 2][e & 3] = *(const LAS f32x4*)(scs + e * 8);
  SBAR();
  step(x1, wb1, x0, wb0, false);
  STAMP(3); STAMPC(1);
  rs *= rs;
  const unsigned ho = WS_L + layer * L_SZ + L_H + ((panel * 8 + mq * 2 + (r32 >> 4)) * 216 + (ft0 + f0) * 2 + h) * 256 + (r32 & 15) * 16;
#pragma unroll
  for (int f = 0; f < NF; ++f) {
    unsigned p[2][2];
#pragma unroll
    for (int e = 0; e < 2; ++e) {
      const f32x16 a = acc[f];
      f32x4 gt = f32x4{a[e * 4], a[e * 4 + 1], a[e * 4 + 2], a[e * 4 + 3]} + cs[f & 1][e] * nm;
      const f32x4 up = (f32x4{a[8 + e * 4], a[9 + e * 4], a[10 + e * 4], a[11 + e * 4]} + cs[f & 1][2 + e] * nm) * rs;
      gt = __builtin_elementwise_max(gt, f32x4{0.f, 0.f, 0.f, 0.f}) * up;
      p[e][0] = pk2(gt[0], gt[1]); p[e][1] = pk2(gt[2], gt[3]);
    }
    if (f + 2 < NF) {
#pragma unroll
      for (int e = 0; e < 4; ++e) cs[f & 1][e] = *(const LAS f32x4*)(scs + (f + 2) * 32 + e * 8);
    }
    st16(c.r, ho + f * 512, pair32(p[0][0], p[0][1], p[1][0], p[1][1]));
    SBAR();
  }
  STAMP(4);
  publish(c, M_CNT, 8, flg(FL_GU + layer * 5, panel * 16 + gu_cb(c)), true);
  STAMP(5);
}
template <int NF> DI void gu_consumer(const Ctx& c, int layer, int f0, int mq) {
  if constexpr (GU16 == 1 || (GU16 == 2 && NF < 4) || (GU16 == 3 && NF == 4)) gu_consumer16<NF>(c, layer, f0, mq); else gu_consumer32<NF>(c, layer, f0, mq);
}
DI void mlp_load(const Ctx& c0, int lw, int layer, unsigned iss, bool qkv_next, const uint16_t* wqkv) {
  const int half = lw & 1;
  {
    BARRIER_L(); BARRIER_L();
    if (PROJ2) BARRIER_L();
  }
  { const int w = lw + 8; (void)w; WDUMP(layer * 5 + 2); }
  {
    const Ctx c = fresh(c0);
    const int ft0 = (gu_cb(c) * 27) >> 2;
    u32x4 pk[20];
    auto park = [&](int e0) {
#pragma unroll
      for (int e = e0; e < e0 + 4; ++e) {
        const int q = half * 5 + e % 5;
        pk[e] = ld16s(c.r, c.lane * 16, WS_WD + (pr_cb(c) * 5 + (q >> 1)) * 55296 + (((e / 5) * 2 + (lw >> 1)) * 2 + (q & 1)) * 1024);
      }
    };
    static_assert(GU_NA == 7 && GU_NB == 7, "schedule");
    if (lw >= 2) {
      Ring<7, GU_NB, 20, 2> rg;
#pragma unroll
      for (int p = 0; p < 7; ++p) rg.voff[p] = WS_WGU + (ft0 * 2 + half * 7 + p) * 20480 + c.lane * 16;
      rg.init(c.ws, 1024, 0, half);
      if (half == 0) ln_cols<4>(c, 56, WS_CSG + ft0 * 128, 13824);
      sig_wait(c, iss - layer * 16 + 4, M_CNT4);
      rg.template fill_ahead<1>(c, layer * 10 + 7);
      rg.template range<5, 5>(); rg.steady();
      wait_vm<28>(); BARRIER_L(); park(0);
      wait_vm<21 + 4>(); BARRIER_L(); park(4);
      wait_vm<14 + 8>(); BARRIER_L(); park(8);
      wait_vm<7 + 12>(); BARRIER_L(); park(12);
      wait_vm<16>(); BARRIER_L(); park(16);
    } else {
      Ring<4, GU_NA, 20, 2> rg;
#pragma unroll
      for (int p = 0; p < 4; ++p) rg.voff[p] = WS_L + layer * L_SZ + L_X1 + (gu_panel(c) * 8 + half * 4 + p) * 20480 + c.lane * 16;
      rg.init(c.ws, 1024, GU_A, half);
      rg.template start<3>(c, half, 4 * 20480, flg(FL_WO + layer * 5, gu_panel(c) * 16 + (c.lane & 15)), layer * 10 + 7, iss + 12);
      rg.template head<3>();
      f32x2 st[8];
      wait_vm<16>(); BARRIER_L();
      ln_load(c, WS_ST + (layer * 2 + 1) * STB + gu_panel(c) * 8192, half * 64 + c.lane, st);
      wait_vm<12 + 8>(); BARRIER_L(); park(0); park(4);
      wait_vm<8 + 8 + 8>(); BARRIER_L();
      const f32x2 t = ln_sum(st);
      wait_vm<8>(); BARRIER_L(); ln_put(c, half * 64 + c.lane, t); park(8);
      BARRIER_L(); park(12); park(16);
    }
    BARRIER_L();
#pragma unroll
    for (int e = 0; e < 20; ++e) PIN(pk[e]);
    { const int w = lw + 8; (void)w; WDUMP(layer * 5 + 3); }
    auto dump = [&]() {
#pragma unroll
      for (int e = 0; e < 20; ++e) ldsw16(c.lds + ((e / 5) * 2 + (lw >> 1)) * 10240 + (half * 5 + e % 5) * 1024 + c.lane * 16, pk[e]);
    };
    if (lw >= 2) {
      Ring<5, DN_NB, 27, 2> rg;
#pragma unroll
      for (int p = 0; p < 5; ++p) { const int q = half * 5 + p; rg.voff[p] = WS_WD + (pr_cb(c) * 5 + (q >> 1)) * 55296 + (q & 1) * 1024 + c.lane * 16; }
      rg.init(c.ws + 8 * 2048, 2048, 0, half);
      rg.slot = 8;
      sig_wait(c, layer * 16 + 16, M_CNT);
      rg.template fill<3>(); dump(); rg.template fill<4>();
      BARRIER_L();
      wait_vm<5 * 5>(); BARRIER_L();
      wait_vm<0>(); BARRIER_L();
      rg.base += 6 * 2048; rg.slot = 6;
      rg.template fill<3>();
      BARRIER_L();
      rg.template fill<3>();
      wait_vm<3 * 5>(); BARRIER_L();
      wait_vm<0>(); BARRIER_L();
      BARRIER_L();
      if (PROJ2) BARRIER_L();
    } else {
      sig_wait(c, layer * 16 + 16, M_CNT);
      dump();
      if (lw == 0) { poll(c, flg(FL_GU + layer * 5, (pr_panel(c) >> 1) * 16 + (c.lane & 15))); STAMPL(layer * 5 + 4, 6); }
#pragma unroll 1
      for (int k = 0; k < 7 + PROJ2; ++k) BARRIER_L();
    }
  }
  { const int w = lw + 8; (void)w; WDUMP(layer * 5 + 4); }
  if (qkv_next) qkv_load1(fresh(c0), lw, wqkv);
}

__global__ __launch_bounds__(768, 3) void fused_kernel(const uint16_t* __restrict__ x, const uint16_t* __restrict__ wqkv, const uint16_t* __restrict__ wo,
                                                    const uint16_t* __restrict__ wg, const uint16_t* __restrict__ wu,
                                                    const uint16_t* __restrict__ wd, uint16_t* __restrict__ out, char* __restrict__ ws) {
  extern __shared__ __attribute__((aligned(16))) char smem[];
  const char* pc;
  asm volatile("s_getpc_b64 %0" : "=s"(pc));
  glds1(P0_SCR + 10240 + 1024, (const char*)((u64)pc & ~15ull), ((blockIdx.x >> 3) * 12 + (threadIdx.x >> 6)) % PF_KB * 1024 + (threadIdx.x & 63) * 16);
  Ctx c;
  c.ws = ws; c.lds = (lds_ptr)smem; c.lane = threadIdx.x & 63; c.xcd = blockIdx.x & 7; c.j = blockIdx.x >> 3;
  c.r = __builtin_amdgcn_make_buffer_rsrc(ws, 0, WS_END, 0x00020000);
  const int w = __builtin_amdgcn_readfirstlane(threadIdx.x >> 6);
  SPANST(0);
  c.tgt = 0;
  if (threadIdx.x < 16) *(LAS unsigned*)(c.lds + M_CNT + threadIdx.x * 4) = 0u;
  const bool qkv_on = qkv_cb(c) != 15;
  Ctx c0 = c;
  WDUMP(15);
  if (__builtin_expect(w >= 8, LD_FIRST)) {
    __builtin_amdgcn_s_setprio(3);
    if (qkv_on) qkv_load0(fresh(c0), w - 8, x, wqkv); else BARRIER();
    c0.tgt = epoch(ws);
#pragma unroll 1
    for (int layer = 0; layer < 2; ++layer) {
      const unsigned iss = layer * 24 + (qkv_on ? (layer + 1) * 4 : 0);
      WDUMP(layer * 5);
      att_load(fresh(c0), opq(w - 8), opq(layer), iss, wo, wg, wu, wd);
      WDUMP(layer * 5 + 1);
      mlp_load(c0, opq(w - 8), opq(layer), iss, layer == 0 && qkv_on, wqkv);
    }
  } else {
    if (w < 4) {
      const unsigned l = fresh(c0).lane;
      const char* base = w == 0 ? (const char*)x : w == 1 ? (const char*)wqkv : ws;
      const unsigned off = w < 2 ? l * 38400u : min(((w - 2) * 64 + l) * 524288u + 2048, WS_END);
      if (TLB_IN || w >= 2) asm volatile(GL_HEAD "global_load_lds_dword %2, %1 sc1" ::"s"(P0_SCR + 10240), "s"(base), "v"(off) : "memory");
    }
#pragma unroll 1
    for (int layer = 0; layer < 2; ++layer) {
      const unsigned L = WS_L + layer * L_SZ;
      qkv_compute(fresh(c0), c0.tgt, opq(w), opq(layer), x, wqkv);
      WDUMP(layer * 5);
      att_compute(fresh(c0), opq(w), opq(layer));
      WDUMP(layer * 5 + 1);
      proj_compute<10, WO_NB, 20>(fresh(c0), opq(w), layer * 5 + 2, L + L_O, layer == 0 ? (const char*)x : nullptr, WS_X2, L + L_X1,
                                  WS_ST + (layer * 2 + 1) * STB, nullptr, FL_WO + layer * 5);
      WDUMP(layer * 5 + 2);
      if (w < 4) gu_consumer<4>(fresh(c0), opq(layer), 0, opq(w));
      else if (GU_SKIP && (gu_cb(c0) & 3) == 0) gu_consumer<2>(fresh(c0), opq(layer), 4, opq(w - 4));
      else gu_consumer<3>(fresh(c0), opq(layer), 4, opq(w - 4));
      WDUMP(layer * 5 + 3);
      proj_compute<27, DN_NB, 18>(fresh(c0), opq(w), layer * 5 + 4, L + L_H, nullptr, L + L_X1, WS_X2, WS_ST + 2 * STB, layer ? out : nullptr,
                                  FL_DN + layer * 5);
      WDUMP(layer * 5 + 4);
    }
  }
}

extern "C" void kernel_launch(void* const* d_in, const int* in_sizes, int n_in, void* d_out, int out_size, void* d_ws, size_t ws_size,
                              hipStream_t stream) {
  (void)hipFuncSetAttribute((const void*)fused_kernel, hipFuncAttributeMaxDynamicSharedMemorySize, LDS_BYTES);
  fused_kernel<<<256, 768, LDS_BYTES, stream>>>((const uint16_t*)d_in[0], (const uint16_t*)d_in[1], (const uint16_t*)d_in[2],
                                                (const uint16_t*)d_in[4], (const uint16_t*)d_in[5], (const uint16_t*)d_in[6],
                                                (uint16_t*)d_out, (char*)d_ws);
}
```

```cpp
#include <hip/hip_runtime.h>
#include <hip/hip_bf16.h>
#include <stdint.h>

typedef __attribute__((ext_vector_type(8))) __bf16 bf16x8;
typedef __attribute__((ext_vector_type(16))) __bf16 bf16x16;
typedef __attribute__((ext_vector_type(4))) short s16x4;
typedef __attribute__((ext_vector_type(8))) short s16x8;
typedef __attribute__((ext_vector_type(2))) __bf16 bf16x2;
typedef __attribute__((ext_vector_type(2))) float f32x2;
typedef __attribute__((ext_vector_type(4))) float f32x4;
typedef __attribute__((ext_vector_type(8))) float f32x8;
typedef __attribute__((ext_vector_type(16))) float f32x16;
typedef __attribute__((ext_vector_type(2))) unsigned u32x2;
typedef __attribute__((ext_vector_type(4))) unsigned u32x4;
typedef unsigned long long u64;
#define LAS __attribute__((address_space(3)))
#define GAS __attribute__((address_space(1)))
typedef LAS char* lds_ptr;
typedef __amdgpu_buffer_rsrc_t rsrc_t;
#define DI __device__ __forceinline__

constexpr unsigned XB = 2048u * 640 * 2;
constexpr unsigned WS_EPOCH = 0;
constexpr unsigned WS_FLAG = 4096;
constexpr int FL_P0 = 0, FL_RT = 1, FL_QKV = 2, FL_ATT = 3, FL_WO = 4, FL_GU = 5, FL_DN = 6, FL_RT2 = 12;
constexpr unsigned WS_WO  = WS_FLAG + 13u * 32768;
constexpr unsigned WS_WGU = WS_WO + 640u * 640 * 2;
constexpr unsigned WS_WD  = WS_WGU + 3456u * 640 * 2;
constexpr unsigned WS_CSQ = WS_WD + 640u * 1728 * 2;
constexpr unsigned WS_CSG = WS_CSQ + 8u * 1920 * 4;
constexpr unsigned WS_ST  = WS_CSG + 4u * 3456 * 4;
constexpr unsigned STB = 2048u * 64;
constexpr unsigned WS_X2  = WS_ST + 4 * STB;
constexpr unsigned WS_L   = WS_X2 + XB;
constexpr unsigned L_Q1 = 0, L_Q2 = L_Q1 + 32u * 65536, L_K1 = L_Q2 + 16u * 32768, L_K2 = L_K1 + 32u * 65536, L_V = L_K2 + 16u * 32768,
                   L_O = L_V + XB, L_X1 = L_O + XB, L_H = L_X1 + XB, L_SZ = L_H + 2048u * 1728 * 2;
constexpr unsigned WS_END = WS_L + 2 * L_SZ;

constexpr int LDS_BYTES = 163840, MISC = 159744, M_SST = MISC, M_SCS = MISC + 1024, M_CNT = MISC + 2048, M_CNT2 = M_CNT + 8, M_CNT3 = M_CNT + 16, M_CNT4 = M_CNT + 24, M_SIG = M_CNT + 32, M_ISS = M_CNT + 40, M_TGT = M_CNT + 48;
constexpr int QKV_NB = 5, QKV_NA = 4, QKV_A = QKV_NB * 16384;
constexpr int WO_NB = 10, DN_NB = 15;
constexpr int GU_NB = 7, GU_NA = 7, GU_A = GU_NB * 14336;
constexpr int AT_SLOT = 45056, AT_CONST = 3 * AT_SLOT, AT_SCR = AT_CONST + 10752, AT_MERGE = AT_SLOT;
static_assert(QKV_A + QKV_NA * 16384 <= MISC && DN_NB * 10240 <= MISC && GU_A + GU_NA * 8192 <= MISC, "lds");
static_assert(AT_SCR + 3 * 4096 <= MISC, "lds");

DI unsigned pk2(float a, float b) { f32x2 v = {a, b}; bf16x2 r = __builtin_convertvector(v, bf16x2); return __builtin_bit_cast(unsigned, r); }
DI float bflo(unsigned u) { return __uint_as_float(u << 16); }
DI float bfhi(unsigned u) { return __uint_as_float(u & 0xffff0000u); }
DI f32x4 mfma16(bf16x8 a, bf16x8 b, f32x4 c) { return __builtin_amdgcn_mfma_f32_16x16x32_bf16(a, b, c, 0, 0, 0); }
DI bf16x8 ldsr16(lds_ptr p) { return *(const LAS bf16x8*)p; }
DI bf16x8 ldstr(lds_ptr p, int second) {
  const s16x4 a = __builtin_amdgcn_ds_read_tr16_b64_v4i16((LAS s16x4*)p), b = __builtin_amdgcn_ds_read_tr16_b64_v4i16((LAS s16x4*)(p + second));
  return __builtin_bit_cast(bf16x8, (s16x8)__builtin_shufflevector(a, b, 0, 1, 2, 3, 4, 5, 6, 7));
}
#define BARRIER() asm volatile("s_waitcnt lgkmcnt(0)\n\ts_barrier" ::: "memory")
template <int N> DI void wait_vm() { asm volatile("s_waitcnt vmcnt(%0)" ::"n"(N > 63 ? 63 : N) : "memory"); }
#define BARRIER_NW() asm volatile("s_barrier" ::: "memory")
#define SBAR() __builtin_amdgcn_sched_barrier(0)
#define PIN(x) asm volatile("" : "+v"(x))
#define GL_HEAD "s_mov_b32 m0, %0\n\ts_nop 3\n\t"
#define GL(v, off) "global_load_lds_dwordx4 %" #v ", %1 offset:" #off " sc1\n\t"
DI void glds1(unsigned m, const char* sb, unsigned v0) { asm volatile(GL_HEAD GL(2, 0)::"s"(m), "s"(sb), "v"(v0) : "memory"); }
DI void glds3(unsigned m, const char* sb, unsigned v0, unsigned v1, unsigned v2) {
  asm volatile(GL_HEAD GL(2, 0) GL(3, 1024) GL(4, 2048)::"s"(m), "s"(sb), "v"(v0), "v"(v1), "v"(v2) : "memory");
}
DI void glds4(unsigned m, const char* sb, unsigned v0, unsigned v1, unsigned v2, unsigned v3) {
  asm volatile(GL_HEAD GL(2, 0) GL(3, 1024) GL(4, 2048) GL(5, 3072)::"s"(m), "s"(sb), "v"(v0), "v"(v1), "v"(v2), "v"(v3) : "memory");
}
DI void st16(rsrc_t r, unsigned off, u32x4 v) { __builtin_amdgcn_raw_buffer_store_b128(v, r, off, 0, 16); }
DI void st8(rsrc_t r, unsigned off, u32x2 v) { __builtin_amdgcn_raw_buffer_store_b64(v, r, off, 0, 16); }
DI u32x4 ld16(rsrc_t r, unsigned off) { return __builtin_amdgcn_raw_buffer_load_b128(r, off, 0, 16); }
DI u32x2 ld8(rsrc_t r, unsigned off) { return __builtin_amdgcn_raw_buffer_load_b64(r, off, 0, 16); }
DI u32x4 ld16s(rsrc_t r, unsigned voff, unsigned soff) { return __builtin_amdgcn_raw_buffer_load_b128(r, voff, soff, 16); }
DI void ldsw16(lds_ptr p, u32x4 v) { *(LAS u32x4*)p = v; }
DI u32x4 pair32(unsigned a0, unsigned a1, unsigned b0, unsigned b1) {
  auto x = __builtin_amdgcn_permlane32_swap(a0, b0, false, false);
  auto y = __builtin_amdgcn_permlane32_swap(a1, b1, false, false);
  return u32x4{x[0], y[0], x[1], y[1]};
}
template <int N> DI float ror16(float v) { return __int_as_float(__builtin_amdgcn_update_dpp(0, __float_as_int(v), 0x120 + N, 0xf, 0xf, false)); }
DI float sum32(float v) {
  v += ror16<8>(v); v += ror16<4>(v); v += ror16<2>(v); v += ror16<1>(v);
  auto a = __builtin_amdgcn_permlane16_swap(__float_as_uint(v), __float_as_uint(v), false, false);
  return __uint_as_float(a[0]) + __uint_as_float(a[1]);
}
DI u32x4 pair16(unsigned a0, unsigned a1, unsigned b0, unsigned b1) {
  auto x = __builtin_amdgcn_permlane16_swap(a0, b0, false, false);
  auto y = __builtin_amdgcn_permlane16_swap(a1, b1, false, false);
  return u32x4{x[0], y[0], x[1], y[1]};
}

DI float rows4(float v) {
  auto a = __builtin_amdgcn_permlane16_swap(__float_as_uint(v), __float_as_uint(v), false, false);
  v = __uint_as_float(a[0]) + __uint_as_float(a[1]);
  auto b = __builtin_amdgcn_permlane32_swap(__float_as_uint(v), __float_as_uint(v), false, false);
  return __uint_as_float(b[0]) + __uint_as_float(b[1]);
}

constexpr int WO_AH = 6, DN_AH = 8, ATT_LATE = 4, PF_KB = 56, PACE = 16;
struct Ctx {
  char* ws; rsrc_t r; lds_ptr lds; u64 tgt; int lane, xcd, j;
};
DI Ctx fresh(const Ctx& c0) {
  Ctx c = c0;
  asm volatile("v_mbcnt_lo_u32_b32 %0, -1, 0\n\tv_mbcnt_hi_u32_b32 %0, -1, %0" : "=v"(c.lane), "+s"(c.xcd), "+s"(c.j), "+s"(c.ws));
  return c;
}
DI int opq(int v) { asm volatile("" : "+s"(v)); return v; }
constexpr int SPIN_CAP = 1 << 18;

DI u64 epoch(const char* ws) {
  u64 e; asm volatile("s_nop 4\n\ts_load_dwordx2 %0, %1, 0x0\n\ts_waitcnt lgkmcnt(0)" : "=s"(e) : "s"(ws + WS_EPOCH) : "memory"); return e + 1;
}
DI unsigned flg(int arr, int unit) { return WS_FLAG + (arr * 256 + unit) * 128; }
DI void publish(const Ctx& c, int cnt_off, unsigned n, unsigned to, bool mark = false) {
  unsigned one = 1, iss = M_ISS, cnt = cnt_off, old, so;
  u64 tgt = c.tgt, ex;
  asm volatile("" : "+v"(one), "+v"(iss), "+v"(cnt), "+v"(to), "+v"(tgt));
  if (mark) asm volatile("s_mov_b64 %0, exec\n\ts_mov_b64 exec, 1\n\tds_add_u32 %1, %2\n\ts_mov_b64 exec, %0" : "=&s"(ex) : "v"(iss), "v"(one) : "memory");
  asm volatile("s_mov_b64 %2, exec\n\ts_waitcnt vmcnt(0)\n\ts_mov_b64 exec, 1\n\tds_add_rtn_u32 %0, %3, %4\n\ts_waitcnt lgkmcnt(0)\n\t"
               "v_readfirstlane_b32 %1, %0\n\ts_mov_b64 exec, %2" : "=&v"(old), "=&s"(so), "=&s"(ex) : "v"(cnt), "v"(one) : "memory");
  if (so % n == n - 1)
    asm volatile("s_mov_b64 %0, exec\n\ts_mov_b64 exec, 1\n\ts_nop 2\n\tglobal_store_dwordx2 %1, %2, %3 sc1\n\ts_mov_b64 exec, %0"
                 : "=&s"(ex) : "v"(to), "v"(tgt), "s"(c.ws) : "memory");
}
DI void poll(const Ctx& c, unsigned off) {
#pragma unroll 1
  for (int spin = 0; spin < SPIN_CAP; ++spin) {
    u64 v;
    asm volatile("s_nop 4\n\tglobal_load_dwordx2 %0, %1, %2 sc1\n\ts_sleep 1\n\ts_waitcnt vmcnt(0)" : "=&v"(v) : "v"(off), "s"(c.ws) : "memory");
    if (__all(v == c.tgt)) break;
    __builtin_amdgcn_s_sleep(2);
  }
}
DI void sig_set(const Ctx& c, unsigned v) { if (c.lane == 0) __hip_atomic_store((LAS unsigned*)(c.lds + M_SIG), v, __ATOMIC_RELAXED, __HIP_MEMORY_SCOPE_WORKGROUP); }
DI void sig_wait(const Ctx& c, unsigned v, int off = M_SIG, bool calm = false) {
#pragma unroll 1
  for (int spin = 0; spin < SPIN_CAP * 16; ++spin) {
    if (__builtin_amdgcn_readfirstlane(__hip_atomic_load((LAS unsigned*)(c.lds + off), __ATOMIC_RELAXED, __HIP_MEMORY_SCOPE_WORKGROUP)) >= v) break;
    if (calm) __builtin_amdgcn_s_sleep(8); else if (off != M_SIG) __builtin_amdgcn_s_sleep(1);
  }
  asm volatile("" ::: "memory");
}

template <int PP, int NSLOT, int NS, int LAG = 1>
struct Ring {
  unsigned voff[PP];
  const char* base;
  unsigned adv, l0;
  int slot;
  DI void init(const char* b, unsigned a, unsigned lds0, int half) {
    base = b - 4096; adv = a; l0 = lds0 + half * PP * 1024; slot = 0;
#pragma unroll
    for (int p = 0; p < PP; ++p) voff[p] += 4096 - (p & 3) * 1024;
  }
  DI void issue() {
    const unsigned m = l0 + slot * (2 * PP * 1024);
    glds4(m, base, voff[0], voff[1], voff[2], voff[3]);
    if constexpr (PP == 5) glds1(m + 4096, base, voff[4]);
    if constexpr (PP == 7) glds3(m + 4096, base, voff[4], voff[5], voff[6]);
    if constexpr (PP == 8) glds4(m + 4096, base, voff[4], voff[5], voff[6], voff[7]);
    base += adv; slot = slot == NSLOT - 1 ? 0 : slot + 1;
  }
  template <int D0> DI void start(const Ctx& c, int half, unsigned delta, unsigned flag, unsigned sig, unsigned iss) {
    if (half == 0) {
      sig_wait(c, iss, M_ISS);
      poll(c, flag);
      issue(); base += delta - adv; l0 += PP * 1024; slot = 0; issue(); base -= delta; l0 -= PP * 1024;
      sig_set(c, sig);
    } else { base += adv; slot = 1; sig_wait(c, sig); }
    fill<D0 - 1>();
  }
  template <int N = NSLOT> DI void fill() {
#pragma unroll 1
    for (int s = 0; s < N; ++s) issue();
  }
  template <int NEED, int N = NSLOT> DI void fill_ahead(const Ctx& c, unsigned sig) {
    int n = 0;
#pragma unroll 1
    while (n < N) {
      if (n * PP >= NEED * PP + (NEED ? PACE : 0) &&
          __builtin_amdgcn_readfirstlane(__hip_atomic_load((LAS unsigned*)(c.lds + M_SIG), __ATOMIC_RELAXED, __HIP_MEMORY_SCOPE_WORKGROUP)) >= sig) break;
      issue(); wait_vm<PACE>(); ++n;
    }
    if (n == N) wait_vm<(N - NEED) * PP>();
    BARRIER();
#pragma unroll 1
    for (; n < N; ++n) issue();
  }
  DI void rest() { range<NSLOT - 2, NSLOT - LAG>(); steady(); range<NSLOT - 1 - LAG, 0>(); BARRIER(); }
  template <int R, int E> DI void range() { if constexpr (R >= E) { wait_vm<R * PP>(); BARRIER(); range<R - 1, E>(); } }
  template <int D0 = NSLOT> DI void head() {
    wait_vm<(D0 - 1) * PP>(); BARRIER();
    if constexpr (D0 < NSLOT) fill<NSLOT - D0>();
    range<NSLOT - 2, NSLOT - LAG>();
    steady();
  }
  DI void steady() {
#pragma unroll 1
    for (int k = LAG; k < NS - NSLOT + LAG; ++k) { wait_vm<(NSLOT - 1 - LAG) * PP>(); BARRIER(); issue(); }
  }
  template <int D0 = NSLOT> DI void run() { head<D0>(); range<NSLOT - 1 - LAG, 0>(); BARRIER(); }
};

DI void ln_load(const Ctx& c, unsigned off, int row, f32x2 (&p)[8]) {
#pragma unroll
  for (int i = 0; i < 8; ++i) p[i] = __builtin_bit_cast(f32x2, __builtin_amdgcn_raw_buffer_load_b64(c.r, row * 8, off + i * 1024, 16));
}
DI f32x2 ln_sum(const f32x2 (&p)[8]) {
  f32x2 t = ((p[0] + p[1]) + (p[2] + p[3])) + ((p[4] + p[5]) + (p[6] + p[7]));
  const float m8 = t[0] * 0.125f; float q0 = 0.f, q1 = 0.f;
#pragma unroll
  for (int i = 0; i < 8; i += 2) { const float a = p[i][0] - m8, b = p[i + 1][0] - m8; q0 += a * a; q1 += b * b; }
  t[1] += (q0 + q1) * (1.f / 80.f);
  PIN(t);
  return t;
}
DI void ln_put(const Ctx& c, int row, f32x2 t) {
  *(LAS float*)(c.lds + M_SST + row * 4) = __builtin_amdgcn_rsqf(t[1] * (1.f / 640.f) + 1e-5f);
  *(LAS float*)(c.lds + M_SST + 512 + row * 4) = -t[0] * (1.f / 640.f);
}
template <int NP> DI void ln_cols_sum(const Ctx& c, int n4, const u32x4 (&q)[NP]) {
  f32x4 cs = __builtin_bit_cast(f32x4, q[0]);
#pragma unroll
  for (int i = 1; i < NP; ++i) cs += __builtin_bit_cast(f32x4, q[i]);
  if (c.lane < n4) *(LAS f32x4*)(c.lds + M_SCS + c.lane * 16) = cs;
}
template <int NP> DI void ln_cols(const Ctx& c, int n4, unsigned off, unsigned pitch) {
  u32x4 q[NP];
#pragma unroll
  for (int i = 0; i < NP; ++i) q[i] = ld16(c.r, off + i * pitch + (c.lane < n4 ? c.lane : 0) * 16);
  ln_cols_sum<NP>(c, n4, q);
}

DI int qkv_panel(const Ctx& c) { return (c.xcd & 3) * 4 + (c.j & 3); }
DI int qkv_cb(const Ctx& c) { return (c.xcd >> 2) * 8 + (c.j >> 2); }
DI int pr_panel(const Ctx& c) { return (c.xcd & 3) * 8 + (c.j & 7); }
DI int pr_cb(const Ctx& c) { return (c.xcd >> 2) * 4 + (c.j >> 3); }
DI int gu_panel(const Ctx& c) { return (c.xcd & 1) * 8 + (c.j & 7); }
DI int gu_cb(const Ctx& c) { return (c.xcd >> 1) * 4 + (c.j >> 3); }
DI int att_head(const Ctx& c) { return c.xcd * 4 + (c.j & 3); }
DI int att_qb(const Ctx& c) { return c.j >> 2; }

constexpr int P0_SCR = QKV_A + QKV_NA * 16384;
static_assert(P0_SCR + 10240 <= MISC, "lds");
struct Phase0 {
  u32x4 v[3]; float sum, m2;
  DI void load(const Ctx& c, int hw, const uint16_t* x, const uint16_t* wqkv) {
    const int lane = c.lane, r32 = lane & 31, b = blockIdx.x, row = b * 8 + hw * 2 + (lane >> 5);
    v[0] = *(const u32x4*)(x + row * 640 + r32 * 8); v[1] = *(const u32x4*)(x + row * 640 + 256 + r32 * 8);
    v[2] = *(const u32x4*)(x + row * 640 + 512 + (r32 & 15) * 8);
    if (b < 240) {
      const char* src = (const char*)wqkv + ((b / 30) * 80 + hw * 8) * 3840 + (b % 30) * 128;
      const unsigned vo = (lane >> 3) * 3840 + (lane & 7) * 16;
      glds1(P0_SCR + hw * 1024, src, vo); glds1(P0_SCR + 4096 + hw * 1024, src + 32 * 3840, vo);
      if (hw < 2) glds1(P0_SCR + 8192 + hw * 1024, src + 64 * 3840, vo);
    }
  }
  DI float dev2(int q) const {
    const float mean = sum * (1.f / 640.f); float a = 0.f, b = 0.f;
#pragma unroll
    for (int e = 0; e < 4; ++e) { const float d0 = bflo(v[q][e]) - mean, d1 = bfhi(v[q][e]) - mean; a += d0 * d0; b += d1 * d1; }
    return a + b;
  }
  DI void part1(const Ctx& c) {
    unsigned uo = 0x3f803f80u; PIN(uo);
    const bf16x2 ones = __builtin_bit_cast(bf16x2, uo);
    float s[3];
#pragma unroll
    for (int q = 0; q < 3; ++q) {
      s[q] = 0.f;
#pragma unroll
      for (int e = 0; e < 4; ++e) { const unsigned u = v[q][e]; s[q] = __builtin_amdgcn_fdot2_f32_bf16(__builtin_bit_cast(bf16x2, u), ones, s[q], false); }
    }
    sum = sum32(s[0] + s[1] + ((c.lane & 16) ? 0.f : s[2]));
    m2 = dev2(0);
    asm volatile("" : "+v"(sum), "+v"(m2));
    wait_vm<0>();
  }
  DI void part2(const Ctx& c, int hw) {
    const int lane = c.lane, r32 = lane & 31, b = blockIdx.x, row = b * 8 + hw * 2 + (lane >> 5);
    const float t = dev2(2);
    m2 = sum32(m2 + dev2(1) + ((lane & 16) ? 0.f : t));
    if (r32 < 8) st8(c.r, WS_ST + (row >> 7) * 8192 + r32 * 1024 + (row & 127) * 8, u32x2{__float_as_uint(sum * 0.125f), __float_as_uint(m2 * 0.125f)});
  }
  DI void cols(const Ctx& c, int hw) {
    const int i = c.lane & 15, g = c.lane >> 4, b = blockIdx.x;
    if (b < 240) {
      const unsigned o = 0x3f803f80u, z = g < 2 ? o : 0u;
      lds_ptr p = c.lds + P0_SCR + (g * 8 + (i >> 2)) * 128 + hw * 32 + (i & 3) * 8;
      f32x4 cs = mfma16(ldstr(p, 512), __builtin_bit_cast(bf16x8, u32x4{o, o, o, o}), f32x4{});
      cs = mfma16(ldstr(p + 4096, 512), __builtin_bit_cast(bf16x8, u32x4{o, o, o, o}), cs);
      cs = mfma16(ldstr(p + (g < 2 ? 8192 : 0), 512), __builtin_bit_cast(bf16x8, u32x4{z, z, z, z}), cs);
      if (i == 0) st16(c.r, WS_CSQ + ((b / 30) * 1920 + (b % 30) * 64 + hw * 16 + g * 4) * 4, __builtin_bit_cast(u32x4, cs));
    }
  }
};

template <bool SWAP, class PRE, class MID>
DI void qkv_loop(const Ctx& c, int w, int layer, f32x4 (&acc)[4][4], PRE pre, MID mid) {
  const int lane = c.lane, i = lane & 15, g = lane >> 4, wm = w & 1, wn = w >> 1;
  const int rm = (i >> 3) * 1024 + (i & 7) * 128 + ((g ^ ((i >> 1) & 7)) << 4);
  lds_ptr pa0 = c.lds + QKV_A + wm * 8192 + (layer == 0 ? rm : lane * 16);
  lds_ptr pa1 = c.lds + QKV_A + wm * 8192 + (layer == 0 ? rm ^ 64 : lane * 16 + 1024);
  const int f = (i >> 2) | ((g & 1) << 2), rb = (g * 8 + (i >> 2)) * 256 + (i & 3) * 8;
  lds_ptr pb[4];
#pragma unroll
  for (int t = 0; t < 4; ++t) pb[t] = c.lds + rb + (((wn * 4 + t) ^ f) << 5);
  bf16x8 x0[4], x1[4], w0[4], w1[4];
  auto step = [&](const bf16x8* xa, const bf16x8* wb, bf16x8* xn, bf16x8* wn_, lds_ptr pa, int bo, bool ld) {
#pragma unroll
    for (int q = 0; q < 16; ++q) {
      if (ld) {
        if ((q & 3) == 0) wn_[q >> 2] = ldstr(pb[q >> 2] + bo, 1024);
        if ((q & 3) == 2) xn[q >> 2] = ldsr16(pa + (q >> 2) * 2048);
      }
      const int t = q >> 2, m = q & 3;
      acc[m][t] = SWAP ? mfma16(wb[t], xa[m], acc[m][t]) : mfma16(xa[m], wb[t], acc[m][t]);
      SBAR();
    }
  };

  mid(0); mid(4); mid(8); mid(12);
  BARRIER();
#pragma unroll
  for (int e = 0; e < 4; ++e) { w0[e] = ldstr(pb[e], 1024); x0[e] = ldsr16(pa0 + e * 2048); }
  int sa = 0, sb = 0;
#pragma unroll 1
  for (int st = 0; st < 9; ++st) {
    step(x0, w0, x1, w1, pa1, 8192, true);
    const int da = sa == QKV_NA - 1 ? -(QKV_NA - 1) * 16384 : 16384, db = sb == QKV_NB - 1 ? -(QKV_NB - 1) * 16384 : 16384;
    sa = sa == QKV_NA - 1 ? 0 : sa + 1; sb = sb == QKV_NB - 1 ? 0 : sb + 1;
    pa0 += da; pa1 += da; pb[0] += db; pb[1] += db; pb[2] += db; pb[3] += db;
    BARRIER_NW();
    step(x1, w1, x0, w0, pa0, 0, true);
  }
  step(x0, w0, x1, w1, pa1, 8192, true);
  BARRIER();
  pre(); SBAR();
  step(x1, w1, x0, w0, pa0, 0, false);
}
DI unsigned qkv_w_off(const Ctx& c, int q) {
  const int row = q * 4 + (c.lane >> 4);
  return row * 3840 + qkv_cb(c) * 256 + (((c.lane & 15) ^ (((row & 3) << 1) | (row & 8))) << 4);
}
DI void qkv_helper(Ctx c, u64& tgt, int hw, int layer, const uint16_t* x, const uint16_t* wqkv) {
  const int panel = qkv_panel(c), cb = qkv_cb(c), l = c.lane;
  const bool on = cb != 15;
  if (layer == 0) {
    Phase0 p0;
    p0.load(c, hw, x, wqkv);
    if (on) BARRIER();
    p0.part1(c);
    BARRIER();
    p0.part2(c, hw);
    if (on) BARRIER();
    p0.cols(c, hw);
    c.tgt = tgt = epoch(c.ws);
    if (hw == 0 && l == 0) *(LAS u64*)(c.lds + M_TGT) = tgt;
    if (on) BARRIER();
    publish(c, M_CNT2, 4, flg(FL_P0, blockIdx.x));
    if (!on) return;
    BARRIER(); BARRIER(); BARRIER();
    const unsigned at = flg(FL_P0, hw < 2 ? panel * 16 + hw * 8 + (l & 7) : (l & 7) * 30 + cb * 2 + ((l >> 3) & 1));
    const u64 v = __hip_atomic_load((const GAS u64*)(c.ws + at), __ATOMIC_RELAXED, __HIP_MEMORY_SCOPE_AGENT);
    BARRIER();
    if (!__all(v == c.tgt)) poll(c, at);
    if (hw < 2) {
      f32x2 p[8];
      ln_load(c, WS_ST + panel * 8192, hw * 64 + l, p);
      BARRIER(); BARRIER();
      ln_put(c, hw * 64 + l, ln_sum(p));
    } else if (hw == 2) {
      u32x4 q[8];
#pragma unroll
      for (int i = 0; i < 8; ++i) q[i] = ld16(c.r, WS_CSQ + cb * 512 + i * 7680 + (l & 31) * 16);
      BARRIER(); BARRIER();
      ln_cols_sum<8>(c, 32, q);
    } else { BARRIER(); BARRIER(); }
    BARRIER();
  } else if (on) {
    if (hw == 2) ln_cols<8>(c, 32, WS_CSQ + cb * 512, 7680);
    BARRIER(); BARRIER(); BARRIER(); BARRIER(); BARRIER(); BARRIER();
    if (hw < 2) {
      f32x2 p[8];
      ln_load(c, WS_ST + 2 * STB + panel * 8192, hw * 64 + l, p);
      BARRIER(); BARRIER();
      ln_put(c, hw * 64 + l, ln_sum(p));
    } else { BARRIER(); BARRIER(); }
    BARRIER(); BARRIER(); BARRIER();
  }
}
DI void qkv_compute(Ctx c, u64& tgt, int w, int layer, const uint16_t* x, const uint16_t* wqkv) {
  const int panel = qkv_panel(c), cb = qkv_cb(c);
  if (w >= 4) { qkv_helper(c, tgt, w - 4, layer, x, wqkv); return; }
  if (cb == 15) { if (layer == 0) { tgt = epoch(c.ws); wait_vm<0>(); BARRIER(); } return; }
  const int lane = c.lane, i = lane & 15, g = lane >> 4, wm = w & 1, wn = w >> 1;
  const unsigned L = WS_L + layer * L_SZ;
  f32x4 acc[4][4];
#pragma unroll
  for (int e = 0; e < 16; ++e) acc[e >> 2][e & 3] = f32x4{0.f, 0.f, 0.f, 0.f};
  const LAS float* srs = (const LAS float*)(c.lds + M_SST) + wm * 64;
  const LAS float* scs = (const LAS float*)(c.lds + M_SCS) + wn * 64;
  if (cb < 10) {
    float rs[4], nm[4]; f32x4 cs[4]; unsigned ad[4][2];
    const float qs = cb < 5 ? 0.22360679774997896f * 1.4426950408889634f : 1.f;
    const unsigned i1 = L + (cb < 5 ? L_Q1 : L_K1), i2 = L + (cb < 5 ? L_Q2 : L_K2);
    qkv_loop<true>(c, w, layer, acc, [&]() {
#pragma unroll
      for (int e = 0; e < 4; ++e) { rs[e] = srs[e * 16 + i]; nm[e] = srs[128 + e * 16 + i]; cs[e] = *(const LAS f32x4*)(scs + e * 16 + g * 4); }
    }, [&](int q) {
      const int t = q >> 2, o = (cb % 5) * 16 + wn * 8 + t * 2 + (g >> 1), hp = o / 5, pos = o - hp * 5;
      const unsigned ts = pos == 2 ? 512 : 1024;
      ad[t][0] = (pos == 2 ? i2 + hp * 32768 : i1 + (2 * hp + (pos > 2)) * 65536 + (pos > 2 ? pos - 3 : pos) * 512) +
                 (panel * 4 + wm * 2) * ts + ((g & 1) * 16 + i) * 16;
      ad[t][1] = ad[t][0] + ts;
      asm volatile("" : "+v"(ad[t][0]), "+v"(ad[t][1]));
    });
#pragma unroll
    for (int e = 0; e < 4; ++e) rs[e] *= qs;
#pragma unroll
    for (int t = 0; t < 4; ++t) {
#pragma unroll
      for (int mp = 0; mp < 2; ++mp) {
        const f32x4 u = (acc[mp * 2][t] + cs[t] * nm[mp * 2]) * rs[mp * 2], v = (acc[mp * 2 + 1][t] + cs[t] * nm[mp * 2 + 1]) * rs[mp * 2 + 1];
        st16(c.r, ad[t][mp], pair16(pk2(u[0], u[1]), pk2(u[2], u[3]), pk2(v[0], v[1]), pk2(v[2], v[3])));
      }
    }
  } else {
    float cs[4]; f32x4 rs[4], nm[4]; unsigned ad[4];
    qkv_loop<false>(c, w, layer, acc, [&]() {
#pragma unroll
      for (int e = 0; e < 4; ++e) { cs[e] = scs[e * 16 + i]; rs[e] = *(const LAS f32x4*)(srs + e * 16 + g * 4); nm[e] = *(const LAS f32x4*)(srs + 128 + e * 16 + g * 4); }
    }, [&](int q) {
      const int t = q >> 2, cv = (cb - 10) * 128 + wn * 64 + t * 16 + i, head = cv / 20, dim = cv - head * 20;
      const int d = (head & 1) ? (dim < 4 ? dim + 16 : dim - 4) : dim;
      ad[t] = L + L_V + head * 81920 + (panel * 8 + wm * 4 + (g & 1)) * 640 + (g >> 1) * 320 + d * 16;
      PIN(ad[t]);
    });
#pragma unroll
    for (int t = 0; t < 4; ++t) {
      const unsigned a0 = ad[t];
#pragma unroll
      for (int mp = 0; mp < 2; ++mp) {
        const f32x4 u = (acc[mp * 2][t] + nm[mp * 2] * cs[t]) * rs[mp * 2], v = (acc[mp * 2 + 1][t] + nm[mp * 2 + 1] * cs[t]) * rs[mp * 2 + 1];
        st16(c.r, a0 + mp * 1280, pair16(pk2(u[0], u[1]), pk2(u[2], u[3]), pk2(v[0], v[1]), pk2(v[2], v[3])));
      }
    }
  }
  if (layer == 0) {
    const u32x2 t = *(const LAS u32x2*)(c.lds + M_TGT);
    c.tgt = tgt = (u64)(unsigned)__builtin_amdgcn_readfirstlane(t[1]) << 32 | (unsigned)__builtin_amdgcn_readfirstlane(t[0]);
  }
  publish(c, M_CNT4, 4, flg(FL_QKV + layer * 5, panel * 16 + cb), true);
}
DI void qkv_load0(const Ctx& c, int lw, const uint16_t* x, const uint16_t* wqkv) {
  const int half = lw & 1;
  if (lw >= 2) {
    Ring<8, QKV_NB, 10, 2> rg;
#pragma unroll
    for (int p = 0; p < 8; ++p) rg.voff[p] = qkv_w_off(c, half * 8 + p);
    rg.init((const char*)wqkv, 64 * 3840, 0, half);
    rg.template fill<2>(); wait_vm<8>(); BARRIER(); rg.issue(); wait_vm<8>(); BARRIER(); rg.template fill<2>();
    rg.steady(); rg.template range<2, 0>(); BARRIER();
  } else {
    Ring<8, QKV_NA, 10, 2> rg;
#pragma unroll
    for (int p = 0; p < 8; ++p) {
      const int mt = qkv_panel(c) * 8 + half * 4 + (p >> 1), r16 = (p & 1) * 8 + (c.lane >> 3);
      rg.voff[p] = (mt * 16 + r16) * 1280 + (((c.lane & 7) ^ ((r16 >> 1) & 7)) << 4);
    }
    rg.init((const char*)x, 128, QKV_A, half);
    rg.template fill<2>(); wait_vm<8>(); BARRIER(); rg.issue(); wait_vm<8>(); BARRIER(); rg.issue();
    rg.steady(); rg.template range<1, 0>(); BARRIER();
  }
}
DI void qkv_load1(const Ctx& c, int lw, const uint16_t* wqkv) {
  const int half = lw & 1;
  static_assert(QKV_NA == 4 && QKV_NB == 5, "schedule");
  if (lw >= 2) {
    Ring<8, QKV_NB, 10, 2> rg;
#pragma unroll
    for (int p = 0; p < 8; ++p) rg.voff[p] = qkv_w_off(c, half * 8 + p);
    rg.init((const char*)wqkv, 64 * 3840, 0, half);
    sig_wait(c, 12, M_CNT4);
    rg.template fill_ahead<1>(c, 6);
    rg.rest();
  } else {
    Ring<8, QKV_NA, 10, 2> rg;
#pragma unroll
    for (int p = 0; p < 8; ++p) rg.voff[p] = WS_X2 + (qkv_panel(c) * 8 + half * 4 + (p >> 1)) * 20480 + (p & 1) * 1024 + c.lane * 16;
    rg.init(c.ws, 2048, QKV_A, half);
    rg.template start<2>(c, half, 4 * 20480, flg(FL_DN, qkv_panel(c) * 16 + (c.lane & 15)), 6, 28);
    rg.template run<2>();
  }
}

DI f32x16 qk8(s16x4 k2, s16x4 q2) { return __builtin_amdgcn_mfma_f32_32x32x8bf16_1k(k2, q2, f32x16{}, 0, 0, 0); }
DI f32x16 qk16(bf16x8 k1, bf16x8 q1, f32x16 s) { return __builtin_amdgcn_mfma_f32_32x32x16_bf16(k1, q1, s, 0, 0, 0); }
DI f32x16 pv_lo(bf16x8 v, f32x16 p, f32x16 o) {
  return __builtin_amdgcn_smfmac_f32_32x32x32_bf16(v, __builtin_bit_cast(bf16x16, (f32x8)__builtin_shufflevector(p, p, 0, 1, 2, 3, 4, 5, 6, 7)), o, 0xDDDD, 0, 0);
}
DI f32x16 pv_hi(bf16x8 v, f32x16 p, f32x16 o) {
  return __builtin_amdgcn_smfmac_f32_32x32x32_bf16(v, __builtin_bit_cast(bf16x16, (f32x8)__builtin_shufflevector(p, p, 8, 9, 10, 11, 12, 13, 14, 15)), o, 0xDDDD, 0, 0);
}
DI float swap32(float v, int h) {
  auto r = __builtin_amdgcn_permlane32_swap(__float_as_uint(v), __float_as_uint(v), false, false);
  return __uint_as_float(h ? r[0] : r[1]);
}
#define EXP4(X, i) do { X[i] = __builtin_amdgcn_exp2f(X[i]); X[i + 1] = __builtin_amdgcn_exp2f(X[i + 1]); X[i + 2] = __builtin_amdgcn_exp2f(X[i + 2]); \
                        X[i + 3] = __builtin_amdgcn_exp2f(X[i + 3]); PIN(X); SBAR(); } while (0)
DI void att_compute(const Ctx& c, int w, int layer) {
  const int lane = c.lane, head = att_head(c), qb = att_qb(c);
  const int qp = w & 3, kp = w >> 2, r32 = lane & 31, h = lane >> 5;
  lds_ptr lds = c.lds;
  const unsigned L = WS_L + layer * L_SZ;
  for (int s = w * 64 + lane; s < 672; s += 512) {
    const unsigned v = s % 20 == 0 ? 0x3f803f80u : 0u;
    *(LAS u32x4*)(lds + AT_CONST + s * 16) = u32x4{v, v, v, v};
  }
  lds_ptr pk1 = lds + kp * 8192 + lane * 16;
  lds_ptr pk2_ = h ? lds + AT_CONST + 16 : lds + 16384 + kp * 4096 + r32 * 16 + (head & 1) * 8;
  lds_ptr pv = (r32 >= 20 ? lds + AT_CONST : lds + 24576 + kp * 10240) + h * 320 + r32 * 16;
  const int vmask = r32 >= 20 ? 0 : -1, kmask = h ? 0 : -1;
  const int qt = qb * 8 + qp * 2 + kp, tok = qt * 32 + r32;
  unsigned ob16 = L + L_O + (tok >> 4) * 20480 + (head * 2 + h) * 256 + (tok & 15) * 16, ob8 = L + L_O + (tok >> 4) * 20480 + 16384 + head * 128 + (tok & 15) * 8;
  asm volatile("" : "+v"(ob16), "+v"(ob8));
  f32x16 o0 = f32x16{}, o1 = f32x16{};
  f32x16 s0, s1, s2, s3 = f32x16{};
  BARRIER();
  const bf16x8 q1a = ldsr16(lds + 2 * AT_SLOT + (qp * 2 + kp) * 1024 + lane * 16), q1b = ldsr16(lds + 2 * AT_SLOT + (qp * 2 + 1 - kp) * 1024 + lane * 16);
  lds_ptr pq2 = h ? lds + AT_CONST + 16 : lds + 2 * AT_SLOT + 8192 + qp * 1024 + r32 * 16 + (head & 1) * 8;
  const s16x4 q2a = *(const LAS s16x4*)(pq2 + kp * 512), q2b = *(const LAS s16x4*)(pq2 + 512 - kp * 512);
  bf16x8 k1 = ldsr16(pk1), vlo = ldsr16(pv), vhi = bf16x8{};
  s16x4 k2 = *(const LAS s16x4*)pk2_;
  s0 = qk16(k1, q1a, qk8(k2, q2a));
#define PH_EVEN(SC, SN, SP, KLOAD, VOFF) do { \
    SN = qk8(k2, q2b); SBAR(); EXP4(SC, 0); SN = qk16(k1, q1b, SN); SBAR(); KLOAD; SBAR(); EXP4(SC, 4); \
    o1 = pv_hi(vhi, SP, o1); SBAR(); vhi = ldsr16(pv + (VOFF) + 640); SBAR(); EXP4(SC, 8); o0 = pv_lo(vlo, SC, o0); SBAR(); EXP4(SC, 12); } while (0)
#define PH_ODD(SC, SN, SP, VLOAD) do { \
    SN = qk8(k2, q2a); SBAR(); EXP4(SC, 0); SN = qk16(k1, q1a, SN); SBAR(); EXP4(SC, 4); \
    o0 = pv_hi(vhi, SP, o0); SBAR(); EXP4(SC, 8); o1 = pv_lo(vlo, SC, o1); SBAR(); VLOAD; SBAR(); EXP4(SC, 12); } while (0)
#define KLD(t) do { k1 = ldsr16(pk1 + (t) * 1024); k2 = *(const LAS s16x4*)(pk2_ + (t) * 512); } while (0)
#define PRI(v) do { __builtin_amdgcn_s_setprio(v); SBAR(); } while (0)
#define TWO(t) PRI(1); PH_EVEN(s0, s1, s3, KLD((t) + 1), (t) * 1280); PH_ODD(s1, s2, s0, vlo = ldsr16(pv + ((t) + 1) * 1280));
  for (int grp = 0; grp < 4; ++grp) {
    TWO(0) PH_EVEN(s2, s3, s1, if (grp == 0) BARRIER_NW(); KLD(2), 1280); PRI(0); PH_ODD(s3, s0, s2, vlo = ldsr16(pv + 2 * 1280));
    TWO(2) PH_EVEN(s2, s3, s1, KLD(4), 3 * 1280); PRI(0); PH_ODD(s3, s0, s2, vlo = ldsr16(pv + 4 * 1280));
    TWO(4) PH_EVEN(s2, s3, s1, KLD(6), 5 * 1280); PRI(0); PH_ODD(s3, s0, s2, vlo = ldsr16(pv + 6 * 1280));
    TWO(6)
    const int d = grp == 2 ? -2 * AT_SLOT : grp == 3 ? 0 : AT_SLOT;
    PH_EVEN(s2, s3, s1, pk1 += d; pk2_ += (d & kmask); if (grp != 3) BARRIER(); KLD(0), 7 * 1280);
    PRI(0); PH_ODD(s3, s0, s2, pv += (d & vmask); vlo = ldsr16(pv));
  }
  o1 = pv_hi(vhi, s3, o1);
  __builtin_amdgcn_s_setprio(0);
  const f32x16 give = o1, keep = o0;
#pragma unroll
  for (int e = 0; e < 3; ++e)
    *(LAS f32x4*)(lds + AT_MERGE + (w * 3 + e) * 1024 + lane * 16) = f32x4{give[4 * e], give[4 * e + 1], give[4 * e + 2], give[4 * e + 3]};
  BARRIER();
  f32x4 a[3];
#pragma unroll
  for (int e = 0; e < 3; ++e)
    a[e] = f32x4{keep[4 * e], keep[4 * e + 1], keep[4 * e + 2], keep[4 * e + 3]} + *(const LAS f32x4*)(lds + AT_MERGE + ((w ^ 4) * 3 + e) * 1024 + lane * 16);
  float l = h ? a[2][0] : 0.f;
  l += swap32(l, h);
  if (__builtin_expect(__any(!(l >= 5.421e-20f && l <= 1.8447e19f)), 0)) {
    const unsigned o2 = (head >> 1) * 32768 + r32 * 16 + (head & 1) * 8;
    const bf16x8 fq1 = __builtin_bit_cast(bf16x8, ld16(c.r, L + L_Q1 + head * 65536 + qt * 1024 + lane * 16));
    u32x2 fq2 = ld8(c.r, L + L_Q2 + o2 + qt * 512);
    if (h) fq2 = u32x2{0u, 0u};
    const unsigned vi = L + L_V + head * 81920 + h * 320 + (r32 < 20 ? r32 : 0) * 16;
    f32x16 oo = f32x16{};
    float m = -INFINITY;
    const u32x4 one8 = {0x3f803f80u, 0x3f803f80u, 0x3f803f80u, 0x3f803f80u};
    for (int t = 0; t < 64; ++t) {
      const u32x4 fk1 = ld16(c.r, L + L_K1 + head * 65536 + t * 1024 + lane * 16);
      const u32x2 fk2 = ld8(c.r, L + L_K2 + o2 + t * 512);
      u32x4 v0 = ld16(c.r, vi + t * 1280), v1 = ld16(c.r, vi + t * 1280 + 640);
      if (r32 >= 20) { v0 = one8; v1 = one8; }
      f32x16 s = qk16(__builtin_bit_cast(bf16x8, fk1), fq1, qk8(__builtin_bit_cast(s16x4, fk2), __builtin_bit_cast(s16x4, fq2)));
      float tm = s[0];
#pragma unroll
      for (int r = 1; r < 16; ++r) tm = fmaxf(tm, s[r]);
      tm = fmaxf(tm, swap32(tm, h));
      const float mn = fmaxf(m, tm), al = __builtin_amdgcn_exp2f(m - mn);
      m = mn;
#pragma unroll
      for (int r = 0; r < 16; ++r) { s[r] = __builtin_amdgcn_exp2f(s[r] - mn); oo[r] *= al; }
      oo = pv_hi(__builtin_bit_cast(bf16x8, v1), s, pv_lo(__builtin_bit_cast(bf16x8, v0), s, oo));
    }
#pragma unroll
    for (int e = 0; e < 3; ++e) a[e] = f32x4{oo[4 * e], oo[4 * e + 1], oo[4 * e + 2], oo[4 * e + 3]};
    l = h ? a[2][0] : 0.f;
    l += swap32(l, h);
  }
  const float inv = __builtin_amdgcn_rcpf(l);
  const unsigned A0 = pk2(a[0][0] * inv, a[0][1] * inv), A1 = pk2(a[0][2] * inv, a[0][3] * inv);
  const unsigned B0 = pk2(a[1][0] * inv, a[1][1] * inv), B1 = pk2(a[1][2] * inv, a[1][3] * inv);
  const u32x2 C = {pk2(a[2][0] * inv, a[2][1] * inv), pk2(a[2][2] * inv, a[2][3] * inv)};
  st16(c.r, ob16, pair32(A0, A1, B0, B1));
  if (!h) st8(c.r, ob8, C);
  publish(c, M_CNT, 8, flg(FL_ATT + layer * 5, qb * 32 + head), true);
}
DI void retile_load(const Ctx& c, int J, u32x4 (&v)[5][4], const uint16_t* wo, const uint16_t* wg, const uint16_t* wu, const uint16_t* wd) {
  const int e = J < 216 ? J : J < 256 ? J - 216 : J - 256;
  const int strip = J < 216 ? (e >> 2) % 27 : e % 10, k0 = J < 216 ? (e & 3) * 5 : min((e / 10) * 5, 49), ld = J < 216 ? 3456 : 1280;
  const rsrc_t r = __builtin_amdgcn_make_buffer_rsrc((void*)(J < 108 ? wg : J < 216 ? wu : J < 256 ? wo : wd), 0,
                                                     J < 216 ? 640 * 3456 : J < 256 ? 640 * 1280 : 1728 * 1280, 0x00020000);
  const unsigned vo = (c.lane >> 3) * ld + (c.lane & 7) * 16;
#pragma unroll
  for (int u = 0; u < 5; ++u)
#pragma unroll
    for (int q = 0; q < 4; ++q) {
      const int ch = (k0 + u) * 4 + q;
      const int row = J < 216 || J >= 256 ? ch * 8 : ch < 64 ? (ch >> 1) * 20 + (ch & 2) * 2 + (ch & 1) * 8 : (ch - 64) * 40 + 16;
      v[u][q] = __builtin_amdgcn_raw_buffer_load_b128(r, vo, row * ld + strip * 128, 0);
    }
}
DI void retile_store(const Ctx& c, int J, lds_ptr scr, const u32x4 (&v)[5][4]) {
  const int e = J < 216 ? J : J < 256 ? J - 216 : J - 256, mat = J >= 108;
  const int strip = J < 216 ? (e >> 2) % 27 : e % 10, k0 = J < 216 ? (e & 3) * 5 : min((e / 10) * 5, 49);
  const unsigned ts = J < 216 ? 40960 : J < 256 ? 20480 : 55296;
  const unsigned dst = (J < 216 ? WS_WGU + mat * 20480 : J < 256 ? WS_WO : WS_WD) + strip * 4 * ts + k0 * 1024;
  const int i = c.lane & 15, g = c.lane >> 4;
  const bf16x8 ones = __builtin_bit_cast(bf16x8, u32x4{0x3f803f80u, 0x3f803f80u, 0x3f803f80u, 0x3f803f80u});
  f32x4 cs[4] = {f32x4{}, f32x4{}, f32x4{}, f32x4{}};
#pragma unroll
  for (int u = 0; u < 5; ++u) {
#pragma unroll
    for (int p = 0; p < 4; ++p) *(LAS u32x4*)(scr + p * 1024 + c.lane * 16) = v[u][p];
#pragma unroll
    for (int t = 0; t < 4; ++t) {
      const bf16x8 f = ldstr(scr + (g * 8 + (i >> 2)) * 128 + t * 32 + (i & 3) * 8, 512);
      __builtin_amdgcn_raw_buffer_store_b128(__builtin_bit_cast(u32x4, f), c.r, c.lane * 16, dst + u * 1024 + t * ts, 16);
      cs[t] = mfma16(f, ones, cs[t]);
    }
  }
  if (J < 216 && i == 0) {
#pragma unroll
    for (int t = 0; t < 4; ++t)
      __builtin_amdgcn_raw_buffer_store_b128(__builtin_bit_cast(u32x4, cs[t]), c.r, g * 16, WS_CSG + ((e & 3) * 3456 + (strip * 8 + t * 2 + mat) * 16) * 4, 16);
  }
}
DI int early_piece(int e) { return e < 4 ? (e & 1) + (e >> 1) * 8 : e < 6 ? 16 + (e - 4) * 4 : e < 9 ? 18 + e : 25 + e; }
DI int late_piece(int e) { return e < 6 ? 2 + e : e < 12 ? 4 + e : e < 15 ? 5 + e : e < 18 ? 6 + e : e < 25 ? 9 + e : 12 + e; }
DI void att_load(const Ctx& c, int lw, int layer, unsigned iss, const uint16_t* wo, const uint16_t* wg, const uint16_t* wu, const uint16_t* wd) {
  const int head = att_head(c), qb = att_qb(c), lane = c.lane;
  const char* Lp = c.ws + WS_L + layer * L_SZ;
  const unsigned vo = lane * 16;
  int grp = qb >> 1;
  auto src_of = [&](int p) {
    return p < 16 ? L_K1 + head * 65536 + grp * 16384 + p * 1024
                  : p < 24 ? L_K2 + (head >> 1) * 32768 + grp * 8192 + (p - 16) * 1024 : L_V + head * 81920 + grp * 20480 + (p - 24) * 1024;
  };
  unsigned v0[14], m0[11], vg[11], sg[11];
#pragma unroll
  for (int q = 0; q < 11; ++q) {
    const int p = q < 3 ? early_piece(lw * 3 + q) : late_piece(lw * 8 + q - 3), pg = lw * 11 + q;
    v0[q] = src_of(p) + vo; m0[q] = p * 1024;
    vg[q] = src_of(pg) + vo; sg[q] = pg < 16 ? 16384 : pg < 24 ? 8192 : 20480;
    asm volatile("" : "+v"(v0[q]), "+s"(m0[q]));
  }
#pragma unroll
  for (int q = 0; q < 3; ++q) {
    const int p = lw * 3 + q;
    v0[11 + q] = (p < 8 ? L_Q1 + head * 65536 + qb * 8192 + p * 1024 : L_Q2 + (head >> 1) * 32768 + qb * 4096 + (p - 8) * 1024) + vo - q * 1024;
    PIN(v0[11 + q]);
  }
  auto issue = [&](int slot) {
#pragma unroll
    for (int q = 0; q < 11; ++q) { vg[q] += grp == 3 ? -3 * sg[q] : sg[q]; glds1(slot * AT_SLOT + (lw * 11 + q) * 1024, Lp, vg[q]); }
    grp = (grp + 1) & 3;
  };
  const int o0 = (head >> 1) * 5 + (head & 1) * 2, c0 = head * 20;
  auto kv_cb = [&](int sel) { return sel == 0 ? 5 + (o0 >> 4) : sel == 1 ? 5 + ((o0 + 2) >> 4) : sel == 2 ? 10 + (c0 >> 7) : 10 + ((c0 + 19) >> 7); };
  if (lw == 0) {
    sig_wait(c, iss, M_ISS);
    poll(c, flg(FL_QKV + layer * 5, lane & 16 ? (qb * 2 + (lane & 1)) * 16 + ((o0 + (lane & 2)) >> 4) : (grp * 4 + (lane & 3)) * 16 + kv_cb((lane >> 2) & 3)));
    sig_set(c, layer * 5 + 2);
  } else sig_wait(c, layer * 5 + 2);
  glds3(2 * AT_SLOT + lw * 3072, Lp, v0[11], v0[12], v0[13]);
#pragma unroll
  for (int q = 0; q < 3; ++q) glds1(m0[q], Lp, v0[q]);
#pragma unroll
  for (int q = 3; q < 3 + ATT_LATE; ++q) glds1(m0[q], Lp, v0[q]);
  wait_vm<ATT_LATE>(); BARRIER();
#pragma unroll
  for (int q = 3 + ATT_LATE; q < 11; ++q) glds1(m0[q], Lp, v0[q]);
  wait_vm<0>(); BARRIER();
  if (lw == 0) { poll(c, flg(FL_QKV + layer * 5, (lane & 15) * 16 + kv_cb(lane >> 4))); sig_set(c, layer * 5 + 3); } else sig_wait(c, layer * 5 + 3, M_SIG, true);
  issue(1);
  const int J = (lw - 1) * 128 + c.j * 4 + (c.xcd >> 1);
  const bool rt = layer == 0 && lw > 0 && J < 366 && !(c.xcd & 1);
  u32x4 v[5][4];
  if (rt) { retile_load(c, J, v, wo, wg, wu, wd); wait_vm<20>(); } else wait_vm<0>();
  BARRIER();
  issue(2);
  if (rt) retile_store(c, J, c.lds + AT_SCR + (lw - 1) * 4096, v);
  if (layer == 0 && lw > 0) publish(c, M_CNT3, 3, flg(FL_RT, blockIdx.x));
  wait_vm<0>(); BARRIER();
  issue(0);
  wait_vm<0>();
  if (layer == 0) {
    if (lw == 0) {
      if (c.j == 0) {
#pragma unroll 1
        for (int q = 0; q < 4; ++q) poll(c, flg(FL_RT, q * 64 + lane));
        if (lane < 32) __hip_atomic_store((GAS u64*)(c.ws + flg(FL_RT2, lane * 8 + c.xcd)), c.tgt, __ATOMIC_RELAXED, __HIP_MEMORY_SCOPE_AGENT);
      }
      poll(c, flg(FL_RT2, blockIdx.x));
      if (blockIdx.x == 0 && lane == 0) *(u64*)(c.ws + WS_EPOCH) = c.tgt;
    }
  }
  BARRIER();
  u32x4 pk[25];
#pragma unroll
  for (int e = 0; e < 25; ++e) pk[e] = ld16s(c.r, vo, WS_WO + (pr_cb(c) * 5 + e % 5) * 20480 + (lw * 5 + e / 5) * 1024);
#pragma unroll 1
  for (unsigned q = lw; q < 35; q += 4) glds1(AT_SCR + lw * 1024, c.ws + WS_WGU + ((gu_cb(c) * 27) >> 2) * 40960 + ((c.j & 7) * 35 + q) * 1024, vo);
  wait_vm<0>();
  BARRIER();
#pragma unroll
  for (int e = 0; e < 25; ++e) PIN(pk[e]);
  auto dump = [&]() {
#pragma unroll
    for (int e = 0; e < 25; ++e) { const int k = lw * 5 + e / 5; ldsw16(c.lds + (k >> 1) * 10240 + (k & 1) * 1024 + (e % 5) * 2048 + vo, pk[e]); }
  };
  sig_wait(c, layer * 16 + 8, M_CNT);
  dump();
  if (lw == 0) poll(c, flg(FL_ATT + layer * 5, (pr_panel(c) >> 2) * 32 + (lane & 31)));
}

template <int NS, int NB, int WIN>
DI void proj_compute(const Ctx& c, int w, unsigned a_off, const char* res_rm, unsigned res_off, unsigned out_off, unsigned st_off,
                     uint16_t* out, int arr) {
  constexpr bool RES = NS <= NB;
  const int panel = pr_panel(c), cb = pr_cb(c);
  if (w >= 4) {
    if constexpr (RES) { BARRIER(); BARRIER(); }
    else {
      static_assert(RES || (NS == 27 && NB == 15 && WIN == 18), "schedule");
      const int par = (w >> 1) & 1, half = w & 1;
      u32x4 pk[15];
#pragma unroll
      for (int e = 0; e < 15; ++e) {
        const int q = half * 5 + e % 5;
        pk[e] = ld16s(c.r, c.lane * 16, WS_WD + (cb * 5 + (q >> 1)) * 55296 + ((15 + par + (e / 5) * 2) * 2 + (q & 1)) * 1024);
      }
      lds_ptr pw = c.lds + par * 10240 + half * 5120 + c.lane * 16;
      BARRIER(); BARRIER();
#pragma unroll
      for (int e = 0; e < 10; ++e) ldsw16(pw + (e / 5) * 20480 + (e % 5) * 1024, pk[e]);
      BARRIER();
#pragma unroll
      for (int e = 10; e < 15; ++e) ldsw16(pw + 40960 + (e % 5) * 1024, pk[e]);
      BARRIER(); BARRIER(); BARRIER(); BARRIER();
    }
    return;
  }
  const int lane = c.lane, i = lane & 15, g = lane >> 4, mt = panel * 4 + w;
  u32x2 res[5];
#pragma unroll
  for (int t = 0; t < 5; ++t)
    res[t] = res_rm ? *(const u32x2*)(res_rm + (mt * 16 + i) * 1280 + cb * 160 + t * 32 + g * 8)
                    : ld8(c.r, res_off + (mt * 80 + cb * 10 + t * 2 + (g >> 1)) * 256 + i * 16 + (g & 1) * 8);
  f32x4 acc[5];
#pragma unroll
  for (int t = 0; t < 5; ++t) { acc[t] = f32x4{bflo(res[t][0]), bfhi(res[t][0]), bflo(res[t][1]), bfhi(res[t][1])}; PIN(acc[t]); }
  float piv;
  { const f32x4 s4 = (acc[0] + acc[1]) + (acc[2] + acc[3]) + acc[4]; piv = rows4((s4[0] + s4[1]) + (s4[2] + s4[3])) * (1.f / 80.f); }
  const int ch = (g & 1) * 2 + (g >> 1);
  unsigned xo = out ? (mt * 16 + i) * 1280 + cb * 160 + ch * 16 : out_off + (mt * 80 + cb * 10 + ch) * 256 + i * 16;
  unsigned so = st_off + (mt >> 3) * 8192 + cb * 1024 + ((mt & 7) * 16 + i) * 8;
  asm volatile("" : "+v"(piv), "+v"(xo), "+v"(so));
  lds_ptr pb = c.lds + lane * 16;
  unsigned av = a_off + mt * (NS * 2048) + lane * 16;
  u32x4 a[WIN];
  bf16x8 f[3][5];
  int sb = 0;
  BARRIER();
  constexpr int AH = RES ? WO_AH : DN_AH;
  auto lda = [&](int k) {
    if (RES && k >= 16) {
      const unsigned o = a_off + mt * 20480 + 16384 + (k - 16) * 1024 + g * 256 + i * 8;
      const u32x2 lo = ld8(c.r, o), hi = ld8(c.r, o + 128);
      a[k % WIN] = u32x4{lo[0], lo[1], hi[0], hi[1]};
    } else a[k % WIN] = ld16(c.r, av + k * 1024);
  };
#pragma unroll
  for (int k = 0; k < AH; ++k) lda(k);
#pragma unroll
  for (int t = 0; t < 5; ++t) { f[0][t] = ldsr16(pb + t * 2048); f[1][t] = ldsr16(pb + t * 2048 + 1024); }
  SBAR();
  auto window = [&](bool first, bool last) {
#pragma unroll
    for (int k = 0; k < WIN; ++k) {
      const bool ld = !(last && k + 2 >= WIN);
      if ((k & 1) == 0) {
        if (RES) pb += 10240;
        else { pb += sb == NB - 1 ? -(NB - 1) * 10240 : 10240; sb = sb == NB - 1 ? 0 : sb + 1; }
        if (!ld) BARRIER(); else if (!RES && (k == 10 || (k == 0 && !first))) BARRIER_NW();
      }
#pragma unroll
      for (int t = 0; t < 5; ++t) {
        if (ld) f[(k + 2) % 3][t] = ldsr16(pb + t * 2048 + (k & 1) * 1024);
        acc[t] = mfma16(f[k % 3][t], __builtin_bit_cast(bf16x8, a[k]), acc[t]);
        SBAR();
      }
      if (!last || k + AH < WIN) { lda(k + AH); SBAR(); }
    }
    av += WIN * 1024;
  };
  static_assert(2 * NS % WIN == 0 && (2 * NS == WIN || WIN % 6 == 0), "window");
#pragma unroll 1
  for (int b = 1; b < 2 * NS / WIN; ++b) window(b == 1, false);
  window(false, true);
  u32x2 o[5];
#pragma unroll
  for (int t = 0; t < 5; ++t) {
    o[t][0] = pk2(acc[t][0], acc[t][1]); o[t][1] = pk2(acc[t][2], acc[t][3]);
  }
  const u32x4 p0 = pair16(o[0][0], o[0][1], o[1][0], o[1][1]), p1 = pair16(o[2][0], o[2][1], o[3][0], o[3][1]),
              p2 = pair16(o[4][0], o[4][1], o[4][0], o[4][1]);
  if (out) {
    char* po = (char*)out + xo;
    *(u32x4*)po = p0; *(u32x4*)(po + 64) = p1;
    if (!(g & 1)) *(u32x4*)(po + 128) = p2;
    return;
  }
  st16(c.r, xo, p0); st16(c.r, xo + 1024, p1);
  if (!(g & 1)) st16(c.r, xo + 2048, p2);
  f32x4 s4 = acc[0] - piv, q4 = s4 * s4;
#pragma unroll
  for (int t = 1; t < 5; ++t) { const f32x4 d = acc[t] - piv; s4 += d; q4 += d * d; }
  const float sd = rows4((s4[0] + s4[1]) + (s4[2] + s4[3])), sq = rows4((q4[0] + q4[1]) + (q4[2] + q4[3]));
  if (g == 0) st8(c.r, so, u32x2{__float_as_uint(sd + 80.f * piv), __float_as_uint(fmaxf(sq - sd * sd * (1.f / 80.f), 0.f))});
  publish(c, M_CNT4, 4, flg(arr, panel * 8 + cb), true);
}
template <int NF>
DI void gu_consumer(const Ctx& c, int layer, int f0, int mq) {
  const int panel = gu_panel(c), ft0 = (gu_cb(c) * 27) >> 2;
  const int lane = c.lane, i = lane & 15, g = lane >> 4;
  f32x4 acc[2][2 * NF];
#pragma unroll
  for (int m = 0; m < 2; ++m)
#pragma unroll
    for (int t = 0; t < 2 * NF; ++t) acc[m][t] = f32x4{0.f, 0.f, 0.f, 0.f};
  lds_ptr pa = c.lds + GU_A + mq * 2048 + lane * 16, pb = c.lds + f0 * 2048 + lane * 16;
  bf16x8 x0[2], x1[2], wa[NF], wb0[NF], wb1[NF];
  auto step = [&](const bf16x8* xa, const bf16x8* wb, bf16x8* xn, bf16x8* wn_, bool ld) {
#pragma unroll
    for (int q = 0; q < 4 * NF; ++q) {
      const int t = q >> 1, m = q & 1;
      if (ld) {
        if (q < 2) xn[q] = ldsr16(pa + q * 1024);
        else if (q < 2 * NF) { const int e = NF == 4 ? (q == 3 ? 0 : q == 4 ? 1 : q == 6 ? 2 : q == 7 ? 3 : -1) : NF == 3 ? q - 3 : q - 2; if (e >= 0) wn_[e] = ldsr16(pb + (NF + e) * 1024); }
        else if (q & 1) wa[(q - 2 * NF) >> 1] = ldsr16(pb + ((q - 2 * NF) >> 1) * 1024);
      }
      acc[m][t] = mfma16(t < NF ? wa[t] : wb[t - NF], xa[m], acc[m][t]);
      SBAR();
    }
  };
  int sl = 0;
  auto adv = [&]() {
    pa += sl == GU_NA - 1 ? -(GU_NA - 1) * 8192 : 8192; pb += sl == GU_NB - 1 ? -(GU_NB - 1) * 14336 : 14336;
    sl = sl == GU_NA - 1 ? 0 : sl + 1;
  };
  unsigned ho = WS_L + layer * L_SZ + L_H + ((panel * 8 + mq * 2 + (g & 1)) * 216 + (ft0 + f0) * 2 + (g >> 1)) * 256 + i * 16;
  PIN(ho);
  BARRIER();
  x0[0] = ldsr16(pa); x0[1] = ldsr16(pa + 1024);
#pragma unroll
  for (int t = 0; t < NF; ++t) { wa[t] = ldsr16(pb + t * 1024); wb0[t] = ldsr16(pb + (NF + t) * 1024); }
#pragma unroll 1
  for (int st = 0; st < 9; ++st) {
    adv(); BARRIER_NW(); step(x0, wb0, x1, wb1, true);
    adv(); BARRIER_NW(); step(x1, wb1, x0, wb0, true);
  }
  adv(); BARRIER_NW(); step(x0, wb0, x1, wb1, true);
  BARRIER();
  const LAS float* srs = (const LAS float*)(c.lds + M_SST) + mq * 32 + i;
  const LAS float* scs = (const LAS float*)(c.lds + M_SCS) + f0 * 32 + g * 4;
  float rs[2] = {srs[0], srs[16]}; const float nm[2] = {srs[128], srs[144]};
  f32x4 cs[2 * NF];
#pragma unroll
  for (int e = 0; e < 2 * NF; ++e) cs[e] = *(const LAS f32x4*)(scs + e * 16);
  SBAR();
  step(x1, wb1, x0, wb0, false);
  rs[0] *= rs[0]; rs[1] *= rs[1];
#pragma unroll
  for (int f = 0; f < NF; ++f) {
    unsigned p[2][2];
#pragma unroll
    for (int m = 0; m < 2; ++m) {
      f32x4 gt = acc[m][2 * f] + cs[2 * f] * nm[m];
      const f32x4 up = (acc[m][2 * f + 1] + cs[2 * f + 1] * nm[m]) * rs[m];
      gt = __builtin_elementwise_max(gt, f32x4{0.f, 0.f, 0.f, 0.f}) * up;
      p[m][0] = pk2(gt[0], gt[1]); p[m][1] = pk2(gt[2], gt[3]);
    }
    st16(c.r, ho + f * 512, pair16(p[0][0], p[0][1], p[1][0], p[1][1]));
    SBAR();
  }
  publish(c, M_CNT, 8, flg(FL_GU + layer * 5, panel * 16 + gu_cb(c)), true);
}
DI void mlp_load(const Ctx& c0, int lw, int layer, unsigned iss, bool qkv_next, const uint16_t* wqkv) {
  const int half = lw & 1;
  {
    BARRIER(); BARRIER();
  }
  {
    const Ctx c = fresh(c0);
    const int ft0 = (gu_cb(c) * 27) >> 2;
    u32x4 pk[20];
    auto park = [&](int e0) {
#pragma unroll
      for (int e = e0; e < e0 + 4; ++e) {
        const int q = half * 5 + e % 5;
        pk[e] = ld16s(c.r, c.lane * 16, WS_WD + (pr_cb(c) * 5 + (q >> 1)) * 55296 + (((e / 5) * 2 + (lw >> 1)) * 2 + (q & 1)) * 1024);
      }
    };
    static_assert(GU_NA == 7 && GU_NB == 7, "schedule");
    if (lw >= 2) {
      Ring<7, GU_NB, 20, 2> rg;
#pragma unroll
      for (int p = 0; p < 7; ++p) rg.voff[p] = WS_WGU + (ft0 * 2 + half * 7 + p) * 20480 + c.lane * 16;
      rg.init(c.ws, 1024, 0, half);
      if (half == 0) ln_cols<4>(c, 56, WS_CSG + ft0 * 128, 13824);
      sig_wait(c, iss - layer * 16 + 4, M_CNT4);
      rg.template fill_ahead<1>(c, layer * 5 + 4);
      rg.template range<5, 5>(); rg.steady();
      wait_vm<28>(); BARRIER(); park(0);
      wait_vm<21 + 4>(); BARRIER(); park(4);
      wait_vm<14 + 8>(); BARRIER(); park(8);
      wait_vm<7 + 12>(); BARRIER(); park(12);
      wait_vm<16>(); BARRIER(); park(16);
    } else {
      Ring<4, GU_NA, 20, 2> rg;
#pragma unroll
      for (int p = 0; p < 4; ++p) rg.voff[p] = WS_L + layer * L_SZ + L_X1 + (gu_panel(c) * 8 + half * 4 + p) * 20480 + c.lane * 16;
      rg.init(c.ws, 1024, GU_A, half);
      rg.template start<3>(c, half, 4 * 20480, flg(FL_WO + layer * 5, gu_panel(c) * 16 + (c.lane & 15)), layer * 5 + 4, iss + 12);
      rg.template head<3>();
      f32x2 st[8];
      wait_vm<16>(); BARRIER();
      ln_load(c, WS_ST + (layer * 2 + 1) * STB + gu_panel(c) * 8192, half * 64 + c.lane, st);
      wait_vm<12 + 8>(); BARRIER(); park(0); park(4);
      wait_vm<8 + 8 + 8>(); BARRIER();
      const f32x2 t = ln_sum(st);
      wait_vm<8>(); BARRIER(); ln_put(c, half * 64 + c.lane, t); park(8);
      BARRIER(); park(12); park(16);
    }
    BARRIER();
#pragma unroll
    for (int e = 0; e < 20; ++e) PIN(pk[e]);
    auto dump = [&]() {
#pragma unroll
      for (int e = 0; e < 20; ++e) ldsw16(c.lds + ((e / 5) * 2 + (lw >> 1)) * 10240 + (half * 5 + e % 5) * 1024 + c.lane * 16, pk[e]);
    };
    if (lw >= 2) {
      Ring<5, DN_NB, 27, 2> rg;
#pragma unroll
      for (int p = 0; p < 5; ++p) { const int q = half * 5 + p; rg.voff[p] = WS_WD + (pr_cb(c) * 5 + (q >> 1)) * 55296 + (q & 1) * 1024 + c.lane * 16; }
      rg.init(c.ws + 8 * 2048, 2048, 0, half);
      rg.slot = 8;
      sig_wait(c, layer * 16 + 16, M_CNT);
      rg.template fill<3>(); dump(); rg.template fill<4>();
      BARRIER();
      wait_vm<5 * 5>(); BARRIER();
      wait_vm<0>(); BARRIER();
      rg.base += 6 * 2048; rg.slot = 6;
      rg.template fill<3>();
      BARRIER();
      rg.template fill<3>();
      wait_vm<3 * 5>(); BARRIER();
      wait_vm<0>(); BARRIER();
      BARRIER();
    } else {
      sig_wait(c, layer * 16 + 16, M_CNT);
      dump();
      if (lw == 0) poll(c, flg(FL_GU + layer * 5, (pr_panel(c) >> 1) * 16 + (c.lane & 15)));
#pragma unroll 1
      for (int k = 0; k < 7; ++k) BARRIER();
    }
  }
  if (qkv_next) qkv_load1(fresh(c0), lw, wqkv);
}

__global__ __launch_bounds__(768, 3) void fused_kernel(const uint16_t* __restrict__ x, const uint16_t* __restrict__ wqkv, const uint16_t* __restrict__ wo,
                                                    const uint16_t* __restrict__ wg, const uint16_t* __restrict__ wu,
                                                    const uint16_t* __restrict__ wd, uint16_t* __restrict__ out, char* __restrict__ ws) {
  extern __shared__ __attribute__((aligned(16))) char smem[];
  const char* pc;
  asm volatile("s_getpc_b64 %0" : "=s"(pc));
  glds1(P0_SCR + 10240 + 1024, (const char*)((u64)pc & ~15ull), ((blockIdx.x >> 3) * 12 + (threadIdx.x >> 6)) % PF_KB * 1024 + (threadIdx.x & 63) * 16);
  Ctx c;
  c.ws = ws; c.lds = (lds_ptr)smem; c.lane = threadIdx.x & 63; c.xcd = blockIdx.x & 7; c.j = blockIdx.x >> 3;
  c.r = __builtin_amdgcn_make_buffer_rsrc(ws, 0, WS_END, 0x00020000);
  const int w = __builtin_amdgcn_readfirstlane(threadIdx.x >> 6);
  c.tgt = 0;
  if (threadIdx.x < 16) *(LAS unsigned*)(c.lds + M_CNT + threadIdx.x * 4) = 0u;
  const bool qkv_on = qkv_cb(c) != 15;
  Ctx c0 = c;
  if (__builtin_expect(w >= 8, 1)) {
    __builtin_amdgcn_s_setprio(3);
    if (qkv_on) qkv_load0(fresh(c0), w - 8, x, wqkv); else BARRIER();
    c0.tgt = epoch(ws);
#pragma unroll 1
    for (int layer = 0; layer < 2; ++layer) {
      const unsigned iss = layer * 24 + (qkv_on ? (layer + 1) * 4 : 0);
      att_load(fresh(c0), opq(w - 8), opq(layer), iss, wo, wg, wu, wd);
      mlp_load(c0, opq(w - 8), opq(layer), iss, layer == 0 && qkv_on, wqkv);
    }
  } else {
    if (w == 2 || w == 3)
      asm volatile(GL_HEAD "global_load_lds_dword %2, %1 sc1" ::"s"(P0_SCR + 10240), "s"(ws),
                   "v"(min(((w - 2) * 64 + fresh(c0).lane) * 524288u + 2048, WS_END)) : "memory");
#pragma unroll 1
    for (int layer = 0; layer < 2; ++layer) {
      const unsigned L = WS_L + layer * L_SZ;
      qkv_compute(fresh(c0), c0.tgt, opq(w), opq(layer), x, wqkv);
      att_compute(fresh(c0), opq(w), opq(layer));
      proj_compute<10, WO_NB, 20>(fresh(c0), opq(w), L + L_O, layer == 0 ? (const char*)x : nullptr, WS_X2, L + L_X1,
                                  WS_ST + (layer * 2 + 1) * STB, nullptr, FL_WO + layer * 5);
      if (w < 4) gu_consumer<4>(fresh(c0), opq(layer), 0, opq(w));
      else if ((gu_cb(c0) & 3) == 0) gu_consumer<2>(fresh(c0), opq(layer), 4, opq(w - 4));
      else gu_consumer<3>(fresh(c0), opq(layer), 4, opq(w - 4));
      proj_compute<27, DN_NB, 18>(fresh(c0), opq(w), L + L_H, nullptr, L + L_X1, WS_X2, WS_ST + 2 * STB, layer ? out : nullptr,
                                  FL_DN + layer * 5);
    }
  }
}

extern "C" void kernel_launch(void* const* d_in, const int* in_sizes, int n_in, void* d_out, int out_size, void* d_ws, size_t ws_size,
                              hipStream_t stream) {
  (void)hipFuncSetAttribute((const void*)fused_kernel, hipFuncAttributeMaxDynamicSharedMemorySize, LDS_BYTES);
  fused_kernel<<<256, 768, LDS_BYTES, stream>>>((const uint16_t*)d_in[0], (const uint16_t*)d_in[1], (const uint16_t*)d_in[2],
                                                (const uint16_t*)d_in[4], (const uint16_t*)d_in[5], (const uint16_t*)d_in[6],
                                                (uint16_t*)d_out, (char*)d_ws);
}
```
